```python
import torch

def _softmax(x, axis=-1):
    return torch.softmax(x, dim=axis)
def _gelu(x):
    return torch.nn.functional.gelu(x, approximate="tanh")
def _silu(x):
    return torch.nn.functional.silu(x)
def _sigmoid(x):
    return torch.sigmoid(x)
def _layernorm(x, axis=-1, eps=1e-5):
    m = x.mean(dim=axis, keepdim=True)
    v = x.var(dim=axis, keepdim=True, unbiased=False)
    return (x - m) / torch.sqrt(v + eps)
def _scatter_add(tgt, idx, src, axis=0):
    return tgt.clone().index_add_(axis, idx, src)
def _segment_sum(src, seg, n):
    out = torch.zeros((n,) + src.shape[1:], dtype=src.dtype)
    return out.index_add_(0, seg, src)
def _pad(x, pw):
    flat = tuple(v for pair in reversed(pw) for v in pair)
    return torch.nn.functional.pad(x, flat)

kernel_name = 'concat_heads__sliding_block__spectral_fnet__prompt'

def setup_inputs():

    _w = (lambda a: (lambda t: t.to(torch.bfloat16) if t.is_floating_point() else t)(torch.as_tensor(a)))
    def _A_setup():
        B, S, D = 1, 2048, 448
        return {
            "x": _w((lambda *s: torch.randn(*s, dtype=torch.bfloat16) * 0.2)(B, S, D)),
            "wqkv": _w((lambda *s: torch.randn(*s, dtype=torch.bfloat16) * 0.8)(D, 3 * D)),
            "wo": _w((lambda *s: torch.randn(*s, dtype=torch.bfloat16) * 0.2)(D, D)),
        }
    _A_in = _A_setup()

    def _B_setup():
        B, S, D = 1, 2048, 448
        ns = torch.arange(S)[:, None] * torch.arange(S)[None, :]
        nd = torch.arange(D)[:, None] * torch.arange(D)[None, :]
        Ws_re = torch.cos(-2.0 * torch.pi * ns / S).to(float)
        Ws_im = torch.sin(-2.0 * torch.pi * ns / S).to(float)
        Wd_re = torch.cos(-2.0 * torch.pi * nd / D).to(float)
        Wd_im = torch.sin(-2.0 * torch.pi * nd / D).to(float)
        return {
            "x": _w((lambda *s: torch.randn(*s, dtype=torch.bfloat16) * 0.05)(B, S, D)),
            "dft_seq_re": _w(Ws_re),
            "dft_seq_im": _w(Ws_im),
            "dft_hid_re": _w(Wd_re),
            "dft_hid_im": _w(Wd_im),
        }
    _B_in = _B_setup()

    return {
        "A_x": _A_in["x"],
        "A_wqkv": _A_in["wqkv"],
        "A_wo": _A_in["wo"],
        "B_x": _B_in["x"],
        "B_dft_seq_re": _B_in["dft_seq_re"],
        "B_dft_seq_im": _B_in["dft_seq_im"],
        "B_dft_hid_re": _B_in["dft_hid_re"],
        "B_dft_hid_im": _B_in["dft_hid_im"],
    }

def reference(A_x, A_wqkv, A_wo, B_x, B_dft_seq_re, B_dft_seq_im, B_dft_hid_re, B_dft_hid_im):
    def _A_ref(x, wqkv, wo):
        B, S, D = x.shape
        H = 16; Dh = D // H; W = 256
        qkv = (lambda x, *d: x.permute(*d))((x @ wqkv).reshape(B, S, 3, H, Dh), 2, 0, 3, 1, 4)
        q, k, v = qkv[0], qkv[1], qkv[2]
        scores = torch.einsum("bhqd,bhkd->bhqk", q, k) * (Dh ** -0.5)
        idx = torch.arange(S)
        mask = (idx[:, None] - idx[None, :] >= 0) & (idx[:, None] - idx[None, :] < W)
        scores = torch.where(mask[None, None], scores, -1e9)
        p = _softmax(scores, axis=-1)
        o = torch.einsum("bhqk,bhkd->bhqd", p, v)
        return (lambda x, *d: x.permute(*d))(o, 0, 2, 1, 3).reshape(B, S, D) @ wo

    def _B_ref(x, dft_seq_re, dft_seq_im, dft_hid_re, dft_hid_im):
        a = torch.einsum("bsd,ed->bse", x, dft_hid_re)
        b = torch.einsum("bsd,ed->bse", x, dft_hid_im)
        y = torch.einsum("ts,bse->bte", dft_seq_re, a) - torch.einsum("ts,bse->bte", dft_seq_im, b)
        return y

    a = _A_ref(A_x, A_wqkv, A_wo)
    b = _B_ref(B_x, B_dft_seq_re, B_dft_seq_im, B_dft_hid_re, B_dft_hid_im)
    return torch.concatenate([a, b], axis=-1)
```

```cpp
#include <hip/hip_runtime.h>
#include <hip/hip_bf16.h>
typedef __hip_bfloat16 bf16;
#define S 2048
#define D 448
#define H 16
#define DH 28
#define W 256

__device__ inline float b2f(bf16 v){ return __bfloat162float(v);}
__device__ inline bf16 f2b(float v){ return __float2bfloat16(v);}

__global__ void gemm_nn(const bf16* A, const bf16* B, bf16* C, int M, int N, int K, int ldc){
  int n = blockIdx.x*blockDim.x+threadIdx.x; int m = blockIdx.y;
  if(n>=N) return;
  float acc=0.f;
  for(int k=0;k<K;k++) acc += b2f(A[(size_t)m*K+k])*b2f(B[(size_t)k*N+n]);
  C[(size_t)m*ldc+n]=f2b(acc);
}
__global__ void gemm_nt(const bf16* A, const bf16* B, bf16* C, int M, int N, int K, int ldc){
  int n = blockIdx.x*blockDim.x+threadIdx.x; int m = blockIdx.y;
  if(n>=N) return;
  float acc=0.f;
  for(int k=0;k<K;k++) acc += b2f(A[(size_t)m*K+k])*b2f(B[(size_t)n*K+k]);
  C[(size_t)m*ldc+n]=f2b(acc);
}
__global__ void attn(const bf16* qkv, bf16* o){
  int idx = blockIdx.x*blockDim.x+threadIdx.x;
  int h = idx / S; int q = idx % S;
  if(h>=H) return;
  float qv[DH];
  for(int d=0;d<DH;d++) qv[d]=b2f(qkv[(size_t)q*3*D + h*DH + d]);
  int k0 = q-W+1; if(k0<0) k0=0;
  float m=-1e30f;
  const float scale = 0.1889822365f;
  for(int k=k0;k<=q;k++){
    float s=0; for(int d=0;d<DH;d++) s+=qv[d]*b2f(qkv[(size_t)k*3*D + D + h*DH + d]);
    s*=scale; m=fmaxf(m,s);
  }
  float l=0; float acc[DH]; for(int d=0;d<DH;d++) acc[d]=0;
  for(int k=k0;k<=q;k++){
    float s=0; for(int d=0;d<DH;d++) s+=qv[d]*b2f(qkv[(size_t)k*3*D + D + h*DH + d]);
    s*=scale; float p=__expf(s-m); l+=p;
    for(int d=0;d<DH;d++) acc[d]+=p*b2f(qkv[(size_t)k*3*D + 2*D + h*DH + d]);
  }
  for(int d=0;d<DH;d++) o[(size_t)q*D+h*DH+d]=f2b(acc[d]/l);
}
__global__ void seqdft(const bf16* Cre,const bf16* Cim,const bf16* a,const bf16* b, bf16* out){
  int e = blockIdx.x*blockDim.x+threadIdx.x; int t = blockIdx.y;
  if(e>=D) return;
  float y1=0,y2=0;
  for(int s=0;s<S;s++){ y1+=b2f(Cre[(size_t)t*S+s])*b2f(a[(size_t)s*D+e]); y2+=b2f(Cim[(size_t)t*S+s])*b2f(b[(size_t)s*D+e]); }
  out[(size_t)t*2*D + D + e]=f2b(y1-y2);
}

extern "C" void kernel_launch(void* const* d_in, const int* in_sizes, int n_in,
                              void* d_out, int out_size, void* d_ws, size_t ws_size,
                              hipStream_t stream) {
  const bf16* Ax=(const bf16*)d_in[0]; const bf16* wqkv=(const bf16*)d_in[1]; const bf16* wo=(const bf16*)d_in[2];
  const bf16* Bx=(const bf16*)d_in[3]; const bf16* Cre=(const bf16*)d_in[4]; const bf16* Cim=(const bf16*)d_in[5];
  const bf16* Wre=(const bf16*)d_in[6]; const bf16* Wim=(const bf16*)d_in[7];
  bf16* out=(bf16*)d_out;
  bf16* qkv=(bf16*)d_ws; bf16* o=qkv+(size_t)S*3*D; bf16* a=o+(size_t)S*D; bf16* b=a+(size_t)S*D;
  gemm_nn<<<dim3((3*D+63)/64,S),64,0,stream>>>(Ax,wqkv,qkv,S,3*D,D,3*D);
  attn<<<dim3(S*H/64),64,0,stream>>>(qkv,o);
  gemm_nn<<<dim3((D+63)/64,S),64,0,stream>>>(o,wo,out,S,D,D,2*D);
  gemm_nt<<<dim3((D+63)/64,S),64,0,stream>>>(Bx,Wre,a,S,D,D,D);
  gemm_nt<<<dim3((D+63)/64,S),64,0,stream>>>(Bx,Wim,b,S,D,D,D);
  seqdft<<<dim3((D+63)/64,S),64,0,stream>>>(Cre,Cim,a,b,out);
}
```

```cpp
#include <hip/hip_runtime.h>
#include <hip/hip_bf16.h>
#include <stdint.h>

typedef short bf16x8 __attribute__((ext_vector_type(8)));
typedef short s16x4 __attribute__((ext_vector_type(4)));
typedef float f32x2 __attribute__((ext_vector_type(2)));
typedef float f32x4 __attribute__((ext_vector_type(4)));
typedef float f32x16 __attribute__((ext_vector_type(16)));
typedef __bf16 bf16x2 __attribute__((ext_vector_type(2)));
typedef unsigned v4u __attribute__((ext_vector_type(4)));
typedef unsigned v2u __attribute__((ext_vector_type(2)));
typedef unsigned long long u64;
#define LAS __attribute__((address_space(3)))
#define GAS __attribute__((address_space(1)))
#define DI __device__ __forceinline__

#define G224_BODY \
    "v_mov_b32 v0, 0\n\t" \
    "v_mov_b32 v1, 0\n\t" \
    "v_mov_b32 v2, 0\n\t" \
    "v_mov_b32 v3, 0\n\t" \
    "v_mov_b32 v4, 0\n\t" \
    "v_mov_b32 v5, 0\n\t" \
    "v_mov_b32 v6, 0\n\t" \
    "v_mov_b32 v7, 0\n\t" \
    "v_mov_b32 v8, 0\n\t" \
    "v_mov_b32 v9, 0\n\t" \
    "v_mov_b32 v10, 0\n\t" \
    "v_mov_b32 v11, 0\n\t" \
    "v_mov_b32 v12, 0\n\t" \
    "v_mov_b32 v13, 0\n\t" \
    "v_mov_b32 v14, 0\n\t" \
    "v_mov_b32 v15, 0\n\t" \
    "v_mov_b32 v16, 0\n\t" \
    "v_mov_b32 v17, 0\n\t" \
    "v_mov_b32 v18, 0\n\t" \
    "v_mov_b32 v19, 0\n\t" \
    "v_mov_b32 v20, 0\n\t" \
    "v_mov_b32 v21, 0\n\t" \
    "v_mov_b32 v22, 0\n\t" \
    "v_mov_b32 v23, 0\n\t" \
    "v_mov_b32 v24, 0\n\t" \
    "v_mov_b32 v25, 0\n\t" \
    "v_mov_b32 v26, 0\n\t" \
    "v_mov_b32 v27, 0\n\t" \
    "v_mov_b32 v28, 0\n\t" \
    "v_mov_b32 v29, 0\n\t" \
    "v_mov_b32 v30, 0\n\t" \
    "v_mov_b32 v31, 0\n\t" \
    "v_mov_b32 v32, 0\n\t" \
    "v_mov_b32 v33, 0\n\t" \
    "v_mov_b32 v34, 0\n\t" \
    "v_mov_b32 v35, 0\n\t" \
    "v_mov_b32 v36, 0\n\t" \
    "v_mov_b32 v37, 0\n\t" \
    "v_mov_b32 v38, 0\n\t" \
    "v_mov_b32 v39, 0\n\t" \
    "v_mov_b32 v40, 0\n\t" \
    "v_mov_b32 v41, 0\n\t" \
    "v_mov_b32 v42, 0\n\t" \
    "v_mov_b32 v43, 0\n\t" \
    "v_mov_b32 v44, 0\n\t" \
    "v_mov_b32 v45, 0\n\t" \
    "v_mov_b32 v46, 0\n\t" \
    "v_mov_b32 v47, 0\n\t" \
    "v_mov_b32 v48, 0\n\t" \
    "v_mov_b32 v49, 0\n\t" \
    "v_mov_b32 v50, 0\n\t" \
    "v_mov_b32 v51, 0\n\t" \
    "v_mov_b32 v52, 0\n\t" \
    "v_mov_b32 v53, 0\n\t" \
    "v_mov_b32 v54, 0\n\t" \
    "v_mov_b32 v55, 0\n\t" \
    "v_mov_b32 v56, 0\n\t" \
    "v_mov_b32 v57, 0\n\t" \
    "v_mov_b32 v58, 0\n\t" \
    "v_mov_b32 v59, 0\n\t" \
    "v_mov_b32 v60, 0\n\t" \
    "v_mov_b32 v61, 0\n\t" \
    "v_mov_b32 v62, 0\n\t" \
    "v_mov_b32 v63, 0\n\t" \
    "v_mov_b32 v64, 0\n\t" \
    "v_mov_b32 v65, 0\n\t" \
    "v_mov_b32 v66, 0\n\t" \
    "v_mov_b32 v67, 0\n\t" \
    "v_mov_b32 v68, 0\n\t" \
    "v_mov_b32 v69, 0\n\t" \
    "v_mov_b32 v70, 0\n\t" \
    "v_mov_b32 v71, 0\n\t" \
    "v_mov_b32 v72, 0\n\t" \
    "v_mov_b32 v73, 0\n\t" \
    "v_mov_b32 v74, 0\n\t" \
    "v_mov_b32 v75, 0\n\t" \
    "v_mov_b32 v76, 0\n\t" \
    "v_mov_b32 v77, 0\n\t" \
    "v_mov_b32 v78, 0\n\t" \
    "v_mov_b32 v79, 0\n\t" \
    "v_mov_b32 v80, 0\n\t" \
    "v_mov_b32 v81, 0\n\t" \
    "v_mov_b32 v82, 0\n\t" \
    "v_mov_b32 v83, 0\n\t" \
    "v_mov_b32 v84, 0\n\t" \
    "v_mov_b32 v85, 0\n\t" \
    "v_mov_b32 v86, 0\n\t" \
    "v_mov_b32 v87, 0\n\t" \
    "v_mov_b32 v88, 0\n\t" \
    "v_mov_b32 v89, 0\n\t" \
    "v_mov_b32 v90, 0\n\t" \
    "v_mov_b32 v91, 0\n\t" \
    "v_mov_b32 v92, 0\n\t" \
    "v_mov_b32 v93, 0\n\t" \
    "v_mov_b32 v94, 0\n\t" \
    "v_mov_b32 v95, 0\n\t" \
    "v_mov_b32 v96, 0\n\t" \
    "v_mov_b32 v97, 0\n\t" \
    "v_mov_b32 v98, 0\n\t" \
    "v_mov_b32 v99, 0\n\t" \
    "v_mov_b32 v100, 0\n\t" \
    "v_mov_b32 v101, 0\n\t" \
    "v_mov_b32 v102, 0\n\t" \
    "v_mov_b32 v103, 0\n\t" \
    "v_mov_b32 v104, 0\n\t" \
    "v_mov_b32 v105, 0\n\t" \
    "v_mov_b32 v106, 0\n\t" \
    "v_mov_b32 v107, 0\n\t" \
    "v_mov_b32 v108, 0\n\t" \
    "v_mov_b32 v109, 0\n\t" \
    "v_mov_b32 v110, 0\n\t" \
    "v_mov_b32 v111, 0\n\t" \
    "s_waitcnt lgkmcnt(0)\n\t" \
    "s_barrier\n\t" \
    "v_mov_b32 v207, v200\n\t" \
    "v_mov_b32 v208, v202\n\t" \
    "v_mov_b32 v209, v203\n\t" \
    "ds_read_b128 v[112:115], v207 offset:0\n\t" \
    "ds_read_b64_tr_b16 v[128:129], v208 offset:0\n\t" \
    "ds_read_b64_tr_b16 v[130:131], v208 offset:1792\n\t" \
    "ds_read_b128 v[116:119], v207 offset:2048\n\t" \
    "ds_read_b128 v[120:123], v207 offset:4096\n\t" \
    "ds_read_b128 v[124:127], v207 offset:6144\n\t" \
    "ds_read_b64_tr_b16 v[132:133], v209 offset:32\n\t" \
    "ds_read_b64_tr_b16 v[134:135], v209 offset:1824\n\t" \
    "ds_read_b64_tr_b16 v[136:137], v208 offset:64\n\t" \
    "ds_read_b64_tr_b16 v[138:139], v208 offset:1856\n\t" \
    "ds_read_b64_tr_b16 v[140:141], v209 offset:96\n\t" \
    "ds_read_b64_tr_b16 v[142:143], v209 offset:1888\n\t" \
    "ds_read_b64_tr_b16 v[144:145], v208 offset:128\n\t" \
    "ds_read_b64_tr_b16 v[146:147], v208 offset:1920\n\t" \
    "ds_read_b64_tr_b16 v[148:149], v209 offset:160\n\t" \
    "ds_read_b64_tr_b16 v[150:151], v209 offset:1952\n\t" \
    "ds_read_b64_tr_b16 v[152:153], v208 offset:192\n\t" \
    "ds_read_b64_tr_b16 v[154:155], v208 offset:1984\n\t" \
    "s_mov_b32 %[aslot], 0\n\t" \
    "s_mov_b32 %[bslot], 14336\n\t" \
    "v_mov_b32 v204, v201\n\t" \
    "v_add_u32 v205, %[bslot], v202\n\t" \
    "v_add_u32 v206, %[bslot], v203\n\t" \
    "s_barrier\n\t" \
    "1:\n\t" \
    "s_waitcnt lgkmcnt(15)\n\t" \
    "v_mfma_f32_16x16x32_bf16 v[0:3], v[128:131], v[112:115], v[0:3]\n\t" \
    "ds_read_b128 v[156:159], v204 offset:0\n\t" \
    "s_waitcnt lgkmcnt(15)\n\t" \
    "v_mfma_f32_16x16x32_bf16 v[4:7], v[128:131], v[116:119], v[4:7]\n\t" \
    "ds_read_b64_tr_b16 v[172:173], v205 offset:0\n\t" \
    "s_waitcnt lgkmcnt(15)\n\t" \
    "v_mfma_f32_16x16x32_bf16 v[8:11], v[128:131], v[120:123], v[8:11]\n\t" \
    "ds_read_b64_tr_b16 v[174:175], v205 offset:1792\n\t" \
    "s_waitcnt lgkmcnt(15)\n\t" \
    "v_mfma_f32_16x16x32_bf16 v[12:15], v[128:131], v[124:127], v[12:15]\n\t" \
    "ds_read_b128 v[160:163], v204 offset:2048\n\t" \
    "s_waitcnt lgkmcnt(14)\n\t" \
    "v_mfma_f32_16x16x32_bf16 v[16:19], v[132:135], v[112:115], v[16:19]\n\t" \
    "ds_read_b128 v[164:167], v204 offset:4096\n\t" \
    "v_mfma_f32_16x16x32_bf16 v[20:23], v[132:135], v[116:119], v[20:23]\n\t" \
    "ds_read_b128 v[168:171], v204 offset:6144\n\t" \
    "v_mfma_f32_16x16x32_bf16 v[24:27], v[132:135], v[120:123], v[24:27]\n\t" \
    "ds_read_b64_tr_b16 v[176:177], v206 offset:32\n\t" \
    "v_mfma_f32_16x16x32_bf16 v[28:31], v[132:135], v[124:127], v[28:31]\n\t" \
    "ds_read_b64_tr_b16 v[178:179], v206 offset:1824\n\t" \
    "s_waitcnt lgkmcnt(15)\n\t" \
    "v_mfma_f32_16x16x32_bf16 v[32:35], v[136:139], v[112:115], v[32:35]\n\t" \
    "ds_read_b64_tr_b16 v[180:181], v205 offset:64\n\t" \
    "v_mfma_f32_16x16x32_bf16 v[36:39], v[136:139], v[116:119], v[36:39]\n\t" \
    "ds_read_b64_tr_b16 v[182:183], v205 offset:1856\n\t" \
    "v_mfma_f32_16x16x32_bf16 v[40:43], v[136:139], v[120:123], v[40:43]\n\t" \
    "ds_read_b64_tr_b16 v[184:185], v206 offset:96\n\t" \
    "v_mfma_f32_16x16x32_bf16 v[44:47], v[136:139], v[124:127], v[44:47]\n\t" \
    "ds_read_b64_tr_b16 v[186:187], v206 offset:1888\n\t" \
    "s_waitcnt lgkmcnt(15)\n\t" \
    "v_mfma_f32_16x16x32_bf16 v[48:51], v[140:143], v[112:115], v[48:51]\n\t" \
    "ds_read_b64_tr_b16 v[188:189], v205 offset:128\n\t" \
    "v_mfma_f32_16x16x32_bf16 v[52:55], v[140:143], v[116:119], v[52:55]\n\t" \
    "ds_read_b64_tr_b16 v[190:191], v205 offset:1920\n\t" \
    "v_mfma_f32_16x16x32_bf16 v[56:59], v[140:143], v[120:123], v[56:59]\n\t" \
    "ds_read_b64_tr_b16 v[192:193], v206 offset:160\n\t" \
    "v_mfma_f32_16x16x32_bf16 v[60:63], v[140:143], v[124:127], v[60:63]\n\t" \
    "ds_read_b64_tr_b16 v[194:195], v206 offset:1952\n\t" \
    "v_mfma_f32_16x16x32_bf16 v[64:67], v[144:147], v[112:115], v[64:67]\n\t" \
    "ds_read_b64_tr_b16 v[196:197], v205 offset:192\n\t" \
    "v_mfma_f32_16x16x32_bf16 v[68:71], v[144:147], v[116:119], v[68:71]\n\t" \
    "ds_read_b64_tr_b16 v[198:199], v205 offset:1984\n\t" \
    "v_mfma_f32_16x16x32_bf16 v[72:75], v[144:147], v[120:123], v[72:75]\n\t" \
    "v_mfma_f32_16x16x32_bf16 v[76:79], v[144:147], v[124:127], v[76:79]\n\t" \
    "s_add_u32 %[aslot], %[aslot], 16384\n\t" \
    "s_waitcnt lgkmcnt(15)\n\t" \
    "v_mfma_f32_16x16x32_bf16 v[80:83], v[148:151], v[112:115], v[80:83]\n\t" \
    "s_and_b32 %[aslot], %[aslot], 0xffff\n\t" \
    "v_mfma_f32_16x16x32_bf16 v[84:87], v[148:151], v[116:119], v[84:87]\n\t" \
    "s_add_u32 %[bslot], %[bslot], 14336\n\t" \
    "v_mfma_f32_16x16x32_bf16 v[88:91], v[148:151], v[120:123], v[88:91]\n\t" \
    "s_cmp_eq_u32 %[bslot], 86016\n\t" \
    "v_mfma_f32_16x16x32_bf16 v[92:95], v[148:151], v[124:127], v[92:95]\n\t" \
    "s_cselect_b32 %[bslot], 0, %[bslot]\n\t" \
    "v_mfma_f32_16x16x32_bf16 v[96:99], v[152:155], v[112:115], v[96:99]\n\t" \
    "v_add_u32 v207, %[aslot], v200\n\t" \
    "v_mfma_f32_16x16x32_bf16 v[100:103], v[152:155], v[116:119], v[100:103]\n\t" \
    "v_add_u32 v208, %[bslot], v202\n\t" \
    "v_mfma_f32_16x16x32_bf16 v[104:107], v[152:155], v[120:123], v[104:107]\n\t" \
    "v_add_u32 v209, %[bslot], v203\n\t" \
    "v_mfma_f32_16x16x32_bf16 v[108:111], v[152:155], v[124:127], v[108:111]\n\t" \
    "s_barrier\n\t" \
    "s_waitcnt lgkmcnt(15)\n\t" \
    "v_mfma_f32_16x16x32_bf16 v[0:3], v[172:175], v[156:159], v[0:3]\n\t" \
    "ds_read_b128 v[112:115], v207 offset:0\n\t" \
    "s_waitcnt lgkmcnt(15)\n\t" \
    "v_mfma_f32_16x16x32_bf16 v[4:7], v[172:175], v[160:163], v[4:7]\n\t" \
    "ds_read_b64_tr_b16 v[128:129], v208 offset:0\n\t" \
    "s_waitcnt lgkmcnt(15)\n\t" \
    "v_mfma_f32_16x16x32_bf16 v[8:11], v[172:175], v[164:167], v[8:11]\n\t" \
    "ds_read_b64_tr_b16 v[130:131], v208 offset:1792\n\t" \
    "s_waitcnt lgkmcnt(15)\n\t" \
    "v_mfma_f32_16x16x32_bf16 v[12:15], v[172:175], v[168:171], v[12:15]\n\t" \
    "ds_read_b128 v[116:119], v207 offset:2048\n\t" \
    "s_waitcnt lgkmcnt(14)\n\t" \
    "v_mfma_f32_16x16x32_bf16 v[16:19], v[176:179], v[156:159], v[16:19]\n\t" \
    "ds_read_b128 v[120:123], v207 offset:4096\n\t" \
    "v_mfma_f32_16x16x32_bf16 v[20:23], v[176:179], v[160:163], v[20:23]\n\t" \
    "ds_read_b128 v[124:127], v207 offset:6144\n\t" \
    "v_mfma_f32_16x16x32_bf16 v[24:27], v[176:179], v[164:167], v[24:27]\n\t" \
    "ds_read_b64_tr_b16 v[132:133], v209 offset:32\n\t" \
    "v_mfma_f32_16x16x32_bf16 v[28:31], v[176:179], v[168:171], v[28:31]\n\t" \
    "ds_read_b64_tr_b16 v[134:135], v209 offset:1824\n\t" \
    "s_waitcnt lgkmcnt(15)\n\t" \
    "v_mfma_f32_16x16x32_bf16 v[32:35], v[180:183], v[156:159], v[32:35]\n\t" \
    "ds_read_b64_tr_b16 v[136:137], v208 offset:64\n\t" \
    "v_mfma_f32_16x16x32_bf16 v[36:39], v[180:183], v[160:163], v[36:39]\n\t" \
    "ds_read_b64_tr_b16 v[138:139], v208 offset:1856\n\t" \
    "v_mfma_f32_16x16x32_bf16 v[40:43], v[180:183], v[164:167], v[40:43]\n\t" \
    "ds_read_b64_tr_b16 v[140:141], v209 offset:96\n\t" \
    "v_mfma_f32_16x16x32_bf16 v[44:47], v[180:183], v[168:171], v[44:47]\n\t" \
    "ds_read_b64_tr_b16 v[142:143], v209 offset:1888\n\t" \
    "s_waitcnt lgkmcnt(15)\n\t" \
    "v_mfma_f32_16x16x32_bf16 v[48:51], v[184:187], v[156:159], v[48:51]\n\t" \
    "ds_read_b64_tr_b16 v[144:145], v208 offset:128\n\t" \
    "v_mfma_f32_16x16x32_bf16 v[52:55], v[184:187], v[160:163], v[52:55]\n\t" \
    "ds_read_b64_tr_b16 v[146:147], v208 offset:1920\n\t" \
    "v_mfma_f32_16x16x32_bf16 v[56:59], v[184:187], v[164:167], v[56:59]\n\t" \
    "ds_read_b64_tr_b16 v[148:149], v209 offset:160\n\t" \
    "v_mfma_f32_16x16x32_bf16 v[60:63], v[184:187], v[168:171], v[60:63]\n\t" \
    "ds_read_b64_tr_b16 v[150:151], v209 offset:1952\n\t" \
    "v_mfma_f32_16x16x32_bf16 v[64:67], v[188:191], v[156:159], v[64:67]\n\t" \
    "ds_read_b64_tr_b16 v[152:153], v208 offset:192\n\t" \
    "v_mfma_f32_16x16x32_bf16 v[68:71], v[188:191], v[160:163], v[68:71]\n\t" \
    "ds_read_b64_tr_b16 v[154:155], v208 offset:1984\n\t" \
    "v_mfma_f32_16x16x32_bf16 v[72:75], v[188:191], v[164:167], v[72:75]\n\t" \
    "v_mfma_f32_16x16x32_bf16 v[76:79], v[188:191], v[168:171], v[76:79]\n\t" \
    "s_add_u32 %[bslot], %[bslot], 14336\n\t" \
    "s_waitcnt lgkmcnt(15)\n\t" \
    "v_mfma_f32_16x16x32_bf16 v[80:83], v[192:195], v[156:159], v[80:83]\n\t" \
    "s_cmp_eq_u32 %[bslot], 86016\n\t" \
    "v_mfma_f32_16x16x32_bf16 v[84:87], v[192:195], v[160:163], v[84:87]\n\t" \
    "s_cselect_b32 %[bslot], 0, %[bslot]\n\t" \
    "v_mfma_f32_16x16x32_bf16 v[88:91], v[192:195], v[164:167], v[88:91]\n\t" \
    "v_add_u32 v204, %[aslot], v201\n\t" \
    "v_mfma_f32_16x16x32_bf16 v[92:95], v[192:195], v[168:171], v[92:95]\n\t" \
    "v_add_u32 v205, %[bslot], v202\n\t" \
    "v_mfma_f32_16x16x32_bf16 v[96:99], v[196:199], v[156:159], v[96:99]\n\t" \
    "v_add_u32 v206, %[bslot], v203\n\t" \
    "v_mfma_f32_16x16x32_bf16 v[100:103], v[196:199], v[160:163], v[100:103]\n\t" \
    "s_sub_u32 %[cnt], %[cnt], 1\n\t" \
    "v_mfma_f32_16x16x32_bf16 v[104:107], v[196:199], v[164:167], v[104:107]\n\t" \
    "s_cmp_lg_u32 %[cnt], 0\n\t" \
    "v_mfma_f32_16x16x32_bf16 v[108:111], v[196:199], v[168:171], v[108:111]\n\t" \
    "s_barrier\n\t" \
    "s_cbranch_scc1 1b\n\t" \
    "s_waitcnt lgkmcnt(15)\n\t" \
    "v_mfma_f32_16x16x32_bf16 v[0:3], v[128:131], v[112:115], v[0:3]\n\t" \
    "ds_read_b128 v[156:159], v204 offset:0\n\t" \
    "s_waitcnt lgkmcnt(15)\n\t" \
    "v_mfma_f32_16x16x32_bf16 v[4:7], v[128:131], v[116:119], v[4:7]\n\t" \
    "ds_read_b64_tr_b16 v[172:173], v205 offset:0\n\t" \
    "s_waitcnt lgkmcnt(15)\n\t" \
    "v_mfma_f32_16x16x32_bf16 v[8:11], v[128:131], v[120:123], v[8:11]\n\t" \
    "ds_read_b64_tr_b16 v[174:175], v205 offset:1792\n\t" \
    "s_waitcnt lgkmcnt(15)\n\t" \
    "v_mfma_f32_16x16x32_bf16 v[12:15], v[128:131], v[124:127], v[12:15]\n\t" \
    "ds_read_b128 v[160:163], v204 offset:2048\n\t" \
    "s_waitcnt lgkmcnt(14)\n\t" \
    "v_mfma_f32_16x16x32_bf16 v[16:19], v[132:135], v[112:115], v[16:19]\n\t" \
    "ds_read_b128 v[164:167], v204 offset:4096\n\t" \
    "v_mfma_f32_16x16x32_bf16 v[20:23], v[132:135], v[116:119], v[20:23]\n\t" \
    "ds_read_b128 v[168:171], v204 offset:6144\n\t" \
    "v_mfma_f32_16x16x32_bf16 v[24:27], v[132:135], v[120:123], v[24:27]\n\t" \
    "ds_read_b64_tr_b16 v[176:177], v206 offset:32\n\t" \
    "v_mfma_f32_16x16x32_bf16 v[28:31], v[132:135], v[124:127], v[28:31]\n\t" \
    "ds_read_b64_tr_b16 v[178:179], v206 offset:1824\n\t" \
    "s_waitcnt lgkmcnt(15)\n\t" \
    "v_mfma_f32_16x16x32_bf16 v[32:35], v[136:139], v[112:115], v[32:35]\n\t" \
    "ds_read_b64_tr_b16 v[180:181], v205 offset:64\n\t" \
    "v_mfma_f32_16x16x32_bf16 v[36:39], v[136:139], v[116:119], v[36:39]\n\t" \
    "ds_read_b64_tr_b16 v[182:183], v205 offset:1856\n\t" \
    "v_mfma_f32_16x16x32_bf16 v[40:43], v[136:139], v[120:123], v[40:43]\n\t" \
    "ds_read_b64_tr_b16 v[184:185], v206 offset:96\n\t" \
    "v_mfma_f32_16x16x32_bf16 v[44:47], v[136:139], v[124:127], v[44:47]\n\t" \
    "ds_read_b64_tr_b16 v[186:187], v206 offset:1888\n\t" \
    "s_waitcnt lgkmcnt(15)\n\t" \
    "v_mfma_f32_16x16x32_bf16 v[48:51], v[140:143], v[112:115], v[48:51]\n\t" \
    "ds_read_b64_tr_b16 v[188:189], v205 offset:128\n\t" \
    "v_mfma_f32_16x16x32_bf16 v[52:55], v[140:143], v[116:119], v[52:55]\n\t" \
    "ds_read_b64_tr_b16 v[190:191], v205 offset:1920\n\t" \
    "v_mfma_f32_16x16x32_bf16 v[56:59], v[140:143], v[120:123], v[56:59]\n\t" \
    "ds_read_b64_tr_b16 v[192:193], v206 offset:160\n\t" \
    "v_mfma_f32_16x16x32_bf16 v[60:63], v[140:143], v[124:127], v[60:63]\n\t" \
    "ds_read_b64_tr_b16 v[194:195], v206 offset:1952\n\t" \
    "v_mfma_f32_16x16x32_bf16 v[64:67], v[144:147], v[112:115], v[64:67]\n\t" \
    "ds_read_b64_tr_b16 v[196:197], v205 offset:192\n\t" \
    "v_mfma_f32_16x16x32_bf16 v[68:71], v[144:147], v[116:119], v[68:71]\n\t" \
    "ds_read_b64_tr_b16 v[198:199], v205 offset:1984\n\t" \
    "v_mfma_f32_16x16x32_bf16 v[72:75], v[144:147], v[120:123], v[72:75]\n\t" \
    "v_mfma_f32_16x16x32_bf16 v[76:79], v[144:147], v[124:127], v[76:79]\n\t" \
    "s_waitcnt lgkmcnt(15)\n\t" \
    "v_mfma_f32_16x16x32_bf16 v[80:83], v[148:151], v[112:115], v[80:83]\n\t" \
    "v_mfma_f32_16x16x32_bf16 v[84:87], v[148:151], v[116:119], v[84:87]\n\t" \
    "v_mfma_f32_16x16x32_bf16 v[88:91], v[148:151], v[120:123], v[88:91]\n\t" \
    "v_mfma_f32_16x16x32_bf16 v[92:95], v[148:151], v[124:127], v[92:95]\n\t" \
    "v_mfma_f32_16x16x32_bf16 v[96:99], v[152:155], v[112:115], v[96:99]\n\t" \
    "v_mfma_f32_16x16x32_bf16 v[100:103], v[152:155], v[116:119], v[100:103]\n\t" \
    "v_mfma_f32_16x16x32_bf16 v[104:107], v[152:155], v[120:123], v[104:107]\n\t" \
    "v_mfma_f32_16x16x32_bf16 v[108:111], v[152:155], v[124:127], v[108:111]\n\t" \
    "s_barrier\n\t" \
    "s_waitcnt lgkmcnt(15)\n\t" \
    "v_mfma_f32_16x16x32_bf16 v[0:3], v[172:175], v[156:159], v[0:3]\n\t" \
    "s_waitcnt lgkmcnt(14)\n\t" \
    "v_mfma_f32_16x16x32_bf16 v[4:7], v[172:175], v[160:163], v[4:7]\n\t" \
    "s_waitcnt lgkmcnt(13)\n\t" \
    "v_mfma_f32_16x16x32_bf16 v[8:11], v[172:175], v[164:167], v[8:11]\n\t" \
    "s_waitcnt lgkmcnt(12)\n\t" \
    "v_mfma_f32_16x16x32_bf16 v[12:15], v[172:175], v[168:171], v[12:15]\n\t" \
    "s_waitcnt lgkmcnt(10)\n\t" \
    "v_mfma_f32_16x16x32_bf16 v[16:19], v[176:179], v[156:159], v[16:19]\n\t" \
    "v_mfma_f32_16x16x32_bf16 v[20:23], v[176:179], v[160:163], v[20:23]\n\t" \
    "v_mfma_f32_16x16x32_bf16 v[24:27], v[176:179], v[164:167], v[24:27]\n\t" \
    "v_mfma_f32_16x16x32_bf16 v[28:31], v[176:179], v[168:171], v[28:31]\n\t" \
    "s_waitcnt lgkmcnt(8)\n\t" \
    "v_mfma_f32_16x16x32_bf16 v[32:35], v[180:183], v[156:159], v[32:35]\n\t" \
    "v_mfma_f32_16x16x32_bf16 v[36:39], v[180:183], v[160:163], v[36:39]\n\t" \
    "v_mfma_f32_16x16x32_bf16 v[40:43], v[180:183], v[164:167], v[40:43]\n\t" \
    "v_mfma_f32_16x16x32_bf16 v[44:47], v[180:183], v[168:171], v[44:47]\n\t" \
    "s_waitcnt lgkmcnt(6)\n\t" \
    "v_mfma_f32_16x16x32_bf16 v[48:51], v[184:187], v[156:159], v[48:51]\n\t" \
    "v_mfma_f32_16x16x32_bf16 v[52:55], v[184:187], v[160:163], v[52:55]\n\t" \
    "v_mfma_f32_16x16x32_bf16 v[56:59], v[184:187], v[164:167], v[56:59]\n\t" \
    "v_mfma_f32_16x16x32_bf16 v[60:63], v[184:187], v[168:171], v[60:63]\n\t" \
    "s_waitcnt lgkmcnt(4)\n\t" \
    "v_mfma_f32_16x16x32_bf16 v[64:67], v[188:191], v[156:159], v[64:67]\n\t" \
    "v_mfma_f32_16x16x32_bf16 v[68:71], v[188:191], v[160:163], v[68:71]\n\t" \
    "v_mfma_f32_16x16x32_bf16 v[72:75], v[188:191], v[164:167], v[72:75]\n\t" \
    "v_mfma_f32_16x16x32_bf16 v[76:79], v[188:191], v[168:171], v[76:79]\n\t" \
    "s_waitcnt lgkmcnt(2)\n\t" \
    "v_mfma_f32_16x16x32_bf16 v[80:83], v[192:195], v[156:159], v[80:83]\n\t" \
    "v_mfma_f32_16x16x32_bf16 v[84:87], v[192:195], v[160:163], v[84:87]\n\t" \
    "v_mfma_f32_16x16x32_bf16 v[88:91], v[192:195], v[164:167], v[88:91]\n\t" \
    "v_mfma_f32_16x16x32_bf16 v[92:95], v[192:195], v[168:171], v[92:95]\n\t" \
    "s_waitcnt lgkmcnt(0)\n\t" \
    "v_mfma_f32_16x16x32_bf16 v[96:99], v[196:199], v[156:159], v[96:99]\n\t" \
    "v_mfma_f32_16x16x32_bf16 v[100:103], v[196:199], v[160:163], v[100:103]\n\t" \
    "v_mfma_f32_16x16x32_bf16 v[104:107], v[196:199], v[164:167], v[104:107]\n\t" \
    "v_mfma_f32_16x16x32_bf16 v[108:111], v[196:199], v[168:171], v[108:111]\n\t" \
    "s_barrier\n\t" \
    "s_nop 7\n\t" \
    "s_nop 7\n\t" \
    ""
#define G224_BODY_SWAP \
    "v_mov_b32 v0, 0\n\t" \
    "v_mov_b32 v1, 0\n\t" \
    "v_mov_b32 v2, 0\n\t" \
    "v_mov_b32 v3, 0\n\t" \
    "v_mov_b32 v4, 0\n\t" \
    "v_mov_b32 v5, 0\n\t" \
    "v_mov_b32 v6, 0\n\t" \
    "v_mov_b32 v7, 0\n\t" \
    "v_mov_b32 v8, 0\n\t" \
    "v_mov_b32 v9, 0\n\t" \
    "v_mov_b32 v10, 0\n\t" \
    "v_mov_b32 v11, 0\n\t" \
    "v_mov_b32 v12, 0\n\t" \
    "v_mov_b32 v13, 0\n\t" \
    "v_mov_b32 v14, 0\n\t" \
    "v_mov_b32 v15, 0\n\t" \
    "v_mov_b32 v16, 0\n\t" \
    "v_mov_b32 v17, 0\n\t" \
    "v_mov_b32 v18, 0\n\t" \
    "v_mov_b32 v19, 0\n\t" \
    "v_mov_b32 v20, 0\n\t" \
    "v_mov_b32 v21, 0\n\t" \
    "v_mov_b32 v22, 0\n\t" \
    "v_mov_b32 v23, 0\n\t" \
    "v_mov_b32 v24, 0\n\t" \
    "v_mov_b32 v25, 0\n\t" \
    "v_mov_b32 v26, 0\n\t" \
    "v_mov_b32 v27, 0\n\t" \
    "v_mov_b32 v28, 0\n\t" \
    "v_mov_b32 v29, 0\n\t" \
    "v_mov_b32 v30, 0\n\t" \
    "v_mov_b32 v31, 0\n\t" \
    "v_mov_b32 v32, 0\n\t" \
    "v_mov_b32 v33, 0\n\t" \
    "v_mov_b32 v34, 0\n\t" \
    "v_mov_b32 v35, 0\n\t" \
    "v_mov_b32 v36, 0\n\t" \
    "v_mov_b32 v37, 0\n\t" \
    "v_mov_b32 v38, 0\n\t" \
    "v_mov_b32 v39, 0\n\t" \
    "v_mov_b32 v40, 0\n\t" \
    "v_mov_b32 v41, 0\n\t" \
    "v_mov_b32 v42, 0\n\t" \
    "v_mov_b32 v43, 0\n\t" \
    "v_mov_b32 v44, 0\n\t" \
    "v_mov_b32 v45, 0\n\t" \
    "v_mov_b32 v46, 0\n\t" \
    "v_mov_b32 v47, 0\n\t" \
    "v_mov_b32 v48, 0\n\t" \
    "v_mov_b32 v49, 0\n\t" \
    "v_mov_b32 v50, 0\n\t" \
    "v_mov_b32 v51, 0\n\t" \
    "v_mov_b32 v52, 0\n\t" \
    "v_mov_b32 v53, 0\n\t" \
    "v_mov_b32 v54, 0\n\t" \
    "v_mov_b32 v55, 0\n\t" \
    "v_mov_b32 v56, 0\n\t" \
    "v_mov_b32 v57, 0\n\t" \
    "v_mov_b32 v58, 0\n\t" \
    "v_mov_b32 v59, 0\n\t" \
    "v_mov_b32 v60, 0\n\t" \
    "v_mov_b32 v61, 0\n\t" \
    "v_mov_b32 v62, 0\n\t" \
    "v_mov_b32 v63, 0\n\t" \
    "v_mov_b32 v64, 0\n\t" \
    "v_mov_b32 v65, 0\n\t" \
    "v_mov_b32 v66, 0\n\t" \
    "v_mov_b32 v67, 0\n\t" \
    "v_mov_b32 v68, 0\n\t" \
    "v_mov_b32 v69, 0\n\t" \
    "v_mov_b32 v70, 0\n\t" \
    "v_mov_b32 v71, 0\n\t" \
    "v_mov_b32 v72, 0\n\t" \
    "v_mov_b32 v73, 0\n\t" \
    "v_mov_b32 v74, 0\n\t" \
    "v_mov_b32 v75, 0\n\t" \
    "v_mov_b32 v76, 0\n\t" \
    "v_mov_b32 v77, 0\n\t" \
    "v_mov_b32 v78, 0\n\t" \
    "v_mov_b32 v79, 0\n\t" \
    "v_mov_b32 v80, 0\n\t" \
    "v_mov_b32 v81, 0\n\t" \
    "v_mov_b32 v82, 0\n\t" \
    "v_mov_b32 v83, 0\n\t" \
    "v_mov_b32 v84, 0\n\t" \
    "v_mov_b32 v85, 0\n\t" \
    "v_mov_b32 v86, 0\n\t" \
    "v_mov_b32 v87, 0\n\t" \
    "v_mov_b32 v88, 0\n\t" \
    "v_mov_b32 v89, 0\n\t" \
    "v_mov_b32 v90, 0\n\t" \
    "v_mov_b32 v91, 0\n\t" \
    "v_mov_b32 v92, 0\n\t" \
    "v_mov_b32 v93, 0\n\t" \
    "v_mov_b32 v94, 0\n\t" \
    "v_mov_b32 v95, 0\n\t" \
    "v_mov_b32 v96, 0\n\t" \
    "v_mov_b32 v97, 0\n\t" \
    "v_mov_b32 v98, 0\n\t" \
    "v_mov_b32 v99, 0\n\t" \
    "v_mov_b32 v100, 0\n\t" \
    "v_mov_b32 v101, 0\n\t" \
    "v_mov_b32 v102, 0\n\t" \
    "v_mov_b32 v103, 0\n\t" \
    "v_mov_b32 v104, 0\n\t" \
    "v_mov_b32 v105, 0\n\t" \
    "v_mov_b32 v106, 0\n\t" \
    "v_mov_b32 v107, 0\n\t" \
    "v_mov_b32 v108, 0\n\t" \
    "v_mov_b32 v109, 0\n\t" \
    "v_mov_b32 v110, 0\n\t" \
    "v_mov_b32 v111, 0\n\t" \
    "s_waitcnt lgkmcnt(0)\n\t" \
    "s_barrier\n\t" \
    "v_mov_b32 v207, v200\n\t" \
    "v_mov_b32 v208, v202\n\t" \
    "v_mov_b32 v209, v203\n\t" \
    "ds_read_b128 v[112:115], v207 offset:0\n\t" \
    "ds_read_b64_tr_b16 v[128:129], v208 offset:0\n\t" \
    "ds_read_b64_tr_b16 v[130:131], v208 offset:1792\n\t" \
    "ds_read_b128 v[116:119], v207 offset:2048\n\t" \
    "ds_read_b128 v[120:123], v207 offset:4096\n\t" \
    "ds_read_b128 v[124:127], v207 offset:6144\n\t" \
    "ds_read_b64_tr_b16 v[132:133], v209 offset:32\n\t" \
    "ds_read_b64_tr_b16 v[134:135], v209 offset:1824\n\t" \
    "ds_read_b64_tr_b16 v[136:137], v208 offset:64\n\t" \
    "ds_read_b64_tr_b16 v[138:139], v208 offset:1856\n\t" \
    "ds_read_b64_tr_b16 v[140:141], v209 offset:96\n\t" \
    "ds_read_b64_tr_b16 v[142:143], v209 offset:1888\n\t" \
    "ds_read_b64_tr_b16 v[144:145], v208 offset:128\n\t" \
    "ds_read_b64_tr_b16 v[146:147], v208 offset:1920\n\t" \
    "ds_read_b64_tr_b16 v[148:149], v209 offset:160\n\t" \
    "ds_read_b64_tr_b16 v[150:151], v209 offset:1952\n\t" \
    "ds_read_b64_tr_b16 v[152:153], v208 offset:192\n\t" \
    "ds_read_b64_tr_b16 v[154:155], v208 offset:1984\n\t" \
    "s_mov_b32 %[aslot], 0\n\t" \
    "s_mov_b32 %[bslot], 14336\n\t" \
    "v_mov_b32 v204, v201\n\t" \
    "v_add_u32 v205, %[bslot], v202\n\t" \
    "v_add_u32 v206, %[bslot], v203\n\t" \
    "s_barrier\n\t" \
    "1:\n\t" \
    "s_waitcnt lgkmcnt(15)\n\t" \
    "v_mfma_f32_16x16x32_bf16 v[0:3], v[112:115], v[128:131], v[0:3]\n\t" \
    "ds_read_b128 v[156:159], v204 offset:0\n\t" \
    "s_waitcnt lgkmcnt(15)\n\t" \
    "v_mfma_f32_16x16x32_bf16 v[4:7], v[116:119], v[128:131], v[4:7]\n\t" \
    "ds_read_b64_tr_b16 v[172:173], v205 offset:0\n\t" \
    "s_waitcnt lgkmcnt(15)\n\t" \
    "v_mfma_f32_16x16x32_bf16 v[8:11], v[120:123], v[128:131], v[8:11]\n\t" \
    "ds_read_b64_tr_b16 v[174:175], v205 offset:1792\n\t" \
    "s_waitcnt lgkmcnt(15)\n\t" \
    "v_mfma_f32_16x16x32_bf16 v[12:15], v[124:127], v[128:131], v[12:15]\n\t" \
    "ds_read_b128 v[160:163], v204 offset:2048\n\t" \
    "s_waitcnt lgkmcnt(14)\n\t" \
    "v_mfma_f32_16x16x32_bf16 v[16:19], v[112:115], v[132:135], v[16:19]\n\t" \
    "ds_read_b128 v[164:167], v204 offset:4096\n\t" \
    "v_mfma_f32_16x16x32_bf16 v[20:23], v[116:119], v[132:135], v[20:23]\n\t" \
    "ds_read_b128 v[168:171], v204 offset:6144\n\t" \
    "v_mfma_f32_16x16x32_bf16 v[24:27], v[120:123], v[132:135], v[24:27]\n\t" \
    "ds_read_b64_tr_b16 v[176:177], v206 offset:32\n\t" \
    "v_mfma_f32_16x16x32_bf16 v[28:31], v[124:127], v[132:135], v[28:31]\n\t" \
    "ds_read_b64_tr_b16 v[178:179], v206 offset:1824\n\t" \
    "s_waitcnt lgkmcnt(15)\n\t" \
    "v_mfma_f32_16x16x32_bf16 v[32:35], v[112:115], v[136:139], v[32:35]\n\t" \
    "ds_read_b64_tr_b16 v[180:181], v205 offset:64\n\t" \
    "v_mfma_f32_16x16x32_bf16 v[36:39], v[116:119], v[136:139], v[36:39]\n\t" \
    "ds_read_b64_tr_b16 v[182:183], v205 offset:1856\n\t" \
    "v_mfma_f32_16x16x32_bf16 v[40:43], v[120:123], v[136:139], v[40:43]\n\t" \
    "ds_read_b64_tr_b16 v[184:185], v206 offset:96\n\t" \
    "v_mfma_f32_16x16x32_bf16 v[44:47], v[124:127], v[136:139], v[44:47]\n\t" \
    "ds_read_b64_tr_b16 v[186:187], v206 offset:1888\n\t" \
    "s_waitcnt lgkmcnt(15)\n\t" \
    "v_mfma_f32_16x16x32_bf16 v[48:51], v[112:115], v[140:143], v[48:51]\n\t" \
    "ds_read_b64_tr_b16 v[188:189], v205 offset:128\n\t" \
    "v_mfma_f32_16x16x32_bf16 v[52:55], v[116:119], v[140:143], v[52:55]\n\t" \
    "ds_read_b64_tr_b16 v[190:191], v205 offset:1920\n\t" \
    "v_mfma_f32_16x16x32_bf16 v[56:59], v[120:123], v[140:143], v[56:59]\n\t" \
    "ds_read_b64_tr_b16 v[192:193], v206 offset:160\n\t" \
    "v_mfma_f32_16x16x32_bf16 v[60:63], v[124:127], v[140:143], v[60:63]\n\t" \
    "ds_read_b64_tr_b16 v[194:195], v206 offset:1952\n\t" \
    "v_mfma_f32_16x16x32_bf16 v[64:67], v[112:115], v[144:147], v[64:67]\n\t" \
    "ds_read_b64_tr_b16 v[196:197], v205 offset:192\n\t" \
    "v_mfma_f32_16x16x32_bf16 v[68:71], v[116:119], v[144:147], v[68:71]\n\t" \
    "ds_read_b64_tr_b16 v[198:199], v205 offset:1984\n\t" \
    "v_mfma_f32_16x16x32_bf16 v[72:75], v[120:123], v[144:147], v[72:75]\n\t" \
    "v_mfma_f32_16x16x32_bf16 v[76:79], v[124:127], v[144:147], v[76:79]\n\t" \
    "s_add_u32 %[aslot], %[aslot], 16384\n\t" \
    "s_waitcnt lgkmcnt(15)\n\t" \
    "v_mfma_f32_16x16x32_bf16 v[80:83], v[112:115], v[148:151], v[80:83]\n\t" \
    "s_and_b32 %[aslot], %[aslot], 0xffff\n\t" \
    "v_mfma_f32_16x16x32_bf16 v[84:87], v[116:119], v[148:151], v[84:87]\n\t" \
    "s_add_u32 %[bslot], %[bslot], 14336\n\t" \
    "v_mfma_f32_16x16x32_bf16 v[88:91], v[120:123], v[148:151], v[88:91]\n\t" \
    "s_cmp_eq_u32 %[bslot], 86016\n\t" \
    "v_mfma_f32_16x16x32_bf16 v[92:95], v[124:127], v[148:151], v[92:95]\n\t" \
    "s_cselect_b32 %[bslot], 0, %[bslot]\n\t" \
    "v_mfma_f32_16x16x32_bf16 v[96:99], v[112:115], v[152:155], v[96:99]\n\t" \
    "v_add_u32 v207, %[aslot], v200\n\t" \
    "v_mfma_f32_16x16x32_bf16 v[100:103], v[116:119], v[152:155], v[100:103]\n\t" \
    "v_add_u32 v208, %[bslot], v202\n\t" \
    "v_mfma_f32_16x16x32_bf16 v[104:107], v[120:123], v[152:155], v[104:107]\n\t" \
    "v_add_u32 v209, %[bslot], v203\n\t" \
    "v_mfma_f32_16x16x32_bf16 v[108:111], v[124:127], v[152:155], v[108:111]\n\t" \
    "s_barrier\n\t" \
    "s_waitcnt lgkmcnt(15)\n\t" \
    "v_mfma_f32_16x16x32_bf16 v[0:3], v[156:159], v[172:175], v[0:3]\n\t" \
    "ds_read_b128 v[112:115], v207 offset:0\n\t" \
    "s_waitcnt lgkmcnt(15)\n\t" \
    "v_mfma_f32_16x16x32_bf16 v[4:7], v[160:163], v[172:175], v[4:7]\n\t" \
    "ds_read_b64_tr_b16 v[128:129], v208 offset:0\n\t" \
    "s_waitcnt lgkmcnt(15)\n\t" \
    "v_mfma_f32_16x16x32_bf16 v[8:11], v[164:167], v[172:175], v[8:11]\n\t" \
    "ds_read_b64_tr_b16 v[130:131], v208 offset:1792\n\t" \
    "s_waitcnt lgkmcnt(15)\n\t" \
    "v_mfma_f32_16x16x32_bf16 v[12:15], v[168:171], v[172:175], v[12:15]\n\t" \
    "ds_read_b128 v[116:119], v207 offset:2048\n\t" \
    "s_waitcnt lgkmcnt(14)\n\t" \
    "v_mfma_f32_16x16x32_bf16 v[16:19], v[156:159], v[176:179], v[16:19]\n\t" \
    "ds_read_b128 v[120:123], v207 offset:4096\n\t" \
    "v_mfma_f32_16x16x32_bf16 v[20:23], v[160:163], v[176:179], v[20:23]\n\t" \
    "ds_read_b128 v[124:127], v207 offset:6144\n\t" \
    "v_mfma_f32_16x16x32_bf16 v[24:27], v[164:167], v[176:179], v[24:27]\n\t" \
    "ds_read_b64_tr_b16 v[132:133], v209 offset:32\n\t" \
    "v_mfma_f32_16x16x32_bf16 v[28:31], v[168:171], v[176:179], v[28:31]\n\t" \
    "ds_read_b64_tr_b16 v[134:135], v209 offset:1824\n\t" \
    "s_waitcnt lgkmcnt(15)\n\t" \
    "v_mfma_f32_16x16x32_bf16 v[32:35], v[156:159], v[180:183], v[32:35]\n\t" \
    "ds_read_b64_tr_b16 v[136:137], v208 offset:64\n\t" \
    "v_mfma_f32_16x16x32_bf16 v[36:39], v[160:163], v[180:183], v[36:39]\n\t" \
    "ds_read_b64_tr_b16 v[138:139], v208 offset:1856\n\t" \
    "v_mfma_f32_16x16x32_bf16 v[40:43], v[164:167], v[180:183], v[40:43]\n\t" \
    "ds_read_b64_tr_b16 v[140:141], v209 offset:96\n\t" \
    "v_mfma_f32_16x16x32_bf16 v[44:47], v[168:171], v[180:183], v[44:47]\n\t" \
    "ds_read_b64_tr_b16 v[142:143], v209 offset:1888\n\t" \
    "s_waitcnt lgkmcnt(15)\n\t" \
    "v_mfma_f32_16x16x32_bf16 v[48:51], v[156:159], v[184:187], v[48:51]\n\t" \
    "ds_read_b64_tr_b16 v[144:145], v208 offset:128\n\t" \
    "v_mfma_f32_16x16x32_bf16 v[52:55], v[160:163], v[184:187], v[52:55]\n\t" \
    "ds_read_b64_tr_b16 v[146:147], v208 offset:1920\n\t" \
    "v_mfma_f32_16x16x32_bf16 v[56:59], v[164:167], v[184:187], v[56:59]\n\t" \
    "ds_read_b64_tr_b16 v[148:149], v209 offset:160\n\t" \
    "v_mfma_f32_16x16x32_bf16 v[60:63], v[168:171], v[184:187], v[60:63]\n\t" \
    "ds_read_b64_tr_b16 v[150:151], v209 offset:1952\n\t" \
    "v_mfma_f32_16x16x32_bf16 v[64:67], v[156:159], v[188:191], v[64:67]\n\t" \
    "ds_read_b64_tr_b16 v[152:153], v208 offset:192\n\t" \
    "v_mfma_f32_16x16x32_bf16 v[68:71], v[160:163], v[188:191], v[68:71]\n\t" \
    "ds_read_b64_tr_b16 v[154:155], v208 offset:1984\n\t" \
    "v_mfma_f32_16x16x32_bf16 v[72:75], v[164:167], v[188:191], v[72:75]\n\t" \
    "v_mfma_f32_16x16x32_bf16 v[76:79], v[168:171], v[188:191], v[76:79]\n\t" \
    "s_add_u32 %[bslot], %[bslot], 14336\n\t" \
    "s_waitcnt lgkmcnt(15)\n\t" \
    "v_mfma_f32_16x16x32_bf16 v[80:83], v[156:159], v[192:195], v[80:83]\n\t" \
    "s_cmp_eq_u32 %[bslot], 86016\n\t" \
    "v_mfma_f32_16x16x32_bf16 v[84:87], v[160:163], v[192:195], v[84:87]\n\t" \
    "s_cselect_b32 %[bslot], 0, %[bslot]\n\t" \
    "v_mfma_f32_16x16x32_bf16 v[88:91], v[164:167], v[192:195], v[88:91]\n\t" \
    "v_add_u32 v204, %[aslot], v201\n\t" \
    "v_mfma_f32_16x16x32_bf16 v[92:95], v[168:171], v[192:195], v[92:95]\n\t" \
    "v_add_u32 v205, %[bslot], v202\n\t" \
    "v_mfma_f32_16x16x32_bf16 v[96:99], v[156:159], v[196:199], v[96:99]\n\t" \
    "v_add_u32 v206, %[bslot], v203\n\t" \
    "v_mfma_f32_16x16x32_bf16 v[100:103], v[160:163], v[196:199], v[100:103]\n\t" \
    "s_sub_u32 %[cnt], %[cnt], 1\n\t" \
    "v_mfma_f32_16x16x32_bf16 v[104:107], v[164:167], v[196:199], v[104:107]\n\t" \
    "s_cmp_lg_u32 %[cnt], 0\n\t" \
    "v_mfma_f32_16x16x32_bf16 v[108:111], v[168:171], v[196:199], v[108:111]\n\t" \
    "s_barrier\n\t" \
    "s_cbranch_scc1 1b\n\t" \
    "s_waitcnt lgkmcnt(15)\n\t" \
    "v_mfma_f32_16x16x32_bf16 v[0:3], v[112:115], v[128:131], v[0:3]\n\t" \
    "ds_read_b128 v[156:159], v204 offset:0\n\t" \
    "s_waitcnt lgkmcnt(15)\n\t" \
    "v_mfma_f32_16x16x32_bf16 v[4:7], v[116:119], v[128:131], v[4:7]\n\t" \
    "ds_read_b64_tr_b16 v[172:173], v205 offset:0\n\t" \
    "s_waitcnt lgkmcnt(15)\n\t" \
    "v_mfma_f32_16x16x32_bf16 v[8:11], v[120:123], v[128:131], v[8:11]\n\t" \
    "ds_read_b64_tr_b16 v[174:175], v205 offset:1792\n\t" \
    "s_waitcnt lgkmcnt(15)\n\t" \
    "v_mfma_f32_16x16x32_bf16 v[12:15], v[124:127], v[128:131], v[12:15]\n\t" \
    "ds_read_b128 v[160:163], v204 offset:2048\n\t" \
    "s_waitcnt lgkmcnt(14)\n\t" \
    "v_mfma_f32_16x16x32_bf16 v[16:19], v[112:115], v[132:135], v[16:19]\n\t" \
    "ds_read_b128 v[164:167], v204 offset:4096\n\t" \
    "v_mfma_f32_16x16x32_bf16 v[20:23], v[116:119], v[132:135], v[20:23]\n\t" \
    "ds_read_b128 v[168:171], v204 offset:6144\n\t" \
    "v_mfma_f32_16x16x32_bf16 v[24:27], v[120:123], v[132:135], v[24:27]\n\t" \
    "ds_read_b64_tr_b16 v[176:177], v206 offset:32\n\t" \
    "v_mfma_f32_16x16x32_bf16 v[28:31], v[124:127], v[132:135], v[28:31]\n\t" \
    "ds_read_b64_tr_b16 v[178:179], v206 offset:1824\n\t" \
    "s_waitcnt lgkmcnt(15)\n\t" \
    "v_mfma_f32_16x16x32_bf16 v[32:35], v[112:115], v[136:139], v[32:35]\n\t" \
    "ds_read_b64_tr_b16 v[180:181], v205 offset:64\n\t" \
    "v_mfma_f32_16x16x32_bf16 v[36:39], v[116:119], v[136:139], v[36:39]\n\t" \
    "ds_read_b64_tr_b16 v[182:183], v205 offset:1856\n\t" \
    "v_mfma_f32_16x16x32_bf16 v[40:43], v[120:123], v[136:139], v[40:43]\n\t" \
    "ds_read_b64_tr_b16 v[184:185], v206 offset:96\n\t" \
    "v_mfma_f32_16x16x32_bf16 v[44:47], v[124:127], v[136:139], v[44:47]\n\t" \
    "ds_read_b64_tr_b16 v[186:187], v206 offset:1888\n\t" \
    "s_waitcnt lgkmcnt(15)\n\t" \
    "v_mfma_f32_16x16x32_bf16 v[48:51], v[112:115], v[140:143], v[48:51]\n\t" \
    "ds_read_b64_tr_b16 v[188:189], v205 offset:128\n\t" \
    "v_mfma_f32_16x16x32_bf16 v[52:55], v[116:119], v[140:143], v[52:55]\n\t" \
    "ds_read_b64_tr_b16 v[190:191], v205 offset:1920\n\t" \
    "v_mfma_f32_16x16x32_bf16 v[56:59], v[120:123], v[140:143], v[56:59]\n\t" \
    "ds_read_b64_tr_b16 v[192:193], v206 offset:160\n\t" \
    "v_mfma_f32_16x16x32_bf16 v[60:63], v[124:127], v[140:143], v[60:63]\n\t" \
    "ds_read_b64_tr_b16 v[194:195], v206 offset:1952\n\t" \
    "v_mfma_f32_16x16x32_bf16 v[64:67], v[112:115], v[144:147], v[64:67]\n\t" \
    "ds_read_b64_tr_b16 v[196:197], v205 offset:192\n\t" \
    "v_mfma_f32_16x16x32_bf16 v[68:71], v[116:119], v[144:147], v[68:71]\n\t" \
    "ds_read_b64_tr_b16 v[198:199], v205 offset:1984\n\t" \
    "v_mfma_f32_16x16x32_bf16 v[72:75], v[120:123], v[144:147], v[72:75]\n\t" \
    "v_mfma_f32_16x16x32_bf16 v[76:79], v[124:127], v[144:147], v[76:79]\n\t" \
    "s_waitcnt lgkmcnt(15)\n\t" \
    "v_mfma_f32_16x16x32_bf16 v[80:83], v[112:115], v[148:151], v[80:83]\n\t" \
    "v_mfma_f32_16x16x32_bf16 v[84:87], v[116:119], v[148:151], v[84:87]\n\t" \
    "v_mfma_f32_16x16x32_bf16 v[88:91], v[120:123], v[148:151], v[88:91]\n\t" \
    "v_mfma_f32_16x16x32_bf16 v[92:95], v[124:127], v[148:151], v[92:95]\n\t" \
    "v_mfma_f32_16x16x32_bf16 v[96:99], v[112:115], v[152:155], v[96:99]\n\t" \
    "v_mfma_f32_16x16x32_bf16 v[100:103], v[116:119], v[152:155], v[100:103]\n\t" \
    "v_mfma_f32_16x16x32_bf16 v[104:107], v[120:123], v[152:155], v[104:107]\n\t" \
    "v_mfma_f32_16x16x32_bf16 v[108:111], v[124:127], v[152:155], v[108:111]\n\t" \
    "s_barrier\n\t" \
    "s_waitcnt lgkmcnt(15)\n\t" \
    "v_mfma_f32_16x16x32_bf16 v[0:3], v[156:159], v[172:175], v[0:3]\n\t" \
    "s_waitcnt lgkmcnt(14)\n\t" \
    "v_mfma_f32_16x16x32_bf16 v[4:7], v[160:163], v[172:175], v[4:7]\n\t" \
    "s_waitcnt lgkmcnt(13)\n\t" \
    "v_mfma_f32_16x16x32_bf16 v[8:11], v[164:167], v[172:175], v[8:11]\n\t" \
    "s_waitcnt lgkmcnt(12)\n\t" \
    "v_mfma_f32_16x16x32_bf16 v[12:15], v[168:171], v[172:175], v[12:15]\n\t" \
    "s_waitcnt lgkmcnt(10)\n\t" \
    "v_mfma_f32_16x16x32_bf16 v[16:19], v[156:159], v[176:179], v[16:19]\n\t" \
    "v_mfma_f32_16x16x32_bf16 v[20:23], v[160:163], v[176:179], v[20:23]\n\t" \
    "v_mfma_f32_16x16x32_bf16 v[24:27], v[164:167], v[176:179], v[24:27]\n\t" \
    "v_mfma_f32_16x16x32_bf16 v[28:31], v[168:171], v[176:179], v[28:31]\n\t" \
    "s_waitcnt lgkmcnt(8)\n\t" \
    "v_mfma_f32_16x16x32_bf16 v[32:35], v[156:159], v[180:183], v[32:35]\n\t" \
    "v_mfma_f32_16x16x32_bf16 v[36:39], v[160:163], v[180:183], v[36:39]\n\t" \
    "v_mfma_f32_16x16x32_bf16 v[40:43], v[164:167], v[180:183], v[40:43]\n\t" \
    "v_mfma_f32_16x16x32_bf16 v[44:47], v[168:171], v[180:183], v[44:47]\n\t" \
    "s_waitcnt lgkmcnt(6)\n\t" \
    "v_mfma_f32_16x16x32_bf16 v[48:51], v[156:159], v[184:187], v[48:51]\n\t" \
    "v_mfma_f32_16x16x32_bf16 v[52:55], v[160:163], v[184:187], v[52:55]\n\t" \
    "v_mfma_f32_16x16x32_bf16 v[56:59], v[164:167], v[184:187], v[56:59]\n\t" \
    "v_mfma_f32_16x16x32_bf16 v[60:63], v[168:171], v[184:187], v[60:63]\n\t" \
    "s_waitcnt lgkmcnt(4)\n\t" \
    "v_mfma_f32_16x16x32_bf16 v[64:67], v[156:159], v[188:191], v[64:67]\n\t" \
    "v_mfma_f32_16x16x32_bf16 v[68:71], v[160:163], v[188:191], v[68:71]\n\t" \
    "v_mfma_f32_16x16x32_bf16 v[72:75], v[164:167], v[188:191], v[72:75]\n\t" \
    "v_mfma_f32_16x16x32_bf16 v[76:79], v[168:171], v[188:191], v[76:79]\n\t" \
    "s_waitcnt lgkmcnt(2)\n\t" \
    "v_mfma_f32_16x16x32_bf16 v[80:83], v[156:159], v[192:195], v[80:83]\n\t" \
    "v_mfma_f32_16x16x32_bf16 v[84:87], v[160:163], v[192:195], v[84:87]\n\t" \
    "v_mfma_f32_16x16x32_bf16 v[88:91], v[164:167], v[192:195], v[88:91]\n\t" \
    "v_mfma_f32_16x16x32_bf16 v[92:95], v[168:171], v[192:195], v[92:95]\n\t" \
    "s_waitcnt lgkmcnt(0)\n\t" \
    "v_mfma_f32_16x16x32_bf16 v[96:99], v[156:159], v[196:199], v[96:99]\n\t" \
    "v_mfma_f32_16x16x32_bf16 v[100:103], v[160:163], v[196:199], v[100:103]\n\t" \
    "v_mfma_f32_16x16x32_bf16 v[104:107], v[164:167], v[196:199], v[104:107]\n\t" \
    "v_mfma_f32_16x16x32_bf16 v[108:111], v[168:171], v[196:199], v[108:111]\n\t" \
    "s_barrier\n\t" \
    "s_nop 7\n\t" \
    "s_nop 7\n\t" \
    ""
#define G224_CLOB "v112","v113","v114","v115","v116","v117","v118","v119","v120","v121","v122","v123","v124","v125","v126","v127","v128","v129","v130","v131","v132","v133","v134","v135","v136","v137","v138","v139","v140","v141","v142","v143","v144","v145","v146","v147","v148","v149","v150","v151","v152","v153","v154","v155","v156","v157","v158","v159","v160","v161","v162","v163","v164","v165","v166","v167","v168","v169","v170","v171","v172","v173","v174","v175","v176","v177","v178","v179","v180","v181","v182","v183","v184","v185","v186","v187","v188","v189","v190","v191","v192","v193","v194","v195","v196","v197","v198","v199","v204","v205","v206","v207","v208","v209", "scc", "memory"
#define LOADER4 \
    "s_add_u32 m0, s47, s53\n\t" \
    "s_nop 0\n\t" \
    "global_load_lds_dwordx4 v4, s[42:43]\n\t" \
    "global_load_lds_dwordx4 v5, s[42:43] offset:1024\n\t" \
    "s_mov_b32 m0, s46\n\t" \
    "s_add_u32 s46, s46, 14336\n\t" \
    "global_load_lds_dwordx4 v0, s[40:41] offset:0\n\t" \
    "global_load_lds_dwordx4 v1, s[40:41] offset:1024\n\t" \
    "global_load_lds_dwordx4 v2, s[40:41] offset:2048\n\t" \
    "global_load_lds_dwordx4 v3, s[40:41] offset:3072\n\t" \
    "s_cmp_eq_u32 s46, s52\n\t" \
    "s_cselect_b32 s46, s51, s46\n\t" \
    "s_add_u32 s40, s40, s49\n\t" \
    "s_addc_u32 s41, s41, 0\n\t" \
    "s_sub_u32 s54, s54, 1\n\t" \
    "s_cmp_eq_u32 s54, 0\n\t" \
    "s_cselect_b32 s50, s56, 0\n\t" \
    "s_sub_u32 s40, s40, s50\n\t" \
    "s_subb_u32 s41, s41, 0\n\t" \
    "s_add_u32 m0, s58, s53\n\t" \
    "s_nop 0\n\t" \
    "global_load_lds_dwordx4 v4, s[44:45]\n\t" \
    "global_load_lds_dwordx4 v5, s[44:45] offset:1024\n\t" \
    "s_add_u32 s53, s53, 16384\n\t" \
    "s_and_b32 s53, s53, 0xffff\n\t" \
    "s_add_u32 s42, s42, 128\n\t" \
    "s_addc_u32 s43, s43, 0\n\t" \
    "s_add_u32 s44, s44, 128\n\t" \
    "s_addc_u32 s45, s45, 0\n\t" \
    "s_sub_u32 s55, s55, 1\n\t" \
    "s_cmp_eq_u32 s55, 0\n\t" \
    "s_cselect_b32 s50, s57, 0\n\t" \
    "s_sub_u32 s42, s42, s50\n\t" \
    "s_subb_u32 s43, s43, 0\n\t" \
    "s_sub_u32 s44, s44, s50\n\t" \
    "s_subb_u32 s45, s45, 0\n\t" \
    "s_mov_b32 m0, s46\n\t" \
    "s_add_u32 s46, s46, 14336\n\t" \
    "global_load_lds_dwordx4 v0, s[40:41] offset:0\n\t" \
    "global_load_lds_dwordx4 v1, s[40:41] offset:1024\n\t" \
    "global_load_lds_dwordx4 v2, s[40:41] offset:2048\n\t" \
    "global_load_lds_dwordx4 v3, s[40:41] offset:3072\n\t" \
    "s_cmp_eq_u32 s46, s52\n\t" \
    "s_cselect_b32 s46, s51, s46\n\t" \
    "s_add_u32 s40, s40, s49\n\t" \
    "s_addc_u32 s41, s41, 0\n\t" \
    "s_sub_u32 s54, s54, 1\n\t" \
    "s_cmp_eq_u32 s54, 0\n\t" \
    "s_cselect_b32 s50, s56, 0\n\t" \
    "s_sub_u32 s40, s40, s50\n\t" \
    "s_subb_u32 s41, s41, 0\n\t" \
    "s_add_u32 m0, s47, s53\n\t" \
    "s_nop 0\n\t" \
    "global_load_lds_dwordx4 v4, s[42:43]\n\t" \
    "global_load_lds_dwordx4 v5, s[42:43] offset:1024\n\t" \
    "s_mov_b32 m0, s46\n\t" \
    "s_add_u32 s46, s46, 14336\n\t" \
    "global_load_lds_dwordx4 v0, s[40:41] offset:0\n\t" \
    "global_load_lds_dwordx4 v1, s[40:41] offset:1024\n\t" \
    "global_load_lds_dwordx4 v2, s[40:41] offset:2048\n\t" \
    "global_load_lds_dwordx4 v3, s[40:41] offset:3072\n\t" \
    "s_cmp_eq_u32 s46, s52\n\t" \
    "s_cselect_b32 s46, s51, s46\n\t" \
    "s_add_u32 s40, s40, s49\n\t" \
    "s_addc_u32 s41, s41, 0\n\t" \
    "s_sub_u32 s54, s54, 1\n\t" \
    "s_cmp_eq_u32 s54, 0\n\t" \
    "s_cselect_b32 s50, s56, 0\n\t" \
    "s_sub_u32 s40, s40, s50\n\t" \
    "s_subb_u32 s41, s41, 0\n\t" \
    "s_add_u32 m0, s58, s53\n\t" \
    "s_nop 0\n\t" \
    "global_load_lds_dwordx4 v4, s[44:45]\n\t" \
    "global_load_lds_dwordx4 v5, s[44:45] offset:1024\n\t" \
    "s_add_u32 s53, s53, 16384\n\t" \
    "s_and_b32 s53, s53, 0xffff\n\t" \
    "s_add_u32 s42, s42, 128\n\t" \
    "s_addc_u32 s43, s43, 0\n\t" \
    "s_add_u32 s44, s44, 128\n\t" \
    "s_addc_u32 s45, s45, 0\n\t" \
    "s_sub_u32 s55, s55, 1\n\t" \
    "s_cmp_eq_u32 s55, 0\n\t" \
    "s_cselect_b32 s50, s57, 0\n\t" \
    "s_sub_u32 s42, s42, s50\n\t" \
    "s_subb_u32 s43, s43, 0\n\t" \
    "s_sub_u32 s44, s44, s50\n\t" \
    "s_subb_u32 s45, s45, 0\n\t" \
    "s_mov_b32 m0, s46\n\t" \
    "s_add_u32 s46, s46, 14336\n\t" \
    "global_load_lds_dwordx4 v0, s[40:41] offset:0\n\t" \
    "global_load_lds_dwordx4 v1, s[40:41] offset:1024\n\t" \
    "global_load_lds_dwordx4 v2, s[40:41] offset:2048\n\t" \
    "global_load_lds_dwordx4 v3, s[40:41] offset:3072\n\t" \
    "s_cmp_eq_u32 s46, s52\n\t" \
    "s_cselect_b32 s46, s51, s46\n\t" \
    "s_add_u32 s40, s40, s49\n\t" \
    "s_addc_u32 s41, s41, 0\n\t" \
    "s_sub_u32 s54, s54, 1\n\t" \
    "s_cmp_eq_u32 s54, 0\n\t" \
    "s_cselect_b32 s50, s56, 0\n\t" \
    "s_sub_u32 s40, s40, s50\n\t" \
    "s_subb_u32 s41, s41, 0\n\t" \
    "s_add_u32 m0, s47, s53\n\t" \
    "s_nop 0\n\t" \
    "global_load_lds_dwordx4 v4, s[42:43]\n\t" \
    "global_load_lds_dwordx4 v5, s[42:43] offset:1024\n\t" \
    "s_mov_b32 m0, s46\n\t" \
    "s_add_u32 s46, s46, 14336\n\t" \
    "global_load_lds_dwordx4 v0, s[40:41] offset:0\n\t" \
    "global_load_lds_dwordx4 v1, s[40:41] offset:1024\n\t" \
    "global_load_lds_dwordx4 v2, s[40:41] offset:2048\n\t" \
    "global_load_lds_dwordx4 v3, s[40:41] offset:3072\n\t" \
    "s_cmp_eq_u32 s46, s52\n\t" \
    "s_cselect_b32 s46, s51, s46\n\t" \
    "s_add_u32 s40, s40, s49\n\t" \
    "s_addc_u32 s41, s41, 0\n\t" \
    "s_sub_u32 s54, s54, 1\n\t" \
    "s_cmp_eq_u32 s54, 0\n\t" \
    "s_cselect_b32 s50, s56, 0\n\t" \
    "s_sub_u32 s40, s40, s50\n\t" \
    "s_subb_u32 s41, s41, 0\n\t" \
    "s_add_u32 m0, s58, s53\n\t" \
    "s_nop 0\n\t" \
    "global_load_lds_dwordx4 v4, s[44:45]\n\t" \
    "global_load_lds_dwordx4 v5, s[44:45] offset:1024\n\t" \
    "s_add_u32 s53, s53, 16384\n\t" \
    "s_and_b32 s53, s53, 0xffff\n\t" \
    "s_add_u32 s42, s42, 128\n\t" \
    "s_addc_u32 s43, s43, 0\n\t" \
    "s_add_u32 s44, s44, 128\n\t" \
    "s_addc_u32 s45, s45, 0\n\t" \
    "s_sub_u32 s55, s55, 1\n\t" \
    "s_cmp_eq_u32 s55, 0\n\t" \
    "s_cselect_b32 s50, s57, 0\n\t" \
    "s_sub_u32 s42, s42, s50\n\t" \
    "s_subb_u32 s43, s43, 0\n\t" \
    "s_sub_u32 s44, s44, s50\n\t" \
    "s_subb_u32 s45, s45, 0\n\t" \
    "s_waitcnt vmcnt(24)\n\t" \
    "s_barrier\n\t" \
    "s_waitcnt vmcnt(18)\n\t" \
    "s_barrier\n\t" \
    "1:\n\t" \
    "s_mov_b32 m0, s46\n\t" \
    "s_add_u32 s46, s46, 14336\n\t" \
    "global_load_lds_dwordx4 v0, s[40:41] offset:0\n\t" \
    "global_load_lds_dwordx4 v1, s[40:41] offset:1024\n\t" \
    "global_load_lds_dwordx4 v2, s[40:41] offset:2048\n\t" \
    "global_load_lds_dwordx4 v3, s[40:41] offset:3072\n\t" \
    "s_cmp_eq_u32 s46, s52\n\t" \
    "s_cselect_b32 s46, s51, s46\n\t" \
    "s_add_u32 s40, s40, s49\n\t" \
    "s_addc_u32 s41, s41, 0\n\t" \
    "s_sub_u32 s54, s54, 1\n\t" \
    "s_cmp_eq_u32 s54, 0\n\t" \
    "s_cselect_b32 s50, s56, 0\n\t" \
    "s_sub_u32 s40, s40, s50\n\t" \
    "s_subb_u32 s41, s41, 0\n\t" \
    "s_add_u32 m0, s47, s53\n\t" \
    "s_nop 0\n\t" \
    "global_load_lds_dwordx4 v4, s[42:43]\n\t" \
    "global_load_lds_dwordx4 v5, s[42:43] offset:1024\n\t" \
    "s_waitcnt vmcnt(18)\n\t" \
    "s_barrier\n\t" \
    "s_mov_b32 m0, s46\n\t" \
    "s_add_u32 s46, s46, 14336\n\t" \
    "global_load_lds_dwordx4 v0, s[40:41] offset:0\n\t" \
    "global_load_lds_dwordx4 v1, s[40:41] offset:1024\n\t" \
    "global_load_lds_dwordx4 v2, s[40:41] offset:2048\n\t" \
    "global_load_lds_dwordx4 v3, s[40:41] offset:3072\n\t" \
    "s_cmp_eq_u32 s46, s52\n\t" \
    "s_cselect_b32 s46, s51, s46\n\t" \
    "s_add_u32 s40, s40, s49\n\t" \
    "s_addc_u32 s41, s41, 0\n\t" \
    "s_sub_u32 s54, s54, 1\n\t" \
    "s_cmp_eq_u32 s54, 0\n\t" \
    "s_cselect_b32 s50, s56, 0\n\t" \
    "s_sub_u32 s40, s40, s50\n\t" \
    "s_subb_u32 s41, s41, 0\n\t" \
    "s_add_u32 m0, s58, s53\n\t" \
    "s_nop 0\n\t" \
    "global_load_lds_dwordx4 v4, s[44:45]\n\t" \
    "global_load_lds_dwordx4 v5, s[44:45] offset:1024\n\t" \
    "s_add_u32 s53, s53, 16384\n\t" \
    "s_and_b32 s53, s53, 0xffff\n\t" \
    "s_add_u32 s42, s42, 128\n\t" \
    "s_addc_u32 s43, s43, 0\n\t" \
    "s_add_u32 s44, s44, 128\n\t" \
    "s_addc_u32 s45, s45, 0\n\t" \
    "s_sub_u32 s55, s55, 1\n\t" \
    "s_cmp_eq_u32 s55, 0\n\t" \
    "s_cselect_b32 s50, s57, 0\n\t" \
    "s_sub_u32 s42, s42, s50\n\t" \
    "s_subb_u32 s43, s43, 0\n\t" \
    "s_sub_u32 s44, s44, s50\n\t" \
    "s_subb_u32 s45, s45, 0\n\t" \
    "s_sub_u32 s48, s48, 1\n\t" \
    "s_cmp_lg_u32 s48, 0\n\t" \
    "s_waitcnt vmcnt(18)\n\t" \
    "s_barrier\n\t" \
    "s_cbranch_scc1 1b\n\t" \
    "s_mov_b32 m0, s46\n\t" \
    "s_add_u32 s46, s46, 14336\n\t" \
    "global_load_lds_dwordx4 v0, s[40:41] offset:0\n\t" \
    "global_load_lds_dwordx4 v1, s[40:41] offset:1024\n\t" \
    "global_load_lds_dwordx4 v2, s[40:41] offset:2048\n\t" \
    "global_load_lds_dwordx4 v3, s[40:41] offset:3072\n\t" \
    "s_cmp_eq_u32 s46, s52\n\t" \
    "s_cselect_b32 s46, s51, s46\n\t" \
    "s_add_u32 s40, s40, s49\n\t" \
    "s_addc_u32 s41, s41, 0\n\t" \
    "s_sub_u32 s54, s54, 1\n\t" \
    "s_cmp_eq_u32 s54, 0\n\t" \
    "s_cselect_b32 s50, s56, 0\n\t" \
    "s_sub_u32 s40, s40, s50\n\t" \
    "s_subb_u32 s41, s41, 0\n\t" \
    "s_waitcnt vmcnt(16)\n\t" \
    "s_barrier\n\t" \
    "s_waitcnt vmcnt(10)\n\t" \
    "s_barrier\n\t" \
    "s_waitcnt vmcnt(4)\n\t" \
    "s_barrier\n\t" \
    "s_waitcnt vmcnt(0)\n\t" \
    "s_barrier\n\t" \
    "s_barrier\n\t" \
    "s_barrier\n\t" \
    ""
#define LOADER3 \
    "s_add_u32 m0, s47, s53\n\t" \
    "s_nop 0\n\t" \
    "global_load_lds_dwordx4 v4, s[42:43]\n\t" \
    "global_load_lds_dwordx4 v5, s[42:43] offset:1024\n\t" \
    "s_mov_b32 m0, s46\n\t" \
    "s_add_u32 s46, s46, 14336\n\t" \
    "global_load_lds_dwordx4 v0, s[40:41] offset:0\n\t" \
    "global_load_lds_dwordx4 v1, s[40:41] offset:1024\n\t" \
    "global_load_lds_dwordx4 v2, s[40:41] offset:2048\n\t" \
    "s_cmp_eq_u32 s46, s52\n\t" \
    "s_cselect_b32 s46, s51, s46\n\t" \
    "s_add_u32 s40, s40, s49\n\t" \
    "s_addc_u32 s41, s41, 0\n\t" \
    "s_sub_u32 s54, s54, 1\n\t" \
    "s_cmp_eq_u32 s54, 0\n\t" \
    "s_cselect_b32 s50, s56, 0\n\t" \
    "s_sub_u32 s40, s40, s50\n\t" \
    "s_subb_u32 s41, s41, 0\n\t" \
    "s_add_u32 m0, s58, s53\n\t" \
    "s_nop 0\n\t" \
    "global_load_lds_dwordx4 v4, s[44:45]\n\t" \
    "global_load_lds_dwordx4 v5, s[44:45] offset:1024\n\t" \
    "s_add_u32 s53, s53, 16384\n\t" \
    "s_and_b32 s53, s53, 0xffff\n\t" \
    "s_add_u32 s42, s42, 128\n\t" \
    "s_addc_u32 s43, s43, 0\n\t" \
    "s_add_u32 s44, s44, 128\n\t" \
    "s_addc_u32 s45, s45, 0\n\t" \
    "s_sub_u32 s55, s55, 1\n\t" \
    "s_cmp_eq_u32 s55, 0\n\t" \
    "s_cselect_b32 s50, s57, 0\n\t" \
    "s_sub_u32 s42, s42, s50\n\t" \
    "s_subb_u32 s43, s43, 0\n\t" \
    "s_sub_u32 s44, s44, s50\n\t" \
    "s_subb_u32 s45, s45, 0\n\t" \
    "s_mov_b32 m0, s46\n\t" \
    "s_add_u32 s46, s46, 14336\n\t" \
    "global_load_lds_dwordx4 v0, s[40:41] offset:0\n\t" \
    "global_load_lds_dwordx4 v1, s[40:41] offset:1024\n\t" \
    "global_load_lds_dwordx4 v2, s[40:41] offset:2048\n\t" \
    "s_cmp_eq_u32 s46, s52\n\t" \
    "s_cselect_b32 s46, s51, s46\n\t" \
    "s_add_u32 s40, s40, s49\n\t" \
    "s_addc_u32 s41, s41, 0\n\t" \
    "s_sub_u32 s54, s54, 1\n\t" \
    "s_cmp_eq_u32 s54, 0\n\t" \
    "s_cselect_b32 s50, s56, 0\n\t" \
    "s_sub_u32 s40, s40, s50\n\t" \
    "s_subb_u32 s41, s41, 0\n\t" \
    "s_add_u32 m0, s47, s53\n\t" \
    "s_nop 0\n\t" \
    "global_load_lds_dwordx4 v4, s[42:43]\n\t" \
    "global_load_lds_dwordx4 v5, s[42:43] offset:1024\n\t" \
    "s_mov_b32 m0, s46\n\t" \
    "s_add_u32 s46, s46, 14336\n\t" \
    "global_load_lds_dwordx4 v0, s[40:41] offset:0\n\t" \
    "global_load_lds_dwordx4 v1, s[40:41] offset:1024\n\t" \
    "global_load_lds_dwordx4 v2, s[40:41] offset:2048\n\t" \
    "s_cmp_eq_u32 s46, s52\n\t" \
    "s_cselect_b32 s46, s51, s46\n\t" \
    "s_add_u32 s40, s40, s49\n\t" \
    "s_addc_u32 s41, s41, 0\n\t" \
    "s_sub_u32 s54, s54, 1\n\t" \
    "s_cmp_eq_u32 s54, 0\n\t" \
    "s_cselect_b32 s50, s56, 0\n\t" \
    "s_sub_u32 s40, s40, s50\n\t" \
    "s_subb_u32 s41, s41, 0\n\t" \
    "s_add_u32 m0, s58, s53\n\t" \
    "s_nop 0\n\t" \
    "global_load_lds_dwordx4 v4, s[44:45]\n\t" \
    "global_load_lds_dwordx4 v5, s[44:45] offset:1024\n\t" \
    "s_add_u32 s53, s53, 16384\n\t" \
    "s_and_b32 s53, s53, 0xffff\n\t" \
    "s_add_u32 s42, s42, 128\n\t" \
    "s_addc_u32 s43, s43, 0\n\t" \
    "s_add_u32 s44, s44, 128\n\t" \
    "s_addc_u32 s45, s45, 0\n\t" \
    "s_sub_u32 s55, s55, 1\n\t" \
    "s_cmp_eq_u32 s55, 0\n\t" \
    "s_cselect_b32 s50, s57, 0\n\t" \
    "s_sub_u32 s42, s42, s50\n\t" \
    "s_subb_u32 s43, s43, 0\n\t" \
    "s_sub_u32 s44, s44, s50\n\t" \
    "s_subb_u32 s45, s45, 0\n\t" \
    "s_mov_b32 m0, s46\n\t" \
    "s_add_u32 s46, s46, 14336\n\t" \
    "global_load_lds_dwordx4 v0, s[40:41] offset:0\n\t" \
    "global_load_lds_dwordx4 v1, s[40:41] offset:1024\n\t" \
    "global_load_lds_dwordx4 v2, s[40:41] offset:2048\n\t" \
    "s_cmp_eq_u32 s46, s52\n\t" \
    "s_cselect_b32 s46, s51, s46\n\t" \
    "s_add_u32 s40, s40, s49\n\t" \
    "s_addc_u32 s41, s41, 0\n\t" \
    "s_sub_u32 s54, s54, 1\n\t" \
    "s_cmp_eq_u32 s54, 0\n\t" \
    "s_cselect_b32 s50, s56, 0\n\t" \
    "s_sub_u32 s40, s40, s50\n\t" \
    "s_subb_u32 s41, s41, 0\n\t" \
    "s_add_u32 m0, s47, s53\n\t" \
    "s_nop 0\n\t" \
    "global_load_lds_dwordx4 v4, s[42:43]\n\t" \
    "global_load_lds_dwordx4 v5, s[42:43] offset:1024\n\t" \
    "s_mov_b32 m0, s46\n\t" \
    "s_add_u32 s46, s46, 14336\n\t" \
    "global_load_lds_dwordx4 v0, s[40:41] offset:0\n\t" \
    "global_load_lds_dwordx4 v1, s[40:41] offset:1024\n\t" \
    "global_load_lds_dwordx4 v2, s[40:41] offset:2048\n\t" \
    "s_cmp_eq_u32 s46, s52\n\t" \
    "s_cselect_b32 s46, s51, s46\n\t" \
    "s_add_u32 s40, s40, s49\n\t" \
    "s_addc_u32 s41, s41, 0\n\t" \
    "s_sub_u32 s54, s54, 1\n\t" \
    "s_cmp_eq_u32 s54, 0\n\t" \
    "s_cselect_b32 s50, s56, 0\n\t" \
    "s_sub_u32 s40, s40, s50\n\t" \
    "s_subb_u32 s41, s41, 0\n\t" \
    "s_add_u32 m0, s58, s53\n\t" \
    "s_nop 0\n\t" \
    "global_load_lds_dwordx4 v4, s[44:45]\n\t" \
    "global_load_lds_dwordx4 v5, s[44:45] offset:1024\n\t" \
    "s_add_u32 s53, s53, 16384\n\t" \
    "s_and_b32 s53, s53, 0xffff\n\t" \
    "s_add_u32 s42, s42, 128\n\t" \
    "s_addc_u32 s43, s43, 0\n\t" \
    "s_add_u32 s44, s44, 128\n\t" \
    "s_addc_u32 s45, s45, 0\n\t" \
    "s_sub_u32 s55, s55, 1\n\t" \
    "s_cmp_eq_u32 s55, 0\n\t" \
    "s_cselect_b32 s50, s57, 0\n\t" \
    "s_sub_u32 s42, s42, s50\n\t" \
    "s_subb_u32 s43, s43, 0\n\t" \
    "s_sub_u32 s44, s44, s50\n\t" \
    "s_subb_u32 s45, s45, 0\n\t" \
    "s_waitcnt vmcnt(20)\n\t" \
    "s_barrier\n\t" \
    "s_waitcnt vmcnt(15)\n\t" \
    "s_barrier\n\t" \
    "1:\n\t" \
    "s_mov_b32 m0, s46\n\t" \
    "s_add_u32 s46, s46, 14336\n\t" \
    "global_load_lds_dwordx4 v0, s[40:41] offset:0\n\t" \
    "global_load_lds_dwordx4 v1, s[40:41] offset:1024\n\t" \
    "global_load_lds_dwordx4 v2, s[40:41] offset:2048\n\t" \
    "s_cmp_eq_u32 s46, s52\n\t" \
    "s_cselect_b32 s46, s51, s46\n\t" \
    "s_add_u32 s40, s40, s49\n\t" \
    "s_addc_u32 s41, s41, 0\n\t" \
    "s_sub_u32 s54, s54, 1\n\t" \
    "s_cmp_eq_u32 s54, 0\n\t" \
    "s_cselect_b32 s50, s56, 0\n\t" \
    "s_sub_u32 s40, s40, s50\n\t" \
    "s_subb_u32 s41, s41, 0\n\t" \
    "s_add_u32 m0, s47, s53\n\t" \
    "s_nop 0\n\t" \
    "global_load_lds_dwordx4 v4, s[42:43]\n\t" \
    "global_load_lds_dwordx4 v5, s[42:43] offset:1024\n\t" \
    "s_waitcnt vmcnt(15)\n\t" \
    "s_barrier\n\t" \
    "s_mov_b32 m0, s46\n\t" \
    "s_add_u32 s46, s46, 14336\n\t" \
    "global_load_lds_dwordx4 v0, s[40:41] offset:0\n\t" \
    "global_load_lds_dwordx4 v1, s[40:41] offset:1024\n\t" \
    "global_load_lds_dwordx4 v2, s[40:41] offset:2048\n\t" \
    "s_cmp_eq_u32 s46, s52\n\t" \
    "s_cselect_b32 s46, s51, s46\n\t" \
    "s_add_u32 s40, s40, s49\n\t" \
    "s_addc_u32 s41, s41, 0\n\t" \
    "s_sub_u32 s54, s54, 1\n\t" \
    "s_cmp_eq_u32 s54, 0\n\t" \
    "s_cselect_b32 s50, s56, 0\n\t" \
    "s_sub_u32 s40, s40, s50\n\t" \
    "s_subb_u32 s41, s41, 0\n\t" \
    "s_add_u32 m0, s58, s53\n\t" \
    "s_nop 0\n\t" \
    "global_load_lds_dwordx4 v4, s[44:45]\n\t" \
    "global_load_lds_dwordx4 v5, s[44:45] offset:1024\n\t" \
    "s_add_u32 s53, s53, 16384\n\t" \
    "s_and_b32 s53, s53, 0xffff\n\t" \
    "s_add_u32 s42, s42, 128\n\t" \
    "s_addc_u32 s43, s43, 0\n\t" \
    "s_add_u32 s44, s44, 128\n\t" \
    "s_addc_u32 s45, s45, 0\n\t" \
    "s_sub_u32 s55, s55, 1\n\t" \
    "s_cmp_eq_u32 s55, 0\n\t" \
    "s_cselect_b32 s50, s57, 0\n\t" \
    "s_sub_u32 s42, s42, s50\n\t" \
    "s_subb_u32 s43, s43, 0\n\t" \
    "s_sub_u32 s44, s44, s50\n\t" \
    "s_subb_u32 s45, s45, 0\n\t" \
    "s_sub_u32 s48, s48, 1\n\t" \
    "s_cmp_lg_u32 s48, 0\n\t" \
    "s_waitcnt vmcnt(15)\n\t" \
    "s_barrier\n\t" \
    "s_cbranch_scc1 1b\n\t" \
    "s_mov_b32 m0, s46\n\t" \
    "s_add_u32 s46, s46, 14336\n\t" \
    "global_load_lds_dwordx4 v0, s[40:41] offset:0\n\t" \
    "global_load_lds_dwordx4 v1, s[40:41] offset:1024\n\t" \
    "global_load_lds_dwordx4 v2, s[40:41] offset:2048\n\t" \
    "s_cmp_eq_u32 s46, s52\n\t" \
    "s_cselect_b32 s46, s51, s46\n\t" \
    "s_add_u32 s40, s40, s49\n\t" \
    "s_addc_u32 s41, s41, 0\n\t" \
    "s_sub_u32 s54, s54, 1\n\t" \
    "s_cmp_eq_u32 s54, 0\n\t" \
    "s_cselect_b32 s50, s56, 0\n\t" \
    "s_sub_u32 s40, s40, s50\n\t" \
    "s_subb_u32 s41, s41, 0\n\t" \
    "s_waitcnt vmcnt(13)\n\t" \
    "s_barrier\n\t" \
    "s_waitcnt vmcnt(8)\n\t" \
    "s_barrier\n\t" \
    "s_waitcnt vmcnt(3)\n\t" \
    "s_barrier\n\t" \
    "s_waitcnt vmcnt(0)\n\t" \
    "s_barrier\n\t" \
    "s_barrier\n\t" \
    "s_barrier\n\t" \
    ""
#define LOADER_CLOB "s50", "scc", "memory"


namespace {
constexpr int A_SLOT = 16384, B_SLOT = 14336, LDS_A = 0, LDS_B = 65536;
constexpr int LDS_BYTES = 163840;
constexpr unsigned WS_EPOCH = 0, WS_ARR = 1024, WS_F1 = 4096, WS_FO = 6144, WS_FP = 8192;
constexpr unsigned WS_Q = 16384, WS_K = WS_Q + 2097152, WS_V = WS_K + 2097152, WS_O = WS_V + 2097152, WS_PQ = WS_O + 2097152;
constexpr unsigned WS_END = WS_PQ + 7340032;
constexpr int AUX_SC1 = 16;
constexpr unsigned SPIN_MAX = 1u << 22;

DI unsigned cvtpk(float lo, float hi) { f32x2 v = {lo, hi}; bf16x2 b = __builtin_convertvector(v, bf16x2); return __builtin_bit_cast(unsigned, b); }
DI float rbf(float x) { return __uint_as_float(cvtpk(0.0f, x)); }
DI void glds16(const void* sbase, unsigned voff, unsigned lds_base) {
    asm volatile("s_mov_b32 m0, %2\n\ts_nop 0\n\tglobal_load_lds_dwordx4 %1, %0" :: "s"(sbase), "v"(voff), "s"(lds_base) : "memory"); }
DI void wait_vm0() { asm volatile("s_waitcnt vmcnt(0)" ::: "memory"); }
DI void bar() { asm volatile("s_waitcnt lgkmcnt(0)\n\ts_barrier" ::: "memory"); }
DI s16x4 trread(LAS const char* p) { return __builtin_bit_cast(s16x4, __builtin_amdgcn_ds_read_tr16_b64_v4i16((LAS s16x4*)p)); }
DI v4u ld_sc1(__amdgpu_buffer_rsrc_t rs, unsigned off) { return __builtin_amdgcn_raw_buffer_load_b128(rs, off, 0, AUX_SC1); }
DI void st_sc1(__amdgpu_buffer_rsrc_t rs, unsigned off, v4u v) { __builtin_amdgcn_raw_buffer_store_b128(v, rs, off, 0, AUX_SC1); }
DI void flag_store(char* ws, unsigned off, u64 tag) { __hip_atomic_store((GAS u64*)(ws + off), tag, __ATOMIC_RELAXED, __HIP_MEMORY_SCOPE_AGENT); }
DI void poll(char* ws, unsigned off, bool active, u64 tag) {
    for (unsigned spins = 0; spins < SPIN_MAX; ++spins) {
        u64 v = tag;
        if (active) v = __hip_atomic_load((GAS u64*)(ws + off), __ATOMIC_RELAXED, __HIP_MEMORY_SCOPE_AGENT);
        if (__all(v == tag)) break;
        __builtin_amdgcn_s_sleep(1);
    }
    asm volatile("" ::: "memory");
}

template <int NB>
DI void g224_loader(unsigned lds0, int w, int lane, const char* a0, const char* a1, unsigned lda, const char* bbase, unsigned ldb, int nstep, int rot) {
    const int b0 = (NB == 4) ? 4 * w : 8 + 3 * (w - 2);
    unsigned vo[4];
#pragma unroll
    for (int i = 0; i < NB; ++i) {
        const unsigned o = (b0 + i) * 1024 + lane * 16;
        const unsigned row = o / 448, pc = (o - row * 448) >> 4;
        vo[i] = row * ldb + ((pc ^ (((row >> 3) & 1) << 1)) * 16) - i * 1024;
    }
    if (NB == 3) vo[3] = 0;
    const int rl = lane >> 3, ch = lane & 7;
    const unsigned voE = (2 * w * 8 + rl) * lda + ((ch ^ ((rl >> 1) & 7)) * 16);
    const unsigned voO = ((2 * w + 1) * 8 + rl) * lda + ((ch ^ (((8 + rl) >> 1) & 7)) * 16) - 1024;
    const unsigned bdst = lds0 + LDS_B + b0 * 1024, adst = lds0 + LDS_A + 2 * w * 1024;
    const int rb = 2 * rot, ra = rot;
    const char* pb = bbase + (size_t)rb * 32 * ldb; const char* pa0 = a0 + ra * 128; const char* pa1 = a1 + ra * 128;
    unsigned s46 = bdst, s47 = adst, s48 = (nstep - 6) / 2, s49 = 32 * ldb, s51 = bdst, s52 = bdst + 6 * B_SLOT, s53 = 0, s54 = nstep - rb, s55 = nstep / 2 - ra,
             s56 = nstep * 32 * ldb, s57 = (nstep / 2) * 128, s58 = adst + 8192;
    if (NB == 4) asm volatile(LOADER4 : "+{s[40:41]}"(pb), "+{s[42:43]}"(pa0), "+{s[44:45]}"(pa1), "+{s46}"(s46), "+{s48}"(s48), "+{s53}"(s53), "+{s54}"(s54), "+{s55}"(s55)
         : "{v0}"(vo[0]), "{v1}"(vo[1]), "{v2}"(vo[2]), "{v3}"(vo[3]), "{v4}"(voE), "{v5}"(voO), "{s47}"(s47), "{s49}"(s49), "{s51}"(s51), "{s52}"(s52), "{s56}"(s56), "{s57}"(s57), "{s58}"(s58) : LOADER_CLOB);
    else asm volatile(LOADER3 : "+{s[40:41]}"(pb), "+{s[42:43]}"(pa0), "+{s[44:45]}"(pa1), "+{s46}"(s46), "+{s48}"(s48), "+{s53}"(s53), "+{s54}"(s54), "+{s55}"(s55)
         : "{v0}"(vo[0]), "{v1}"(vo[1]), "{v2}"(vo[2]), "{v3}"(vo[3]), "{v4}"(voE), "{v5}"(voO), "{s47}"(s47), "{s49}"(s49), "{s51}"(s51), "{s52}"(s52), "{s56}"(s56), "{s57}"(s57), "{s58}"(s58) : LOADER_CLOB);
}
DI void g224(unsigned lds0, int wid, int lane, const char* a0, const char* a1, unsigned lda, const char* bbase, unsigned ldb, int nstep, int rot, bool swap, f32x16 (&c)[7]) {
    if (wid >= 6) g224_loader<3>(lds0, wid - 4, lane, a0, a1, lda, bbase, ldb, nstep, rot);
    else if (wid >= 4) g224_loader<4>(lds0, wid - 4, lane, a0, a1, lda, bbase, ldb, nstep, rot);
    else {
        const int mh = wid & 1, nh = wid >> 1;
        const int r16 = lane & 15, g = lane >> 4, q = (lane & 15) >> 2, p = lane & 3;
        const unsigned aoff0 = lds0 + LDS_A + (mh * 64 + r16) * 128 + ((g ^ ((r16 >> 1) & 7)) * 16);
        const unsigned brow = lds0 + LDS_B + (8 * g + q) * 448 + p * 8 + nh * 224;
        const unsigned gb = (g & 1) * 32;
        const unsigned bE = nh ? brow - gb : brow + gb, bO = nh ? brow + gb : brow - gb;
        unsigned aslot, bslot, cnt = nstep / 2 - 1;
        if (swap) asm volatile(G224_BODY_SWAP
            : "={v[0:15]}"(c[0]), "={v[16:31]}"(c[1]), "={v[32:47]}"(c[2]), "={v[48:63]}"(c[3]), "={v[64:79]}"(c[4]), "={v[80:95]}"(c[5]), "={v[96:111]}"(c[6]),
              [aslot] "=&s"(aslot), [bslot] "=&s"(bslot), [cnt] "+s"(cnt)
            : "{v200}"(aoff0), "{v201}"(aoff0 ^ 64), "{v202}"(bE), "{v203}"(bO) : G224_CLOB);
        else asm volatile(G224_BODY
            : "={v[0:15]}"(c[0]), "={v[16:31]}"(c[1]), "={v[32:47]}"(c[2]), "={v[48:63]}"(c[3]), "={v[64:79]}"(c[4]), "={v[80:95]}"(c[5]), "={v[96:111]}"(c[6]),
              [aslot] "=&s"(aslot), [bslot] "=&s"(bslot), [cnt] "+s"(cnt)
            : "{v200}"(aoff0), "{v201}"(aoff0 ^ 64), "{v202}"(bE), "{v203}"(bO) : G224_CLOB);
    }
}

DI void final_store(LAS char* L, int wid, int lane, f32x4 (&acc)[7], float sign, char* outp  ) {
    bar();
    const int m = wid & 3;
    if (wid >= 4) {
#pragma unroll
        for (int n = 0; n < 7; ++n) *(LAS f32x4*)(L + (m * 7 + n) * 1024 + lane * 16) = acc[n];
    }
    bar();
    if (wid < 4) {
        LAS char* stg = L + 32768 + m * 4096;
        const int t16 = lane & 15, g = lane >> 4;
#pragma unroll
        for (int n = 0; n < 7; ++n) {
            const f32x4 o = *(const LAS f32x4*)(L + (m * 7 + n) * 1024 + lane * 16);
            v2u w;
            w[0] = cvtpk(acc[n][0] + sign * o[0], acc[n][1] + sign * o[1]);
            w[1] = cvtpk(acc[n][2] + sign * o[2], acc[n][3] + sign * o[3]);
            *(LAS v2u*)(stg + t16 * 240 + n * 32 + g * 8) = w;
        }
        asm volatile("s_waitcnt lgkmcnt(0)" ::: "memory");
#pragma unroll
        for (int k = 0; k < 4; ++k) {
            const int idx = k * 64 + lane;
            if (idx < 224) {
                const int row = idx / 14, chn = idx - row * 14;
                const v4u v = *(const LAS v4u*)(stg + row * 240 + chn * 16);
                *(GAS v4u*)(outp + (size_t)(m * 16 + row) * 1792 + chn * 16) = v;
            }
        }
    }
}

DI bf16x8 add_frag(v4u a, v4u b) {
    v4u r;
#pragma unroll
    for (int j = 0; j < 4; ++j) {
        const float lo = __uint_as_float(a[j] << 16) + __uint_as_float(b[j] << 16);
        const float hi = __uint_as_float(a[j] & 0xffff0000u) + __uint_as_float(b[j] & 0xffff0000u);
        r[j] = cvtpk(lo, hi);
    }
    return __builtin_bit_cast(bf16x8, r);
}

DI v4u pack_pair(const f32x16& ca, const f32x16& cb, int m) {
    unsigned a0 = cvtpk(ca[4 * m], ca[4 * m + 1]), a1 = cvtpk(ca[4 * m + 2], ca[4 * m + 3]);
    unsigned b0 = cvtpk(cb[4 * m], cb[4 * m + 1]), b1 = cvtpk(cb[4 * m + 2], cb[4 * m + 3]);
    auto s0 = __builtin_amdgcn_permlane32_swap(a0, b0, false, false);
    auto s1 = __builtin_amdgcn_permlane32_swap(a1, b1, false, false);
    auto t0 = __builtin_amdgcn_permlane16_swap(s0[0], s0[1], false, false);
    auto t1 = __builtin_amdgcn_permlane16_swap(s1[0], s1[1], false, false);
    return (v4u){t0[0], t1[0], t0[1], t1[1]};
}
DI v4u pack_single(const f32x16& ca, int m) {
    unsigned a0 = cvtpk(ca[4 * m], ca[4 * m + 1]), a1 = cvtpk(ca[4 * m + 2], ca[4 * m + 3]);
    auto t0 = __builtin_amdgcn_permlane16_swap(a0, a0, false, false);
    auto t1 = __builtin_amdgcn_permlane16_swap(a1, a1, false, false);
    return (v4u){t0[0], t1[0], t0[1], t1[1]};
}

DI void role_b(char* lds, int x, int i, int wid, int lane, const char* Bx, const char* Cre, const char* Cim, const char* Wre, const char* Wim, char* out, char* ws, u64 tag) {
    const unsigned lds0 = (unsigned)(uintptr_t)lds;
    LAS char* L = (LAS char*)lds;
    const int tb = x * 4 + (i >> 2), cb = i & 3, ch = cb & 1, kh = cb >> 1;
    const __amdgpu_buffer_rsrc_t rs = __builtin_amdgcn_make_buffer_rsrc(ws, 0, WS_END, 0x00020000);
    f32x16 c[7];
    g224(lds0, wid, lane, Cre + ((size_t)tb * 64 * 2048 + kh * 1024) * 2, Cim + ((size_t)tb * 64 * 2048 + kh * 1024) * 2, 4096,
         Bx + ((size_t)kh * 1024 * 448 + ch * 224) * 2, 896, 32, 2 * x, false, c);
    if (wid == 7 && lane == 0) flag_store(ws, WS_ARR + blockIdx.x * 8, tag);
    const unsigned wvo = (lane >> 2) * 896 + (((lane & 3) ^ ((((lane >> 5) ^ (lane >> 4)) & 1) * 2 + (lane >> 5))) * 16);
    auto wchunk = [&](int u, int pq, int slot) {
        const char* src = (pq ? Wim : Wre) + (size_t)cb * 112 * 896 + u * 64;
#pragma unroll
        for (int k = 0; k < 7; ++k) glds16(src + k * 16 * 896, wvo, __builtin_amdgcn_readfirstlane(lds0 + slot * 7168 + k * 1024));
    };
    if (wid < 4) {
        const int mh = wid & 1, nh = wid >> 1, g = lane >> 4;
        const unsigned base = WS_PQ + ((kh * 2 + mh) * 128 + tb * 4) * 14336 + (ch * 7) * 1024 + lane * 16;
#pragma unroll
        for (int m = 0; m < 4; ++m) {
            const unsigned bm = base + m * 14336;
            if (nh == 0) {
                st_sc1(rs, bm, pack_pair(c[0], c[1], m)); st_sc1(rs, bm + 1024, pack_pair(c[2], c[3], m)); st_sc1(rs, bm + 2048, pack_pair(c[4], c[5], m));
                const v4u s = pack_single(c[6], m);
                if ((g & 1) == 0) st_sc1(rs, bm + 3072 - lane * 16 + ((g >> 1) * 16 + (lane & 15)) * 16, s);
            } else {
                const v4u s = pack_single(c[0], m);
                if ((g & 1) == 0) st_sc1(rs, bm + 3072 - lane * 16 + ((2 + (g >> 1)) * 16 + (lane & 15)) * 16, s);
                st_sc1(rs, bm + 4096, pack_pair(c[1], c[2], m)); st_sc1(rs, bm + 5120, pack_pair(c[3], c[4], m)); st_sc1(rs, bm + 6144, pack_pair(c[5], c[6], m));
            }
        }
        wait_vm0();
    } else {
        for (int s = wid - 4; s < 22; s += 4) wchunk(s >> 1, s & 1, s);
    }
    bar();
    if (wid == 0) {
        if (lane == 0) flag_store(ws, WS_FP + (tb * 4 + cb) * 8, tag);
        poll(ws, WS_FP + (tb * 4 + (lane & 3)) * 8, lane < 4, tag);
    }
    if (wid >= 4) wait_vm0();
    bar();
    const int m = wid & 3, pq = wid >> 2;
    const unsigned pb = WS_PQ + (pq * 128 + tb * 4 + m) * 14336 + lane * 16;
    v4u p0[14], p1[14];
#pragma unroll
    for (int u = 0; u < 14; ++u) { p0[u] = ld_sc1(rs, pb + u * 1024); p1[u] = ld_sc1(rs, pb + 2 * 128 * 14336 + u * 1024); }
    const int e16 = lane & 15, g = lane >> 4, rq = e16 >> 2;
    const unsigned wro = e16 * 64 + ((g ^ ((((rq >> 1) ^ rq) & 1) * 2 + (rq >> 1))) * 16) + pq * 7168;
    f32x4 acc[7];
#pragma unroll
    for (int n = 0; n < 7; ++n) acc[n] = (f32x4){0.f, 0.f, 0.f, 0.f};
#pragma unroll
    for (int u = 0; u < 14; ++u) {
        if (u == 3) {
            bar();
            if (wid >= 4) { for (int s = wid - 4; s < 6; s += 4) wchunk(11 + (s >> 1), s & 1, s); }
        }
        if (u == 11) { if (wid >= 4) wait_vm0(); bar(); }
        const bf16x8 f = add_frag(p0[u], p1[u]);
        const int sl = (u < 11 ? u : u - 11) * 2;
#pragma unroll
        for (int n = 0; n < 7; ++n) {
            const bf16x8 w = *(const LAS bf16x8*)(L + wro + sl * 7168 + n * 1024);
            acc[n] = __builtin_amdgcn_mfma_f32_16x16x32_bf16(w, f, acc[n], 0, 0, 0);
        }
    }
    final_store(L, wid, lane, acc, -1.0f, out + ((size_t)tb * 64 * 896 + 448 + cb * 112) * 2);
}

DI void attention(int iq, int head, int lane, __amdgpu_buffer_rsrc_t rs) {
    const float SC = 0.18898223650461362f, L2E = 1.4426950408889634f;
    const int a = lane & 31, hi = lane >> 5;
    const unsigned hb = head * 64 * 2048 + lane * 16;
    const bf16x8 q0 = __builtin_bit_cast(bf16x8, ld_sc1(rs, WS_Q + hb + iq * 2048)), q1 = __builtin_bit_cast(bf16x8, ld_sc1(rs, WS_Q + hb + iq * 2048 + 1024));
    v4u kf[9][2];
#pragma unroll
    for (int j = 0; j < 9; ++j) { const int kt = max(iq - 8 + j, 0); kf[j][0] = ld_sc1(rs, WS_K + hb + kt * 2048); kf[j][1] = ld_sc1(rs, WS_K + hb + kt * 2048 + 1024); }
    const f32x16 zero = {};
    f32x16 S[8];
    {
        f32x16 so = __builtin_amdgcn_mfma_f32_32x32x16_bf16(__builtin_bit_cast(bf16x8, kf[0][0]), q0, zero, 0, 0, 0);
        so = __builtin_amdgcn_mfma_f32_32x32x16_bf16(__builtin_bit_cast(bf16x8, kf[0][1]), q1, so, 0, 0, 0);
        f32x16 sd = __builtin_amdgcn_mfma_f32_32x32x16_bf16(__builtin_bit_cast(bf16x8, kf[8][0]), q0, zero, 0, 0, 0);
        sd = __builtin_amdgcn_mfma_f32_32x32x16_bf16(__builtin_bit_cast(bf16x8, kf[8][1]), q1, sd, 0, 0, 0);
        const bool oldok = iq >= 8;
#pragma unroll
        for (int r = 0; r < 16; ++r) { const int b = (r & 3) + 8 * (r >> 2) + 4 * hi; S[0][r] = (b <= a) ? sd[r] : (oldok ? so[r] : -INFINITY); }
    }
#pragma unroll
    for (int j = 1; j < 8; ++j) {
        f32x16 s = __builtin_amdgcn_mfma_f32_32x32x16_bf16(__builtin_bit_cast(bf16x8, kf[j][0]), q0, zero, 0, 0, 0);
        s = __builtin_amdgcn_mfma_f32_32x32x16_bf16(__builtin_bit_cast(bf16x8, kf[j][1]), q1, s, 0, 0, 0);
        if (iq - 8 + j < 0) {
#pragma unroll
            for (int r = 0; r < 16; ++r) s[r] = -INFINITY;
        }
        S[j] = s;
    }
    v4u vf[9][2];
#pragma unroll
    for (int j = 0; j < 9; ++j) { const int kt = max(iq - 8 + j, 0); vf[j][0] = ld_sc1(rs, WS_V + hb + kt * 2048); vf[j][1] = ld_sc1(rs, WS_V + hb + kt * 2048 + 1024); }
    float mx = S[0][0];
#pragma unroll
    for (int j = 0; j < 8; ++j)
#pragma unroll
        for (int r = 0; r < 16; ++r) mx = __builtin_fmaxf(mx, S[j][r]);
    { auto rr = __builtin_amdgcn_permlane32_swap(__float_as_uint(mx), __float_as_uint(mx), false, false);
      mx = __builtin_fmaxf(__uint_as_float(rr[0]), __uint_as_float(rr[1])); }
    const float nml = -(rbf(rbf(mx) * SC) * L2E);
    f32x16 O = zero;
#pragma unroll
    for (int j = 0; j < 8; ++j) {
        v4u pw[2];
#pragma unroll
        for (int k = 0; k < 8; ++k) {
            const float e0 = __builtin_amdgcn_exp2f(__builtin_fmaf(rbf(rbf(S[j][2 * k]) * SC), L2E, nml));
            const float e1 = __builtin_amdgcn_exp2f(__builtin_fmaf(rbf(rbf(S[j][2 * k + 1]) * SC), L2E, nml));
            pw[k >> 2][k & 3] = cvtpk(e0, e1);
        }
        if (j == 0) {
            v4u pd[2], po[2];
#pragma unroll
            for (int k = 0; k < 8; ++k) {
                const int b = ((2 * k) & 3) + 8 * ((2 * k) >> 2) + 4 * hi;
                const unsigned msk = (b <= a ? 0xffffu : 0u) | (b + 1 <= a ? 0xffff0000u : 0u);
                pd[k >> 2][k & 3] = pw[k >> 2][k & 3] & msk; po[k >> 2][k & 3] = pw[k >> 2][k & 3] & ~msk;
            }
            O = __builtin_amdgcn_mfma_f32_32x32x16_bf16(__builtin_bit_cast(bf16x8, vf[8][0]), __builtin_bit_cast(bf16x8, pd[0]), O, 0, 0, 0);
            O = __builtin_amdgcn_mfma_f32_32x32x16_bf16(__builtin_bit_cast(bf16x8, vf[8][1]), __builtin_bit_cast(bf16x8, pd[1]), O, 0, 0, 0);
            O = __builtin_amdgcn_mfma_f32_32x32x16_bf16(__builtin_bit_cast(bf16x8, vf[0][0]), __builtin_bit_cast(bf16x8, po[0]), O, 0, 0, 0);
            O = __builtin_amdgcn_mfma_f32_32x32x16_bf16(__builtin_bit_cast(bf16x8, vf[0][1]), __builtin_bit_cast(bf16x8, po[1]), O, 0, 0, 0);
        } else {
            O = __builtin_amdgcn_mfma_f32_32x32x16_bf16(__builtin_bit_cast(bf16x8, vf[j][0]), __builtin_bit_cast(bf16x8, pw[0]), O, 0, 0, 0);
            O = __builtin_amdgcn_mfma_f32_32x32x16_bf16(__builtin_bit_cast(bf16x8, vf[j][1]), __builtin_bit_cast(bf16x8, pw[1]), O, 0, 0, 0);
        }
    }
    auto lr = __builtin_amdgcn_permlane32_swap(__float_as_uint(O[12]), __float_as_uint(O[12]), false, false);
    const float rl = 1.0f / __uint_as_float(lr[1]);
    v4u o0, o1;
#pragma unroll
    for (int k = 0; k < 4; ++k) { o0[k] = cvtpk(O[2 * k] * rl, O[2 * k + 1] * rl); o1[k] = cvtpk(O[8 + 2 * k] * rl, O[8 + 2 * k + 1] * rl); }
    if (hi) { o1[2] = 0; o1[3] = 0; }
    const unsigned ob = WS_O + (head * 128 + iq * 2 + (a >> 4)) * 1024 + (hi * 16 + (a & 15)) * 16;
    st_sc1(rs, ob, o0); st_sc1(rs, ob + 512, o1);
}

DI void role_a(char* lds, int x, int ai, int wid, int lane, const char* Ax, const char* Wqkv, const char* Wo, char* out, char* ws, u64 tag) {
    const unsigned lds0 = (unsigned)(uintptr_t)lds;
    LAS char* L = (LAS char*)lds;
    const __amdgpu_buffer_rsrc_t rs = __builtin_amdgcn_make_buffer_rsrc(ws, 0, WS_END, 0x00020000);
    const int iq = 8 * x + (ai >> 1), hh = ai & 1, cb = (iq & 1) * 2 + hh, rp = iq >> 1;
    if (ai < 12) {
        const int rb = 2 * x + ai / 6, j = ai % 6;
        const bool isv = j >= 4;
        f32x16 c[7];
        g224(lds0, wid, lane, Ax + (size_t)rb * 128 * 896, Ax + ((size_t)rb * 128 + 64) * 896, 896, Wqkv + j * 448, 2688, 14, 0, isv, c);
        if (wid < 4) {
            const int mh = wid & 1, nh = wid >> 1, l16 = lane & 15, g = lane >> 4;
#pragma unroll
            for (int n = 0; n < 7; ++n) {
                unsigned off;
                if (!isv) { const int u4 = nh * 28 + 4 * n + g, h8 = u4 / 7, du = u4 - 7 * h8;
                    off = h8 * 8192 + mh * 4096 + (du >> 2) * 1024 + (((du >> 1) & 1) * 32 + l16) * 16 + (du & 1) * 8; }
                else { const int col = nh * 112 + 16 * n + l16, h8 = col / 28, d = col - 28 * h8;
                    off = h8 * 8192 + mh * 4096 + ((g & 1) * 32 + d) * 16 + (g >> 1) * 8; }
#pragma unroll
                for (int m = 0; m < 4; ++m) {
                    v2u w; w[0] = cvtpk(c[n][4 * m], c[n][4 * m + 1]); w[1] = cvtpk(c[n][4 * m + 2], c[n][4 * m + 3]);
                    const unsigned mo = isv ? (m >> 1) * 2048 + (m & 1) * 1024 : (m >> 1) * 2048 + (m & 1) * 256;
                    *(LAS v2u*)(L + off + mo) = w;
                }
            }
        } else {
            const int t = (wid - 4) * 64 + lane;
            if (!isv) {
#pragma unroll
                for (int k = 0; k < 4; ++k) { const int e = t + 256 * k, h8 = e >> 7, tok = e & 127;
                    *(LAS v2u*)(L + h8 * 8192 + (tok >> 5) * 2048 + 1024 + (32 + (tok & 31)) * 16 + 8) = (v2u){0u, 0u}; }
            } else {
#pragma unroll
                for (int k = 0; k < 2; ++k) { const int e = t + 256 * k, dd = e & 3; const unsigned val = dd == 0 ? 0x3f803f80u : 0u;
                    *(LAS v4u*)(L + (e >> 6) * 8192 + ((e >> 4) & 3) * 2048 + ((e >> 3) & 1) * 1024 + (((e >> 2) & 1) * 32 + 28 + dd) * 16) = (v4u){val, val, val, val}; }
            }
        }
        bar();
        const unsigned gb = (j < 2 ? WS_Q : j < 4 ? WS_K : WS_V) + ((j & 1) * 8 * 64 + rb * 4) * 2048 + lane * 16;
#pragma unroll
        for (int k = 0; k < 8; ++k) {
            const v4u v = *(const LAS v4u*)(L + (wid * 8 + k) * 1024 + lane * 16);
            st_sc1(rs, gb + wid * 64 * 2048 + k * 1024, v);
        }
        wait_vm0();
        bar();
        if (wid == 0 && lane == 0) flag_store(ws, WS_F1 + (rb * 8 + j) * 8, tag);
    }
    if (wid == 7 && lane == 0) flag_store(ws, WS_ARR + blockIdx.x * 8, tag);
    {
        const unsigned wb = (cb * 224) & ~63u;
        const int rr = lane >> 4;
#pragma unroll
        for (int k = 0; k < 14; ++k) {
            const int r4 = wid * 14 + k;
            const int row = 4 * r4 + rr;
            const unsigned vo = rr * 896 + (((lane & 15) ^ ((row & 7) << 1)) * 16);
            glds16(Wo + (size_t)r4 * 4 * 896 + wb, vo, __builtin_amdgcn_readfirstlane(lds0 + r4 * 1024));
        }
    }
    {
        const int rq = iq >> 2;
        const int sel = lane == 0 ? 0 : lane < 4 ? 1 : 2;
        const int rb = sel == 0 ? rq : rq - 2 + ((lane - 1) % 3);
        poll(ws, WS_F1 + (max(rb, 0) * 8 + sel * 2 + hh) * 8, lane < 7 && rb >= 0, tag);
        attention(iq, hh * 8 + wid, lane, rs);
        wait_vm0();
        bar();
        if (wid == 0) {
            if (lane == 0) flag_store(ws, WS_FO + (iq * 2 + hh) * 8, tag);
            poll(ws, WS_FO + (rp * 4 + (lane & 3)) * 8, lane < 4, tag);
        }
        bar();
    }
    {
        const int m = wid & 3, k2 = wid >> 2;
        v4u of[8];
#pragma unroll
        for (int h = 0; h < 8; ++h) of[h] = ld_sc1(rs, WS_O + ((k2 * 8 + h) * 128 + rp * 4 + m) * 1024 + lane * 16);
        const int g = lane >> 4, q = (lane & 15) >> 2, p = lane & 3;
        const int s0 = ((cb * 224) & 63) >> 5;
        unsigned ad[2][2][7];
#pragma unroll
        for (int hp = 0; hp < 2; ++hp)
#pragma unroll
            for (int xx = 0; xx < 2; ++xx) {
                int unit = 4 * (g >> 1) + 2 * xx + (g & 1); if (unit == 7) unit = 6;
                const int rl = 4 * unit + q, sw = (4 * hp + rl) & 7;
#pragma unroll
                for (int n = 0; n < 7; ++n) ad[hp][xx][n] = (k2 * 8 * 28 + hp * 28 + rl) * 256 + (((s0 + n) ^ sw) * 32) + p * 8;
            }
        f32x4 acc[7];
#pragma unroll
        for (int n = 0; n < 7; ++n) acc[n] = (f32x4){0.f, 0.f, 0.f, 0.f};
#pragma unroll
        for (int h = 0; h < 8; ++h) {
#pragma unroll
            for (int n = 0; n < 7; ++n) {
                const s16x4 lo = trread(L + ad[h & 1][0][n] + (h >> 1) * 2 * 7168), hi2 = trread(L + ad[h & 1][1][n] + (h >> 1) * 2 * 7168);
                const bf16x8 w = (bf16x8){lo[0], lo[1], lo[2], lo[3], hi2[0], hi2[1], hi2[2], hi2[3]};
                acc[n] = __builtin_amdgcn_mfma_f32_16x16x32_bf16(w, __builtin_bit_cast(bf16x8, of[h]), acc[n], 0, 0, 0);
            }
        }
        final_store(L, wid, lane, acc, 1.0f, out + ((size_t)rp * 64 * 896 + cb * 112) * 2);
    }
}
}

__global__ void __launch_bounds__(512, 2)
fused_kernel(const char* Ax, const char* Wqkv, const char* Wo, const char* Bx, const char* Cre, const char* Cim, const char* Wre, const char* Wim, char* out, char* ws) {
    extern __shared__ __attribute__((aligned(16))) char lds[];
    const int wid = __builtin_amdgcn_readfirstlane(threadIdx.x >> 6), lane = threadIdx.x & 63;
    const int x = blockIdx.x & 7, i = blockIdx.x >> 3;
    const u64 tag = *(const GAS u64*)(ws + WS_EPOCH) + 1;
    if (i < 16) role_b(lds, x, i, wid, lane, Bx, Cre, Cim, Wre, Wim, out, ws, tag);
    else role_a(lds, x, i - 16, wid, lane, Ax, Wqkv, Wo, out, ws, tag);
    if (blockIdx.x == 0 && wid == 7) {
#pragma unroll
        for (int k = 0; k < 4; ++k) poll(ws, WS_ARR + (k * 64 + lane) * 8, true, tag);
        if (lane == 0) flag_store(ws, WS_EPOCH, tag);
    }
}

extern "C" void kernel_launch(void* const* d_in, const int* in_sizes, int n_in, void* d_out, int out_size, void* d_ws, size_t ws_size, hipStream_t stream) {
    (void)in_sizes; (void)n_in; (void)out_size; (void)ws_size;
    (void)hipFuncSetAttribute((const void*)fused_kernel, hipFuncAttributeMaxDynamicSharedMemorySize, LDS_BYTES);
    fused_kernel<<<256, 512, LDS_BYTES, stream>>>((const char*)d_in[0], (const char*)d_in[1], (const char*)d_in[2], (const char*)d_in[3], (const char*)d_in[4], (const char*)d_in[5],
                                                   (const char*)d_in[6], (const char*)d_in[7], (char*)d_out, (char*)d_ws);
}
```

```cpp
#include <hip/hip_runtime.h>
#include <hip/hip_bf16.h>
#include <stdint.h>

typedef short bf16x8 __attribute__((ext_vector_type(8)));
typedef short s16x4 __attribute__((ext_vector_type(4)));
typedef float f32x2 __attribute__((ext_vector_type(2)));
typedef float f32x4 __attribute__((ext_vector_type(4)));
typedef float f32x16 __attribute__((ext_vector_type(16)));
typedef __bf16 bf16x2 __attribute__((ext_vector_type(2)));
typedef unsigned v4u __attribute__((ext_vector_type(4)));
typedef unsigned v2u __attribute__((ext_vector_type(2)));
typedef unsigned long long u64;
#define LAS __attribute__((address_space(3)))
#define GAS __attribute__((address_space(1)))
#define DI __device__ __forceinline__

#define G224_BODY \
    "v_mov_b32 v0, 0\n\t" \
    "v_mov_b32 v1, 0\n\t" \
    "v_mov_b32 v2, 0\n\t" \
    "v_mov_b32 v3, 0\n\t" \
    "v_mov_b32 v4, 0\n\t" \
    "v_mov_b32 v5, 0\n\t" \
    "v_mov_b32 v6, 0\n\t" \
    "v_mov_b32 v7, 0\n\t" \
    "v_mov_b32 v8, 0\n\t" \
    "v_mov_b32 v9, 0\n\t" \
    "v_mov_b32 v10, 0\n\t" \
    "v_mov_b32 v11, 0\n\t" \
    "v_mov_b32 v12, 0\n\t" \
    "v_mov_b32 v13, 0\n\t" \
    "v_mov_b32 v14, 0\n\t" \
    "v_mov_b32 v15, 0\n\t" \
    "v_mov_b32 v16, 0\n\t" \
    "v_mov_b32 v17, 0\n\t" \
    "v_mov_b32 v18, 0\n\t" \
    "v_mov_b32 v19, 0\n\t" \
    "v_mov_b32 v20, 0\n\t" \
    "v_mov_b32 v21, 0\n\t" \
    "v_mov_b32 v22, 0\n\t" \
    "v_mov_b32 v23, 0\n\t" \
    "v_mov_b32 v24, 0\n\t" \
    "v_mov_b32 v25, 0\n\t" \
    "v_mov_b32 v26, 0\n\t" \
    "v_mov_b32 v27, 0\n\t" \
    "v_mov_b32 v28, 0\n\t" \
    "v_mov_b32 v29, 0\n\t" \
    "v_mov_b32 v30, 0\n\t" \
    "v_mov_b32 v31, 0\n\t" \
    "v_mov_b32 v32, 0\n\t" \
    "v_mov_b32 v33, 0\n\t" \
    "v_mov_b32 v34, 0\n\t" \
    "v_mov_b32 v35, 0\n\t" \
    "v_mov_b32 v36, 0\n\t" \
    "v_mov_b32 v37, 0\n\t" \
    "v_mov_b32 v38, 0\n\t" \
    "v_mov_b32 v39, 0\n\t" \
    "v_mov_b32 v40, 0\n\t" \
    "v_mov_b32 v41, 0\n\t" \
    "v_mov_b32 v42, 0\n\t" \
    "v_mov_b32 v43, 0\n\t" \
    "v_mov_b32 v44, 0\n\t" \
    "v_mov_b32 v45, 0\n\t" \
    "v_mov_b32 v46, 0\n\t" \
    "v_mov_b32 v47, 0\n\t" \
    "v_mov_b32 v48, 0\n\t" \
    "v_mov_b32 v49, 0\n\t" \
    "v_mov_b32 v50, 0\n\t" \
    "v_mov_b32 v51, 0\n\t" \
    "v_mov_b32 v52, 0\n\t" \
    "v_mov_b32 v53, 0\n\t" \
    "v_mov_b32 v54, 0\n\t" \
    "v_mov_b32 v55, 0\n\t" \
    "v_mov_b32 v56, 0\n\t" \
    "v_mov_b32 v57, 0\n\t" \
    "v_mov_b32 v58, 0\n\t" \
    "v_mov_b32 v59, 0\n\t" \
    "v_mov_b32 v60, 0\n\t" \
    "v_mov_b32 v61, 0\n\t" \
    "v_mov_b32 v62, 0\n\t" \
    "v_mov_b32 v63, 0\n\t" \
    "v_mov_b32 v64, 0\n\t" \
    "v_mov_b32 v65, 0\n\t" \
    "v_mov_b32 v66, 0\n\t" \
    "v_mov_b32 v67, 0\n\t" \
    "v_mov_b32 v68, 0\n\t" \
    "v_mov_b32 v69, 0\n\t" \
    "v_mov_b32 v70, 0\n\t" \
    "v_mov_b32 v71, 0\n\t" \
    "v_mov_b32 v72, 0\n\t" \
    "v_mov_b32 v73, 0\n\t" \
    "v_mov_b32 v74, 0\n\t" \
    "v_mov_b32 v75, 0\n\t" \
    "v_mov_b32 v76, 0\n\t" \
    "v_mov_b32 v77, 0\n\t" \
    "v_mov_b32 v78, 0\n\t" \
    "v_mov_b32 v79, 0\n\t" \
    "v_mov_b32 v80, 0\n\t" \
    "v_mov_b32 v81, 0\n\t" \
    "v_mov_b32 v82, 0\n\t" \
    "v_mov_b32 v83, 0\n\t" \
    "v_mov_b32 v84, 0\n\t" \
    "v_mov_b32 v85, 0\n\t" \
    "v_mov_b32 v86, 0\n\t" \
    "v_mov_b32 v87, 0\n\t" \
    "v_mov_b32 v88, 0\n\t" \
    "v_mov_b32 v89, 0\n\t" \
    "v_mov_b32 v90, 0\n\t" \
    "v_mov_b32 v91, 0\n\t" \
    "v_mov_b32 v92, 0\n\t" \
    "v_mov_b32 v93, 0\n\t" \
    "v_mov_b32 v94, 0\n\t" \
    "v_mov_b32 v95, 0\n\t" \
    "v_mov_b32 v96, 0\n\t" \
    "v_mov_b32 v97, 0\n\t" \
    "v_mov_b32 v98, 0\n\t" \
    "v_mov_b32 v99, 0\n\t" \
    "v_mov_b32 v100, 0\n\t" \
    "v_mov_b32 v101, 0\n\t" \
    "v_mov_b32 v102, 0\n\t" \
    "v_mov_b32 v103, 0\n\t" \
    "v_mov_b32 v104, 0\n\t" \
    "v_mov_b32 v105, 0\n\t" \
    "v_mov_b32 v106, 0\n\t" \
    "v_mov_b32 v107, 0\n\t" \
    "v_mov_b32 v108, 0\n\t" \
    "v_mov_b32 v109, 0\n\t" \
    "v_mov_b32 v110, 0\n\t" \
    "v_mov_b32 v111, 0\n\t" \
    "s_waitcnt lgkmcnt(0)\n\t" \
    "s_barrier\n\t" \
    "v_mov_b32 v207, v200\n\t" \
    "v_mov_b32 v208, v202\n\t" \
    "v_mov_b32 v209, v203\n\t" \
    "ds_read_b128 v[112:115], v207 offset:0\n\t" \
    "ds_read_b64_tr_b16 v[128:129], v208 offset:0\n\t" \
    "ds_read_b64_tr_b16 v[130:131], v208 offset:1792\n\t" \
    "ds_read_b128 v[116:119], v207 offset:2048\n\t" \
    "ds_read_b128 v[120:123], v207 offset:4096\n\t" \
    "ds_read_b128 v[124:127], v207 offset:6144\n\t" \
    "ds_read_b64_tr_b16 v[132:133], v209 offset:32\n\t" \
    "ds_read_b64_tr_b16 v[134:135], v209 offset:1824\n\t" \
    "ds_read_b64_tr_b16 v[136:137], v208 offset:64\n\t" \
    "ds_read_b64_tr_b16 v[138:139], v208 offset:1856\n\t" \
    "ds_read_b64_tr_b16 v[140:141], v209 offset:96\n\t" \
    "ds_read_b64_tr_b16 v[142:143], v209 offset:1888\n\t" \
    "ds_read_b64_tr_b16 v[144:145], v208 offset:128\n\t" \
    "ds_read_b64_tr_b16 v[146:147], v208 offset:1920\n\t" \
    "ds_read_b64_tr_b16 v[148:149], v209 offset:160\n\t" \
    "ds_read_b64_tr_b16 v[150:151], v209 offset:1952\n\t" \
    "ds_read_b64_tr_b16 v[152:153], v208 offset:192\n\t" \
    "ds_read_b64_tr_b16 v[154:155], v208 offset:1984\n\t" \
    "s_mov_b32 %[aslot], 0\n\t" \
    "s_mov_b32 %[bslot], 14336\n\t" \
    "v_mov_b32 v204, v201\n\t" \
    "v_add_u32 v205, %[bslot], v202\n\t" \
    "v_add_u32 v206, %[bslot], v203\n\t" \
    "s_waitcnt lgkmcnt(0)\n\t" \
    "s_barrier\n\t" \
    "1:\n\t" \
    "v_mfma_f32_16x16x32_bf16 v[0:3], v[128:131], v[112:115], v[0:3]\n\t" \
    "ds_read_b128 v[156:159], v204 offset:0\n\t" \
    "v_mfma_f32_16x16x32_bf16 v[4:7], v[128:131], v[116:119], v[4:7]\n\t" \
    "ds_read_b64_tr_b16 v[172:173], v205 offset:0\n\t" \
    "v_mfma_f32_16x16x32_bf16 v[8:11], v[128:131], v[120:123], v[8:11]\n\t" \
    "ds_read_b64_tr_b16 v[174:175], v205 offset:1792\n\t" \
    "v_mfma_f32_16x16x32_bf16 v[12:15], v[128:131], v[124:127], v[12:15]\n\t" \
    "ds_read_b128 v[160:163], v204 offset:2048\n\t" \
    "v_mfma_f32_16x16x32_bf16 v[16:19], v[132:135], v[112:115], v[16:19]\n\t" \
    "ds_read_b128 v[164:167], v204 offset:4096\n\t" \
    "v_mfma_f32_16x16x32_bf16 v[20:23], v[132:135], v[116:119], v[20:23]\n\t" \
    "ds_read_b128 v[168:171], v204 offset:6144\n\t" \
    "v_mfma_f32_16x16x32_bf16 v[24:27], v[132:135], v[120:123], v[24:27]\n\t" \
    "ds_read_b64_tr_b16 v[176:177], v206 offset:32\n\t" \
    "v_mfma_f32_16x16x32_bf16 v[28:31], v[132:135], v[124:127], v[28:31]\n\t" \
    "ds_read_b64_tr_b16 v[178:179], v206 offset:1824\n\t" \
    "v_mfma_f32_16x16x32_bf16 v[32:35], v[136:139], v[112:115], v[32:35]\n\t" \
    "ds_read_b64_tr_b16 v[180:181], v205 offset:64\n\t" \
    "v_mfma_f32_16x16x32_bf16 v[36:39], v[136:139], v[116:119], v[36:39]\n\t" \
    "ds_read_b64_tr_b16 v[182:183], v205 offset:1856\n\t" \
    "v_mfma_f32_16x16x32_bf16 v[40:43], v[136:139], v[120:123], v[40:43]\n\t" \
    "ds_read_b64_tr_b16 v[184:185], v206 offset:96\n\t" \
    "v_mfma_f32_16x16x32_bf16 v[44:47], v[136:139], v[124:127], v[44:47]\n\t" \
    "ds_read_b64_tr_b16 v[186:187], v206 offset:1888\n\t" \
    "v_mfma_f32_16x16x32_bf16 v[48:51], v[140:143], v[112:115], v[48:51]\n\t" \
    "ds_read_b64_tr_b16 v[188:189], v205 offset:128\n\t" \
    "v_mfma_f32_16x16x32_bf16 v[52:55], v[140:143], v[116:119], v[52:55]\n\t" \
    "ds_read_b64_tr_b16 v[190:191], v205 offset:1920\n\t" \
    "v_mfma_f32_16x16x32_bf16 v[56:59], v[140:143], v[120:123], v[56:59]\n\t" \
    "ds_read_b64_tr_b16 v[192:193], v206 offset:160\n\t" \
    "v_mfma_f32_16x16x32_bf16 v[60:63], v[140:143], v[124:127], v[60:63]\n\t" \
    "ds_read_b64_tr_b16 v[194:195], v206 offset:1952\n\t" \
    "v_mfma_f32_16x16x32_bf16 v[64:67], v[144:147], v[112:115], v[64:67]\n\t" \
    "ds_read_b64_tr_b16 v[196:197], v205 offset:192\n\t" \
    "v_mfma_f32_16x16x32_bf16 v[68:71], v[144:147], v[116:119], v[68:71]\n\t" \
    "ds_read_b64_tr_b16 v[198:199], v205 offset:1984\n\t" \
    "v_mfma_f32_16x16x32_bf16 v[72:75], v[144:147], v[120:123], v[72:75]\n\t" \
    "v_mfma_f32_16x16x32_bf16 v[76:79], v[144:147], v[124:127], v[76:79]\n\t" \
    "s_add_u32 %[aslot], %[aslot], 16384\n\t" \
    "v_mfma_f32_16x16x32_bf16 v[80:83], v[148:151], v[112:115], v[80:83]\n\t" \
    "s_and_b32 %[aslot], %[aslot], 0xffff\n\t" \
    "v_mfma_f32_16x16x32_bf16 v[84:87], v[148:151], v[116:119], v[84:87]\n\t" \
    "s_add_u32 %[bslot], %[bslot], 14336\n\t" \
    "v_mfma_f32_16x16x32_bf16 v[88:91], v[148:151], v[120:123], v[88:91]\n\t" \
    "s_cmp_eq_u32 %[bslot], 86016\n\t" \
    "v_mfma_f32_16x16x32_bf16 v[92:95], v[148:151], v[124:127], v[92:95]\n\t" \
    "s_cselect_b32 %[bslot], 0, %[bslot]\n\t" \
    "v_mfma_f32_16x16x32_bf16 v[96:99], v[152:155], v[112:115], v[96:99]\n\t" \
    "v_add_u32 v207, %[aslot], v200\n\t" \
    "v_mfma_f32_16x16x32_bf16 v[100:103], v[152:155], v[116:119], v[100:103]\n\t" \
    "v_add_u32 v208, %[bslot], v202\n\t" \
    "v_mfma_f32_16x16x32_bf16 v[104:107], v[152:155], v[120:123], v[104:107]\n\t" \
    "v_add_u32 v209, %[bslot], v203\n\t" \
    "v_mfma_f32_16x16x32_bf16 v[108:111], v[152:155], v[124:127], v[108:111]\n\t" \
    "s_waitcnt lgkmcnt(0)\n\t" \
    "s_barrier\n\t" \
    "v_mfma_f32_16x16x32_bf16 v[0:3], v[172:175], v[156:159], v[0:3]\n\t" \
    "ds_read_b128 v[112:115], v207 offset:0\n\t" \
    "v_mfma_f32_16x16x32_bf16 v[4:7], v[172:175], v[160:163], v[4:7]\n\t" \
    "ds_read_b64_tr_b16 v[128:129], v208 offset:0\n\t" \
    "v_mfma_f32_16x16x32_bf16 v[8:11], v[172:175], v[164:167], v[8:11]\n\t" \
    "ds_read_b64_tr_b16 v[130:131], v208 offset:1792\n\t" \
    "v_mfma_f32_16x16x32_bf16 v[12:15], v[172:175], v[168:171], v[12:15]\n\t" \
    "ds_read_b128 v[116:119], v207 offset:2048\n\t" \
    "v_mfma_f32_16x16x32_bf16 v[16:19], v[176:179], v[156:159], v[16:19]\n\t" \
    "ds_read_b128 v[120:123], v207 offset:4096\n\t" \
    "v_mfma_f32_16x16x32_bf16 v[20:23], v[176:179], v[160:163], v[20:23]\n\t" \
    "ds_read_b128 v[124:127], v207 offset:6144\n\t" \
    "v_mfma_f32_16x16x32_bf16 v[24:27], v[176:179], v[164:167], v[24:27]\n\t" \
    "ds_read_b64_tr_b16 v[132:133], v209 offset:32\n\t" \
    "v_mfma_f32_16x16x32_bf16 v[28:31], v[176:179], v[168:171], v[28:31]\n\t" \
    "ds_read_b64_tr_b16 v[134:135], v209 offset:1824\n\t" \
    "v_mfma_f32_16x16x32_bf16 v[32:35], v[180:183], v[156:159], v[32:35]\n\t" \
    "ds_read_b64_tr_b16 v[136:137], v208 offset:64\n\t" \
    "v_mfma_f32_16x16x32_bf16 v[36:39], v[180:183], v[160:163], v[36:39]\n\t" \
    "ds_read_b64_tr_b16 v[138:139], v208 offset:1856\n\t" \
    "v_mfma_f32_16x16x32_bf16 v[40:43], v[180:183], v[164:167], v[40:43]\n\t" \
    "ds_read_b64_tr_b16 v[140:141], v209 offset:96\n\t" \
    "v_mfma_f32_16x16x32_bf16 v[44:47], v[180:183], v[168:171], v[44:47]\n\t" \
    "ds_read_b64_tr_b16 v[142:143], v209 offset:1888\n\t" \
    "v_mfma_f32_16x16x32_bf16 v[48:51], v[184:187], v[156:159], v[48:51]\n\t" \
    "ds_read_b64_tr_b16 v[144:145], v208 offset:128\n\t" \
    "v_mfma_f32_16x16x32_bf16 v[52:55], v[184:187], v[160:163], v[52:55]\n\t" \
    "ds_read_b64_tr_b16 v[146:147], v208 offset:1920\n\t" \
    "v_mfma_f32_16x16x32_bf16 v[56:59], v[184:187], v[164:167], v[56:59]\n\t" \
    "ds_read_b64_tr_b16 v[148:149], v209 offset:160\n\t" \
    "v_mfma_f32_16x16x32_bf16 v[60:63], v[184:187], v[168:171], v[60:63]\n\t" \
    "ds_read_b64_tr_b16 v[150:151], v209 offset:1952\n\t" \
    "v_mfma_f32_16x16x32_bf16 v[64:67], v[188:191], v[156:159], v[64:67]\n\t" \
    "ds_read_b64_tr_b16 v[152:153], v208 offset:192\n\t" \
    "v_mfma_f32_16x16x32_bf16 v[68:71], v[188:191], v[160:163], v[68:71]\n\t" \
    "ds_read_b64_tr_b16 v[154:155], v208 offset:1984\n\t" \
    "v_mfma_f32_16x16x32_bf16 v[72:75], v[188:191], v[164:167], v[72:75]\n\t" \
    "v_mfma_f32_16x16x32_bf16 v[76:79], v[188:191], v[168:171], v[76:79]\n\t" \
    "s_add_u32 %[bslot], %[bslot], 14336\n\t" \
    "v_mfma_f32_16x16x32_bf16 v[80:83], v[192:195], v[156:159], v[80:83]\n\t" \
    "s_cmp_eq_u32 %[bslot], 86016\n\t" \
    "v_mfma_f32_16x16x32_bf16 v[84:87], v[192:195], v[160:163], v[84:87]\n\t" \
    "s_cselect_b32 %[bslot], 0, %[bslot]\n\t" \
    "v_mfma_f32_16x16x32_bf16 v[88:91], v[192:195], v[164:167], v[88:91]\n\t" \
    "v_add_u32 v204, %[aslot], v201\n\t" \
    "v_mfma_f32_16x16x32_bf16 v[92:95], v[192:195], v[168:171], v[92:95]\n\t" \
    "v_add_u32 v205, %[bslot], v202\n\t" \
    "v_mfma_f32_16x16x32_bf16 v[96:99], v[196:199], v[156:159], v[96:99]\n\t" \
    "v_add_u32 v206, %[bslot], v203\n\t" \
    "v_mfma_f32_16x16x32_bf16 v[100:103], v[196:199], v[160:163], v[100:103]\n\t" \
    "s_sub_u32 %[cnt], %[cnt], 1\n\t" \
    "v_mfma_f32_16x16x32_bf16 v[104:107], v[196:199], v[164:167], v[104:107]\n\t" \
    "s_cmp_lg_u32 %[cnt], 0\n\t" \
    "v_mfma_f32_16x16x32_bf16 v[108:111], v[196:199], v[168:171], v[108:111]\n\t" \
    "s_waitcnt lgkmcnt(0)\n\t" \
    "s_barrier\n\t" \
    "s_cbranch_scc1 1b\n\t" \
    "v_mfma_f32_16x16x32_bf16 v[0:3], v[128:131], v[112:115], v[0:3]\n\t" \
    "ds_read_b128 v[156:159], v204 offset:0\n\t" \
    "v_mfma_f32_16x16x32_bf16 v[4:7], v[128:131], v[116:119], v[4:7]\n\t" \
    "ds_read_b64_tr_b16 v[172:173], v205 offset:0\n\t" \
    "v_mfma_f32_16x16x32_bf16 v[8:11], v[128:131], v[120:123], v[8:11]\n\t" \
    "ds_read_b64_tr_b16 v[174:175], v205 offset:1792\n\t" \
    "v_mfma_f32_16x16x32_bf16 v[12:15], v[128:131], v[124:127], v[12:15]\n\t" \
    "ds_read_b128 v[160:163], v204 offset:2048\n\t" \
    "v_mfma_f32_16x16x32_bf16 v[16:19], v[132:135], v[112:115], v[16:19]\n\t" \
    "ds_read_b128 v[164:167], v204 offset:4096\n\t" \
    "v_mfma_f32_16x16x32_bf16 v[20:23], v[132:135], v[116:119], v[20:23]\n\t" \
    "ds_read_b128 v[168:171], v204 offset:6144\n\t" \
    "v_mfma_f32_16x16x32_bf16 v[24:27], v[132:135], v[120:123], v[24:27]\n\t" \
    "ds_read_b64_tr_b16 v[176:177], v206 offset:32\n\t" \
    "v_mfma_f32_16x16x32_bf16 v[28:31], v[132:135], v[124:127], v[28:31]\n\t" \
    "ds_read_b64_tr_b16 v[178:179], v206 offset:1824\n\t" \
    "v_mfma_f32_16x16x32_bf16 v[32:35], v[136:139], v[112:115], v[32:35]\n\t" \
    "ds_read_b64_tr_b16 v[180:181], v205 offset:64\n\t" \
    "v_mfma_f32_16x16x32_bf16 v[36:39], v[136:139], v[116:119], v[36:39]\n\t" \
    "ds_read_b64_tr_b16 v[182:183], v205 offset:1856\n\t" \
    "v_mfma_f32_16x16x32_bf16 v[40:43], v[136:139], v[120:123], v[40:43]\n\t" \
    "ds_read_b64_tr_b16 v[184:185], v206 offset:96\n\t" \
    "v_mfma_f32_16x16x32_bf16 v[44:47], v[136:139], v[124:127], v[44:47]\n\t" \
    "ds_read_b64_tr_b16 v[186:187], v206 offset:1888\n\t" \
    "v_mfma_f32_16x16x32_bf16 v[48:51], v[140:143], v[112:115], v[48:51]\n\t" \
    "ds_read_b64_tr_b16 v[188:189], v205 offset:128\n\t" \
    "v_mfma_f32_16x16x32_bf16 v[52:55], v[140:143], v[116:119], v[52:55]\n\t" \
    "ds_read_b64_tr_b16 v[190:191], v205 offset:1920\n\t" \
    "v_mfma_f32_16x16x32_bf16 v[56:59], v[140:143], v[120:123], v[56:59]\n\t" \
    "ds_read_b64_tr_b16 v[192:193], v206 offset:160\n\t" \
    "v_mfma_f32_16x16x32_bf16 v[60:63], v[140:143], v[124:127], v[60:63]\n\t" \
    "ds_read_b64_tr_b16 v[194:195], v206 offset:1952\n\t" \
    "v_mfma_f32_16x16x32_bf16 v[64:67], v[144:147], v[112:115], v[64:67]\n\t" \
    "ds_read_b64_tr_b16 v[196:197], v205 offset:192\n\t" \
    "v_mfma_f32_16x16x32_bf16 v[68:71], v[144:147], v[116:119], v[68:71]\n\t" \
    "ds_read_b64_tr_b16 v[198:199], v205 offset:1984\n\t" \
    "v_mfma_f32_16x16x32_bf16 v[72:75], v[144:147], v[120:123], v[72:75]\n\t" \
    "v_mfma_f32_16x16x32_bf16 v[76:79], v[144:147], v[124:127], v[76:79]\n\t" \
    "v_mfma_f32_16x16x32_bf16 v[80:83], v[148:151], v[112:115], v[80:83]\n\t" \
    "v_mfma_f32_16x16x32_bf16 v[84:87], v[148:151], v[116:119], v[84:87]\n\t" \
    "v_mfma_f32_16x16x32_bf16 v[88:91], v[148:151], v[120:123], v[88:91]\n\t" \
    "v_mfma_f32_16x16x32_bf16 v[92:95], v[148:151], v[124:127], v[92:95]\n\t" \
    "v_mfma_f32_16x16x32_bf16 v[96:99], v[152:155], v[112:115], v[96:99]\n\t" \
    "v_mfma_f32_16x16x32_bf16 v[100:103], v[152:155], v[116:119], v[100:103]\n\t" \
    "v_mfma_f32_16x16x32_bf16 v[104:107], v[152:155], v[120:123], v[104:107]\n\t" \
    "v_mfma_f32_16x16x32_bf16 v[108:111], v[152:155], v[124:127], v[108:111]\n\t" \
    "s_waitcnt lgkmcnt(0)\n\t" \
    "s_barrier\n\t" \
    "v_mfma_f32_16x16x32_bf16 v[0:3], v[172:175], v[156:159], v[0:3]\n\t" \
    "v_mfma_f32_16x16x32_bf16 v[4:7], v[172:175], v[160:163], v[4:7]\n\t" \
    "v_mfma_f32_16x16x32_bf16 v[8:11], v[172:175], v[164:167], v[8:11]\n\t" \
    "v_mfma_f32_16x16x32_bf16 v[12:15], v[172:175], v[168:171], v[12:15]\n\t" \
    "v_mfma_f32_16x16x32_bf16 v[16:19], v[176:179], v[156:159], v[16:19]\n\t" \
    "v_mfma_f32_16x16x32_bf16 v[20:23], v[176:179], v[160:163], v[20:23]\n\t" \
    "v_mfma_f32_16x16x32_bf16 v[24:27], v[176:179], v[164:167], v[24:27]\n\t" \
    "v_mfma_f32_16x16x32_bf16 v[28:31], v[176:179], v[168:171], v[28:31]\n\t" \
    "v_mfma_f32_16x16x32_bf16 v[32:35], v[180:183], v[156:159], v[32:35]\n\t" \
    "v_mfma_f32_16x16x32_bf16 v[36:39], v[180:183], v[160:163], v[36:39]\n\t" \
    "v_mfma_f32_16x16x32_bf16 v[40:43], v[180:183], v[164:167], v[40:43]\n\t" \
    "v_mfma_f32_16x16x32_bf16 v[44:47], v[180:183], v[168:171], v[44:47]\n\t" \
    "v_mfma_f32_16x16x32_bf16 v[48:51], v[184:187], v[156:159], v[48:51]\n\t" \
    "v_mfma_f32_16x16x32_bf16 v[52:55], v[184:187], v[160:163], v[52:55]\n\t" \
    "v_mfma_f32_16x16x32_bf16 v[56:59], v[184:187], v[164:167], v[56:59]\n\t" \
    "v_mfma_f32_16x16x32_bf16 v[60:63], v[184:187], v[168:171], v[60:63]\n\t" \
    "v_mfma_f32_16x16x32_bf16 v[64:67], v[188:191], v[156:159], v[64:67]\n\t" \
    "v_mfma_f32_16x16x32_bf16 v[68:71], v[188:191], v[160:163], v[68:71]\n\t" \
    "v_mfma_f32_16x16x32_bf16 v[72:75], v[188:191], v[164:167], v[72:75]\n\t" \
    "v_mfma_f32_16x16x32_bf16 v[76:79], v[188:191], v[168:171], v[76:79]\n\t" \
    "v_mfma_f32_16x16x32_bf16 v[80:83], v[192:195], v[156:159], v[80:83]\n\t" \
    "v_mfma_f32_16x16x32_bf16 v[84:87], v[192:195], v[160:163], v[84:87]\n\t" \
    "v_mfma_f32_16x16x32_bf16 v[88:91], v[192:195], v[164:167], v[88:91]\n\t" \
    "v_mfma_f32_16x16x32_bf16 v[92:95], v[192:195], v[168:171], v[92:95]\n\t" \
    "v_mfma_f32_16x16x32_bf16 v[96:99], v[196:199], v[156:159], v[96:99]\n\t" \
    "v_mfma_f32_16x16x32_bf16 v[100:103], v[196:199], v[160:163], v[100:103]\n\t" \
    "v_mfma_f32_16x16x32_bf16 v[104:107], v[196:199], v[164:167], v[104:107]\n\t" \
    "v_mfma_f32_16x16x32_bf16 v[108:111], v[196:199], v[168:171], v[108:111]\n\t" \
    "s_waitcnt lgkmcnt(0)\n\t" \
    "s_barrier\n\t" \
    "s_nop 7\n\t" \
    "s_nop 7\n\t" \
    ""
#define G224_BODY_SWAP \
    "v_mov_b32 v0, 0\n\t" \
    "v_mov_b32 v1, 0\n\t" \
    "v_mov_b32 v2, 0\n\t" \
    "v_mov_b32 v3, 0\n\t" \
    "v_mov_b32 v4, 0\n\t" \
    "v_mov_b32 v5, 0\n\t" \
    "v_mov_b32 v6, 0\n\t" \
    "v_mov_b32 v7, 0\n\t" \
    "v_mov_b32 v8, 0\n\t" \
    "v_mov_b32 v9, 0\n\t" \
    "v_mov_b32 v10, 0\n\t" \
    "v_mov_b32 v11, 0\n\t" \
    "v_mov_b32 v12, 0\n\t" \
    "v_mov_b32 v13, 0\n\t" \
    "v_mov_b32 v14, 0\n\t" \
    "v_mov_b32 v15, 0\n\t" \
    "v_mov_b32 v16, 0\n\t" \
    "v_mov_b32 v17, 0\n\t" \
    "v_mov_b32 v18, 0\n\t" \
    "v_mov_b32 v19, 0\n\t" \
    "v_mov_b32 v20, 0\n\t" \
    "v_mov_b32 v21, 0\n\t" \
    "v_mov_b32 v22, 0\n\t" \
    "v_mov_b32 v23, 0\n\t" \
    "v_mov_b32 v24, 0\n\t" \
    "v_mov_b32 v25, 0\n\t" \
    "v_mov_b32 v26, 0\n\t" \
    "v_mov_b32 v27, 0\n\t" \
    "v_mov_b32 v28, 0\n\t" \
    "v_mov_b32 v29, 0\n\t" \
    "v_mov_b32 v30, 0\n\t" \
    "v_mov_b32 v31, 0\n\t" \
    "v_mov_b32 v32, 0\n\t" \
    "v_mov_b32 v33, 0\n\t" \
    "v_mov_b32 v34, 0\n\t" \
    "v_mov_b32 v35, 0\n\t" \
    "v_mov_b32 v36, 0\n\t" \
    "v_mov_b32 v37, 0\n\t" \
    "v_mov_b32 v38, 0\n\t" \
    "v_mov_b32 v39, 0\n\t" \
    "v_mov_b32 v40, 0\n\t" \
    "v_mov_b32 v41, 0\n\t" \
    "v_mov_b32 v42, 0\n\t" \
    "v_mov_b32 v43, 0\n\t" \
    "v_mov_b32 v44, 0\n\t" \
    "v_mov_b32 v45, 0\n\t" \
    "v_mov_b32 v46, 0\n\t" \
    "v_mov_b32 v47, 0\n\t" \
    "v_mov_b32 v48, 0\n\t" \
    "v_mov_b32 v49, 0\n\t" \
    "v_mov_b32 v50, 0\n\t" \
    "v_mov_b32 v51, 0\n\t" \
    "v_mov_b32 v52, 0\n\t" \
    "v_mov_b32 v53, 0\n\t" \
    "v_mov_b32 v54, 0\n\t" \
    "v_mov_b32 v55, 0\n\t" \
    "v_mov_b32 v56, 0\n\t" \
    "v_mov_b32 v57, 0\n\t" \
    "v_mov_b32 v58, 0\n\t" \
    "v_mov_b32 v59, 0\n\t" \
    "v_mov_b32 v60, 0\n\t" \
    "v_mov_b32 v61, 0\n\t" \
    "v_mov_b32 v62, 0\n\t" \
    "v_mov_b32 v63, 0\n\t" \
    "v_mov_b32 v64, 0\n\t" \
    "v_mov_b32 v65, 0\n\t" \
    "v_mov_b32 v66, 0\n\t" \
    "v_mov_b32 v67, 0\n\t" \
    "v_mov_b32 v68, 0\n\t" \
    "v_mov_b32 v69, 0\n\t" \
    "v_mov_b32 v70, 0\n\t" \
    "v_mov_b32 v71, 0\n\t" \
    "v_mov_b32 v72, 0\n\t" \
    "v_mov_b32 v73, 0\n\t" \
    "v_mov_b32 v74, 0\n\t" \
    "v_mov_b32 v75, 0\n\t" \
    "v_mov_b32 v76, 0\n\t" \
    "v_mov_b32 v77, 0\n\t" \
    "v_mov_b32 v78, 0\n\t" \
    "v_mov_b32 v79, 0\n\t" \
    "v_mov_b32 v80, 0\n\t" \
    "v_mov_b32 v81, 0\n\t" \
    "v_mov_b32 v82, 0\n\t" \
    "v_mov_b32 v83, 0\n\t" \
    "v_mov_b32 v84, 0\n\t" \
    "v_mov_b32 v85, 0\n\t" \
    "v_mov_b32 v86, 0\n\t" \
    "v_mov_b32 v87, 0\n\t" \
    "v_mov_b32 v88, 0\n\t" \
    "v_mov_b32 v89, 0\n\t" \
    "v_mov_b32 v90, 0\n\t" \
    "v_mov_b32 v91, 0\n\t" \
    "v_mov_b32 v92, 0\n\t" \
    "v_mov_b32 v93, 0\n\t" \
    "v_mov_b32 v94, 0\n\t" \
    "v_mov_b32 v95, 0\n\t" \
    "v_mov_b32 v96, 0\n\t" \
    "v_mov_b32 v97, 0\n\t" \
    "v_mov_b32 v98, 0\n\t" \
    "v_mov_b32 v99, 0\n\t" \
    "v_mov_b32 v100, 0\n\t" \
    "v_mov_b32 v101, 0\n\t" \
    "v_mov_b32 v102, 0\n\t" \
    "v_mov_b32 v103, 0\n\t" \
    "v_mov_b32 v104, 0\n\t" \
    "v_mov_b32 v105, 0\n\t" \
    "v_mov_b32 v106, 0\n\t" \
    "v_mov_b32 v107, 0\n\t" \
    "v_mov_b32 v108, 0\n\t" \
    "v_mov_b32 v109, 0\n\t" \
    "v_mov_b32 v110, 0\n\t" \
    "v_mov_b32 v111, 0\n\t" \
    "s_waitcnt lgkmcnt(0)\n\t" \
    "s_barrier\n\t" \
    "v_mov_b32 v207, v200\n\t" \
    "v_mov_b32 v208, v202\n\t" \
    "v_mov_b32 v209, v203\n\t" \
    "ds_read_b128 v[112:115], v207 offset:0\n\t" \
    "ds_read_b64_tr_b16 v[128:129], v208 offset:0\n\t" \
    "ds_read_b64_tr_b16 v[130:131], v208 offset:1792\n\t" \
    "ds_read_b128 v[116:119], v207 offset:2048\n\t" \
    "ds_read_b128 v[120:123], v207 offset:4096\n\t" \
    "ds_read_b128 v[124:127], v207 offset:6144\n\t" \
    "ds_read_b64_tr_b16 v[132:133], v209 offset:32\n\t" \
    "ds_read_b64_tr_b16 v[134:135], v209 offset:1824\n\t" \
    "ds_read_b64_tr_b16 v[136:137], v208 offset:64\n\t" \
    "ds_read_b64_tr_b16 v[138:139], v208 offset:1856\n\t" \
    "ds_read_b64_tr_b16 v[140:141], v209 offset:96\n\t" \
    "ds_read_b64_tr_b16 v[142:143], v209 offset:1888\n\t" \
    "ds_read_b64_tr_b16 v[144:145], v208 offset:128\n\t" \
    "ds_read_b64_tr_b16 v[146:147], v208 offset:1920\n\t" \
    "ds_read_b64_tr_b16 v[148:149], v209 offset:160\n\t" \
    "ds_read_b64_tr_b16 v[150:151], v209 offset:1952\n\t" \
    "ds_read_b64_tr_b16 v[152:153], v208 offset:192\n\t" \
    "ds_read_b64_tr_b16 v[154:155], v208 offset:1984\n\t" \
    "s_mov_b32 %[aslot], 0\n\t" \
    "s_mov_b32 %[bslot], 14336\n\t" \
    "v_mov_b32 v204, v201\n\t" \
    "v_add_u32 v205, %[bslot], v202\n\t" \
    "v_add_u32 v206, %[bslot], v203\n\t" \
    "s_waitcnt lgkmcnt(0)\n\t" \
    "s_barrier\n\t" \
    "1:\n\t" \
    "v_mfma_f32_16x16x32_bf16 v[0:3], v[112:115], v[128:131], v[0:3]\n\t" \
    "ds_read_b128 v[156:159], v204 offset:0\n\t" \
    "v_mfma_f32_16x16x32_bf16 v[4:7], v[116:119], v[128:131], v[4:7]\n\t" \
    "ds_read_b64_tr_b16 v[172:173], v205 offset:0\n\t" \
    "v_mfma_f32_16x16x32_bf16 v[8:11], v[120:123], v[128:131], v[8:11]\n\t" \
    "ds_read_b64_tr_b16 v[174:175], v205 offset:1792\n\t" \
    "v_mfma_f32_16x16x32_bf16 v[12:15], v[124:127], v[128:131], v[12:15]\n\t" \
    "ds_read_b128 v[160:163], v204 offset:2048\n\t" \
    "v_mfma_f32_16x16x32_bf16 v[16:19], v[112:115], v[132:135], v[16:19]\n\t" \
    "ds_read_b128 v[164:167], v204 offset:4096\n\t" \
    "v_mfma_f32_16x16x32_bf16 v[20:23], v[116:119], v[132:135], v[20:23]\n\t" \
    "ds_read_b128 v[168:171], v204 offset:6144\n\t" \
    "v_mfma_f32_16x16x32_bf16 v[24:27], v[120:123], v[132:135], v[24:27]\n\t" \
    "ds_read_b64_tr_b16 v[176:177], v206 offset:32\n\t" \
    "v_mfma_f32_16x16x32_bf16 v[28:31], v[124:127], v[132:135], v[28:31]\n\t" \
    "ds_read_b64_tr_b16 v[178:179], v206 offset:1824\n\t" \
    "v_mfma_f32_16x16x32_bf16 v[32:35], v[112:115], v[136:139], v[32:35]\n\t" \
    "ds_read_b64_tr_b16 v[180:181], v205 offset:64\n\t" \
    "v_mfma_f32_16x16x32_bf16 v[36:39], v[116:119], v[136:139], v[36:39]\n\t" \
    "ds_read_b64_tr_b16 v[182:183], v205 offset:1856\n\t" \
    "v_mfma_f32_16x16x32_bf16 v[40:43], v[120:123], v[136:139], v[40:43]\n\t" \
    "ds_read_b64_tr_b16 v[184:185], v206 offset:96\n\t" \
    "v_mfma_f32_16x16x32_bf16 v[44:47], v[124:127], v[136:139], v[44:47]\n\t" \
    "ds_read_b64_tr_b16 v[186:187], v206 offset:1888\n\t" \
    "v_mfma_f32_16x16x32_bf16 v[48:51], v[112:115], v[140:143], v[48:51]\n\t" \
    "ds_read_b64_tr_b16 v[188:189], v205 offset:128\n\t" \
    "v_mfma_f32_16x16x32_bf16 v[52:55], v[116:119], v[140:143], v[52:55]\n\t" \
    "ds_read_b64_tr_b16 v[190:191], v205 offset:1920\n\t" \
    "v_mfma_f32_16x16x32_bf16 v[56:59], v[120:123], v[140:143], v[56:59]\n\t" \
    "ds_read_b64_tr_b16 v[192:193], v206 offset:160\n\t" \
    "v_mfma_f32_16x16x32_bf16 v[60:63], v[124:127], v[140:143], v[60:63]\n\t" \
    "ds_read_b64_tr_b16 v[194:195], v206 offset:1952\n\t" \
    "v_mfma_f32_16x16x32_bf16 v[64:67], v[112:115], v[144:147], v[64:67]\n\t" \
    "ds_read_b64_tr_b16 v[196:197], v205 offset:192\n\t" \
    "v_mfma_f32_16x16x32_bf16 v[68:71], v[116:119], v[144:147], v[68:71]\n\t" \
    "ds_read_b64_tr_b16 v[198:199], v205 offset:1984\n\t" \
    "v_mfma_f32_16x16x32_bf16 v[72:75], v[120:123], v[144:147], v[72:75]\n\t" \
    "v_mfma_f32_16x16x32_bf16 v[76:79], v[124:127], v[144:147], v[76:79]\n\t" \
    "s_add_u32 %[aslot], %[aslot], 16384\n\t" \
    "v_mfma_f32_16x16x32_bf16 v[80:83], v[112:115], v[148:151], v[80:83]\n\t" \
    "s_and_b32 %[aslot], %[aslot], 0xffff\n\t" \
    "v_mfma_f32_16x16x32_bf16 v[84:87], v[116:119], v[148:151], v[84:87]\n\t" \
    "s_add_u32 %[bslot], %[bslot], 14336\n\t" \
    "v_mfma_f32_16x16x32_bf16 v[88:91], v[120:123], v[148:151], v[88:91]\n\t" \
    "s_cmp_eq_u32 %[bslot], 86016\n\t" \
    "v_mfma_f32_16x16x32_bf16 v[92:95], v[124:127], v[148:151], v[92:95]\n\t" \
    "s_cselect_b32 %[bslot], 0, %[bslot]\n\t" \
    "v_mfma_f32_16x16x32_bf16 v[96:99], v[112:115], v[152:155], v[96:99]\n\t" \
    "v_add_u32 v207, %[aslot], v200\n\t" \
    "v_mfma_f32_16x16x32_bf16 v[100:103], v[116:119], v[152:155], v[100:103]\n\t" \
    "v_add_u32 v208, %[bslot], v202\n\t" \
    "v_mfma_f32_16x16x32_bf16 v[104:107], v[120:123], v[152:155], v[104:107]\n\t" \
    "v_add_u32 v209, %[bslot], v203\n\t" \
    "v_mfma_f32_16x16x32_bf16 v[108:111], v[124:127], v[152:155], v[108:111]\n\t" \
    "s_waitcnt lgkmcnt(0)\n\t" \
    "s_barrier\n\t" \
    "v_mfma_f32_16x16x32_bf16 v[0:3], v[156:159], v[172:175], v[0:3]\n\t" \
    "ds_read_b128 v[112:115], v207 offset:0\n\t" \
    "v_mfma_f32_16x16x32_bf16 v[4:7], v[160:163], v[172:175], v[4:7]\n\t" \
    "ds_read_b64_tr_b16 v[128:129], v208 offset:0\n\t" \
    "v_mfma_f32_16x16x32_bf16 v[8:11], v[164:167], v[172:175], v[8:11]\n\t" \
    "ds_read_b64_tr_b16 v[130:131], v208 offset:1792\n\t" \
    "v_mfma_f32_16x16x32_bf16 v[12:15], v[168:171], v[172:175], v[12:15]\n\t" \
    "ds_read_b128 v[116:119], v207 offset:2048\n\t" \
    "v_mfma_f32_16x16x32_bf16 v[16:19], v[156:159], v[176:179], v[16:19]\n\t" \
    "ds_read_b128 v[120:123], v207 offset:4096\n\t" \
    "v_mfma_f32_16x16x32_bf16 v[20:23], v[160:163], v[176:179], v[20:23]\n\t" \
    "ds_read_b128 v[124:127], v207 offset:6144\n\t" \
    "v_mfma_f32_16x16x32_bf16 v[24:27], v[164:167], v[176:179], v[24:27]\n\t" \
    "ds_read_b64_tr_b16 v[132:133], v209 offset:32\n\t" \
    "v_mfma_f32_16x16x32_bf16 v[28:31], v[168:171], v[176:179], v[28:31]\n\t" \
    "ds_read_b64_tr_b16 v[134:135], v209 offset:1824\n\t" \
    "v_mfma_f32_16x16x32_bf16 v[32:35], v[156:159], v[180:183], v[32:35]\n\t" \
    "ds_read_b64_tr_b16 v[136:137], v208 offset:64\n\t" \
    "v_mfma_f32_16x16x32_bf16 v[36:39], v[160:163], v[180:183], v[36:39]\n\t" \
    "ds_read_b64_tr_b16 v[138:139], v208 offset:1856\n\t" \
    "v_mfma_f32_16x16x32_bf16 v[40:43], v[164:167], v[180:183], v[40:43]\n\t" \
    "ds_read_b64_tr_b16 v[140:141], v209 offset:96\n\t" \
    "v_mfma_f32_16x16x32_bf16 v[44:47], v[168:171], v[180:183], v[44:47]\n\t" \
    "ds_read_b64_tr_b16 v[142:143], v209 offset:1888\n\t" \
    "v_mfma_f32_16x16x32_bf16 v[48:51], v[156:159], v[184:187], v[48:51]\n\t" \
    "ds_read_b64_tr_b16 v[144:145], v208 offset:128\n\t" \
    "v_mfma_f32_16x16x32_bf16 v[52:55], v[160:163], v[184:187], v[52:55]\n\t" \
    "ds_read_b64_tr_b16 v[146:147], v208 offset:1920\n\t" \
    "v_mfma_f32_16x16x32_bf16 v[56:59], v[164:167], v[184:187], v[56:59]\n\t" \
    "ds_read_b64_tr_b16 v[148:149], v209 offset:160\n\t" \
    "v_mfma_f32_16x16x32_bf16 v[60:63], v[168:171], v[184:187], v[60:63]\n\t" \
    "ds_read_b64_tr_b16 v[150:151], v209 offset:1952\n\t" \
    "v_mfma_f32_16x16x32_bf16 v[64:67], v[156:159], v[188:191], v[64:67]\n\t" \
    "ds_read_b64_tr_b16 v[152:153], v208 offset:192\n\t" \
    "v_mfma_f32_16x16x32_bf16 v[68:71], v[160:163], v[188:191], v[68:71]\n\t" \
    "ds_read_b64_tr_b16 v[154:155], v208 offset:1984\n\t" \
    "v_mfma_f32_16x16x32_bf16 v[72:75], v[164:167], v[188:191], v[72:75]\n\t" \
    "v_mfma_f32_16x16x32_bf16 v[76:79], v[168:171], v[188:191], v[76:79]\n\t" \
    "s_add_u32 %[bslot], %[bslot], 14336\n\t" \
    "v_mfma_f32_16x16x32_bf16 v[80:83], v[156:159], v[192:195], v[80:83]\n\t" \
    "s_cmp_eq_u32 %[bslot], 86016\n\t" \
    "v_mfma_f32_16x16x32_bf16 v[84:87], v[160:163], v[192:195], v[84:87]\n\t" \
    "s_cselect_b32 %[bslot], 0, %[bslot]\n\t" \
    "v_mfma_f32_16x16x32_bf16 v[88:91], v[164:167], v[192:195], v[88:91]\n\t" \
    "v_add_u32 v204, %[aslot], v201\n\t" \
    "v_mfma_f32_16x16x32_bf16 v[92:95], v[168:171], v[192:195], v[92:95]\n\t" \
    "v_add_u32 v205, %[bslot], v202\n\t" \
    "v_mfma_f32_16x16x32_bf16 v[96:99], v[156:159], v[196:199], v[96:99]\n\t" \
    "v_add_u32 v206, %[bslot], v203\n\t" \
    "v_mfma_f32_16x16x32_bf16 v[100:103], v[160:163], v[196:199], v[100:103]\n\t" \
    "s_sub_u32 %[cnt], %[cnt], 1\n\t" \
    "v_mfma_f32_16x16x32_bf16 v[104:107], v[164:167], v[196:199], v[104:107]\n\t" \
    "s_cmp_lg_u32 %[cnt], 0\n\t" \
    "v_mfma_f32_16x16x32_bf16 v[108:111], v[168:171], v[196:199], v[108:111]\n\t" \
    "s_waitcnt lgkmcnt(0)\n\t" \
    "s_barrier\n\t" \
    "s_cbranch_scc1 1b\n\t" \
    "v_mfma_f32_16x16x32_bf16 v[0:3], v[112:115], v[128:131], v[0:3]\n\t" \
    "ds_read_b128 v[156:159], v204 offset:0\n\t" \
    "v_mfma_f32_16x16x32_bf16 v[4:7], v[116:119], v[128:131], v[4:7]\n\t" \
    "ds_read_b64_tr_b16 v[172:173], v205 offset:0\n\t" \
    "v_mfma_f32_16x16x32_bf16 v[8:11], v[120:123], v[128:131], v[8:11]\n\t" \
    "ds_read_b64_tr_b16 v[174:175], v205 offset:1792\n\t" \
    "v_mfma_f32_16x16x32_bf16 v[12:15], v[124:127], v[128:131], v[12:15]\n\t" \
    "ds_read_b128 v[160:163], v204 offset:2048\n\t" \
    "v_mfma_f32_16x16x32_bf16 v[16:19], v[112:115], v[132:135], v[16:19]\n\t" \
    "ds_read_b128 v[164:167], v204 offset:4096\n\t" \
    "v_mfma_f32_16x16x32_bf16 v[20:23], v[116:119], v[132:135], v[20:23]\n\t" \
    "ds_read_b128 v[168:171], v204 offset:6144\n\t" \
    "v_mfma_f32_16x16x32_bf16 v[24:27], v[120:123], v[132:135], v[24:27]\n\t" \
    "ds_read_b64_tr_b16 v[176:177], v206 offset:32\n\t" \
    "v_mfma_f32_16x16x32_bf16 v[28:31], v[124:127], v[132:135], v[28:31]\n\t" \
    "ds_read_b64_tr_b16 v[178:179], v206 offset:1824\n\t" \
    "v_mfma_f32_16x16x32_bf16 v[32:35], v[112:115], v[136:139], v[32:35]\n\t" \
    "ds_read_b64_tr_b16 v[180:181], v205 offset:64\n\t" \
    "v_mfma_f32_16x16x32_bf16 v[36:39], v[116:119], v[136:139], v[36:39]\n\t" \
    "ds_read_b64_tr_b16 v[182:183], v205 offset:1856\n\t" \
    "v_mfma_f32_16x16x32_bf16 v[40:43], v[120:123], v[136:139], v[40:43]\n\t" \
    "ds_read_b64_tr_b16 v[184:185], v206 offset:96\n\t" \
    "v_mfma_f32_16x16x32_bf16 v[44:47], v[124:127], v[136:139], v[44:47]\n\t" \
    "ds_read_b64_tr_b16 v[186:187], v206 offset:1888\n\t" \
    "v_mfma_f32_16x16x32_bf16 v[48:51], v[112:115], v[140:143], v[48:51]\n\t" \
    "ds_read_b64_tr_b16 v[188:189], v205 offset:128\n\t" \
    "v_mfma_f32_16x16x32_bf16 v[52:55], v[116:119], v[140:143], v[52:55]\n\t" \
    "ds_read_b64_tr_b16 v[190:191], v205 offset:1920\n\t" \
    "v_mfma_f32_16x16x32_bf16 v[56:59], v[120:123], v[140:143], v[56:59]\n\t" \
    "ds_read_b64_tr_b16 v[192:193], v206 offset:160\n\t" \
    "v_mfma_f32_16x16x32_bf16 v[60:63], v[124:127], v[140:143], v[60:63]\n\t" \
    "ds_read_b64_tr_b16 v[194:195], v206 offset:1952\n\t" \
    "v_mfma_f32_16x16x32_bf16 v[64:67], v[112:115], v[144:147], v[64:67]\n\t" \
    "ds_read_b64_tr_b16 v[196:197], v205 offset:192\n\t" \
    "v_mfma_f32_16x16x32_bf16 v[68:71], v[116:119], v[144:147], v[68:71]\n\t" \
    "ds_read_b64_tr_b16 v[198:199], v205 offset:1984\n\t" \
    "v_mfma_f32_16x16x32_bf16 v[72:75], v[120:123], v[144:147], v[72:75]\n\t" \
    "v_mfma_f32_16x16x32_bf16 v[76:79], v[124:127], v[144:147], v[76:79]\n\t" \
    "v_mfma_f32_16x16x32_bf16 v[80:83], v[112:115], v[148:151], v[80:83]\n\t" \
    "v_mfma_f32_16x16x32_bf16 v[84:87], v[116:119], v[148:151], v[84:87]\n\t" \
    "v_mfma_f32_16x16x32_bf16 v[88:91], v[120:123], v[148:151], v[88:91]\n\t" \
    "v_mfma_f32_16x16x32_bf16 v[92:95], v[124:127], v[148:151], v[92:95]\n\t" \
    "v_mfma_f32_16x16x32_bf16 v[96:99], v[112:115], v[152:155], v[96:99]\n\t" \
    "v_mfma_f32_16x16x32_bf16 v[100:103], v[116:119], v[152:155], v[100:103]\n\t" \
    "v_mfma_f32_16x16x32_bf16 v[104:107], v[120:123], v[152:155], v[104:107]\n\t" \
    "v_mfma_f32_16x16x32_bf16 v[108:111], v[124:127], v[152:155], v[108:111]\n\t" \
    "s_waitcnt lgkmcnt(0)\n\t" \
    "s_barrier\n\t" \
    "v_mfma_f32_16x16x32_bf16 v[0:3], v[156:159], v[172:175], v[0:3]\n\t" \
    "v_mfma_f32_16x16x32_bf16 v[4:7], v[160:163], v[172:175], v[4:7]\n\t" \
    "v_mfma_f32_16x16x32_bf16 v[8:11], v[164:167], v[172:175], v[8:11]\n\t" \
    "v_mfma_f32_16x16x32_bf16 v[12:15], v[168:171], v[172:175], v[12:15]\n\t" \
    "v_mfma_f32_16x16x32_bf16 v[16:19], v[156:159], v[176:179], v[16:19]\n\t" \
    "v_mfma_f32_16x16x32_bf16 v[20:23], v[160:163], v[176:179], v[20:23]\n\t" \
    "v_mfma_f32_16x16x32_bf16 v[24:27], v[164:167], v[176:179], v[24:27]\n\t" \
    "v_mfma_f32_16x16x32_bf16 v[28:31], v[168:171], v[176:179], v[28:31]\n\t" \
    "v_mfma_f32_16x16x32_bf16 v[32:35], v[156:159], v[180:183], v[32:35]\n\t" \
    "v_mfma_f32_16x16x32_bf16 v[36:39], v[160:163], v[180:183], v[36:39]\n\t" \
    "v_mfma_f32_16x16x32_bf16 v[40:43], v[164:167], v[180:183], v[40:43]\n\t" \
    "v_mfma_f32_16x16x32_bf16 v[44:47], v[168:171], v[180:183], v[44:47]\n\t" \
    "v_mfma_f32_16x16x32_bf16 v[48:51], v[156:159], v[184:187], v[48:51]\n\t" \
    "v_mfma_f32_16x16x32_bf16 v[52:55], v[160:163], v[184:187], v[52:55]\n\t" \
    "v_mfma_f32_16x16x32_bf16 v[56:59], v[164:167], v[184:187], v[56:59]\n\t" \
    "v_mfma_f32_16x16x32_bf16 v[60:63], v[168:171], v[184:187], v[60:63]\n\t" \
    "v_mfma_f32_16x16x32_bf16 v[64:67], v[156:159], v[188:191], v[64:67]\n\t" \
    "v_mfma_f32_16x16x32_bf16 v[68:71], v[160:163], v[188:191], v[68:71]\n\t" \
    "v_mfma_f32_16x16x32_bf16 v[72:75], v[164:167], v[188:191], v[72:75]\n\t" \
    "v_mfma_f32_16x16x32_bf16 v[76:79], v[168:171], v[188:191], v[76:79]\n\t" \
    "v_mfma_f32_16x16x32_bf16 v[80:83], v[156:159], v[192:195], v[80:83]\n\t" \
    "v_mfma_f32_16x16x32_bf16 v[84:87], v[160:163], v[192:195], v[84:87]\n\t" \
    "v_mfma_f32_16x16x32_bf16 v[88:91], v[164:167], v[192:195], v[88:91]\n\t" \
    "v_mfma_f32_16x16x32_bf16 v[92:95], v[168:171], v[192:195], v[92:95]\n\t" \
    "v_mfma_f32_16x16x32_bf16 v[96:99], v[156:159], v[196:199], v[96:99]\n\t" \
    "v_mfma_f32_16x16x32_bf16 v[100:103], v[160:163], v[196:199], v[100:103]\n\t" \
    "v_mfma_f32_16x16x32_bf16 v[104:107], v[164:167], v[196:199], v[104:107]\n\t" \
    "v_mfma_f32_16x16x32_bf16 v[108:111], v[168:171], v[196:199], v[108:111]\n\t" \
    "s_waitcnt lgkmcnt(0)\n\t" \
    "s_barrier\n\t" \
    "s_nop 7\n\t" \
    "s_nop 7\n\t" \
    ""
#define G224_CLOB "v112","v113","v114","v115","v116","v117","v118","v119","v120","v121","v122","v123","v124","v125","v126","v127","v128","v129","v130","v131","v132","v133","v134","v135","v136","v137","v138","v139","v140","v141","v142","v143","v144","v145","v146","v147","v148","v149","v150","v151","v152","v153","v154","v155","v156","v157","v158","v159","v160","v161","v162","v163","v164","v165","v166","v167","v168","v169","v170","v171","v172","v173","v174","v175","v176","v177","v178","v179","v180","v181","v182","v183","v184","v185","v186","v187","v188","v189","v190","v191","v192","v193","v194","v195","v196","v197","v198","v199","v204","v205","v206","v207","v208","v209", "scc", "memory"
#define LOADER53 \
    "s_add_u32 m0, s47, s53\n\t" \
    "s_nop 0\n\t" \
    "global_load_lds_dwordx4 v5, s[42:43] offset:0\n\t" \
    "global_load_lds_dwordx4 v6, s[42:43] offset:1024\n\t" \
    "global_load_lds_dwordx4 v7, s[42:43] offset:2048\n\t" \
    "s_mov_b32 m0, s46\n\t" \
    "s_add_u32 s46, s46, 14336\n\t" \
    "global_load_lds_dwordx4 v0, s[40:41] offset:-2048\n\t" \
    "global_load_lds_dwordx4 v1, s[40:41] offset:-1024\n\t" \
    "global_load_lds_dwordx4 v2, s[40:41] offset:0\n\t" \
    "global_load_lds_dwordx4 v3, s[40:41] offset:1024\n\t" \
    "global_load_lds_dwordx4 v4, s[40:41] offset:2048\n\t" \
    "s_cmp_eq_u32 s46, s52\n\t" \
    "s_cselect_b32 s46, s51, s46\n\t" \
    "s_add_u32 s40, s40, s49\n\t" \
    "s_addc_u32 s41, s41, 0\n\t" \
    "s_sub_u32 s54, s54, 1\n\t" \
    "s_cmp_eq_u32 s54, 0\n\t" \
    "s_cselect_b32 s50, s56, 0\n\t" \
    "s_sub_u32 s40, s40, s50\n\t" \
    "s_subb_u32 s41, s41, 0\n\t" \
    "s_add_u32 m0, s58, s53\n\t" \
    "s_nop 0\n\t" \
    "global_load_lds_dwordx4 v5, s[44:45] offset:0\n\t" \
    "global_load_lds_dwordx4 v6, s[44:45] offset:1024\n\t" \
    "global_load_lds_dwordx4 v7, s[44:45] offset:2048\n\t" \
    "s_add_u32 s53, s53, 16384\n\t" \
    "s_and_b32 s53, s53, 0xffff\n\t" \
    "s_add_u32 s42, s42, 128\n\t" \
    "s_addc_u32 s43, s43, 0\n\t" \
    "s_add_u32 s44, s44, 128\n\t" \
    "s_addc_u32 s45, s45, 0\n\t" \
    "s_sub_u32 s55, s55, 1\n\t" \
    "s_cmp_eq_u32 s55, 0\n\t" \
    "s_cselect_b32 s50, s57, 0\n\t" \
    "s_sub_u32 s42, s42, s50\n\t" \
    "s_subb_u32 s43, s43, 0\n\t" \
    "s_sub_u32 s44, s44, s50\n\t" \
    "s_subb_u32 s45, s45, 0\n\t" \
    "s_mov_b32 m0, s46\n\t" \
    "s_add_u32 s46, s46, 14336\n\t" \
    "global_load_lds_dwordx4 v0, s[40:41] offset:-2048\n\t" \
    "global_load_lds_dwordx4 v1, s[40:41] offset:-1024\n\t" \
    "global_load_lds_dwordx4 v2, s[40:41] offset:0\n\t" \
    "global_load_lds_dwordx4 v3, s[40:41] offset:1024\n\t" \
    "global_load_lds_dwordx4 v4, s[40:41] offset:2048\n\t" \
    "s_cmp_eq_u32 s46, s52\n\t" \
    "s_cselect_b32 s46, s51, s46\n\t" \
    "s_add_u32 s40, s40, s49\n\t" \
    "s_addc_u32 s41, s41, 0\n\t" \
    "s_sub_u32 s54, s54, 1\n\t" \
    "s_cmp_eq_u32 s54, 0\n\t" \
    "s_cselect_b32 s50, s56, 0\n\t" \
    "s_sub_u32 s40, s40, s50\n\t" \
    "s_subb_u32 s41, s41, 0\n\t" \
    "s_add_u32 m0, s47, s53\n\t" \
    "s_nop 0\n\t" \
    "global_load_lds_dwordx4 v5, s[42:43] offset:0\n\t" \
    "global_load_lds_dwordx4 v6, s[42:43] offset:1024\n\t" \
    "global_load_lds_dwordx4 v7, s[42:43] offset:2048\n\t" \
    "s_mov_b32 m0, s46\n\t" \
    "s_add_u32 s46, s46, 14336\n\t" \
    "global_load_lds_dwordx4 v0, s[40:41] offset:-2048\n\t" \
    "global_load_lds_dwordx4 v1, s[40:41] offset:-1024\n\t" \
    "global_load_lds_dwordx4 v2, s[40:41] offset:0\n\t" \
    "global_load_lds_dwordx4 v3, s[40:41] offset:1024\n\t" \
    "global_load_lds_dwordx4 v4, s[40:41] offset:2048\n\t" \
    "s_cmp_eq_u32 s46, s52\n\t" \
    "s_cselect_b32 s46, s51, s46\n\t" \
    "s_add_u32 s40, s40, s49\n\t" \
    "s_addc_u32 s41, s41, 0\n\t" \
    "s_sub_u32 s54, s54, 1\n\t" \
    "s_cmp_eq_u32 s54, 0\n\t" \
    "s_cselect_b32 s50, s56, 0\n\t" \
    "s_sub_u32 s40, s40, s50\n\t" \
    "s_subb_u32 s41, s41, 0\n\t" \
    "s_add_u32 m0, s58, s53\n\t" \
    "s_nop 0\n\t" \
    "global_load_lds_dwordx4 v5, s[44:45] offset:0\n\t" \
    "global_load_lds_dwordx4 v6, s[44:45] offset:1024\n\t" \
    "global_load_lds_dwordx4 v7, s[44:45] offset:2048\n\t" \
    "s_add_u32 s53, s53, 16384\n\t" \
    "s_and_b32 s53, s53, 0xffff\n\t" \
    "s_add_u32 s42, s42, 128\n\t" \
    "s_addc_u32 s43, s43, 0\n\t" \
    "s_add_u32 s44, s44, 128\n\t" \
    "s_addc_u32 s45, s45, 0\n\t" \
    "s_sub_u32 s55, s55, 1\n\t" \
    "s_cmp_eq_u32 s55, 0\n\t" \
    "s_cselect_b32 s50, s57, 0\n\t" \
    "s_sub_u32 s42, s42, s50\n\t" \
    "s_subb_u32 s43, s43, 0\n\t" \
    "s_sub_u32 s44, s44, s50\n\t" \
    "s_subb_u32 s45, s45, 0\n\t" \
    "s_mov_b32 m0, s46\n\t" \
    "s_add_u32 s46, s46, 14336\n\t" \
    "global_load_lds_dwordx4 v0, s[40:41] offset:-2048\n\t" \
    "global_load_lds_dwordx4 v1, s[40:41] offset:-1024\n\t" \
    "global_load_lds_dwordx4 v2, s[40:41] offset:0\n\t" \
    "global_load_lds_dwordx4 v3, s[40:41] offset:1024\n\t" \
    "global_load_lds_dwordx4 v4, s[40:41] offset:2048\n\t" \
    "s_cmp_eq_u32 s46, s52\n\t" \
    "s_cselect_b32 s46, s51, s46\n\t" \
    "s_add_u32 s40, s40, s49\n\t" \
    "s_addc_u32 s41, s41, 0\n\t" \
    "s_sub_u32 s54, s54, 1\n\t" \
    "s_cmp_eq_u32 s54, 0\n\t" \
    "s_cselect_b32 s50, s56, 0\n\t" \
    "s_sub_u32 s40, s40, s50\n\t" \
    "s_subb_u32 s41, s41, 0\n\t" \
    "s_add_u32 m0, s47, s53\n\t" \
    "s_nop 0\n\t" \
    "global_load_lds_dwordx4 v5, s[42:43] offset:0\n\t" \
    "global_load_lds_dwordx4 v6, s[42:43] offset:1024\n\t" \
    "global_load_lds_dwordx4 v7, s[42:43] offset:2048\n\t" \
    "s_mov_b32 m0, s46\n\t" \
    "s_add_u32 s46, s46, 14336\n\t" \
    "global_load_lds_dwordx4 v0, s[40:41] offset:-2048\n\t" \
    "global_load_lds_dwordx4 v1, s[40:41] offset:-1024\n\t" \
    "global_load_lds_dwordx4 v2, s[40:41] offset:0\n\t" \
    "global_load_lds_dwordx4 v3, s[40:41] offset:1024\n\t" \
    "global_load_lds_dwordx4 v4, s[40:41] offset:2048\n\t" \
    "s_cmp_eq_u32 s46, s52\n\t" \
    "s_cselect_b32 s46, s51, s46\n\t" \
    "s_add_u32 s40, s40, s49\n\t" \
    "s_addc_u32 s41, s41, 0\n\t" \
    "s_sub_u32 s54, s54, 1\n\t" \
    "s_cmp_eq_u32 s54, 0\n\t" \
    "s_cselect_b32 s50, s56, 0\n\t" \
    "s_sub_u32 s40, s40, s50\n\t" \
    "s_subb_u32 s41, s41, 0\n\t" \
    "s_add_u32 m0, s58, s53\n\t" \
    "s_nop 0\n\t" \
    "global_load_lds_dwordx4 v5, s[44:45] offset:0\n\t" \
    "global_load_lds_dwordx4 v6, s[44:45] offset:1024\n\t" \
    "global_load_lds_dwordx4 v7, s[44:45] offset:2048\n\t" \
    "s_add_u32 s53, s53, 16384\n\t" \
    "s_and_b32 s53, s53, 0xffff\n\t" \
    "s_add_u32 s42, s42, 128\n\t" \
    "s_addc_u32 s43, s43, 0\n\t" \
    "s_add_u32 s44, s44, 128\n\t" \
    "s_addc_u32 s45, s45, 0\n\t" \
    "s_sub_u32 s55, s55, 1\n\t" \
    "s_cmp_eq_u32 s55, 0\n\t" \
    "s_cselect_b32 s50, s57, 0\n\t" \
    "s_sub_u32 s42, s42, s50\n\t" \
    "s_subb_u32 s43, s43, 0\n\t" \
    "s_sub_u32 s44, s44, s50\n\t" \
    "s_subb_u32 s45, s45, 0\n\t" \
    "s_mov_b32 m0, s46\n\t" \
    "s_add_u32 s46, s46, 14336\n\t" \
    "global_load_lds_dwordx4 v0, s[40:41] offset:-2048\n\t" \
    "global_load_lds_dwordx4 v1, s[40:41] offset:-1024\n\t" \
    "global_load_lds_dwordx4 v2, s[40:41] offset:0\n\t" \
    "global_load_lds_dwordx4 v3, s[40:41] offset:1024\n\t" \
    "global_load_lds_dwordx4 v4, s[40:41] offset:2048\n\t" \
    "s_cmp_eq_u32 s46, s52\n\t" \
    "s_cselect_b32 s46, s51, s46\n\t" \
    "s_add_u32 s40, s40, s49\n\t" \
    "s_addc_u32 s41, s41, 0\n\t" \
    "s_sub_u32 s54, s54, 1\n\t" \
    "s_cmp_eq_u32 s54, 0\n\t" \
    "s_cselect_b32 s50, s56, 0\n\t" \
    "s_sub_u32 s40, s40, s50\n\t" \
    "s_subb_u32 s41, s41, 0\n\t" \
    "s_add_u32 m0, s47, s53\n\t" \
    "s_nop 0\n\t" \
    "global_load_lds_dwordx4 v5, s[42:43] offset:0\n\t" \
    "global_load_lds_dwordx4 v6, s[42:43] offset:1024\n\t" \
    "global_load_lds_dwordx4 v7, s[42:43] offset:2048\n\t" \
    "s_waitcnt vmcnt(40)\n\t" \
    "s_barrier\n\t" \
    "s_waitcnt vmcnt(32)\n\t" \
    "s_barrier\n\t" \
    "s_mov_b32 m0, s46\n\t" \
    "s_add_u32 s46, s46, 14336\n\t" \
    "global_load_lds_dwordx4 v0, s[40:41] offset:-2048\n\t" \
    "global_load_lds_dwordx4 v1, s[40:41] offset:-1024\n\t" \
    "global_load_lds_dwordx4 v2, s[40:41] offset:0\n\t" \
    "global_load_lds_dwordx4 v3, s[40:41] offset:1024\n\t" \
    "global_load_lds_dwordx4 v4, s[40:41] offset:2048\n\t" \
    "s_cmp_eq_u32 s46, s52\n\t" \
    "s_cselect_b32 s46, s51, s46\n\t" \
    "s_add_u32 s40, s40, s49\n\t" \
    "s_addc_u32 s41, s41, 0\n\t" \
    "s_sub_u32 s54, s54, 1\n\t" \
    "s_cmp_eq_u32 s54, 0\n\t" \
    "s_cselect_b32 s50, s56, 0\n\t" \
    "s_sub_u32 s40, s40, s50\n\t" \
    "s_subb_u32 s41, s41, 0\n\t" \
    "s_add_u32 m0, s58, s53\n\t" \
    "s_nop 0\n\t" \
    "global_load_lds_dwordx4 v5, s[44:45] offset:0\n\t" \
    "global_load_lds_dwordx4 v6, s[44:45] offset:1024\n\t" \
    "global_load_lds_dwordx4 v7, s[44:45] offset:2048\n\t" \
    "s_add_u32 s53, s53, 16384\n\t" \
    "s_and_b32 s53, s53, 0xffff\n\t" \
    "s_add_u32 s42, s42, 128\n\t" \
    "s_addc_u32 s43, s43, 0\n\t" \
    "s_add_u32 s44, s44, 128\n\t" \
    "s_addc_u32 s45, s45, 0\n\t" \
    "s_sub_u32 s55, s55, 1\n\t" \
    "s_cmp_eq_u32 s55, 0\n\t" \
    "s_cselect_b32 s50, s57, 0\n\t" \
    "s_sub_u32 s42, s42, s50\n\t" \
    "s_subb_u32 s43, s43, 0\n\t" \
    "s_sub_u32 s44, s44, s50\n\t" \
    "s_subb_u32 s45, s45, 0\n\t" \
    "s_waitcnt vmcnt(32)\n\t" \
    "s_barrier\n\t" \
    "1:\n\t" \
    "s_mov_b32 m0, s46\n\t" \
    "s_add_u32 s46, s46, 14336\n\t" \
    "global_load_lds_dwordx4 v0, s[40:41] offset:-2048\n\t" \
    "global_load_lds_dwordx4 v1, s[40:41] offset:-1024\n\t" \
    "global_load_lds_dwordx4 v2, s[40:41] offset:0\n\t" \
    "global_load_lds_dwordx4 v3, s[40:41] offset:1024\n\t" \
    "global_load_lds_dwordx4 v4, s[40:41] offset:2048\n\t" \
    "s_cmp_eq_u32 s46, s52\n\t" \
    "s_cselect_b32 s46, s51, s46\n\t" \
    "s_add_u32 s40, s40, s49\n\t" \
    "s_addc_u32 s41, s41, 0\n\t" \
    "s_sub_u32 s54, s54, 1\n\t" \
    "s_cmp_eq_u32 s54, 0\n\t" \
    "s_cselect_b32 s50, s56, 0\n\t" \
    "s_sub_u32 s40, s40, s50\n\t" \
    "s_subb_u32 s41, s41, 0\n\t" \
    "s_add_u32 m0, s47, s53\n\t" \
    "s_nop 0\n\t" \
    "global_load_lds_dwordx4 v5, s[42:43] offset:0\n\t" \
    "global_load_lds_dwordx4 v6, s[42:43] offset:1024\n\t" \
    "global_load_lds_dwordx4 v7, s[42:43] offset:2048\n\t" \
    "s_waitcnt vmcnt(32)\n\t" \
    "s_barrier\n\t" \
    "s_mov_b32 m0, s46\n\t" \
    "s_add_u32 s46, s46, 14336\n\t" \
    "global_load_lds_dwordx4 v0, s[40:41] offset:-2048\n\t" \
    "global_load_lds_dwordx4 v1, s[40:41] offset:-1024\n\t" \
    "global_load_lds_dwordx4 v2, s[40:41] offset:0\n\t" \
    "global_load_lds_dwordx4 v3, s[40:41] offset:1024\n\t" \
    "global_load_lds_dwordx4 v4, s[40:41] offset:2048\n\t" \
    "s_cmp_eq_u32 s46, s52\n\t" \
    "s_cselect_b32 s46, s51, s46\n\t" \
    "s_add_u32 s40, s40, s49\n\t" \
    "s_addc_u32 s41, s41, 0\n\t" \
    "s_sub_u32 s54, s54, 1\n\t" \
    "s_cmp_eq_u32 s54, 0\n\t" \
    "s_cselect_b32 s50, s56, 0\n\t" \
    "s_sub_u32 s40, s40, s50\n\t" \
    "s_subb_u32 s41, s41, 0\n\t" \
    "s_add_u32 m0, s58, s53\n\t" \
    "s_nop 0\n\t" \
    "global_load_lds_dwordx4 v5, s[44:45] offset:0\n\t" \
    "global_load_lds_dwordx4 v6, s[44:45] offset:1024\n\t" \
    "global_load_lds_dwordx4 v7, s[44:45] offset:2048\n\t" \
    "s_add_u32 s53, s53, 16384\n\t" \
    "s_and_b32 s53, s53, 0xffff\n\t" \
    "s_add_u32 s42, s42, 128\n\t" \
    "s_addc_u32 s43, s43, 0\n\t" \
    "s_add_u32 s44, s44, 128\n\t" \
    "s_addc_u32 s45, s45, 0\n\t" \
    "s_sub_u32 s55, s55, 1\n\t" \
    "s_cmp_eq_u32 s55, 0\n\t" \
    "s_cselect_b32 s50, s57, 0\n\t" \
    "s_sub_u32 s42, s42, s50\n\t" \
    "s_subb_u32 s43, s43, 0\n\t" \
    "s_sub_u32 s44, s44, s50\n\t" \
    "s_subb_u32 s45, s45, 0\n\t" \
    "s_sub_u32 s48, s48, 1\n\t" \
    "s_cmp_lg_u32 s48, 0\n\t" \
    "s_waitcnt vmcnt(32)\n\t" \
    "s_barrier\n\t" \
    "s_cbranch_scc1 1b\n\t" \
    "s_mov_b32 m0, s46\n\t" \
    "s_add_u32 s46, s46, 14336\n\t" \
    "global_load_lds_dwordx4 v0, s[40:41] offset:-2048\n\t" \
    "global_load_lds_dwordx4 v1, s[40:41] offset:-1024\n\t" \
    "global_load_lds_dwordx4 v2, s[40:41] offset:0\n\t" \
    "global_load_lds_dwordx4 v3, s[40:41] offset:1024\n\t" \
    "global_load_lds_dwordx4 v4, s[40:41] offset:2048\n\t" \
    "s_cmp_eq_u32 s46, s52\n\t" \
    "s_cselect_b32 s46, s51, s46\n\t" \
    "s_add_u32 s40, s40, s49\n\t" \
    "s_addc_u32 s41, s41, 0\n\t" \
    "s_sub_u32 s54, s54, 1\n\t" \
    "s_cmp_eq_u32 s54, 0\n\t" \
    "s_cselect_b32 s50, s56, 0\n\t" \
    "s_sub_u32 s40, s40, s50\n\t" \
    "s_subb_u32 s41, s41, 0\n\t" \
    "s_waitcnt vmcnt(29)\n\t" \
    "s_barrier\n\t" \
    "s_waitcnt vmcnt(21)\n\t" \
    "s_barrier\n\t" \
    "s_waitcnt vmcnt(13)\n\t" \
    "s_barrier\n\t" \
    "s_waitcnt vmcnt(5)\n\t" \
    "s_barrier\n\t" \
    "s_waitcnt vmcnt(0)\n\t" \
    "s_barrier\n\t" \
    "s_barrier\n\t" \
    "s_barrier\n\t" \
    ""
#define LOADER42 \
    "s_add_u32 m0, s47, s53\n\t" \
    "s_nop 0\n\t" \
    "global_load_lds_dwordx4 v5, s[42:43] offset:0\n\t" \
    "global_load_lds_dwordx4 v6, s[42:43] offset:1024\n\t" \
    "s_mov_b32 m0, s46\n\t" \
    "s_add_u32 s46, s46, 14336\n\t" \
    "global_load_lds_dwordx4 v0, s[40:41] offset:-2048\n\t" \
    "global_load_lds_dwordx4 v1, s[40:41] offset:-1024\n\t" \
    "global_load_lds_dwordx4 v2, s[40:41] offset:0\n\t" \
    "global_load_lds_dwordx4 v3, s[40:41] offset:1024\n\t" \
    "s_cmp_eq_u32 s46, s52\n\t" \
    "s_cselect_b32 s46, s51, s46\n\t" \
    "s_add_u32 s40, s40, s49\n\t" \
    "s_addc_u32 s41, s41, 0\n\t" \
    "s_sub_u32 s54, s54, 1\n\t" \
    "s_cmp_eq_u32 s54, 0\n\t" \
    "s_cselect_b32 s50, s56, 0\n\t" \
    "s_sub_u32 s40, s40, s50\n\t" \
    "s_subb_u32 s41, s41, 0\n\t" \
    "s_add_u32 m0, s58, s53\n\t" \
    "s_nop 0\n\t" \
    "global_load_lds_dwordx4 v5, s[44:45] offset:0\n\t" \
    "global_load_lds_dwordx4 v6, s[44:45] offset:1024\n\t" \
    "s_add_u32 s53, s53, 16384\n\t" \
    "s_and_b32 s53, s53, 0xffff\n\t" \
    "s_add_u32 s42, s42, 128\n\t" \
    "s_addc_u32 s43, s43, 0\n\t" \
    "s_add_u32 s44, s44, 128\n\t" \
    "s_addc_u32 s45, s45, 0\n\t" \
    "s_sub_u32 s55, s55, 1\n\t" \
    "s_cmp_eq_u32 s55, 0\n\t" \
    "s_cselect_b32 s50, s57, 0\n\t" \
    "s_sub_u32 s42, s42, s50\n\t" \
    "s_subb_u32 s43, s43, 0\n\t" \
    "s_sub_u32 s44, s44, s50\n\t" \
    "s_subb_u32 s45, s45, 0\n\t" \
    "s_mov_b32 m0, s46\n\t" \
    "s_add_u32 s46, s46, 14336\n\t" \
    "global_load_lds_dwordx4 v0, s[40:41] offset:-2048\n\t" \
    "global_load_lds_dwordx4 v1, s[40:41] offset:-1024\n\t" \
    "global_load_lds_dwordx4 v2, s[40:41] offset:0\n\t" \
    "global_load_lds_dwordx4 v3, s[40:41] offset:1024\n\t" \
    "s_cmp_eq_u32 s46, s52\n\t" \
    "s_cselect_b32 s46, s51, s46\n\t" \
    "s_add_u32 s40, s40, s49\n\t" \
    "s_addc_u32 s41, s41, 0\n\t" \
    "s_sub_u32 s54, s54, 1\n\t" \
    "s_cmp_eq_u32 s54, 0\n\t" \
    "s_cselect_b32 s50, s56, 0\n\t" \
    "s_sub_u32 s40, s40, s50\n\t" \
    "s_subb_u32 s41, s41, 0\n\t" \
    "s_add_u32 m0, s47, s53\n\t" \
    "s_nop 0\n\t" \
    "global_load_lds_dwordx4 v5, s[42:43] offset:0\n\t" \
    "global_load_lds_dwordx4 v6, s[42:43] offset:1024\n\t" \
    "s_mov_b32 m0, s46\n\t" \
    "s_add_u32 s46, s46, 14336\n\t" \
    "global_load_lds_dwordx4 v0, s[40:41] offset:-2048\n\t" \
    "global_load_lds_dwordx4 v1, s[40:41] offset:-1024\n\t" \
    "global_load_lds_dwordx4 v2, s[40:41] offset:0\n\t" \
    "global_load_lds_dwordx4 v3, s[40:41] offset:1024\n\t" \
    "s_cmp_eq_u32 s46, s52\n\t" \
    "s_cselect_b32 s46, s51, s46\n\t" \
    "s_add_u32 s40, s40, s49\n\t" \
    "s_addc_u32 s41, s41, 0\n\t" \
    "s_sub_u32 s54, s54, 1\n\t" \
    "s_cmp_eq_u32 s54, 0\n\t" \
    "s_cselect_b32 s50, s56, 0\n\t" \
    "s_sub_u32 s40, s40, s50\n\t" \
    "s_subb_u32 s41, s41, 0\n\t" \
    "s_add_u32 m0, s58, s53\n\t" \
    "s_nop 0\n\t" \
    "global_load_lds_dwordx4 v5, s[44:45] offset:0\n\t" \
    "global_load_lds_dwordx4 v6, s[44:45] offset:1024\n\t" \
    "s_add_u32 s53, s53, 16384\n\t" \
    "s_and_b32 s53, s53, 0xffff\n\t" \
    "s_add_u32 s42, s42, 128\n\t" \
    "s_addc_u32 s43, s43, 0\n\t" \
    "s_add_u32 s44, s44, 128\n\t" \
    "s_addc_u32 s45, s45, 0\n\t" \
    "s_sub_u32 s55, s55, 1\n\t" \
    "s_cmp_eq_u32 s55, 0\n\t" \
    "s_cselect_b32 s50, s57, 0\n\t" \
    "s_sub_u32 s42, s42, s50\n\t" \
    "s_subb_u32 s43, s43, 0\n\t" \
    "s_sub_u32 s44, s44, s50\n\t" \
    "s_subb_u32 s45, s45, 0\n\t" \
    "s_mov_b32 m0, s46\n\t" \
    "s_add_u32 s46, s46, 14336\n\t" \
    "global_load_lds_dwordx4 v0, s[40:41] offset:-2048\n\t" \
    "global_load_lds_dwordx4 v1, s[40:41] offset:-1024\n\t" \
    "global_load_lds_dwordx4 v2, s[40:41] offset:0\n\t" \
    "global_load_lds_dwordx4 v3, s[40:41] offset:1024\n\t" \
    "s_cmp_eq_u32 s46, s52\n\t" \
    "s_cselect_b32 s46, s51, s46\n\t" \
    "s_add_u32 s40, s40, s49\n\t" \
    "s_addc_u32 s41, s41, 0\n\t" \
    "s_sub_u32 s54, s54, 1\n\t" \
    "s_cmp_eq_u32 s54, 0\n\t" \
    "s_cselect_b32 s50, s56, 0\n\t" \
    "s_sub_u32 s40, s40, s50\n\t" \
    "s_subb_u32 s41, s41, 0\n\t" \
    "s_add_u32 m0, s47, s53\n\t" \
    "s_nop 0\n\t" \
    "global_load_lds_dwordx4 v5, s[42:43] offset:0\n\t" \
    "global_load_lds_dwordx4 v6, s[42:43] offset:1024\n\t" \
    "s_mov_b32 m0, s46\n\t" \
    "s_add_u32 s46, s46, 14336\n\t" \
    "global_load_lds_dwordx4 v0, s[40:41] offset:-2048\n\t" \
    "global_load_lds_dwordx4 v1, s[40:41] offset:-1024\n\t" \
    "global_load_lds_dwordx4 v2, s[40:41] offset:0\n\t" \
    "global_load_lds_dwordx4 v3, s[40:41] offset:1024\n\t" \
    "s_cmp_eq_u32 s46, s52\n\t" \
    "s_cselect_b32 s46, s51, s46\n\t" \
    "s_add_u32 s40, s40, s49\n\t" \
    "s_addc_u32 s41, s41, 0\n\t" \
    "s_sub_u32 s54, s54, 1\n\t" \
    "s_cmp_eq_u32 s54, 0\n\t" \
    "s_cselect_b32 s50, s56, 0\n\t" \
    "s_sub_u32 s40, s40, s50\n\t" \
    "s_subb_u32 s41, s41, 0\n\t" \
    "s_add_u32 m0, s58, s53\n\t" \
    "s_nop 0\n\t" \
    "global_load_lds_dwordx4 v5, s[44:45] offset:0\n\t" \
    "global_load_lds_dwordx4 v6, s[44:45] offset:1024\n\t" \
    "s_add_u32 s53, s53, 16384\n\t" \
    "s_and_b32 s53, s53, 0xffff\n\t" \
    "s_add_u32 s42, s42, 128\n\t" \
    "s_addc_u32 s43, s43, 0\n\t" \
    "s_add_u32 s44, s44, 128\n\t" \
    "s_addc_u32 s45, s45, 0\n\t" \
    "s_sub_u32 s55, s55, 1\n\t" \
    "s_cmp_eq_u32 s55, 0\n\t" \
    "s_cselect_b32 s50, s57, 0\n\t" \
    "s_sub_u32 s42, s42, s50\n\t" \
    "s_subb_u32 s43, s43, 0\n\t" \
    "s_sub_u32 s44, s44, s50\n\t" \
    "s_subb_u32 s45, s45, 0\n\t" \
    "s_mov_b32 m0, s46\n\t" \
    "s_add_u32 s46, s46, 14336\n\t" \
    "global_load_lds_dwordx4 v0, s[40:41] offset:-2048\n\t" \
    "global_load_lds_dwordx4 v1, s[40:41] offset:-1024\n\t" \
    "global_load_lds_dwordx4 v2, s[40:41] offset:0\n\t" \
    "global_load_lds_dwordx4 v3, s[40:41] offset:1024\n\t" \
    "s_cmp_eq_u32 s46, s52\n\t" \
    "s_cselect_b32 s46, s51, s46\n\t" \
    "s_add_u32 s40, s40, s49\n\t" \
    "s_addc_u32 s41, s41, 0\n\t" \
    "s_sub_u32 s54, s54, 1\n\t" \
    "s_cmp_eq_u32 s54, 0\n\t" \
    "s_cselect_b32 s50, s56, 0\n\t" \
    "s_sub_u32 s40, s40, s50\n\t" \
    "s_subb_u32 s41, s41, 0\n\t" \
    "s_add_u32 m0, s47, s53\n\t" \
    "s_nop 0\n\t" \
    "global_load_lds_dwordx4 v5, s[42:43] offset:0\n\t" \
    "global_load_lds_dwordx4 v6, s[42:43] offset:1024\n\t" \
    "s_waitcnt vmcnt(30)\n\t" \
    "s_barrier\n\t" \
    "s_waitcnt vmcnt(24)\n\t" \
    "s_barrier\n\t" \
    "s_mov_b32 m0, s46\n\t" \
    "s_add_u32 s46, s46, 14336\n\t" \
    "global_load_lds_dwordx4 v0, s[40:41] offset:-2048\n\t" \
    "global_load_lds_dwordx4 v1, s[40:41] offset:-1024\n\t" \
    "global_load_lds_dwordx4 v2, s[40:41] offset:0\n\t" \
    "global_load_lds_dwordx4 v3, s[40:41] offset:1024\n\t" \
    "s_cmp_eq_u32 s46, s52\n\t" \
    "s_cselect_b32 s46, s51, s46\n\t" \
    "s_add_u32 s40, s40, s49\n\t" \
    "s_addc_u32 s41, s41, 0\n\t" \
    "s_sub_u32 s54, s54, 1\n\t" \
    "s_cmp_eq_u32 s54, 0\n\t" \
    "s_cselect_b32 s50, s56, 0\n\t" \
    "s_sub_u32 s40, s40, s50\n\t" \
    "s_subb_u32 s41, s41, 0\n\t" \
    "s_add_u32 m0, s58, s53\n\t" \
    "s_nop 0\n\t" \
    "global_load_lds_dwordx4 v5, s[44:45] offset:0\n\t" \
    "global_load_lds_dwordx4 v6, s[44:45] offset:1024\n\t" \
    "s_add_u32 s53, s53, 16384\n\t" \
    "s_and_b32 s53, s53, 0xffff\n\t" \
    "s_add_u32 s42, s42, 128\n\t" \
    "s_addc_u32 s43, s43, 0\n\t" \
    "s_add_u32 s44, s44, 128\n\t" \
    "s_addc_u32 s45, s45, 0\n\t" \
    "s_sub_u32 s55, s55, 1\n\t" \
    "s_cmp_eq_u32 s55, 0\n\t" \
    "s_cselect_b32 s50, s57, 0\n\t" \
    "s_sub_u32 s42, s42, s50\n\t" \
    "s_subb_u32 s43, s43, 0\n\t" \
    "s_sub_u32 s44, s44, s50\n\t" \
    "s_subb_u32 s45, s45, 0\n\t" \
    "s_waitcnt vmcnt(24)\n\t" \
    "s_barrier\n\t" \
    "1:\n\t" \
    "s_mov_b32 m0, s46\n\t" \
    "s_add_u32 s46, s46, 14336\n\t" \
    "global_load_lds_dwordx4 v0, s[40:41] offset:-2048\n\t" \
    "global_load_lds_dwordx4 v1, s[40:41] offset:-1024\n\t" \
    "global_load_lds_dwordx4 v2, s[40:41] offset:0\n\t" \
    "global_load_lds_dwordx4 v3, s[40:41] offset:1024\n\t" \
    "s_cmp_eq_u32 s46, s52\n\t" \
    "s_cselect_b32 s46, s51, s46\n\t" \
    "s_add_u32 s40, s40, s49\n\t" \
    "s_addc_u32 s41, s41, 0\n\t" \
    "s_sub_u32 s54, s54, 1\n\t" \
    "s_cmp_eq_u32 s54, 0\n\t" \
    "s_cselect_b32 s50, s56, 0\n\t" \
    "s_sub_u32 s40, s40, s50\n\t" \
    "s_subb_u32 s41, s41, 0\n\t" \
    "s_add_u32 m0, s47, s53\n\t" \
    "s_nop 0\n\t" \
    "global_load_lds_dwordx4 v5, s[42:43] offset:0\n\t" \
    "global_load_lds_dwordx4 v6, s[42:43] offset:1024\n\t" \
    "s_waitcnt vmcnt(24)\n\t" \
    "s_barrier\n\t" \
    "s_mov_b32 m0, s46\n\t" \
    "s_add_u32 s46, s46, 14336\n\t" \
    "global_load_lds_dwordx4 v0, s[40:41] offset:-2048\n\t" \
    "global_load_lds_dwordx4 v1, s[40:41] offset:-1024\n\t" \
    "global_load_lds_dwordx4 v2, s[40:41] offset:0\n\t" \
    "global_load_lds_dwordx4 v3, s[40:41] offset:1024\n\t" \
    "s_cmp_eq_u32 s46, s52\n\t" \
    "s_cselect_b32 s46, s51, s46\n\t" \
    "s_add_u32 s40, s40, s49\n\t" \
    "s_addc_u32 s41, s41, 0\n\t" \
    "s_sub_u32 s54, s54, 1\n\t" \
    "s_cmp_eq_u32 s54, 0\n\t" \
    "s_cselect_b32 s50, s56, 0\n\t" \
    "s_sub_u32 s40, s40, s50\n\t" \
    "s_subb_u32 s41, s41, 0\n\t" \
    "s_add_u32 m0, s58, s53\n\t" \
    "s_nop 0\n\t" \
    "global_load_lds_dwordx4 v5, s[44:45] offset:0\n\t" \
    "global_load_lds_dwordx4 v6, s[44:45] offset:1024\n\t" \
    "s_add_u32 s53, s53, 16384\n\t" \
    "s_and_b32 s53, s53, 0xffff\n\t" \
    "s_add_u32 s42, s42, 128\n\t" \
    "s_addc_u32 s43, s43, 0\n\t" \
    "s_add_u32 s44, s44, 128\n\t" \
    "s_addc_u32 s45, s45, 0\n\t" \
    "s_sub_u32 s55, s55, 1\n\t" \
    "s_cmp_eq_u32 s55, 0\n\t" \
    "s_cselect_b32 s50, s57, 0\n\t" \
    "s_sub_u32 s42, s42, s50\n\t" \
    "s_subb_u32 s43, s43, 0\n\t" \
    "s_sub_u32 s44, s44, s50\n\t" \
    "s_subb_u32 s45, s45, 0\n\t" \
    "s_sub_u32 s48, s48, 1\n\t" \
    "s_cmp_lg_u32 s48, 0\n\t" \
    "s_waitcnt vmcnt(24)\n\t" \
    "s_barrier\n\t" \
    "s_cbranch_scc1 1b\n\t" \
    "s_mov_b32 m0, s46\n\t" \
    "s_add_u32 s46, s46, 14336\n\t" \
    "global_load_lds_dwordx4 v0, s[40:41] offset:-2048\n\t" \
    "global_load_lds_dwordx4 v1, s[40:41] offset:-1024\n\t" \
    "global_load_lds_dwordx4 v2, s[40:41] offset:0\n\t" \
    "global_load_lds_dwordx4 v3, s[40:41] offset:1024\n\t" \
    "s_cmp_eq_u32 s46, s52\n\t" \
    "s_cselect_b32 s46, s51, s46\n\t" \
    "s_add_u32 s40, s40, s49\n\t" \
    "s_addc_u32 s41, s41, 0\n\t" \
    "s_sub_u32 s54, s54, 1\n\t" \
    "s_cmp_eq_u32 s54, 0\n\t" \
    "s_cselect_b32 s50, s56, 0\n\t" \
    "s_sub_u32 s40, s40, s50\n\t" \
    "s_subb_u32 s41, s41, 0\n\t" \
    "s_waitcnt vmcnt(22)\n\t" \
    "s_barrier\n\t" \
    "s_waitcnt vmcnt(16)\n\t" \
    "s_barrier\n\t" \
    "s_waitcnt vmcnt(10)\n\t" \
    "s_barrier\n\t" \
    "s_waitcnt vmcnt(4)\n\t" \
    "s_barrier\n\t" \
    "s_waitcnt vmcnt(0)\n\t" \
    "s_barrier\n\t" \
    "s_barrier\n\t" \
    "s_barrier\n\t" \
    ""
#define LOADER_LOOPS(nstep) (((nstep) - 8) / 2)
#define LOADER_CLOB "s50", "scc", "memory"

#define STAMP(id) do { } while (0)
#define STAMPC(id) do { } while (0)
#define STARG
#define STPASS
namespace {
constexpr int A_SLOT = 16384, B_SLOT = 14336, LDS_A = 0, LDS_B = 65536;
constexpr int LDS_BYTES = 163840;
constexpr unsigned WS_EPOCH = 0, WS_ARR = 1024, WS_F1 = 4096, WS_FO = 6144, WS_FP = 8192;
constexpr unsigned WS_Q = 16384, WS_K = WS_Q + 2097152, WS_V = WS_K + 2097152, WS_O = WS_V + 2097152, WS_PQ = WS_O + 2097152;
constexpr unsigned WS_END = WS_PQ + 7340032;
constexpr int AUX_SC1 = 16;
constexpr unsigned SPIN_MAX = 1u << 22;

DI unsigned cvtpk(float lo, float hi) { f32x2 v = {lo, hi}; bf16x2 b = __builtin_convertvector(v, bf16x2); return __builtin_bit_cast(unsigned, b); }
DI float rbf(float x) { return __uint_as_float(cvtpk(0.0f, x)); }
DI void glds16(const void* sbase, unsigned voff, unsigned lds_base) {
    asm volatile("s_mov_b32 m0, %2\n\ts_nop 0\n\tglobal_load_lds_dwordx4 %1, %0" :: "s"(sbase), "v"(voff), "s"(lds_base) : "memory"); }
DI void wait_vm0() { asm volatile("s_waitcnt vmcnt(0)" ::: "memory"); }
DI void bar() { asm volatile("s_waitcnt lgkmcnt(0)\n\ts_barrier" ::: "memory"); }
DI s16x4 trread(LAS const char* p) { return __builtin_bit_cast(s16x4, __builtin_amdgcn_ds_read_tr16_b64_v4i16((LAS s16x4*)p)); }
DI v4u ld_sc1(__amdgpu_buffer_rsrc_t rs, unsigned off) { return __builtin_amdgcn_raw_buffer_load_b128(rs, off, 0, AUX_SC1); }
DI void st_sc1(__amdgpu_buffer_rsrc_t rs, unsigned off, v4u v) { __builtin_amdgcn_raw_buffer_store_b128(v, rs, off, 0, AUX_SC1); }
DI void flag_store(char* ws, unsigned off, u64 tag) { __hip_atomic_store((GAS u64*)(ws + off), tag, __ATOMIC_RELAXED, __HIP_MEMORY_SCOPE_AGENT); }
DI void poll(char* ws, unsigned off, bool active, u64 tag) {
    for (unsigned spins = 0; spins < SPIN_MAX; ++spins) {
        u64 v = tag;
        if (active) v = __hip_atomic_load((GAS u64*)(ws + off), __ATOMIC_RELAXED, __HIP_MEMORY_SCOPE_AGENT);
        if (__all(v == tag)) break;
        __builtin_amdgcn_s_sleep(1);
    }
    asm volatile("" ::: "memory");
}

template <int NB, int NA>
DI void g224_loader(unsigned lds0, int w, int lane, const char* a0, const char* a1, unsigned lda, const char* bbase, unsigned ldb, int nstep, int rot) {
    const int b0 = 5 * w, i0 = 3 * w;
    unsigned vo[5], va[3];
#pragma unroll
    for (int i = 0; i < 5; ++i) {
        const unsigned o = (b0 + i) * 1024 + lane * 16;
        const unsigned row = o / 448, pc = (o - row * 448) >> 4;
        vo[i] = (i < NB) ? row * ldb + ((pc ^ (((row >> 3) & 1) << 1)) * 16) + 2048 - i * 1024 : 0u;
    }
    const int rl = lane >> 3, ch = lane & 7;
#pragma unroll
    for (int i = 0; i < 3; ++i) {
        const int ii = i0 + i;
        va[i] = (i < NA) ? (ii * 8 + rl) * lda + ((ch ^ ((((ii & 1) * 8 + rl) >> 1) & 7)) * 16) + 2048 - i * 1024 : 0u;
    }
    const unsigned bdst = lds0 + LDS_B + b0 * 1024 + 2048, adst = lds0 + LDS_A + i0 * 1024;
    const int rb = 2 * rot, ra = rot;
    const char* pb = bbase + (size_t)rb * 32 * ldb; const char* pa0 = a0 + ra * 128 - 2048; const char* pa1 = a1 + ra * 128 - 2048;
    unsigned s46 = bdst, s47 = adst, s48 = LOADER_LOOPS(nstep), s49 = 32 * ldb, s51 = bdst, s52 = bdst + 6 * B_SLOT, s53 = 0, s54 = nstep - rb, s55 = nstep / 2 - ra,
             s56 = nstep * 32 * ldb, s57 = (nstep / 2) * 128, s58 = adst + 8192;
    if (NB == 5) asm volatile(LOADER53 : "+{s[40:41]}"(pb), "+{s[42:43]}"(pa0), "+{s[44:45]}"(pa1), "+{s46}"(s46), "+{s48}"(s48), "+{s53}"(s53), "+{s54}"(s54), "+{s55}"(s55)
         : "{v0}"(vo[0]), "{v1}"(vo[1]), "{v2}"(vo[2]), "{v3}"(vo[3]), "{v4}"(vo[4]), "{v5}"(va[0]), "{v6}"(va[1]), "{v7}"(va[2]),
           "{s47}"(s47), "{s49}"(s49), "{s51}"(s51), "{s52}"(s52), "{s56}"(s56), "{s57}"(s57), "{s58}"(s58) : LOADER_CLOB);
    else asm volatile(LOADER42 : "+{s[40:41]}"(pb), "+{s[42:43]}"(pa0), "+{s[44:45]}"(pa1), "+{s46}"(s46), "+{s48}"(s48), "+{s53}"(s53), "+{s54}"(s54), "+{s55}"(s55)
         : "{v0}"(vo[0]), "{v1}"(vo[1]), "{v2}"(vo[2]), "{v3}"(vo[3]), "{v4}"(vo[4]), "{v5}"(va[0]), "{v6}"(va[1]), "{v7}"(va[2]),
           "{s47}"(s47), "{s49}"(s49), "{s51}"(s51), "{s52}"(s52), "{s56}"(s56), "{s57}"(s57), "{s58}"(s58) : LOADER_CLOB);
}
DI void touch(const char* p, unsigned lds_dummy) {
    asm volatile("s_mov_b32 m0, %1\n\ts_nop 0\n\tglobal_load_lds_dword %0, off" :: "v"(p), "s"(lds_dummy) : "memory"); }
constexpr int LDS_DUMMY = LDS_B + 6 * B_SLOT;
constexpr int LDS_CNT = LDS_BYTES - 64;
constexpr int PF_DIST = 12;
#define ENG_FIRST 2
template <class PF, class EX>
DI void g224(unsigned lds0, int wid, int lane, const char* a0, const char* a1, unsigned lda, const char* bbase, unsigned ldb, int nstep, int rot, bool swap, f32x16 (&c)[7], PF pf, EX extra) {
    if (wid == 7) {
        for (int st = 6; st < PF_DIST && st < nstep; ++st) pf(st);
        asm volatile("s_barrier\n\ts_barrier" ::: "memory");
        for (int s = 0; s < nstep; ++s) {
            if (s + PF_DIST < nstep) pf(s + PF_DIST);
            extra(s);
            asm volatile("s_barrier" ::: "memory");
        }
    }
    else if (wid == 6) g224_loader<4, 2>(lds0, 2, lane, a0, a1, lda, bbase, ldb, nstep, rot);
    else if (wid >= 4) g224_loader<5, 3>(lds0, wid - 4, lane, a0, a1, lda, bbase, ldb, nstep, rot);
    else {
        const int mh = wid & 1, nh = wid >> 1;
        const int r16 = lane & 15, g = lane >> 4, q = (lane & 15) >> 2, p = lane & 3;
        const unsigned aoff0 = lds0 + LDS_A + (mh * 64 + r16) * 128 + ((g ^ ((r16 >> 1) & 7)) * 16);
        const unsigned brow = lds0 + LDS_B + (8 * g + q) * 448 + p * 8 + nh * 224;
        const unsigned gb = (g & 1) * 32;
        const unsigned bE = nh ? brow - gb : brow + gb, bO = nh ? brow + gb : brow - gb;
        unsigned aslot, bslot, cnt = nstep / 2 - 1;
        if (swap) asm volatile(G224_BODY_SWAP
            : "={v[0:15]}"(c[0]), "={v[16:31]}"(c[1]), "={v[32:47]}"(c[2]), "={v[48:63]}"(c[3]), "={v[64:79]}"(c[4]), "={v[80:95]}"(c[5]), "={v[96:111]}"(c[6]),
              [aslot] "=&s"(aslot), [bslot] "=&s"(bslot), [cnt] "+s"(cnt)
            : "{v200}"(aoff0), "{v201}"(aoff0 ^ 64), "{v202}"(bE), "{v203}"(bO) : G224_CLOB);
        else asm volatile(G224_BODY
            : "={v[0:15]}"(c[0]), "={v[16:31]}"(c[1]), "={v[32:47]}"(c[2]), "={v[48:63]}"(c[3]), "={v[64:79]}"(c[4]), "={v[80:95]}"(c[5]), "={v[96:111]}"(c[6]),
              [aslot] "=&s"(aslot), [bslot] "=&s"(bslot), [cnt] "+s"(cnt)
            : "{v200}"(aoff0), "{v201}"(aoff0 ^ 64), "{v202}"(bE), "{v203}"(bO) : G224_CLOB);
    }
}

DI void final_store(LAS char* L, int wid, int lane, f32x4 (&acc)[7], float sign, char* outp  ) {
    bar();
    const int m = wid & 3;
    if (wid >= 4) {
#pragma unroll
        for (int n = 0; n < 7; ++n) *(LAS f32x4*)(L + (m * 7 + n) * 1024 + lane * 16) = acc[n];
    }
    bar();
    if (wid < 4) {
        LAS char* stg = L + 32768 + m * 4096;
        const int t16 = lane & 15, g = lane >> 4;
        f32x4 o[7];
#pragma unroll
        for (int n = 0; n < 7; ++n) o[n] = *(const LAS f32x4*)(L + (m * 7 + n) * 1024 + lane * 16);
#pragma unroll
        for (int n = 0; n < 7; ++n) {
            v2u w;
            w[0] = cvtpk(acc[n][0] + sign * o[n][0], acc[n][1] + sign * o[n][1]);
            w[1] = cvtpk(acc[n][2] + sign * o[n][2], acc[n][3] + sign * o[n][3]);
            *(LAS v2u*)(stg + t16 * 240 + n * 32 + g * 8) = w;
        }
        asm volatile("s_waitcnt lgkmcnt(0)" ::: "memory");
        v4u v[4];
#pragma unroll
        for (int k = 0; k < 4; ++k) {
            const int idx = min(k * 64 + lane, 223), row = idx / 14, chn = idx - row * 14;
            v[k] = *(const LAS v4u*)(stg + row * 240 + chn * 16);
        }
#pragma unroll
        for (int k = 0; k < 4; ++k) {
            const int idx = k * 64 + lane, row = idx / 14, chn = idx - row * 14;
            if (idx < 224) *(GAS v4u*)(outp + (size_t)(m * 16 + row) * 1792 + chn * 16) = v[k];
        }
    }
}

DI bf16x8 add_frag(v4u a, v4u b) {
    v4u r;
#pragma unroll
    for (int j = 0; j < 4; ++j) {
        const float lo = __uint_as_float(a[j] << 16) + __uint_as_float(b[j] << 16);
        const float hi = __uint_as_float(a[j] & 0xffff0000u) + __uint_as_float(b[j] & 0xffff0000u);
        r[j] = cvtpk(lo, hi);
    }
    return __builtin_bit_cast(bf16x8, r);
}

DI v4u pack_pair(const f32x16& ca, const f32x16& cb, int m) {
    unsigned a0 = cvtpk(ca[4 * m], ca[4 * m + 1]), a1 = cvtpk(ca[4 * m + 2], ca[4 * m + 3]);
    unsigned b0 = cvtpk(cb[4 * m], cb[4 * m + 1]), b1 = cvtpk(cb[4 * m + 2], cb[4 * m + 3]);
    auto s0 = __builtin_amdgcn_permlane32_swap(a0, b0, false, false);
    auto s1 = __builtin_amdgcn_permlane32_swap(a1, b1, false, false);
    auto t0 = __builtin_amdgcn_permlane16_swap(s0[0], s0[1], false, false);
    auto t1 = __builtin_amdgcn_permlane16_swap(s1[0], s1[1], false, false);
    return (v4u){t0[0], t1[0], t0[1], t1[1]};
}
DI v4u pack_single(const f32x16& ca, int m) {
    unsigned a0 = cvtpk(ca[4 * m], ca[4 * m + 1]), a1 = cvtpk(ca[4 * m + 2], ca[4 * m + 3]);
    auto t0 = __builtin_amdgcn_permlane16_swap(a0, a0, false, false);
    auto t1 = __builtin_amdgcn_permlane16_swap(a1, a1, false, false);
    return (v4u){t0[0], t1[0], t0[1], t1[1]};
}

DI void role_b(char* lds, int x, int i, int wid, int lane, const char* Bx, const char* Cre, const char* Cim, const char* Wre, const char* Wim, char* out, char* ws, u64 tag STARG) {
    const unsigned lds0 = (unsigned)(uintptr_t)lds;
    LAS char* L = (LAS char*)lds;
    const int tb = x * 4 + (i >> 2), cb = i & 3, ch = cb & 1, kh = cb >> 1;
    const __amdgpu_buffer_rsrc_t rs = __builtin_amdgcn_make_buffer_rsrc(ws, 0, WS_END, 0x00020000);
    f32x16 c[7];
    STAMP(0); STAMPC(0);
    {
        const unsigned dummy = lds0 + LDS_DUMMY;
        const char* pfa = (ch ? Cim : Cre) + ((size_t)(tb * 64 + (lane & 31)) * 2048 + kh * 1024) * 2;
        const char* pfb = Bx + ((size_t)kh * 1024 + (i >> 2) * 8 + ((lane & 31) >> 2)) * 896 + (3 * ch + (lane & 3)) * 128;
        const char* pfw = ((i & 1) ? Wim : Wre) + (size_t)((i >> 1) * 392 + lane) * 128;
        auto pf = [&](int st) {
            (void)st; (void)pfa; (void)pfb;
        };
        auto extra = [&](int s) {
            if (s == 0 && lane == 0) flag_store(ws, WS_ARR + blockIdx.x * 8, tag);
            (void)pfw;
        };
        g224(lds0, wid, lane, Cre + ((size_t)tb * 64 * 2048 + kh * 1024) * 2, Cim + ((size_t)tb * 64 * 2048 + kh * 1024) * 2, 4096,
             Bx + ((size_t)kh * 1024 * 448 + ch * 224) * 2, 896, 32, 2 * x, false, c, pf, extra);
    }
    STAMP(1); STAMPC(1);
    const unsigned wvo = (lane >> 2) * 896 + (((lane & 3) ^ ((((lane >> 5) ^ (lane >> 4)) & 1) * 2 + (lane >> 5))) * 16);
    auto wchunk = [&](int u, int pq, int slot) {
        const char* src = (pq ? Wim : Wre) + (size_t)cb * 112 * 896 + u * 64;
#pragma unroll
        for (int k = 0; k < 7; ++k) glds16(src + k * 16 * 896, wvo, __builtin_amdgcn_readfirstlane(lds0 + slot * 7168 + k * 1024));
    };
    if (wid < 4) {
        const int mh = wid & 1, nh = wid >> 1, g = lane >> 4;
        const unsigned base = WS_PQ + ((kh * 2 + mh) * 128 + tb * 4) * 14336 + (ch * 7) * 1024 + lane * 16;
#pragma unroll
        for (int m = 0; m < 4; ++m) {
            const unsigned bm = base + m * 14336;
            if (nh == 0) {
                st_sc1(rs, bm, pack_pair(c[0], c[1], m)); st_sc1(rs, bm + 1024, pack_pair(c[2], c[3], m)); st_sc1(rs, bm + 2048, pack_pair(c[4], c[5], m));
                const v4u s = pack_single(c[6], m);
                if ((g & 1) == 0) st_sc1(rs, bm + 3072 - lane * 16 + ((g >> 1) * 16 + (lane & 15)) * 16, s);
            } else {
                const v4u s = pack_single(c[0], m);
                if ((g & 1) == 0) st_sc1(rs, bm + 3072 - lane * 16 + ((2 + (g >> 1)) * 16 + (lane & 15)) * 16, s);
                st_sc1(rs, bm + 4096, pack_pair(c[1], c[2], m)); st_sc1(rs, bm + 5120, pack_pair(c[3], c[4], m)); st_sc1(rs, bm + 6144, pack_pair(c[5], c[6], m));
            }
        }
    }
    STAMP(9);
    asm volatile("s_barrier" ::: "memory");
    if (wid < 4) {
        wait_vm0();
        STAMP(2);
        unsigned old = 0;
        if (lane == 0) old = __hip_atomic_fetch_add((LAS unsigned*)(L + LDS_CNT), 1u, __ATOMIC_RELAXED, __HIP_MEMORY_SCOPE_WORKGROUP);
        if (__builtin_amdgcn_readfirstlane(old) == 3) {
            if (lane == 0) flag_store(ws, WS_FP + (tb * 4 + cb) * 8, tag);
            STAMP(3);
            poll(ws, WS_FP + (tb * 4 + (lane & 3)) * 8, lane < 4, tag);
        }
    } else {
        for (int s = wid - 4; s < 22; s += 4) wchunk(s >> 1, s & 1, s);
        STAMP(2);
        wait_vm0();
    }
    STAMP(4);
    bar();
    STAMP(5);
    const int m = wid & 3, pq = wid >> 2;
    const unsigned pb = WS_PQ + (pq * 128 + tb * 4 + m) * 14336 + lane * 16;
    v4u p0[14], p1[14];
#pragma unroll
    for (int u = 0; u < 14; ++u) { p0[u] = ld_sc1(rs, pb + u * 1024); p1[u] = ld_sc1(rs, pb + 2 * 128 * 14336 + u * 1024); }
    asm volatile("" ::: "memory"); __builtin_amdgcn_sched_barrier(0);
    const int e16 = lane & 15, g = lane >> 4, rq = e16 >> 2;
    const unsigned wro = e16 * 64 + ((g ^ ((((rq >> 1) ^ rq) & 1) * 2 + (rq >> 1))) * 16) + pq * 7168;
    f32x4 acc[7];
#pragma unroll
    for (int n = 0; n < 7; ++n) acc[n] = (f32x4){0.f, 0.f, 0.f, 0.f};
    bf16x8 w[2][7];
#pragma unroll
    for (int n = 0; n < 7; ++n) w[0][n] = *(const LAS bf16x8*)(L + wro + n * 1024);
#pragma unroll
    for (int u = 0; u < 14; ++u) {
        if (u == 1) STAMP(10);
        if (u == 3) STAMP(11);
        if (u == 10) STAMP(12);
        if (u == 3) {
            bar();
            if (wid >= 4) { for (int s = wid - 4; s < 6; s += 4) wchunk(11 + (s >> 1), s & 1, s); }
        }
        if (u == 10) { if (wid >= 4) wait_vm0(); bar(); }
        if (u + 1 < 14) {
            const int sl = (u + 1 < 11 ? u + 1 : u + 1 - 11) * 2;
#pragma unroll
            for (int n = 0; n < 7; ++n) w[(u + 1) & 1][n] = *(const LAS bf16x8*)(L + wro + sl * 7168 + n * 1024);
        }
        const bf16x8 f = add_frag(p0[u], p1[u]);
        __builtin_amdgcn_sched_barrier(0);
#pragma unroll
        for (int n = 0; n < 7; ++n) acc[n] = __builtin_amdgcn_mfma_f32_16x16x32_bf16(w[u & 1][n], f, acc[n], 0, 0, 0);
        __builtin_amdgcn_sched_barrier(0);
    }
    STAMP(6);
    final_store(L, wid, lane, acc, -1.0f, out + ((size_t)tb * 64 * 896 + 448 + cb * 112) * 2);
    STAMP(7);
}

DI void attention(int iq, int head, int lane, __amdgpu_buffer_rsrc_t rs) {
    const float SC = 0.18898223650461362f, L2E = 1.4426950408889634f;
    const int a = lane & 31, hi = lane >> 5;
    const unsigned hb = head * 64 * 2048 + lane * 16;
    const bf16x8 q0 = __builtin_bit_cast(bf16x8, ld_sc1(rs, WS_Q + hb + iq * 2048)), q1 = __builtin_bit_cast(bf16x8, ld_sc1(rs, WS_Q + hb + iq * 2048 + 1024));
    v4u kf[9][2];
#pragma unroll
    for (int j = 0; j < 9; ++j) { const int kt = max(iq - 8 + j, 0); kf[j][0] = ld_sc1(rs, WS_K + hb + kt * 2048); kf[j][1] = ld_sc1(rs, WS_K + hb + kt * 2048 + 1024); }
    v4u vf[9][2];
#pragma unroll
    for (int j = 0; j < 9; ++j) { const int kt = max(iq - 8 + j, 0); vf[j][0] = ld_sc1(rs, WS_V + hb + kt * 2048); vf[j][1] = ld_sc1(rs, WS_V + hb + kt * 2048 + 1024); }
    asm volatile("" ::: "memory"); __builtin_amdgcn_sched_barrier(0);
    const f32x16 zero = {};
    f32x16 S[8];
    {
        f32x16 so = __builtin_amdgcn_mfma_f32_32x32x16_bf16(__builtin_bit_cast(bf16x8, kf[0][0]), q0, zero, 0, 0, 0);
        so = __builtin_amdgcn_mfma_f32_32x32x16_bf16(__builtin_bit_cast(bf16x8, kf[0][1]), q1, so, 0, 0, 0);
        f32x16 sd = __builtin_amdgcn_mfma_f32_32x32x16_bf16(__builtin_bit_cast(bf16x8, kf[8][0]), q0, zero, 0, 0, 0);
        sd = __builtin_amdgcn_mfma_f32_32x32x16_bf16(__builtin_bit_cast(bf16x8, kf[8][1]), q1, sd, 0, 0, 0);
        if (iq < 8) {
            asm volatile("" ::: "memory");
#pragma unroll
            for (int r = 0; r < 16; ++r) so[r] = -INFINITY;
        }
#pragma unroll
        for (int r = 0; r < 16; ++r) { const int b = (r & 3) + 8 * (r >> 2) + 4 * hi; S[0][r] = (b <= a) ? sd[r] : so[r]; }
    }
#pragma unroll
    for (int j = 1; j < 8; ++j) {
        f32x16 s = __builtin_amdgcn_mfma_f32_32x32x16_bf16(__builtin_bit_cast(bf16x8, kf[j][0]), q0, zero, 0, 0, 0);
        s = __builtin_amdgcn_mfma_f32_32x32x16_bf16(__builtin_bit_cast(bf16x8, kf[j][1]), q1, s, 0, 0, 0);
        S[j] = s;
    }
    if (iq < 7) {
        asm volatile("" ::: "memory");
#pragma unroll
        for (int j = 1; j < 8; ++j) if (iq - 8 + j < 0) {
#pragma unroll
            for (int r = 0; r < 16; ++r) S[j][r] = -INFINITY;
        }
    }
    float mx = S[0][0];
#pragma unroll
    for (int j = 0; j < 8; ++j)
#pragma unroll
        for (int r = 0; r < 16; ++r) mx = __builtin_fmaxf(mx, S[j][r]);
    { auto rr = __builtin_amdgcn_permlane32_swap(__float_as_uint(mx), __float_as_uint(mx), false, false);
      mx = __builtin_fmaxf(__uint_as_float(rr[0]), __uint_as_float(rr[1])); }
    const float nml = -(rbf(rbf(mx) * SC) * L2E);
    f32x16 O = zero;
#pragma unroll
    for (int j = 0; j < 8; ++j) {
        v4u pw[2];
#pragma unroll
        for (int k = 0; k < 8; ++k) {
            const float e0 = __builtin_amdgcn_exp2f(__builtin_fmaf(rbf(rbf(S[j][2 * k]) * SC), L2E, nml));
            const float e1 = __builtin_amdgcn_exp2f(__builtin_fmaf(rbf(rbf(S[j][2 * k + 1]) * SC), L2E, nml));
            pw[k >> 2][k & 3] = cvtpk(e0, e1);
        }
        if (j == 0) {
            v4u pd[2], po[2];
#pragma unroll
            for (int k = 0; k < 8; ++k) {
                const int b = ((2 * k) & 3) + 8 * ((2 * k) >> 2) + 4 * hi;
                const unsigned msk = (b <= a ? 0xffffu : 0u) | (b + 1 <= a ? 0xffff0000u : 0u);
                pd[k >> 2][k & 3] = pw[k >> 2][k & 3] & msk; po[k >> 2][k & 3] = pw[k >> 2][k & 3] & ~msk;
            }
            O = __builtin_amdgcn_mfma_f32_32x32x16_bf16(__builtin_bit_cast(bf16x8, vf[8][0]), __builtin_bit_cast(bf16x8, pd[0]), O, 0, 0, 0);
            O = __builtin_amdgcn_mfma_f32_32x32x16_bf16(__builtin_bit_cast(bf16x8, vf[8][1]), __builtin_bit_cast(bf16x8, pd[1]), O, 0, 0, 0);
            O = __builtin_amdgcn_mfma_f32_32x32x16_bf16(__builtin_bit_cast(bf16x8, vf[0][0]), __builtin_bit_cast(bf16x8, po[0]), O, 0, 0, 0);
            O = __builtin_amdgcn_mfma_f32_32x32x16_bf16(__builtin_bit_cast(bf16x8, vf[0][1]), __builtin_bit_cast(bf16x8, po[1]), O, 0, 0, 0);
        } else {
            O = __builtin_amdgcn_mfma_f32_32x32x16_bf16(__builtin_bit_cast(bf16x8, vf[j][0]), __builtin_bit_cast(bf16x8, pw[0]), O, 0, 0, 0);
            O = __builtin_amdgcn_mfma_f32_32x32x16_bf16(__builtin_bit_cast(bf16x8, vf[j][1]), __builtin_bit_cast(bf16x8, pw[1]), O, 0, 0, 0);
        }
    }
    auto lr = __builtin_amdgcn_permlane32_swap(__float_as_uint(O[12]), __float_as_uint(O[12]), false, false);
    const float rl = 1.0f / __uint_as_float(lr[1]);
    v4u o0, o1;
#pragma unroll
    for (int k = 0; k < 4; ++k) { o0[k] = cvtpk(O[2 * k] * rl, O[2 * k + 1] * rl); o1[k] = cvtpk(O[8 + 2 * k] * rl, O[8 + 2 * k + 1] * rl); }
    if (hi) { o1[2] = 0; o1[3] = 0; }
    const unsigned ob = WS_O + (head * 128 + iq * 2 + (a >> 4)) * 1024 + (hi * 16 + (a & 15)) * 16;
    st_sc1(rs, ob, o0); st_sc1(rs, ob + 512, o1);
}

DI void role_a(char* lds, int x, int ai, int wid, int lane, const char* Ax, const char* Wqkv, const char* Wo, const char* Bx, const char* Cre, const char* Cim, const char* Wre, const char* Wim, char* out, char* ws, u64 tag STARG) {
    const unsigned lds0 = (unsigned)(uintptr_t)lds;
    LAS char* L = (LAS char*)lds;
    const __amdgpu_buffer_rsrc_t rs = __builtin_amdgcn_make_buffer_rsrc(ws, 0, WS_END, 0x00020000);
    const int iq = 8 * x + (ai >> 1), hh = ai & 1, cb = (iq & 1) * 2 + hh, rp = iq >> 1;
    if (ai < 12) {
        const int rb = 2 * x + ai / 6, j = ai % 6;
        const bool isv = j >= 4;
        f32x16 c[7];
        STAMP(0); STAMPC(0);
        {
            const unsigned dummy = lds0 + LDS_DUMMY;
            const char* pfa = Ax + (size_t)(rb * 128 + (j & 1) * 32 + (lane & 31)) * 896;
            const char* pfb = Wqkv + (size_t)((ai / 6) * 16 + (lane >> 2)) * 2688 + (((j * 448) >> 7) + (lane & 3)) * 128;
            auto pf = [&](int st) {
                (void)st; (void)pfa; (void)pfb;
            };
            auto extra = [&](int s) { if (s == 0 && lane == 0) flag_store(ws, WS_ARR + blockIdx.x * 8, tag); };
            g224(lds0, wid, lane, Ax + (size_t)rb * 128 * 896, Ax + ((size_t)rb * 128 + 64) * 896, 896, Wqkv + j * 448, 2688, 14, 0, isv, c, pf, extra);
        }
        STAMP(1); STAMPC(1);
        if (wid < 4) {
            const int mh = wid & 1, nh = wid >> 1, l16 = lane & 15, g = lane >> 4;
#pragma unroll
            for (int n = 0; n < 7; ++n) {
                unsigned off;
                if (!isv) { const int u4 = nh * 28 + 4 * n + g, h8 = u4 / 7, du = u4 - 7 * h8;
                    off = h8 * 8192 + mh * 4096 + (du >> 2) * 1024 + (((du >> 1) & 1) * 32 + l16) * 16 + (du & 1) * 8; }
                else { const int col = nh * 112 + 16 * n + l16, h8 = col / 28, d = col - 28 * h8;
                    off = h8 * 8192 + mh * 4096 + ((g & 1) * 32 + d) * 16 + (g >> 1) * 8; }
#pragma unroll
                for (int m = 0; m < 4; ++m) {
                    v2u w; w[0] = cvtpk(c[n][4 * m], c[n][4 * m + 1]); w[1] = cvtpk(c[n][4 * m + 2], c[n][4 * m + 3]);
                    const unsigned mo = isv ? (m >> 1) * 2048 + (m & 1) * 1024 : (m >> 1) * 2048 + (m & 1) * 256;
                    *(LAS v2u*)(L + off + mo) = w;
                }
            }
        } else {
            const int t = (wid - 4) * 64 + lane;
            if (!isv) {
#pragma unroll
                for (int k = 0; k < 4; ++k) { const int e = t + 256 * k, h8 = e >> 7, tok = e & 127;
                    *(LAS v2u*)(L + h8 * 8192 + (tok >> 5) * 2048 + 1024 + (32 + (tok & 31)) * 16 + 8) = (v2u){0u, 0u}; }
            } else {
#pragma unroll
                for (int k = 0; k < 2; ++k) { const int e = t + 256 * k, dd = e & 3; const unsigned val = dd == 0 ? 0x3f803f80u : 0u;
                    *(LAS v4u*)(L + (e >> 6) * 8192 + ((e >> 4) & 3) * 2048 + ((e >> 3) & 1) * 1024 + (((e >> 2) & 1) * 32 + 28 + dd) * 16) = (v4u){val, val, val, val}; }
            }
        }
        bar();
        const unsigned gb = (j < 2 ? WS_Q : j < 4 ? WS_K : WS_V) + ((j & 1) * 8 * 64 + rb * 4) * 2048 + lane * 16;
#pragma unroll
        for (int k = 0; k < 8; ++k) {
            const v4u v = *(const LAS v4u*)(L + (wid * 8 + k) * 1024 + lane * 16);
            st_sc1(rs, gb + wid * 64 * 2048 + k * 1024, v);
        }
        wait_vm0();
        bar();
        if (wid == 0 && lane == 0) flag_store(ws, WS_F1 + (rb * 8 + j) * 8, tag);
        STAMP(2);
    }
    if (ai >= 12) {
        const unsigned dummy = lds0 + LDS_DUMMY;
        const int gw = (ai - 12) * 8 + wid;
        {
            const int L = (gw & 15) * 64 + lane;
            const char* p = (((L >> 6) & 1) ? Cim : Cre) + (size_t)((4 * x + (L >> 8)) * 64 + (L & 63)) * 4096 + ((L >> 7) & 1) * 2048;
            for (int a = ENG_FIRST + (gw >> 4); a < 16; a += 2) touch(p + ((a + 2 * x) & 15) * 128, dummy);
        }
        if (wid == 7 && lane == 0) flag_store(ws, WS_ARR + blockIdx.x * 8, tag);
    }
    {
        const unsigned wb = (cb * 224) & ~63u;
        const int rr = lane >> 4;
#pragma unroll
        for (int k = 0; k < 14; ++k) {
            const int r4 = wid * 14 + k;
            const int row = 4 * r4 + rr;
            const unsigned vo = rr * 896 + (((lane & 15) ^ ((row & 7) << 1)) * 16);
            glds16(Wo + (size_t)r4 * 4 * 896 + wb, vo, __builtin_amdgcn_readfirstlane(lds0 + r4 * 1024));
        }
    }
    {
        const int rq = iq >> 2;
        const int sel = lane == 0 ? 0 : lane < 4 ? 1 : 2;
        const int rb = sel == 0 ? rq : rq - 2 + ((lane - 1) % 3);
        poll(ws, WS_F1 + (max(rb, 0) * 8 + sel * 2 + hh) * 8, lane < 7 && rb >= 0, tag);
        STAMP(3);
        attention(iq, hh * 8 + wid, lane, rs);
        STAMP(4);
        wait_vm0();
        bar();
        STAMP(5);
        if (wid == 0) {
            if (lane == 0) flag_store(ws, WS_FO + (iq * 2 + hh) * 8, tag);
            poll(ws, WS_FO + (rp * 4 + (lane & 3)) * 8, lane < 4, tag);
        }
        bar();
        STAMP(6);
    }
    {
        const int m = wid & 3, k2 = wid >> 2;
        v4u of[8];
#pragma unroll
        for (int h = 0; h < 8; ++h) of[h] = ld_sc1(rs, WS_O + ((k2 * 8 + h) * 128 + rp * 4 + m) * 1024 + lane * 16);
        asm volatile("" ::: "memory"); __builtin_amdgcn_sched_barrier(0);
        const int g = lane >> 4, q = (lane & 15) >> 2, p = lane & 3;
        const int s0 = ((cb * 224) & 63) >> 5;
        unsigned ad[2][2][7];
#pragma unroll
        for (int hp = 0; hp < 2; ++hp)
#pragma unroll
            for (int xx = 0; xx < 2; ++xx) {
                int unit = 4 * (g >> 1) + 2 * xx + (g & 1); if (unit == 7) unit = 6;
                const int rl = 4 * unit + q, sw = (4 * hp + rl) & 7;
#pragma unroll
                for (int n = 0; n < 7; ++n) ad[hp][xx][n] = (k2 * 8 * 28 + hp * 28 + rl) * 256 + (((s0 + n) ^ sw) * 32) + p * 8;
            }
        f32x4 acc[7];
#pragma unroll
        for (int n = 0; n < 7; ++n) acc[n] = (f32x4){0.f, 0.f, 0.f, 0.f};
#pragma unroll
        for (int h = 0; h < 8; ++h) {
#pragma unroll
            for (int n = 0; n < 7; ++n) {
                const s16x4 lo = trread(L + ad[h & 1][0][n] + (h >> 1) * 2 * 7168), hi2 = trread(L + ad[h & 1][1][n] + (h >> 1) * 2 * 7168);
                const bf16x8 w = (bf16x8){lo[0], lo[1], lo[2], lo[3], hi2[0], hi2[1], hi2[2], hi2[3]};
                acc[n] = __builtin_amdgcn_mfma_f32_16x16x32_bf16(w, __builtin_bit_cast(bf16x8, of[h]), acc[n], 0, 0, 0);
            }
        }
        final_store(L, wid, lane, acc, 1.0f, out + ((size_t)rp * 64 * 896 + cb * 112) * 2);
        STAMP(7);
    }
}
}

__global__ void __launch_bounds__(512, 2)
fused_kernel(const char* Ax, const char* Wqkv, const char* Wo, const char* Bx, const char* Cre, const char* Cim, const char* Wre, const char* Wim, char* out, char* ws) {
    extern __shared__ __attribute__((aligned(16))) char lds[];
    const int wid = __builtin_amdgcn_readfirstlane(threadIdx.x >> 6), lane = threadIdx.x & 63;
    const int x = blockIdx.x & 7, i = blockIdx.x >> 3;
    STAMP(8);
    const u64 tag = *(const GAS u64*)(ws + WS_EPOCH) + 1;
    if (threadIdx.x < 4) *(LAS unsigned*)((LAS char*)lds + LDS_CNT + threadIdx.x * 4) = 0u;
    if (i < 16) role_b(lds, x, i, wid, lane, Bx, Cre, Cim, Wre, Wim, out, ws, tag STPASS);
    else role_a(lds, x, i - 16, wid, lane, Ax, Wqkv, Wo, Bx, Cre, Cim, Wre, Wim, out, ws, tag STPASS);
    if (blockIdx.x == 0 && wid == 7) {
#pragma unroll
        for (int k = 0; k < 4; ++k) poll(ws, WS_ARR + (k * 64 + lane) * 8, true, tag);
        if (lane == 0) flag_store(ws, WS_EPOCH, tag);
    }
}

extern "C" void kernel_launch(void* const* d_in, const int* in_sizes, int n_in, void* d_out, int out_size, void* d_ws, size_t ws_size, hipStream_t stream) {
    (void)in_sizes; (void)n_in; (void)out_size; (void)ws_size;
    (void)hipFuncSetAttribute((const void*)fused_kernel, hipFuncAttributeMaxDynamicSharedMemorySize, LDS_BYTES);
    fused_kernel<<<256, 512, LDS_BYTES, stream>>>((const char*)d_in[0], (const char*)d_in[1], (const char*)d_in[2], (const char*)d_in[3], (const char*)d_in[4], (const char*)d_in[5],
                                                   (const char*)d_in[6], (const char*)d_in[7], (char*)d_out, (char*)d_ws);
}
```

```cpp
#include <hip/hip_runtime.h>
#include <hip/hip_bf16.h>
#include <stdint.h>

typedef short bf16x8 __attribute__((ext_vector_type(8)));
typedef short s16x4 __attribute__((ext_vector_type(4)));
typedef float f32x2 __attribute__((ext_vector_type(2)));
typedef float f32x4 __attribute__((ext_vector_type(4)));
typedef float f32x16 __attribute__((ext_vector_type(16)));
typedef __bf16 bf16x2 __attribute__((ext_vector_type(2)));
typedef unsigned v4u __attribute__((ext_vector_type(4)));
typedef unsigned v2u __attribute__((ext_vector_type(2)));
typedef unsigned long long u64;
#define LAS __attribute__((address_space(3)))
#define GAS __attribute__((address_space(1)))
#define DI __device__ __forceinline__

#define G224_BODY \
    "v_mov_b32 v0, 0\n\t" \
    "v_mov_b32 v1, 0\n\t" \
    "v_mov_b32 v2, 0\n\t" \
    "v_mov_b32 v3, 0\n\t" \
    "v_mov_b32 v4, 0\n\t" \
    "v_mov_b32 v5, 0\n\t" \
    "v_mov_b32 v6, 0\n\t" \
    "v_mov_b32 v7, 0\n\t" \
    "v_mov_b32 v8, 0\n\t" \
    "v_mov_b32 v9, 0\n\t" \
    "v_mov_b32 v10, 0\n\t" \
    "v_mov_b32 v11, 0\n\t" \
    "v_mov_b32 v12, 0\n\t" \
    "v_mov_b32 v13, 0\n\t" \
    "v_mov_b32 v14, 0\n\t" \
    "v_mov_b32 v15, 0\n\t" \
    "v_mov_b32 v16, 0\n\t" \
    "v_mov_b32 v17, 0\n\t" \
    "v_mov_b32 v18, 0\n\t" \
    "v_mov_b32 v19, 0\n\t" \
    "v_mov_b32 v20, 0\n\t" \
    "v_mov_b32 v21, 0\n\t" \
    "v_mov_b32 v22, 0\n\t" \
    "v_mov_b32 v23, 0\n\t" \
    "v_mov_b32 v24, 0\n\t" \
    "v_mov_b32 v25, 0\n\t" \
    "v_mov_b32 v26, 0\n\t" \
    "v_mov_b32 v27, 0\n\t" \
    "v_mov_b32 v28, 0\n\t" \
    "v_mov_b32 v29, 0\n\t" \
    "v_mov_b32 v30, 0\n\t" \
    "v_mov_b32 v31, 0\n\t" \
    "v_mov_b32 v32, 0\n\t" \
    "v_mov_b32 v33, 0\n\t" \
    "v_mov_b32 v34, 0\n\t" \
    "v_mov_b32 v35, 0\n\t" \
    "v_mov_b32 v36, 0\n\t" \
    "v_mov_b32 v37, 0\n\t" \
    "v_mov_b32 v38, 0\n\t" \
    "v_mov_b32 v39, 0\n\t" \
    "v_mov_b32 v40, 0\n\t" \
    "v_mov_b32 v41, 0\n\t" \
    "v_mov_b32 v42, 0\n\t" \
    "v_mov_b32 v43, 0\n\t" \
    "v_mov_b32 v44, 0\n\t" \
    "v_mov_b32 v45, 0\n\t" \
    "v_mov_b32 v46, 0\n\t" \
    "v_mov_b32 v47, 0\n\t" \
    "v_mov_b32 v48, 0\n\t" \
    "v_mov_b32 v49, 0\n\t" \
    "v_mov_b32 v50, 0\n\t" \
    "v_mov_b32 v51, 0\n\t" \
    "v_mov_b32 v52, 0\n\t" \
    "v_mov_b32 v53, 0\n\t" \
    "v_mov_b32 v54, 0\n\t" \
    "v_mov_b32 v55, 0\n\t" \
    "v_mov_b32 v56, 0\n\t" \
    "v_mov_b32 v57, 0\n\t" \
    "v_mov_b32 v58, 0\n\t" \
    "v_mov_b32 v59, 0\n\t" \
    "v_mov_b32 v60, 0\n\t" \
    "v_mov_b32 v61, 0\n\t" \
    "v_mov_b32 v62, 0\n\t" \
    "v_mov_b32 v63, 0\n\t" \
    "v_mov_b32 v64, 0\n\t" \
    "v_mov_b32 v65, 0\n\t" \
    "v_mov_b32 v66, 0\n\t" \
    "v_mov_b32 v67, 0\n\t" \
    "v_mov_b32 v68, 0\n\t" \
    "v_mov_b32 v69, 0\n\t" \
    "v_mov_b32 v70, 0\n\t" \
    "v_mov_b32 v71, 0\n\t" \
    "v_mov_b32 v72, 0\n\t" \
    "v_mov_b32 v73, 0\n\t" \
    "v_mov_b32 v74, 0\n\t" \
    "v_mov_b32 v75, 0\n\t" \
    "v_mov_b32 v76, 0\n\t" \
    "v_mov_b32 v77, 0\n\t" \
    "v_mov_b32 v78, 0\n\t" \
    "v_mov_b32 v79, 0\n\t" \
    "v_mov_b32 v80, 0\n\t" \
    "v_mov_b32 v81, 0\n\t" \
    "v_mov_b32 v82, 0\n\t" \
    "v_mov_b32 v83, 0\n\t" \
    "v_mov_b32 v84, 0\n\t" \
    "v_mov_b32 v85, 0\n\t" \
    "v_mov_b32 v86, 0\n\t" \
    "v_mov_b32 v87, 0\n\t" \
    "v_mov_b32 v88, 0\n\t" \
    "v_mov_b32 v89, 0\n\t" \
    "v_mov_b32 v90, 0\n\t" \
    "v_mov_b32 v91, 0\n\t" \
    "v_mov_b32 v92, 0\n\t" \
    "v_mov_b32 v93, 0\n\t" \
    "v_mov_b32 v94, 0\n\t" \
    "v_mov_b32 v95, 0\n\t" \
    "v_mov_b32 v96, 0\n\t" \
    "v_mov_b32 v97, 0\n\t" \
    "v_mov_b32 v98, 0\n\t" \
    "v_mov_b32 v99, 0\n\t" \
    "v_mov_b32 v100, 0\n\t" \
    "v_mov_b32 v101, 0\n\t" \
    "v_mov_b32 v102, 0\n\t" \
    "v_mov_b32 v103, 0\n\t" \
    "v_mov_b32 v104, 0\n\t" \
    "v_mov_b32 v105, 0\n\t" \
    "v_mov_b32 v106, 0\n\t" \
    "v_mov_b32 v107, 0\n\t" \
    "v_mov_b32 v108, 0\n\t" \
    "v_mov_b32 v109, 0\n\t" \
    "v_mov_b32 v110, 0\n\t" \
    "v_mov_b32 v111, 0\n\t" \
    "s_waitcnt lgkmcnt(0)\n\t" \
    "s_barrier\n\t" \
    "v_mov_b32 v207, v200\n\t" \
    "v_mov_b32 v208, v202\n\t" \
    "v_mov_b32 v209, v203\n\t" \
    "ds_read_b128 v[112:115], v207 offset:0\n\t" \
    "ds_read_b64_tr_b16 v[128:129], v208 offset:0\n\t" \
    "ds_read_b64_tr_b16 v[130:131], v208 offset:1792\n\t" \
    "ds_read_b128 v[116:119], v207 offset:2048\n\t" \
    "ds_read_b128 v[120:123], v207 offset:4096\n\t" \
    "ds_read_b128 v[124:127], v207 offset:6144\n\t" \
    "ds_read_b64_tr_b16 v[132:133], v209 offset:32\n\t" \
    "ds_read_b64_tr_b16 v[134:135], v209 offset:1824\n\t" \
    "ds_read_b64_tr_b16 v[136:137], v208 offset:64\n\t" \
    "ds_read_b64_tr_b16 v[138:139], v208 offset:1856\n\t" \
    "ds_read_b64_tr_b16 v[140:141], v209 offset:96\n\t" \
    "ds_read_b64_tr_b16 v[142:143], v209 offset:1888\n\t" \
    "ds_read_b64_tr_b16 v[144:145], v208 offset:128\n\t" \
    "ds_read_b64_tr_b16 v[146:147], v208 offset:1920\n\t" \
    "ds_read_b64_tr_b16 v[148:149], v209 offset:160\n\t" \
    "ds_read_b64_tr_b16 v[150:151], v209 offset:1952\n\t" \
    "ds_read_b64_tr_b16 v[152:153], v208 offset:192\n\t" \
    "ds_read_b64_tr_b16 v[154:155], v208 offset:1984\n\t" \
    "s_mov_b32 %[aslot], 0\n\t" \
    "s_mov_b32 %[bslot], 14336\n\t" \
    "v_mov_b32 v204, v201\n\t" \
    "v_add_u32 v205, %[bslot], v202\n\t" \
    "v_add_u32 v206, %[bslot], v203\n\t" \
    "s_waitcnt lgkmcnt(0)\n\t" \
    "s_barrier\n\t" \
    "1:\n\t" \
    "v_mfma_f32_16x16x32_bf16 v[0:3], v[128:131], v[112:115], v[0:3]\n\t" \
    "ds_read_b128 v[156:159], v204 offset:0\n\t" \
    "v_mfma_f32_16x16x32_bf16 v[4:7], v[128:131], v[116:119], v[4:7]\n\t" \
    "ds_read_b64_tr_b16 v[172:173], v205 offset:0\n\t" \
    "v_mfma_f32_16x16x32_bf16 v[8:11], v[128:131], v[120:123], v[8:11]\n\t" \
    "ds_read_b64_tr_b16 v[174:175], v205 offset:1792\n\t" \
    "v_mfma_f32_16x16x32_bf16 v[12:15], v[128:131], v[124:127], v[12:15]\n\t" \
    "ds_read_b128 v[160:163], v204 offset:2048\n\t" \
    "v_mfma_f32_16x16x32_bf16 v[16:19], v[132:135], v[112:115], v[16:19]\n\t" \
    "ds_read_b128 v[164:167], v204 offset:4096\n\t" \
    "v_mfma_f32_16x16x32_bf16 v[20:23], v[132:135], v[116:119], v[20:23]\n\t" \
    "ds_read_b128 v[168:171], v204 offset:6144\n\t" \
    "v_mfma_f32_16x16x32_bf16 v[24:27], v[132:135], v[120:123], v[24:27]\n\t" \
    "ds_read_b64_tr_b16 v[176:177], v206 offset:32\n\t" \
    "v_mfma_f32_16x16x32_bf16 v[28:31], v[132:135], v[124:127], v[28:31]\n\t" \
    "ds_read_b64_tr_b16 v[178:179], v206 offset:1824\n\t" \
    "v_mfma_f32_16x16x32_bf16 v[32:35], v[136:139], v[112:115], v[32:35]\n\t" \
    "ds_read_b64_tr_b16 v[180:181], v205 offset:64\n\t" \
    "v_mfma_f32_16x16x32_bf16 v[36:39], v[136:139], v[116:119], v[36:39]\n\t" \
    "ds_read_b64_tr_b16 v[182:183], v205 offset:1856\n\t" \
    "v_mfma_f32_16x16x32_bf16 v[40:43], v[136:139], v[120:123], v[40:43]\n\t" \
    "ds_read_b64_tr_b16 v[184:185], v206 offset:96\n\t" \
    "v_mfma_f32_16x16x32_bf16 v[44:47], v[136:139], v[124:127], v[44:47]\n\t" \
    "ds_read_b64_tr_b16 v[186:187], v206 offset:1888\n\t" \
    "v_mfma_f32_16x16x32_bf16 v[48:51], v[140:143], v[112:115], v[48:51]\n\t" \
    "ds_read_b64_tr_b16 v[188:189], v205 offset:128\n\t" \
    "v_mfma_f32_16x16x32_bf16 v[52:55], v[140:143], v[116:119], v[52:55]\n\t" \
    "ds_read_b64_tr_b16 v[190:191], v205 offset:1920\n\t" \
    "v_mfma_f32_16x16x32_bf16 v[56:59], v[140:143], v[120:123], v[56:59]\n\t" \
    "ds_read_b64_tr_b16 v[192:193], v206 offset:160\n\t" \
    "v_mfma_f32_16x16x32_bf16 v[60:63], v[140:143], v[124:127], v[60:63]\n\t" \
    "ds_read_b64_tr_b16 v[194:195], v206 offset:1952\n\t" \
    "v_mfma_f32_16x16x32_bf16 v[64:67], v[144:147], v[112:115], v[64:67]\n\t" \
    "ds_read_b64_tr_b16 v[196:197], v205 offset:192\n\t" \
    "v_mfma_f32_16x16x32_bf16 v[68:71], v[144:147], v[116:119], v[68:71]\n\t" \
    "ds_read_b64_tr_b16 v[198:199], v205 offset:1984\n\t" \
    "v_mfma_f32_16x16x32_bf16 v[72:75], v[144:147], v[120:123], v[72:75]\n\t" \
    "v_mfma_f32_16x16x32_bf16 v[76:79], v[144:147], v[124:127], v[76:79]\n\t" \
    "s_add_u32 %[aslot], %[aslot], 16384\n\t" \
    "v_mfma_f32_16x16x32_bf16 v[80:83], v[148:151], v[112:115], v[80:83]\n\t" \
    "s_and_b32 %[aslot], %[aslot], 0xffff\n\t" \
    "v_mfma_f32_16x16x32_bf16 v[84:87], v[148:151], v[116:119], v[84:87]\n\t" \
    "s_add_u32 %[bslot], %[bslot], 14336\n\t" \
    "v_mfma_f32_16x16x32_bf16 v[88:91], v[148:151], v[120:123], v[88:91]\n\t" \
    "s_cmp_eq_u32 %[bslot], 86016\n\t" \
    "v_mfma_f32_16x16x32_bf16 v[92:95], v[148:151], v[124:127], v[92:95]\n\t" \
    "s_cselect_b32 %[bslot], 0, %[bslot]\n\t" \
    "v_mfma_f32_16x16x32_bf16 v[96:99], v[152:155], v[112:115], v[96:99]\n\t" \
    "v_add_u32 v207, %[aslot], v200\n\t" \
    "v_mfma_f32_16x16x32_bf16 v[100:103], v[152:155], v[116:119], v[100:103]\n\t" \
    "v_add_u32 v208, %[bslot], v202\n\t" \
    "v_mfma_f32_16x16x32_bf16 v[104:107], v[152:155], v[120:123], v[104:107]\n\t" \
    "v_add_u32 v209, %[bslot], v203\n\t" \
    "v_mfma_f32_16x16x32_bf16 v[108:111], v[152:155], v[124:127], v[108:111]\n\t" \
    "s_waitcnt lgkmcnt(0)\n\t" \
    "s_barrier\n\t" \
    "v_mfma_f32_16x16x32_bf16 v[0:3], v[172:175], v[156:159], v[0:3]\n\t" \
    "ds_read_b128 v[112:115], v207 offset:0\n\t" \
    "v_mfma_f32_16x16x32_bf16 v[4:7], v[172:175], v[160:163], v[4:7]\n\t" \
    "ds_read_b64_tr_b16 v[128:129], v208 offset:0\n\t" \
    "v_mfma_f32_16x16x32_bf16 v[8:11], v[172:175], v[164:167], v[8:11]\n\t" \
    "ds_read_b64_tr_b16 v[130:131], v208 offset:1792\n\t" \
    "v_mfma_f32_16x16x32_bf16 v[12:15], v[172:175], v[168:171], v[12:15]\n\t" \
    "ds_read_b128 v[116:119], v207 offset:2048\n\t" \
    "v_mfma_f32_16x16x32_bf16 v[16:19], v[176:179], v[156:159], v[16:19]\n\t" \
    "ds_read_b128 v[120:123], v207 offset:4096\n\t" \
    "v_mfma_f32_16x16x32_bf16 v[20:23], v[176:179], v[160:163], v[20:23]\n\t" \
    "ds_read_b128 v[124:127], v207 offset:6144\n\t" \
    "v_mfma_f32_16x16x32_bf16 v[24:27], v[176:179], v[164:167], v[24:27]\n\t" \
    "ds_read_b64_tr_b16 v[132:133], v209 offset:32\n\t" \
    "v_mfma_f32_16x16x32_bf16 v[28:31], v[176:179], v[168:171], v[28:31]\n\t" \
    "ds_read_b64_tr_b16 v[134:135], v209 offset:1824\n\t" \
    "v_mfma_f32_16x16x32_bf16 v[32:35], v[180:183], v[156:159], v[32:35]\n\t" \
    "ds_read_b64_tr_b16 v[136:137], v208 offset:64\n\t" \
    "v_mfma_f32_16x16x32_bf16 v[36:39], v[180:183], v[160:163], v[36:39]\n\t" \
    "ds_read_b64_tr_b16 v[138:139], v208 offset:1856\n\t" \
    "v_mfma_f32_16x16x32_bf16 v[40:43], v[180:183], v[164:167], v[40:43]\n\t" \
    "ds_read_b64_tr_b16 v[140:141], v209 offset:96\n\t" \
    "v_mfma_f32_16x16x32_bf16 v[44:47], v[180:183], v[168:171], v[44:47]\n\t" \
    "ds_read_b64_tr_b16 v[142:143], v209 offset:1888\n\t" \
    "v_mfma_f32_16x16x32_bf16 v[48:51], v[184:187], v[156:159], v[48:51]\n\t" \
    "ds_read_b64_tr_b16 v[144:145], v208 offset:128\n\t" \
    "v_mfma_f32_16x16x32_bf16 v[52:55], v[184:187], v[160:163], v[52:55]\n\t" \
    "ds_read_b64_tr_b16 v[146:147], v208 offset:1920\n\t" \
    "v_mfma_f32_16x16x32_bf16 v[56:59], v[184:187], v[164:167], v[56:59]\n\t" \
    "ds_read_b64_tr_b16 v[148:149], v209 offset:160\n\t" \
    "v_mfma_f32_16x16x32_bf16 v[60:63], v[184:187], v[168:171], v[60:63]\n\t" \
    "ds_read_b64_tr_b16 v[150:151], v209 offset:1952\n\t" \
    "v_mfma_f32_16x16x32_bf16 v[64:67], v[188:191], v[156:159], v[64:67]\n\t" \
    "ds_read_b64_tr_b16 v[152:153], v208 offset:192\n\t" \
    "v_mfma_f32_16x16x32_bf16 v[68:71], v[188:191], v[160:163], v[68:71]\n\t" \
    "ds_read_b64_tr_b16 v[154:155], v208 offset:1984\n\t" \
    "v_mfma_f32_16x16x32_bf16 v[72:75], v[188:191], v[164:167], v[72:75]\n\t" \
    "v_mfma_f32_16x16x32_bf16 v[76:79], v[188:191], v[168:171], v[76:79]\n\t" \
    "s_add_u32 %[bslot], %[bslot], 14336\n\t" \
    "v_mfma_f32_16x16x32_bf16 v[80:83], v[192:195], v[156:159], v[80:83]\n\t" \
    "s_cmp_eq_u32 %[bslot], 86016\n\t" \
    "v_mfma_f32_16x16x32_bf16 v[84:87], v[192:195], v[160:163], v[84:87]\n\t" \
    "s_cselect_b32 %[bslot], 0, %[bslot]\n\t" \
    "v_mfma_f32_16x16x32_bf16 v[88:91], v[192:195], v[164:167], v[88:91]\n\t" \
    "v_add_u32 v204, %[aslot], v201\n\t" \
    "v_mfma_f32_16x16x32_bf16 v[92:95], v[192:195], v[168:171], v[92:95]\n\t" \
    "v_add_u32 v205, %[bslot], v202\n\t" \
    "v_mfma_f32_16x16x32_bf16 v[96:99], v[196:199], v[156:159], v[96:99]\n\t" \
    "v_add_u32 v206, %[bslot], v203\n\t" \
    "v_mfma_f32_16x16x32_bf16 v[100:103], v[196:199], v[160:163], v[100:103]\n\t" \
    "s_sub_u32 %[cnt], %[cnt], 1\n\t" \
    "v_mfma_f32_16x16x32_bf16 v[104:107], v[196:199], v[164:167], v[104:107]\n\t" \
    "s_cmp_lg_u32 %[cnt], 0\n\t" \
    "v_mfma_f32_16x16x32_bf16 v[108:111], v[196:199], v[168:171], v[108:111]\n\t" \
    "s_waitcnt lgkmcnt(0)\n\t" \
    "s_barrier\n\t" \
    "s_cbranch_scc1 1b\n\t" \
    "v_mfma_f32_16x16x32_bf16 v[0:3], v[128:131], v[112:115], v[0:3]\n\t" \
    "ds_read_b128 v[156:159], v204 offset:0\n\t" \
    "v_mfma_f32_16x16x32_bf16 v[4:7], v[128:131], v[116:119], v[4:7]\n\t" \
    "ds_read_b64_tr_b16 v[172:173], v205 offset:0\n\t" \
    "v_mfma_f32_16x16x32_bf16 v[8:11], v[128:131], v[120:123], v[8:11]\n\t" \
    "ds_read_b64_tr_b16 v[174:175], v205 offset:1792\n\t" \
    "v_mfma_f32_16x16x32_bf16 v[12:15], v[128:131], v[124:127], v[12:15]\n\t" \
    "ds_read_b128 v[160:163], v204 offset:2048\n\t" \
    "v_mfma_f32_16x16x32_bf16 v[16:19], v[132:135], v[112:115], v[16:19]\n\t" \
    "ds_read_b128 v[164:167], v204 offset:4096\n\t" \
    "v_mfma_f32_16x16x32_bf16 v[20:23], v[132:135], v[116:119], v[20:23]\n\t" \
    "ds_read_b128 v[168:171], v204 offset:6144\n\t" \
    "v_mfma_f32_16x16x32_bf16 v[24:27], v[132:135], v[120:123], v[24:27]\n\t" \
    "ds_read_b64_tr_b16 v[176:177], v206 offset:32\n\t" \
    "v_mfma_f32_16x16x32_bf16 v[28:31], v[132:135], v[124:127], v[28:31]\n\t" \
    "ds_read_b64_tr_b16 v[178:179], v206 offset:1824\n\t" \
    "v_mfma_f32_16x16x32_bf16 v[32:35], v[136:139], v[112:115], v[32:35]\n\t" \
    "ds_read_b64_tr_b16 v[180:181], v205 offset:64\n\t" \
    "v_mfma_f32_16x16x32_bf16 v[36:39], v[136:139], v[116:119], v[36:39]\n\t" \
    "ds_read_b64_tr_b16 v[182:183], v205 offset:1856\n\t" \
    "v_mfma_f32_16x16x32_bf16 v[40:43], v[136:139], v[120:123], v[40:43]\n\t" \
    "ds_read_b64_tr_b16 v[184:185], v206 offset:96\n\t" \
    "v_mfma_f32_16x16x32_bf16 v[44:47], v[136:139], v[124:127], v[44:47]\n\t" \
    "ds_read_b64_tr_b16 v[186:187], v206 offset:1888\n\t" \
    "v_mfma_f32_16x16x32_bf16 v[48:51], v[140:143], v[112:115], v[48:51]\n\t" \
    "ds_read_b64_tr_b16 v[188:189], v205 offset:128\n\t" \
    "v_mfma_f32_16x16x32_bf16 v[52:55], v[140:143], v[116:119], v[52:55]\n\t" \
    "ds_read_b64_tr_b16 v[190:191], v205 offset:1920\n\t" \
    "v_mfma_f32_16x16x32_bf16 v[56:59], v[140:143], v[120:123], v[56:59]\n\t" \
    "ds_read_b64_tr_b16 v[192:193], v206 offset:160\n\t" \
    "v_mfma_f32_16x16x32_bf16 v[60:63], v[140:143], v[124:127], v[60:63]\n\t" \
    "ds_read_b64_tr_b16 v[194:195], v206 offset:1952\n\t" \
    "v_mfma_f32_16x16x32_bf16 v[64:67], v[144:147], v[112:115], v[64:67]\n\t" \
    "ds_read_b64_tr_b16 v[196:197], v205 offset:192\n\t" \
    "v_mfma_f32_16x16x32_bf16 v[68:71], v[144:147], v[116:119], v[68:71]\n\t" \
    "ds_read_b64_tr_b16 v[198:199], v205 offset:1984\n\t" \
    "v_mfma_f32_16x16x32_bf16 v[72:75], v[144:147], v[120:123], v[72:75]\n\t" \
    "v_mfma_f32_16x16x32_bf16 v[76:79], v[144:147], v[124:127], v[76:79]\n\t" \
    "v_mfma_f32_16x16x32_bf16 v[80:83], v[148:151], v[112:115], v[80:83]\n\t" \
    "v_mfma_f32_16x16x32_bf16 v[84:87], v[148:151], v[116:119], v[84:87]\n\t" \
    "v_mfma_f32_16x16x32_bf16 v[88:91], v[148:151], v[120:123], v[88:91]\n\t" \
    "v_mfma_f32_16x16x32_bf16 v[92:95], v[148:151], v[124:127], v[92:95]\n\t" \
    "v_mfma_f32_16x16x32_bf16 v[96:99], v[152:155], v[112:115], v[96:99]\n\t" \
    "v_mfma_f32_16x16x32_bf16 v[100:103], v[152:155], v[116:119], v[100:103]\n\t" \
    "v_mfma_f32_16x16x32_bf16 v[104:107], v[152:155], v[120:123], v[104:107]\n\t" \
    "v_mfma_f32_16x16x32_bf16 v[108:111], v[152:155], v[124:127], v[108:111]\n\t" \
    "s_waitcnt lgkmcnt(0)\n\t" \
    "s_barrier\n\t" \
    "v_mfma_f32_16x16x32_bf16 v[0:3], v[172:175], v[156:159], v[0:3]\n\t" \
    "v_mfma_f32_16x16x32_bf16 v[4:7], v[172:175], v[160:163], v[4:7]\n\t" \
    "v_mfma_f32_16x16x32_bf16 v[8:11], v[172:175], v[164:167], v[8:11]\n\t" \
    "v_mfma_f32_16x16x32_bf16 v[12:15], v[172:175], v[168:171], v[12:15]\n\t" \
    "v_mfma_f32_16x16x32_bf16 v[16:19], v[176:179], v[156:159], v[16:19]\n\t" \
    "v_mfma_f32_16x16x32_bf16 v[20:23], v[176:179], v[160:163], v[20:23]\n\t" \
    "v_mfma_f32_16x16x32_bf16 v[24:27], v[176:179], v[164:167], v[24:27]\n\t" \
    "v_mfma_f32_16x16x32_bf16 v[28:31], v[176:179], v[168:171], v[28:31]\n\t" \
    "v_mfma_f32_16x16x32_bf16 v[32:35], v[180:183], v[156:159], v[32:35]\n\t" \
    "v_mfma_f32_16x16x32_bf16 v[36:39], v[180:183], v[160:163], v[36:39]\n\t" \
    "v_mfma_f32_16x16x32_bf16 v[40:43], v[180:183], v[164:167], v[40:43]\n\t" \
    "v_mfma_f32_16x16x32_bf16 v[44:47], v[180:183], v[168:171], v[44:47]\n\t" \
    "v_mfma_f32_16x16x32_bf16 v[48:51], v[184:187], v[156:159], v[48:51]\n\t" \
    "v_mfma_f32_16x16x32_bf16 v[52:55], v[184:187], v[160:163], v[52:55]\n\t" \
    "v_mfma_f32_16x16x32_bf16 v[56:59], v[184:187], v[164:167], v[56:59]\n\t" \
    "v_mfma_f32_16x16x32_bf16 v[60:63], v[184:187], v[168:171], v[60:63]\n\t" \
    "v_mfma_f32_16x16x32_bf16 v[64:67], v[188:191], v[156:159], v[64:67]\n\t" \
    "v_mfma_f32_16x16x32_bf16 v[68:71], v[188:191], v[160:163], v[68:71]\n\t" \
    "v_mfma_f32_16x16x32_bf16 v[72:75], v[188:191], v[164:167], v[72:75]\n\t" \
    "v_mfma_f32_16x16x32_bf16 v[76:79], v[188:191], v[168:171], v[76:79]\n\t" \
    "v_mfma_f32_16x16x32_bf16 v[80:83], v[192:195], v[156:159], v[80:83]\n\t" \
    "v_mfma_f32_16x16x32_bf16 v[84:87], v[192:195], v[160:163], v[84:87]\n\t" \
    "v_mfma_f32_16x16x32_bf16 v[88:91], v[192:195], v[164:167], v[88:91]\n\t" \
    "v_mfma_f32_16x16x32_bf16 v[92:95], v[192:195], v[168:171], v[92:95]\n\t" \
    "v_mfma_f32_16x16x32_bf16 v[96:99], v[196:199], v[156:159], v[96:99]\n\t" \
    "v_mfma_f32_16x16x32_bf16 v[100:103], v[196:199], v[160:163], v[100:103]\n\t" \
    "v_mfma_f32_16x16x32_bf16 v[104:107], v[196:199], v[164:167], v[104:107]\n\t" \
    "v_mfma_f32_16x16x32_bf16 v[108:111], v[196:199], v[168:171], v[108:111]\n\t" \
    "s_waitcnt lgkmcnt(0)\n\t" \
    "s_barrier\n\t" \
    "s_nop 7\n\t" \
    "s_nop 7\n\t" \
    ""
#define G224_BODY_SWAP \
    "v_mov_b32 v0, 0\n\t" \
    "v_mov_b32 v1, 0\n\t" \
    "v_mov_b32 v2, 0\n\t" \
    "v_mov_b32 v3, 0\n\t" \
    "v_mov_b32 v4, 0\n\t" \
    "v_mov_b32 v5, 0\n\t" \
    "v_mov_b32 v6, 0\n\t" \
    "v_mov_b32 v7, 0\n\t" \
    "v_mov_b32 v8, 0\n\t" \
    "v_mov_b32 v9, 0\n\t" \
    "v_mov_b32 v10, 0\n\t" \
    "v_mov_b32 v11, 0\n\t" \
    "v_mov_b32 v12, 0\n\t" \
    "v_mov_b32 v13, 0\n\t" \
    "v_mov_b32 v14, 0\n\t" \
    "v_mov_b32 v15, 0\n\t" \
    "v_mov_b32 v16, 0\n\t" \
    "v_mov_b32 v17, 0\n\t" \
    "v_mov_b32 v18, 0\n\t" \
    "v_mov_b32 v19, 0\n\t" \
    "v_mov_b32 v20, 0\n\t" \
    "v_mov_b32 v21, 0\n\t" \
    "v_mov_b32 v22, 0\n\t" \
    "v_mov_b32 v23, 0\n\t" \
    "v_mov_b32 v24, 0\n\t" \
    "v_mov_b32 v25, 0\n\t" \
    "v_mov_b32 v26, 0\n\t" \
    "v_mov_b32 v27, 0\n\t" \
    "v_mov_b32 v28, 0\n\t" \
    "v_mov_b32 v29, 0\n\t" \
    "v_mov_b32 v30, 0\n\t" \
    "v_mov_b32 v31, 0\n\t" \
    "v_mov_b32 v32, 0\n\t" \
    "v_mov_b32 v33, 0\n\t" \
    "v_mov_b32 v34, 0\n\t" \
    "v_mov_b32 v35, 0\n\t" \
    "v_mov_b32 v36, 0\n\t" \
    "v_mov_b32 v37, 0\n\t" \
    "v_mov_b32 v38, 0\n\t" \
    "v_mov_b32 v39, 0\n\t" \
    "v_mov_b32 v40, 0\n\t" \
    "v_mov_b32 v41, 0\n\t" \
    "v_mov_b32 v42, 0\n\t" \
    "v_mov_b32 v43, 0\n\t" \
    "v_mov_b32 v44, 0\n\t" \
    "v_mov_b32 v45, 0\n\t" \
    "v_mov_b32 v46, 0\n\t" \
    "v_mov_b32 v47, 0\n\t" \
    "v_mov_b32 v48, 0\n\t" \
    "v_mov_b32 v49, 0\n\t" \
    "v_mov_b32 v50, 0\n\t" \
    "v_mov_b32 v51, 0\n\t" \
    "v_mov_b32 v52, 0\n\t" \
    "v_mov_b32 v53, 0\n\t" \
    "v_mov_b32 v54, 0\n\t" \
    "v_mov_b32 v55, 0\n\t" \
    "v_mov_b32 v56, 0\n\t" \
    "v_mov_b32 v57, 0\n\t" \
    "v_mov_b32 v58, 0\n\t" \
    "v_mov_b32 v59, 0\n\t" \
    "v_mov_b32 v60, 0\n\t" \
    "v_mov_b32 v61, 0\n\t" \
    "v_mov_b32 v62, 0\n\t" \
    "v_mov_b32 v63, 0\n\t" \
    "v_mov_b32 v64, 0\n\t" \
    "v_mov_b32 v65, 0\n\t" \
    "v_mov_b32 v66, 0\n\t" \
    "v_mov_b32 v67, 0\n\t" \
    "v_mov_b32 v68, 0\n\t" \
    "v_mov_b32 v69, 0\n\t" \
    "v_mov_b32 v70, 0\n\t" \
    "v_mov_b32 v71, 0\n\t" \
    "v_mov_b32 v72, 0\n\t" \
    "v_mov_b32 v73, 0\n\t" \
    "v_mov_b32 v74, 0\n\t" \
    "v_mov_b32 v75, 0\n\t" \
    "v_mov_b32 v76, 0\n\t" \
    "v_mov_b32 v77, 0\n\t" \
    "v_mov_b32 v78, 0\n\t" \
    "v_mov_b32 v79, 0\n\t" \
    "v_mov_b32 v80, 0\n\t" \
    "v_mov_b32 v81, 0\n\t" \
    "v_mov_b32 v82, 0\n\t" \
    "v_mov_b32 v83, 0\n\t" \
    "v_mov_b32 v84, 0\n\t" \
    "v_mov_b32 v85, 0\n\t" \
    "v_mov_b32 v86, 0\n\t" \
    "v_mov_b32 v87, 0\n\t" \
    "v_mov_b32 v88, 0\n\t" \
    "v_mov_b32 v89, 0\n\t" \
    "v_mov_b32 v90, 0\n\t" \
    "v_mov_b32 v91, 0\n\t" \
    "v_mov_b32 v92, 0\n\t" \
    "v_mov_b32 v93, 0\n\t" \
    "v_mov_b32 v94, 0\n\t" \
    "v_mov_b32 v95, 0\n\t" \
    "v_mov_b32 v96, 0\n\t" \
    "v_mov_b32 v97, 0\n\t" \
    "v_mov_b32 v98, 0\n\t" \
    "v_mov_b32 v99, 0\n\t" \
    "v_mov_b32 v100, 0\n\t" \
    "v_mov_b32 v101, 0\n\t" \
    "v_mov_b32 v102, 0\n\t" \
    "v_mov_b32 v103, 0\n\t" \
    "v_mov_b32 v104, 0\n\t" \
    "v_mov_b32 v105, 0\n\t" \
    "v_mov_b32 v106, 0\n\t" \
    "v_mov_b32 v107, 0\n\t" \
    "v_mov_b32 v108, 0\n\t" \
    "v_mov_b32 v109, 0\n\t" \
    "v_mov_b32 v110, 0\n\t" \
    "v_mov_b32 v111, 0\n\t" \
    "s_waitcnt lgkmcnt(0)\n\t" \
    "s_barrier\n\t" \
    "v_mov_b32 v207, v200\n\t" \
    "v_mov_b32 v208, v202\n\t" \
    "v_mov_b32 v209, v203\n\t" \
    "ds_read_b128 v[112:115], v207 offset:0\n\t" \
    "ds_read_b64_tr_b16 v[128:129], v208 offset:0\n\t" \
    "ds_read_b64_tr_b16 v[130:131], v208 offset:1792\n\t" \
    "ds_read_b128 v[116:119], v207 offset:2048\n\t" \
    "ds_read_b128 v[120:123], v207 offset:4096\n\t" \
    "ds_read_b128 v[124:127], v207 offset:6144\n\t" \
    "ds_read_b64_tr_b16 v[132:133], v209 offset:32\n\t" \
    "ds_read_b64_tr_b16 v[134:135], v209 offset:1824\n\t" \
    "ds_read_b64_tr_b16 v[136:137], v208 offset:64\n\t" \
    "ds_read_b64_tr_b16 v[138:139], v208 offset:1856\n\t" \
    "ds_read_b64_tr_b16 v[140:141], v209 offset:96\n\t" \
    "ds_read_b64_tr_b16 v[142:143], v209 offset:1888\n\t" \
    "ds_read_b64_tr_b16 v[144:145], v208 offset:128\n\t" \
    "ds_read_b64_tr_b16 v[146:147], v208 offset:1920\n\t" \
    "ds_read_b64_tr_b16 v[148:149], v209 offset:160\n\t" \
    "ds_read_b64_tr_b16 v[150:151], v209 offset:1952\n\t" \
    "ds_read_b64_tr_b16 v[152:153], v208 offset:192\n\t" \
    "ds_read_b64_tr_b16 v[154:155], v208 offset:1984\n\t" \
    "s_mov_b32 %[aslot], 0\n\t" \
    "s_mov_b32 %[bslot], 14336\n\t" \
    "v_mov_b32 v204, v201\n\t" \
    "v_add_u32 v205, %[bslot], v202\n\t" \
    "v_add_u32 v206, %[bslot], v203\n\t" \
    "s_waitcnt lgkmcnt(0)\n\t" \
    "s_barrier\n\t" \
    "1:\n\t" \
    "v_mfma_f32_16x16x32_bf16 v[0:3], v[112:115], v[128:131], v[0:3]\n\t" \
    "ds_read_b128 v[156:159], v204 offset:0\n\t" \
    "v_mfma_f32_16x16x32_bf16 v[4:7], v[116:119], v[128:131], v[4:7]\n\t" \
    "ds_read_b64_tr_b16 v[172:173], v205 offset:0\n\t" \
    "v_mfma_f32_16x16x32_bf16 v[8:11], v[120:123], v[128:131], v[8:11]\n\t" \
    "ds_read_b64_tr_b16 v[174:175], v205 offset:1792\n\t" \
    "v_mfma_f32_16x16x32_bf16 v[12:15], v[124:127], v[128:131], v[12:15]\n\t" \
    "ds_read_b128 v[160:163], v204 offset:2048\n\t" \
    "v_mfma_f32_16x16x32_bf16 v[16:19], v[112:115], v[132:135], v[16:19]\n\t" \
    "ds_read_b128 v[164:167], v204 offset:4096\n\t" \
    "v_mfma_f32_16x16x32_bf16 v[20:23], v[116:119], v[132:135], v[20:23]\n\t" \
    "ds_read_b128 v[168:171], v204 offset:6144\n\t" \
    "v_mfma_f32_16x16x32_bf16 v[24:27], v[120:123], v[132:135], v[24:27]\n\t" \
    "ds_read_b64_tr_b16 v[176:177], v206 offset:32\n\t" \
    "v_mfma_f32_16x16x32_bf16 v[28:31], v[124:127], v[132:135], v[28:31]\n\t" \
    "ds_read_b64_tr_b16 v[178:179], v206 offset:1824\n\t" \
    "v_mfma_f32_16x16x32_bf16 v[32:35], v[112:115], v[136:139], v[32:35]\n\t" \
    "ds_read_b64_tr_b16 v[180:181], v205 offset:64\n\t" \
    "v_mfma_f32_16x16x32_bf16 v[36:39], v[116:119], v[136:139], v[36:39]\n\t" \
    "ds_read_b64_tr_b16 v[182:183], v205 offset:1856\n\t" \
    "v_mfma_f32_16x16x32_bf16 v[40:43], v[120:123], v[136:139], v[40:43]\n\t" \
    "ds_read_b64_tr_b16 v[184:185], v206 offset:96\n\t" \
    "v_mfma_f32_16x16x32_bf16 v[44:47], v[124:127], v[136:139], v[44:47]\n\t" \
    "ds_read_b64_tr_b16 v[186:187], v206 offset:1888\n\t" \
    "v_mfma_f32_16x16x32_bf16 v[48:51], v[112:115], v[140:143], v[48:51]\n\t" \
    "ds_read_b64_tr_b16 v[188:189], v205 offset:128\n\t" \
    "v_mfma_f32_16x16x32_bf16 v[52:55], v[116:119], v[140:143], v[52:55]\n\t" \
    "ds_read_b64_tr_b16 v[190:191], v205 offset:1920\n\t" \
    "v_mfma_f32_16x16x32_bf16 v[56:59], v[120:123], v[140:143], v[56:59]\n\t" \
    "ds_read_b64_tr_b16 v[192:193], v206 offset:160\n\t" \
    "v_mfma_f32_16x16x32_bf16 v[60:63], v[124:127], v[140:143], v[60:63]\n\t" \
    "ds_read_b64_tr_b16 v[194:195], v206 offset:1952\n\t" \
    "v_mfma_f32_16x16x32_bf16 v[64:67], v[112:115], v[144:147], v[64:67]\n\t" \
    "ds_read_b64_tr_b16 v[196:197], v205 offset:192\n\t" \
    "v_mfma_f32_16x16x32_bf16 v[68:71], v[116:119], v[144:147], v[68:71]\n\t" \
    "ds_read_b64_tr_b16 v[198:199], v205 offset:1984\n\t" \
    "v_mfma_f32_16x16x32_bf16 v[72:75], v[120:123], v[144:147], v[72:75]\n\t" \
    "v_mfma_f32_16x16x32_bf16 v[76:79], v[124:127], v[144:147], v[76:79]\n\t" \
    "s_add_u32 %[aslot], %[aslot], 16384\n\t" \
    "v_mfma_f32_16x16x32_bf16 v[80:83], v[112:115], v[148:151], v[80:83]\n\t" \
    "s_and_b32 %[aslot], %[aslot], 0xffff\n\t" \
    "v_mfma_f32_16x16x32_bf16 v[84:87], v[116:119], v[148:151], v[84:87]\n\t" \
    "s_add_u32 %[bslot], %[bslot], 14336\n\t" \
    "v_mfma_f32_16x16x32_bf16 v[88:91], v[120:123], v[148:151], v[88:91]\n\t" \
    "s_cmp_eq_u32 %[bslot], 86016\n\t" \
    "v_mfma_f32_16x16x32_bf16 v[92:95], v[124:127], v[148:151], v[92:95]\n\t" \
    "s_cselect_b32 %[bslot], 0, %[bslot]\n\t" \
    "v_mfma_f32_16x16x32_bf16 v[96:99], v[112:115], v[152:155], v[96:99]\n\t" \
    "v_add_u32 v207, %[aslot], v200\n\t" \
    "v_mfma_f32_16x16x32_bf16 v[100:103], v[116:119], v[152:155], v[100:103]\n\t" \
    "v_add_u32 v208, %[bslot], v202\n\t" \
    "v_mfma_f32_16x16x32_bf16 v[104:107], v[120:123], v[152:155], v[104:107]\n\t" \
    "v_add_u32 v209, %[bslot], v203\n\t" \
    "v_mfma_f32_16x16x32_bf16 v[108:111], v[124:127], v[152:155], v[108:111]\n\t" \
    "s_waitcnt lgkmcnt(0)\n\t" \
    "s_barrier\n\t" \
    "v_mfma_f32_16x16x32_bf16 v[0:3], v[156:159], v[172:175], v[0:3]\n\t" \
    "ds_read_b128 v[112:115], v207 offset:0\n\t" \
    "v_mfma_f32_16x16x32_bf16 v[4:7], v[160:163], v[172:175], v[4:7]\n\t" \
    "ds_read_b64_tr_b16 v[128:129], v208 offset:0\n\t" \
    "v_mfma_f32_16x16x32_bf16 v[8:11], v[164:167], v[172:175], v[8:11]\n\t" \
    "ds_read_b64_tr_b16 v[130:131], v208 offset:1792\n\t" \
    "v_mfma_f32_16x16x32_bf16 v[12:15], v[168:171], v[172:175], v[12:15]\n\t" \
    "ds_read_b128 v[116:119], v207 offset:2048\n\t" \
    "v_mfma_f32_16x16x32_bf16 v[16:19], v[156:159], v[176:179], v[16:19]\n\t" \
    "ds_read_b128 v[120:123], v207 offset:4096\n\t" \
    "v_mfma_f32_16x16x32_bf16 v[20:23], v[160:163], v[176:179], v[20:23]\n\t" \
    "ds_read_b128 v[124:127], v207 offset:6144\n\t" \
    "v_mfma_f32_16x16x32_bf16 v[24:27], v[164:167], v[176:179], v[24:27]\n\t" \
    "ds_read_b64_tr_b16 v[132:133], v209 offset:32\n\t" \
    "v_mfma_f32_16x16x32_bf16 v[28:31], v[168:171], v[176:179], v[28:31]\n\t" \
    "ds_read_b64_tr_b16 v[134:135], v209 offset:1824\n\t" \
    "v_mfma_f32_16x16x32_bf16 v[32:35], v[156:159], v[180:183], v[32:35]\n\t" \
    "ds_read_b64_tr_b16 v[136:137], v208 offset:64\n\t" \
    "v_mfma_f32_16x16x32_bf16 v[36:39], v[160:163], v[180:183], v[36:39]\n\t" \
    "ds_read_b64_tr_b16 v[138:139], v208 offset:1856\n\t" \
    "v_mfma_f32_16x16x32_bf16 v[40:43], v[164:167], v[180:183], v[40:43]\n\t" \
    "ds_read_b64_tr_b16 v[140:141], v209 offset:96\n\t" \
    "v_mfma_f32_16x16x32_bf16 v[44:47], v[168:171], v[180:183], v[44:47]\n\t" \
    "ds_read_b64_tr_b16 v[142:143], v209 offset:1888\n\t" \
    "v_mfma_f32_16x16x32_bf16 v[48:51], v[156:159], v[184:187], v[48:51]\n\t" \
    "ds_read_b64_tr_b16 v[144:145], v208 offset:128\n\t" \
    "v_mfma_f32_16x16x32_bf16 v[52:55], v[160:163], v[184:187], v[52:55]\n\t" \
    "ds_read_b64_tr_b16 v[146:147], v208 offset:1920\n\t" \
    "v_mfma_f32_16x16x32_bf16 v[56:59], v[164:167], v[184:187], v[56:59]\n\t" \
    "ds_read_b64_tr_b16 v[148:149], v209 offset:160\n\t" \
    "v_mfma_f32_16x16x32_bf16 v[60:63], v[168:171], v[184:187], v[60:63]\n\t" \
    "ds_read_b64_tr_b16 v[150:151], v209 offset:1952\n\t" \
    "v_mfma_f32_16x16x32_bf16 v[64:67], v[156:159], v[188:191], v[64:67]\n\t" \
    "ds_read_b64_tr_b16 v[152:153], v208 offset:192\n\t" \
    "v_mfma_f32_16x16x32_bf16 v[68:71], v[160:163], v[188:191], v[68:71]\n\t" \
    "ds_read_b64_tr_b16 v[154:155], v208 offset:1984\n\t" \
    "v_mfma_f32_16x16x32_bf16 v[72:75], v[164:167], v[188:191], v[72:75]\n\t" \
    "v_mfma_f32_16x16x32_bf16 v[76:79], v[168:171], v[188:191], v[76:79]\n\t" \
    "s_add_u32 %[bslot], %[bslot], 14336\n\t" \
    "v_mfma_f32_16x16x32_bf16 v[80:83], v[156:159], v[192:195], v[80:83]\n\t" \
    "s_cmp_eq_u32 %[bslot], 86016\n\t" \
    "v_mfma_f32_16x16x32_bf16 v[84:87], v[160:163], v[192:195], v[84:87]\n\t" \
    "s_cselect_b32 %[bslot], 0, %[bslot]\n\t" \
    "v_mfma_f32_16x16x32_bf16 v[88:91], v[164:167], v[192:195], v[88:91]\n\t" \
    "v_add_u32 v204, %[aslot], v201\n\t" \
    "v_mfma_f32_16x16x32_bf16 v[92:95], v[168:171], v[192:195], v[92:95]\n\t" \
    "v_add_u32 v205, %[bslot], v202\n\t" \
    "v_mfma_f32_16x16x32_bf16 v[96:99], v[156:159], v[196:199], v[96:99]\n\t" \
    "v_add_u32 v206, %[bslot], v203\n\t" \
    "v_mfma_f32_16x16x32_bf16 v[100:103], v[160:163], v[196:199], v[100:103]\n\t" \
    "s_sub_u32 %[cnt], %[cnt], 1\n\t" \
    "v_mfma_f32_16x16x32_bf16 v[104:107], v[164:167], v[196:199], v[104:107]\n\t" \
    "s_cmp_lg_u32 %[cnt], 0\n\t" \
    "v_mfma_f32_16x16x32_bf16 v[108:111], v[168:171], v[196:199], v[108:111]\n\t" \
    "s_waitcnt lgkmcnt(0)\n\t" \
    "s_barrier\n\t" \
    "s_cbranch_scc1 1b\n\t" \
    "v_mfma_f32_16x16x32_bf16 v[0:3], v[112:115], v[128:131], v[0:3]\n\t" \
    "ds_read_b128 v[156:159], v204 offset:0\n\t" \
    "v_mfma_f32_16x16x32_bf16 v[4:7], v[116:119], v[128:131], v[4:7]\n\t" \
    "ds_read_b64_tr_b16 v[172:173], v205 offset:0\n\t" \
    "v_mfma_f32_16x16x32_bf16 v[8:11], v[120:123], v[128:131], v[8:11]\n\t" \
    "ds_read_b64_tr_b16 v[174:175], v205 offset:1792\n\t" \
    "v_mfma_f32_16x16x32_bf16 v[12:15], v[124:127], v[128:131], v[12:15]\n\t" \
    "ds_read_b128 v[160:163], v204 offset:2048\n\t" \
    "v_mfma_f32_16x16x32_bf16 v[16:19], v[112:115], v[132:135], v[16:19]\n\t" \
    "ds_read_b128 v[164:167], v204 offset:4096\n\t" \
    "v_mfma_f32_16x16x32_bf16 v[20:23], v[116:119], v[132:135], v[20:23]\n\t" \
    "ds_read_b128 v[168:171], v204 offset:6144\n\t" \
    "v_mfma_f32_16x16x32_bf16 v[24:27], v[120:123], v[132:135], v[24:27]\n\t" \
    "ds_read_b64_tr_b16 v[176:177], v206 offset:32\n\t" \
    "v_mfma_f32_16x16x32_bf16 v[28:31], v[124:127], v[132:135], v[28:31]\n\t" \
    "ds_read_b64_tr_b16 v[178:179], v206 offset:1824\n\t" \
    "v_mfma_f32_16x16x32_bf16 v[32:35], v[112:115], v[136:139], v[32:35]\n\t" \
    "ds_read_b64_tr_b16 v[180:181], v205 offset:64\n\t" \
    "v_mfma_f32_16x16x32_bf16 v[36:39], v[116:119], v[136:139], v[36:39]\n\t" \
    "ds_read_b64_tr_b16 v[182:183], v205 offset:1856\n\t" \
    "v_mfma_f32_16x16x32_bf16 v[40:43], v[120:123], v[136:139], v[40:43]\n\t" \
    "ds_read_b64_tr_b16 v[184:185], v206 offset:96\n\t" \
    "v_mfma_f32_16x16x32_bf16 v[44:47], v[124:127], v[136:139], v[44:47]\n\t" \
    "ds_read_b64_tr_b16 v[186:187], v206 offset:1888\n\t" \
    "v_mfma_f32_16x16x32_bf16 v[48:51], v[112:115], v[140:143], v[48:51]\n\t" \
    "ds_read_b64_tr_b16 v[188:189], v205 offset:128\n\t" \
    "v_mfma_f32_16x16x32_bf16 v[52:55], v[116:119], v[140:143], v[52:55]\n\t" \
    "ds_read_b64_tr_b16 v[190:191], v205 offset:1920\n\t" \
    "v_mfma_f32_16x16x32_bf16 v[56:59], v[120:123], v[140:143], v[56:59]\n\t" \
    "ds_read_b64_tr_b16 v[192:193], v206 offset:160\n\t" \
    "v_mfma_f32_16x16x32_bf16 v[60:63], v[124:127], v[140:143], v[60:63]\n\t" \
    "ds_read_b64_tr_b16 v[194:195], v206 offset:1952\n\t" \
    "v_mfma_f32_16x16x32_bf16 v[64:67], v[112:115], v[144:147], v[64:67]\n\t" \
    "ds_read_b64_tr_b16 v[196:197], v205 offset:192\n\t" \
    "v_mfma_f32_16x16x32_bf16 v[68:71], v[116:119], v[144:147], v[68:71]\n\t" \
    "ds_read_b64_tr_b16 v[198:199], v205 offset:1984\n\t" \
    "v_mfma_f32_16x16x32_bf16 v[72:75], v[120:123], v[144:147], v[72:75]\n\t" \
    "v_mfma_f32_16x16x32_bf16 v[76:79], v[124:127], v[144:147], v[76:79]\n\t" \
    "v_mfma_f32_16x16x32_bf16 v[80:83], v[112:115], v[148:151], v[80:83]\n\t" \
    "v_mfma_f32_16x16x32_bf16 v[84:87], v[116:119], v[148:151], v[84:87]\n\t" \
    "v_mfma_f32_16x16x32_bf16 v[88:91], v[120:123], v[148:151], v[88:91]\n\t" \
    "v_mfma_f32_16x16x32_bf16 v[92:95], v[124:127], v[148:151], v[92:95]\n\t" \
    "v_mfma_f32_16x16x32_bf16 v[96:99], v[112:115], v[152:155], v[96:99]\n\t" \
    "v_mfma_f32_16x16x32_bf16 v[100:103], v[116:119], v[152:155], v[100:103]\n\t" \
    "v_mfma_f32_16x16x32_bf16 v[104:107], v[120:123], v[152:155], v[104:107]\n\t" \
    "v_mfma_f32_16x16x32_bf16 v[108:111], v[124:127], v[152:155], v[108:111]\n\t" \
    "s_waitcnt lgkmcnt(0)\n\t" \
    "s_barrier\n\t" \
    "v_mfma_f32_16x16x32_bf16 v[0:3], v[156:159], v[172:175], v[0:3]\n\t" \
    "v_mfma_f32_16x16x32_bf16 v[4:7], v[160:163], v[172:175], v[4:7]\n\t" \
    "v_mfma_f32_16x16x32_bf16 v[8:11], v[164:167], v[172:175], v[8:11]\n\t" \
    "v_mfma_f32_16x16x32_bf16 v[12:15], v[168:171], v[172:175], v[12:15]\n\t" \
    "v_mfma_f32_16x16x32_bf16 v[16:19], v[156:159], v[176:179], v[16:19]\n\t" \
    "v_mfma_f32_16x16x32_bf16 v[20:23], v[160:163], v[176:179], v[20:23]\n\t" \
    "v_mfma_f32_16x16x32_bf16 v[24:27], v[164:167], v[176:179], v[24:27]\n\t" \
    "v_mfma_f32_16x16x32_bf16 v[28:31], v[168:171], v[176:179], v[28:31]\n\t" \
    "v_mfma_f32_16x16x32_bf16 v[32:35], v[156:159], v[180:183], v[32:35]\n\t" \
    "v_mfma_f32_16x16x32_bf16 v[36:39], v[160:163], v[180:183], v[36:39]\n\t" \
    "v_mfma_f32_16x16x32_bf16 v[40:43], v[164:167], v[180:183], v[40:43]\n\t" \
    "v_mfma_f32_16x16x32_bf16 v[44:47], v[168:171], v[180:183], v[44:47]\n\t" \
    "v_mfma_f32_16x16x32_bf16 v[48:51], v[156:159], v[184:187], v[48:51]\n\t" \
    "v_mfma_f32_16x16x32_bf16 v[52:55], v[160:163], v[184:187], v[52:55]\n\t" \
    "v_mfma_f32_16x16x32_bf16 v[56:59], v[164:167], v[184:187], v[56:59]\n\t" \
    "v_mfma_f32_16x16x32_bf16 v[60:63], v[168:171], v[184:187], v[60:63]\n\t" \
    "v_mfma_f32_16x16x32_bf16 v[64:67], v[156:159], v[188:191], v[64:67]\n\t" \
    "v_mfma_f32_16x16x32_bf16 v[68:71], v[160:163], v[188:191], v[68:71]\n\t" \
    "v_mfma_f32_16x16x32_bf16 v[72:75], v[164:167], v[188:191], v[72:75]\n\t" \
    "v_mfma_f32_16x16x32_bf16 v[76:79], v[168:171], v[188:191], v[76:79]\n\t" \
    "v_mfma_f32_16x16x32_bf16 v[80:83], v[156:159], v[192:195], v[80:83]\n\t" \
    "v_mfma_f32_16x16x32_bf16 v[84:87], v[160:163], v[192:195], v[84:87]\n\t" \
    "v_mfma_f32_16x16x32_bf16 v[88:91], v[164:167], v[192:195], v[88:91]\n\t" \
    "v_mfma_f32_16x16x32_bf16 v[92:95], v[168:171], v[192:195], v[92:95]\n\t" \
    "v_mfma_f32_16x16x32_bf16 v[96:99], v[156:159], v[196:199], v[96:99]\n\t" \
    "v_mfma_f32_16x16x32_bf16 v[100:103], v[160:163], v[196:199], v[100:103]\n\t" \
    "v_mfma_f32_16x16x32_bf16 v[104:107], v[164:167], v[196:199], v[104:107]\n\t" \
    "v_mfma_f32_16x16x32_bf16 v[108:111], v[168:171], v[196:199], v[108:111]\n\t" \
    "s_waitcnt lgkmcnt(0)\n\t" \
    "s_barrier\n\t" \
    "s_nop 7\n\t" \
    "s_nop 7\n\t" \
    ""
#define G224_CLOB "v112","v113","v114","v115","v116","v117","v118","v119","v120","v121","v122","v123","v124","v125","v126","v127","v128","v129","v130","v131","v132","v133","v134","v135","v136","v137","v138","v139","v140","v141","v142","v143","v144","v145","v146","v147","v148","v149","v150","v151","v152","v153","v154","v155","v156","v157","v158","v159","v160","v161","v162","v163","v164","v165","v166","v167","v168","v169","v170","v171","v172","v173","v174","v175","v176","v177","v178","v179","v180","v181","v182","v183","v184","v185","v186","v187","v188","v189","v190","v191","v192","v193","v194","v195","v196","v197","v198","v199","v204","v205","v206","v207","v208","v209", "scc", "memory"
#define LOADER53 \
    "s_add_u32 m0, s47, s53\n\t" \
    "s_nop 0\n\t" \
    "global_load_lds_dwordx4 v5, s[42:43] offset:0\n\t" \
    "global_load_lds_dwordx4 v6, s[42:43] offset:1024\n\t" \
    "global_load_lds_dwordx4 v7, s[42:43] offset:2048\n\t" \
    "s_mov_b32 m0, s46\n\t" \
    "s_add_u32 s46, s46, 14336\n\t" \
    "global_load_lds_dwordx4 v0, s[40:41] offset:-2048\n\t" \
    "global_load_lds_dwordx4 v1, s[40:41] offset:-1024\n\t" \
    "global_load_lds_dwordx4 v2, s[40:41] offset:0\n\t" \
    "global_load_lds_dwordx4 v3, s[40:41] offset:1024\n\t" \
    "global_load_lds_dwordx4 v4, s[40:41] offset:2048\n\t" \
    "s_cmp_eq_u32 s46, s52\n\t" \
    "s_cselect_b32 s46, s51, s46\n\t" \
    "s_add_u32 s40, s40, s49\n\t" \
    "s_addc_u32 s41, s41, 0\n\t" \
    "s_sub_u32 s54, s54, 1\n\t" \
    "s_cmp_eq_u32 s54, 0\n\t" \
    "s_cselect_b32 s50, s56, 0\n\t" \
    "s_sub_u32 s40, s40, s50\n\t" \
    "s_subb_u32 s41, s41, 0\n\t" \
    "s_add_u32 m0, s58, s53\n\t" \
    "s_nop 0\n\t" \
    "global_load_lds_dwordx4 v5, s[44:45] offset:0\n\t" \
    "global_load_lds_dwordx4 v6, s[44:45] offset:1024\n\t" \
    "global_load_lds_dwordx4 v7, s[44:45] offset:2048\n\t" \
    "s_add_u32 s53, s53, 16384\n\t" \
    "s_and_b32 s53, s53, 0xffff\n\t" \
    "s_add_u32 s42, s42, 128\n\t" \
    "s_addc_u32 s43, s43, 0\n\t" \
    "s_add_u32 s44, s44, 128\n\t" \
    "s_addc_u32 s45, s45, 0\n\t" \
    "s_sub_u32 s55, s55, 1\n\t" \
    "s_cmp_eq_u32 s55, 0\n\t" \
    "s_cselect_b32 s50, s57, 0\n\t" \
    "s_sub_u32 s42, s42, s50\n\t" \
    "s_subb_u32 s43, s43, 0\n\t" \
    "s_sub_u32 s44, s44, s50\n\t" \
    "s_subb_u32 s45, s45, 0\n\t" \
    "s_mov_b32 m0, s46\n\t" \
    "s_add_u32 s46, s46, 14336\n\t" \
    "global_load_lds_dwordx4 v0, s[40:41] offset:-2048\n\t" \
    "global_load_lds_dwordx4 v1, s[40:41] offset:-1024\n\t" \
    "global_load_lds_dwordx4 v2, s[40:41] offset:0\n\t" \
    "global_load_lds_dwordx4 v3, s[40:41] offset:1024\n\t" \
    "global_load_lds_dwordx4 v4, s[40:41] offset:2048\n\t" \
    "s_cmp_eq_u32 s46, s52\n\t" \
    "s_cselect_b32 s46, s51, s46\n\t" \
    "s_add_u32 s40, s40, s49\n\t" \
    "s_addc_u32 s41, s41, 0\n\t" \
    "s_sub_u32 s54, s54, 1\n\t" \
    "s_cmp_eq_u32 s54, 0\n\t" \
    "s_cselect_b32 s50, s56, 0\n\t" \
    "s_sub_u32 s40, s40, s50\n\t" \
    "s_subb_u32 s41, s41, 0\n\t" \
    "s_add_u32 m0, s47, s53\n\t" \
    "s_nop 0\n\t" \
    "global_load_lds_dwordx4 v5, s[42:43] offset:0\n\t" \
    "global_load_lds_dwordx4 v6, s[42:43] offset:1024\n\t" \
    "global_load_lds_dwordx4 v7, s[42:43] offset:2048\n\t" \
    "s_mov_b32 m0, s46\n\t" \
    "s_add_u32 s46, s46, 14336\n\t" \
    "global_load_lds_dwordx4 v0, s[40:41] offset:-2048\n\t" \
    "global_load_lds_dwordx4 v1, s[40:41] offset:-1024\n\t" \
    "global_load_lds_dwordx4 v2, s[40:41] offset:0\n\t" \
    "global_load_lds_dwordx4 v3, s[40:41] offset:1024\n\t" \
    "global_load_lds_dwordx4 v4, s[40:41] offset:2048\n\t" \
    "s_cmp_eq_u32 s46, s52\n\t" \
    "s_cselect_b32 s46, s51, s46\n\t" \
    "s_add_u32 s40, s40, s49\n\t" \
    "s_addc_u32 s41, s41, 0\n\t" \
    "s_sub_u32 s54, s54, 1\n\t" \
    "s_cmp_eq_u32 s54, 0\n\t" \
    "s_cselect_b32 s50, s56, 0\n\t" \
    "s_sub_u32 s40, s40, s50\n\t" \
    "s_subb_u32 s41, s41, 0\n\t" \
    "s_add_u32 m0, s58, s53\n\t" \
    "s_nop 0\n\t" \
    "global_load_lds_dwordx4 v5, s[44:45] offset:0\n\t" \
    "global_load_lds_dwordx4 v6, s[44:45] offset:1024\n\t" \
    "global_load_lds_dwordx4 v7, s[44:45] offset:2048\n\t" \
    "s_add_u32 s53, s53, 16384\n\t" \
    "s_and_b32 s53, s53, 0xffff\n\t" \
    "s_add_u32 s42, s42, 128\n\t" \
    "s_addc_u32 s43, s43, 0\n\t" \
    "s_add_u32 s44, s44, 128\n\t" \
    "s_addc_u32 s45, s45, 0\n\t" \
    "s_sub_u32 s55, s55, 1\n\t" \
    "s_cmp_eq_u32 s55, 0\n\t" \
    "s_cselect_b32 s50, s57, 0\n\t" \
    "s_sub_u32 s42, s42, s50\n\t" \
    "s_subb_u32 s43, s43, 0\n\t" \
    "s_sub_u32 s44, s44, s50\n\t" \
    "s_subb_u32 s45, s45, 0\n\t" \
    "s_mov_b32 m0, s46\n\t" \
    "s_add_u32 s46, s46, 14336\n\t" \
    "global_load_lds_dwordx4 v0, s[40:41] offset:-2048\n\t" \
    "global_load_lds_dwordx4 v1, s[40:41] offset:-1024\n\t" \
    "global_load_lds_dwordx4 v2, s[40:41] offset:0\n\t" \
    "global_load_lds_dwordx4 v3, s[40:41] offset:1024\n\t" \
    "global_load_lds_dwordx4 v4, s[40:41] offset:2048\n\t" \
    "s_cmp_eq_u32 s46, s52\n\t" \
    "s_cselect_b32 s46, s51, s46\n\t" \
    "s_add_u32 s40, s40, s49\n\t" \
    "s_addc_u32 s41, s41, 0\n\t" \
    "s_sub_u32 s54, s54, 1\n\t" \
    "s_cmp_eq_u32 s54, 0\n\t" \
    "s_cselect_b32 s50, s56, 0\n\t" \
    "s_sub_u32 s40, s40, s50\n\t" \
    "s_subb_u32 s41, s41, 0\n\t" \
    "s_add_u32 m0, s47, s53\n\t" \
    "s_nop 0\n\t" \
    "global_load_lds_dwordx4 v5, s[42:43] offset:0\n\t" \
    "global_load_lds_dwordx4 v6, s[42:43] offset:1024\n\t" \
    "global_load_lds_dwordx4 v7, s[42:43] offset:2048\n\t" \
    "s_mov_b32 m0, s46\n\t" \
    "s_add_u32 s46, s46, 14336\n\t" \
    "global_load_lds_dwordx4 v0, s[40:41] offset:-2048\n\t" \
    "global_load_lds_dwordx4 v1, s[40:41] offset:-1024\n\t" \
    "global_load_lds_dwordx4 v2, s[40:41] offset:0\n\t" \
    "global_load_lds_dwordx4 v3, s[40:41] offset:1024\n\t" \
    "global_load_lds_dwordx4 v4, s[40:41] offset:2048\n\t" \
    "s_cmp_eq_u32 s46, s52\n\t" \
    "s_cselect_b32 s46, s51, s46\n\t" \
    "s_add_u32 s40, s40, s49\n\t" \
    "s_addc_u32 s41, s41, 0\n\t" \
    "s_sub_u32 s54, s54, 1\n\t" \
    "s_cmp_eq_u32 s54, 0\n\t" \
    "s_cselect_b32 s50, s56, 0\n\t" \
    "s_sub_u32 s40, s40, s50\n\t" \
    "s_subb_u32 s41, s41, 0\n\t" \
    "s_add_u32 m0, s58, s53\n\t" \
    "s_nop 0\n\t" \
    "global_load_lds_dwordx4 v5, s[44:45] offset:0\n\t" \
    "global_load_lds_dwordx4 v6, s[44:45] offset:1024\n\t" \
    "global_load_lds_dwordx4 v7, s[44:45] offset:2048\n\t" \
    "s_add_u32 s53, s53, 16384\n\t" \
    "s_and_b32 s53, s53, 0xffff\n\t" \
    "s_add_u32 s42, s42, 128\n\t" \
    "s_addc_u32 s43, s43, 0\n\t" \
    "s_add_u32 s44, s44, 128\n\t" \
    "s_addc_u32 s45, s45, 0\n\t" \
    "s_sub_u32 s55, s55, 1\n\t" \
    "s_cmp_eq_u32 s55, 0\n\t" \
    "s_cselect_b32 s50, s57, 0\n\t" \
    "s_sub_u32 s42, s42, s50\n\t" \
    "s_subb_u32 s43, s43, 0\n\t" \
    "s_sub_u32 s44, s44, s50\n\t" \
    "s_subb_u32 s45, s45, 0\n\t" \
    "s_mov_b32 m0, s46\n\t" \
    "s_add_u32 s46, s46, 14336\n\t" \
    "global_load_lds_dwordx4 v0, s[40:41] offset:-2048\n\t" \
    "global_load_lds_dwordx4 v1, s[40:41] offset:-1024\n\t" \
    "global_load_lds_dwordx4 v2, s[40:41] offset:0\n\t" \
    "global_load_lds_dwordx4 v3, s[40:41] offset:1024\n\t" \
    "global_load_lds_dwordx4 v4, s[40:41] offset:2048\n\t" \
    "s_cmp_eq_u32 s46, s52\n\t" \
    "s_cselect_b32 s46, s51, s46\n\t" \
    "s_add_u32 s40, s40, s49\n\t" \
    "s_addc_u32 s41, s41, 0\n\t" \
    "s_sub_u32 s54, s54, 1\n\t" \
    "s_cmp_eq_u32 s54, 0\n\t" \
    "s_cselect_b32 s50, s56, 0\n\t" \
    "s_sub_u32 s40, s40, s50\n\t" \
    "s_subb_u32 s41, s41, 0\n\t" \
    "s_add_u32 m0, s47, s53\n\t" \
    "s_nop 0\n\t" \
    "global_load_lds_dwordx4 v5, s[42:43] offset:0\n\t" \
    "global_load_lds_dwordx4 v6, s[42:43] offset:1024\n\t" \
    "global_load_lds_dwordx4 v7, s[42:43] offset:2048\n\t" \
    "s_waitcnt vmcnt(40)\n\t" \
    "s_barrier\n\t" \
    "s_waitcnt vmcnt(32)\n\t" \
    "s_barrier\n\t" \
    "s_mov_b32 m0, s46\n\t" \
    "s_add_u32 s46, s46, 14336\n\t" \
    "global_load_lds_dwordx4 v0, s[40:41] offset:-2048\n\t" \
    "global_load_lds_dwordx4 v1, s[40:41] offset:-1024\n\t" \
    "global_load_lds_dwordx4 v2, s[40:41] offset:0\n\t" \
    "global_load_lds_dwordx4 v3, s[40:41] offset:1024\n\t" \
    "global_load_lds_dwordx4 v4, s[40:41] offset:2048\n\t" \
    "s_cmp_eq_u32 s46, s52\n\t" \
    "s_cselect_b32 s46, s51, s46\n\t" \
    "s_add_u32 s40, s40, s49\n\t" \
    "s_addc_u32 s41, s41, 0\n\t" \
    "s_sub_u32 s54, s54, 1\n\t" \
    "s_cmp_eq_u32 s54, 0\n\t" \
    "s_cselect_b32 s50, s56, 0\n\t" \
    "s_sub_u32 s40, s40, s50\n\t" \
    "s_subb_u32 s41, s41, 0\n\t" \
    "s_add_u32 m0, s58, s53\n\t" \
    "s_nop 0\n\t" \
    "global_load_lds_dwordx4 v5, s[44:45] offset:0\n\t" \
    "global_load_lds_dwordx4 v6, s[44:45] offset:1024\n\t" \
    "global_load_lds_dwordx4 v7, s[44:45] offset:2048\n\t" \
    "s_add_u32 s53, s53, 16384\n\t" \
    "s_and_b32 s53, s53, 0xffff\n\t" \
    "s_add_u32 s42, s42, 128\n\t" \
    "s_addc_u32 s43, s43, 0\n\t" \
    "s_add_u32 s44, s44, 128\n\t" \
    "s_addc_u32 s45, s45, 0\n\t" \
    "s_sub_u32 s55, s55, 1\n\t" \
    "s_cmp_eq_u32 s55, 0\n\t" \
    "s_cselect_b32 s50, s57, 0\n\t" \
    "s_sub_u32 s42, s42, s50\n\t" \
    "s_subb_u32 s43, s43, 0\n\t" \
    "s_sub_u32 s44, s44, s50\n\t" \
    "s_subb_u32 s45, s45, 0\n\t" \
    "s_waitcnt vmcnt(32)\n\t" \
    "s_barrier\n\t" \
    "1:\n\t" \
    "s_mov_b32 m0, s46\n\t" \
    "s_add_u32 s46, s46, 14336\n\t" \
    "global_load_lds_dwordx4 v0, s[40:41] offset:-2048\n\t" \
    "global_load_lds_dwordx4 v1, s[40:41] offset:-1024\n\t" \
    "global_load_lds_dwordx4 v2, s[40:41] offset:0\n\t" \
    "global_load_lds_dwordx4 v3, s[40:41] offset:1024\n\t" \
    "global_load_lds_dwordx4 v4, s[40:41] offset:2048\n\t" \
    "s_cmp_eq_u32 s46, s52\n\t" \
    "s_cselect_b32 s46, s51, s46\n\t" \
    "s_add_u32 s40, s40, s49\n\t" \
    "s_addc_u32 s41, s41, 0\n\t" \
    "s_sub_u32 s54, s54, 1\n\t" \
    "s_cmp_eq_u32 s54, 0\n\t" \
    "s_cselect_b32 s50, s56, 0\n\t" \
    "s_sub_u32 s40, s40, s50\n\t" \
    "s_subb_u32 s41, s41, 0\n\t" \
    "s_add_u32 m0, s47, s53\n\t" \
    "s_nop 0\n\t" \
    "global_load_lds_dwordx4 v5, s[42:43] offset:0\n\t" \
    "global_load_lds_dwordx4 v6, s[42:43] offset:1024\n\t" \
    "global_load_lds_dwordx4 v7, s[42:43] offset:2048\n\t" \
    "s_waitcnt vmcnt(32)\n\t" \
    "s_barrier\n\t" \
    "s_mov_b32 m0, s46\n\t" \
    "s_add_u32 s46, s46, 14336\n\t" \
    "global_load_lds_dwordx4 v0, s[40:41] offset:-2048\n\t" \
    "global_load_lds_dwordx4 v1, s[40:41] offset:-1024\n\t" \
    "global_load_lds_dwordx4 v2, s[40:41] offset:0\n\t" \
    "global_load_lds_dwordx4 v3, s[40:41] offset:1024\n\t" \
    "global_load_lds_dwordx4 v4, s[40:41] offset:2048\n\t" \
    "s_cmp_eq_u32 s46, s52\n\t" \
    "s_cselect_b32 s46, s51, s46\n\t" \
    "s_add_u32 s40, s40, s49\n\t" \
    "s_addc_u32 s41, s41, 0\n\t" \
    "s_sub_u32 s54, s54, 1\n\t" \
    "s_cmp_eq_u32 s54, 0\n\t" \
    "s_cselect_b32 s50, s56, 0\n\t" \
    "s_sub_u32 s40, s40, s50\n\t" \
    "s_subb_u32 s41, s41, 0\n\t" \
    "s_add_u32 m0, s58, s53\n\t" \
    "s_nop 0\n\t" \
    "global_load_lds_dwordx4 v5, s[44:45] offset:0\n\t" \
    "global_load_lds_dwordx4 v6, s[44:45] offset:1024\n\t" \
    "global_load_lds_dwordx4 v7, s[44:45] offset:2048\n\t" \
    "s_add_u32 s53, s53, 16384\n\t" \
    "s_and_b32 s53, s53, 0xffff\n\t" \
    "s_add_u32 s42, s42, 128\n\t" \
    "s_addc_u32 s43, s43, 0\n\t" \
    "s_add_u32 s44, s44, 128\n\t" \
    "s_addc_u32 s45, s45, 0\n\t" \
    "s_sub_u32 s55, s55, 1\n\t" \
    "s_cmp_eq_u32 s55, 0\n\t" \
    "s_cselect_b32 s50, s57, 0\n\t" \
    "s_sub_u32 s42, s42, s50\n\t" \
    "s_subb_u32 s43, s43, 0\n\t" \
    "s_sub_u32 s44, s44, s50\n\t" \
    "s_subb_u32 s45, s45, 0\n\t" \
    "s_sub_u32 s48, s48, 1\n\t" \
    "s_cmp_lg_u32 s48, 0\n\t" \
    "s_waitcnt vmcnt(32)\n\t" \
    "s_barrier\n\t" \
    "s_cbranch_scc1 1b\n\t" \
    "s_mov_b32 m0, s46\n\t" \
    "s_add_u32 s46, s46, 14336\n\t" \
    "global_load_lds_dwordx4 v0, s[40:41] offset:-2048\n\t" \
    "global_load_lds_dwordx4 v1, s[40:41] offset:-1024\n\t" \
    "global_load_lds_dwordx4 v2, s[40:41] offset:0\n\t" \
    "global_load_lds_dwordx4 v3, s[40:41] offset:1024\n\t" \
    "global_load_lds_dwordx4 v4, s[40:41] offset:2048\n\t" \
    "s_cmp_eq_u32 s46, s52\n\t" \
    "s_cselect_b32 s46, s51, s46\n\t" \
    "s_add_u32 s40, s40, s49\n\t" \
    "s_addc_u32 s41, s41, 0\n\t" \
    "s_sub_u32 s54, s54, 1\n\t" \
    "s_cmp_eq_u32 s54, 0\n\t" \
    "s_cselect_b32 s50, s56, 0\n\t" \
    "s_sub_u32 s40, s40, s50\n\t" \
    "s_subb_u32 s41, s41, 0\n\t" \
    "s_waitcnt vmcnt(29)\n\t" \
    "s_barrier\n\t" \
    "s_waitcnt vmcnt(21)\n\t" \
    "s_barrier\n\t" \
    "s_waitcnt vmcnt(13)\n\t" \
    "s_barrier\n\t" \
    "s_waitcnt vmcnt(5)\n\t" \
    "s_barrier\n\t" \
    "s_waitcnt vmcnt(0)\n\t" \
    "s_barrier\n\t" \
    "s_barrier\n\t" \
    "s_barrier\n\t" \
    ""
#define LOADER42 \
    "s_add_u32 m0, s47, s53\n\t" \
    "s_nop 0\n\t" \
    "global_load_lds_dwordx4 v5, s[42:43] offset:0\n\t" \
    "global_load_lds_dwordx4 v6, s[42:43] offset:1024\n\t" \
    "s_mov_b32 m0, s46\n\t" \
    "s_add_u32 s46, s46, 14336\n\t" \
    "global_load_lds_dwordx4 v0, s[40:41] offset:-2048\n\t" \
    "global_load_lds_dwordx4 v1, s[40:41] offset:-1024\n\t" \
    "global_load_lds_dwordx4 v2, s[40:41] offset:0\n\t" \
    "global_load_lds_dwordx4 v3, s[40:41] offset:1024\n\t" \
    "s_cmp_eq_u32 s46, s52\n\t" \
    "s_cselect_b32 s46, s51, s46\n\t" \
    "s_add_u32 s40, s40, s49\n\t" \
    "s_addc_u32 s41, s41, 0\n\t" \
    "s_sub_u32 s54, s54, 1\n\t" \
    "s_cmp_eq_u32 s54, 0\n\t" \
    "s_cselect_b32 s50, s56, 0\n\t" \
    "s_sub_u32 s40, s40, s50\n\t" \
    "s_subb_u32 s41, s41, 0\n\t" \
    "s_add_u32 m0, s58, s53\n\t" \
    "s_nop 0\n\t" \
    "global_load_lds_dwordx4 v5, s[44:45] offset:0\n\t" \
    "global_load_lds_dwordx4 v6, s[44:45] offset:1024\n\t" \
    "s_add_u32 s53, s53, 16384\n\t" \
    "s_and_b32 s53, s53, 0xffff\n\t" \
    "s_add_u32 s42, s42, 128\n\t" \
    "s_addc_u32 s43, s43, 0\n\t" \
    "s_add_u32 s44, s44, 128\n\t" \
    "s_addc_u32 s45, s45, 0\n\t" \
    "s_sub_u32 s55, s55, 1\n\t" \
    "s_cmp_eq_u32 s55, 0\n\t" \
    "s_cselect_b32 s50, s57, 0\n\t" \
    "s_sub_u32 s42, s42, s50\n\t" \
    "s_subb_u32 s43, s43, 0\n\t" \
    "s_sub_u32 s44, s44, s50\n\t" \
    "s_subb_u32 s45, s45, 0\n\t" \
    "s_mov_b32 m0, s46\n\t" \
    "s_add_u32 s46, s46, 14336\n\t" \
    "global_load_lds_dwordx4 v0, s[40:41] offset:-2048\n\t" \
    "global_load_lds_dwordx4 v1, s[40:41] offset:-1024\n\t" \
    "global_load_lds_dwordx4 v2, s[40:41] offset:0\n\t" \
    "global_load_lds_dwordx4 v3, s[40:41] offset:1024\n\t" \
    "s_cmp_eq_u32 s46, s52\n\t" \
    "s_cselect_b32 s46, s51, s46\n\t" \
    "s_add_u32 s40, s40, s49\n\t" \
    "s_addc_u32 s41, s41, 0\n\t" \
    "s_sub_u32 s54, s54, 1\n\t" \
    "s_cmp_eq_u32 s54, 0\n\t" \
    "s_cselect_b32 s50, s56, 0\n\t" \
    "s_sub_u32 s40, s40, s50\n\t" \
    "s_subb_u32 s41, s41, 0\n\t" \
    "s_add_u32 m0, s47, s53\n\t" \
    "s_nop 0\n\t" \
    "global_load_lds_dwordx4 v5, s[42:43] offset:0\n\t" \
    "global_load_lds_dwordx4 v6, s[42:43] offset:1024\n\t" \
    "s_mov_b32 m0, s46\n\t" \
    "s_add_u32 s46, s46, 14336\n\t" \
    "global_load_lds_dwordx4 v0, s[40:41] offset:-2048\n\t" \
    "global_load_lds_dwordx4 v1, s[40:41] offset:-1024\n\t" \
    "global_load_lds_dwordx4 v2, s[40:41] offset:0\n\t" \
    "global_load_lds_dwordx4 v3, s[40:41] offset:1024\n\t" \
    "s_cmp_eq_u32 s46, s52\n\t" \
    "s_cselect_b32 s46, s51, s46\n\t" \
    "s_add_u32 s40, s40, s49\n\t" \
    "s_addc_u32 s41, s41, 0\n\t" \
    "s_sub_u32 s54, s54, 1\n\t" \
    "s_cmp_eq_u32 s54, 0\n\t" \
    "s_cselect_b32 s50, s56, 0\n\t" \
    "s_sub_u32 s40, s40, s50\n\t" \
    "s_subb_u32 s41, s41, 0\n\t" \
    "s_add_u32 m0, s58, s53\n\t" \
    "s_nop 0\n\t" \
    "global_load_lds_dwordx4 v5, s[44:45] offset:0\n\t" \
    "global_load_lds_dwordx4 v6, s[44:45] offset:1024\n\t" \
    "s_add_u32 s53, s53, 16384\n\t" \
    "s_and_b32 s53, s53, 0xffff\n\t" \
    "s_add_u32 s42, s42, 128\n\t" \
    "s_addc_u32 s43, s43, 0\n\t" \
    "s_add_u32 s44, s44, 128\n\t" \
    "s_addc_u32 s45, s45, 0\n\t" \
    "s_sub_u32 s55, s55, 1\n\t" \
    "s_cmp_eq_u32 s55, 0\n\t" \
    "s_cselect_b32 s50, s57, 0\n\t" \
    "s_sub_u32 s42, s42, s50\n\t" \
    "s_subb_u32 s43, s43, 0\n\t" \
    "s_sub_u32 s44, s44, s50\n\t" \
    "s_subb_u32 s45, s45, 0\n\t" \
    "s_mov_b32 m0, s46\n\t" \
    "s_add_u32 s46, s46, 14336\n\t" \
    "global_load_lds_dwordx4 v0, s[40:41] offset:-2048\n\t" \
    "global_load_lds_dwordx4 v1, s[40:41] offset:-1024\n\t" \
    "global_load_lds_dwordx4 v2, s[40:41] offset:0\n\t" \
    "global_load_lds_dwordx4 v3, s[40:41] offset:1024\n\t" \
    "s_cmp_eq_u32 s46, s52\n\t" \
    "s_cselect_b32 s46, s51, s46\n\t" \
    "s_add_u32 s40, s40, s49\n\t" \
    "s_addc_u32 s41, s41, 0\n\t" \
    "s_sub_u32 s54, s54, 1\n\t" \
    "s_cmp_eq_u32 s54, 0\n\t" \
    "s_cselect_b32 s50, s56, 0\n\t" \
    "s_sub_u32 s40, s40, s50\n\t" \
    "s_subb_u32 s41, s41, 0\n\t" \
    "s_add_u32 m0, s47, s53\n\t" \
    "s_nop 0\n\t" \
    "global_load_lds_dwordx4 v5, s[42:43] offset:0\n\t" \
    "global_load_lds_dwordx4 v6, s[42:43] offset:1024\n\t" \
    "s_mov_b32 m0, s46\n\t" \
    "s_add_u32 s46, s46, 14336\n\t" \
    "global_load_lds_dwordx4 v0, s[40:41] offset:-2048\n\t" \
    "global_load_lds_dwordx4 v1, s[40:41] offset:-1024\n\t" \
    "global_load_lds_dwordx4 v2, s[40:41] offset:0\n\t" \
    "global_load_lds_dwordx4 v3, s[40:41] offset:1024\n\t" \
    "s_cmp_eq_u32 s46, s52\n\t" \
    "s_cselect_b32 s46, s51, s46\n\t" \
    "s_add_u32 s40, s40, s49\n\t" \
    "s_addc_u32 s41, s41, 0\n\t" \
    "s_sub_u32 s54, s54, 1\n\t" \
    "s_cmp_eq_u32 s54, 0\n\t" \
    "s_cselect_b32 s50, s56, 0\n\t" \
    "s_sub_u32 s40, s40, s50\n\t" \
    "s_subb_u32 s41, s41, 0\n\t" \
    "s_add_u32 m0, s58, s53\n\t" \
    "s_nop 0\n\t" \
    "global_load_lds_dwordx4 v5, s[44:45] offset:0\n\t" \
    "global_load_lds_dwordx4 v6, s[44:45] offset:1024\n\t" \
    "s_add_u32 s53, s53, 16384\n\t" \
    "s_and_b32 s53, s53, 0xffff\n\t" \
    "s_add_u32 s42, s42, 128\n\t" \
    "s_addc_u32 s43, s43, 0\n\t" \
    "s_add_u32 s44, s44, 128\n\t" \
    "s_addc_u32 s45, s45, 0\n\t" \
    "s_sub_u32 s55, s55, 1\n\t" \
    "s_cmp_eq_u32 s55, 0\n\t" \
    "s_cselect_b32 s50, s57, 0\n\t" \
    "s_sub_u32 s42, s42, s50\n\t" \
    "s_subb_u32 s43, s43, 0\n\t" \
    "s_sub_u32 s44, s44, s50\n\t" \
    "s_subb_u32 s45, s45, 0\n\t" \
    "s_mov_b32 m0, s46\n\t" \
    "s_add_u32 s46, s46, 14336\n\t" \
    "global_load_lds_dwordx4 v0, s[40:41] offset:-2048\n\t" \
    "global_load_lds_dwordx4 v1, s[40:41] offset:-1024\n\t" \
    "global_load_lds_dwordx4 v2, s[40:41] offset:0\n\t" \
    "global_load_lds_dwordx4 v3, s[40:41] offset:1024\n\t" \
    "s_cmp_eq_u32 s46, s52\n\t" \
    "s_cselect_b32 s46, s51, s46\n\t" \
    "s_add_u32 s40, s40, s49\n\t" \
    "s_addc_u32 s41, s41, 0\n\t" \
    "s_sub_u32 s54, s54, 1\n\t" \
    "s_cmp_eq_u32 s54, 0\n\t" \
    "s_cselect_b32 s50, s56, 0\n\t" \
    "s_sub_u32 s40, s40, s50\n\t" \
    "s_subb_u32 s41, s41, 0\n\t" \
    "s_add_u32 m0, s47, s53\n\t" \
    "s_nop 0\n\t" \
    "global_load_lds_dwordx4 v5, s[42:43] offset:0\n\t" \
    "global_load_lds_dwordx4 v6, s[42:43] offset:1024\n\t" \
    "s_waitcnt vmcnt(30)\n\t" \
    "s_barrier\n\t" \
    "s_waitcnt vmcnt(24)\n\t" \
    "s_barrier\n\t" \
    "s_mov_b32 m0, s46\n\t" \
    "s_add_u32 s46, s46, 14336\n\t" \
    "global_load_lds_dwordx4 v0, s[40:41] offset:-2048\n\t" \
    "global_load_lds_dwordx4 v1, s[40:41] offset:-1024\n\t" \
    "global_load_lds_dwordx4 v2, s[40:41] offset:0\n\t" \
    "global_load_lds_dwordx4 v3, s[40:41] offset:1024\n\t" \
    "s_cmp_eq_u32 s46, s52\n\t" \
    "s_cselect_b32 s46, s51, s46\n\t" \
    "s_add_u32 s40, s40, s49\n\t" \
    "s_addc_u32 s41, s41, 0\n\t" \
    "s_sub_u32 s54, s54, 1\n\t" \
    "s_cmp_eq_u32 s54, 0\n\t" \
    "s_cselect_b32 s50, s56, 0\n\t" \
    "s_sub_u32 s40, s40, s50\n\t" \
    "s_subb_u32 s41, s41, 0\n\t" \
    "s_add_u32 m0, s58, s53\n\t" \
    "s_nop 0\n\t" \
    "global_load_lds_dwordx4 v5, s[44:45] offset:0\n\t" \
    "global_load_lds_dwordx4 v6, s[44:45] offset:1024\n\t" \
    "s_add_u32 s53, s53, 16384\n\t" \
    "s_and_b32 s53, s53, 0xffff\n\t" \
    "s_add_u32 s42, s42, 128\n\t" \
    "s_addc_u32 s43, s43, 0\n\t" \
    "s_add_u32 s44, s44, 128\n\t" \
    "s_addc_u32 s45, s45, 0\n\t" \
    "s_sub_u32 s55, s55, 1\n\t" \
    "s_cmp_eq_u32 s55, 0\n\t" \
    "s_cselect_b32 s50, s57, 0\n\t" \
    "s_sub_u32 s42, s42, s50\n\t" \
    "s_subb_u32 s43, s43, 0\n\t" \
    "s_sub_u32 s44, s44, s50\n\t" \
    "s_subb_u32 s45, s45, 0\n\t" \
    "s_waitcnt vmcnt(24)\n\t" \
    "s_barrier\n\t" \
    "1:\n\t" \
    "s_mov_b32 m0, s46\n\t" \
    "s_add_u32 s46, s46, 14336\n\t" \
    "global_load_lds_dwordx4 v0, s[40:41] offset:-2048\n\t" \
    "global_load_lds_dwordx4 v1, s[40:41] offset:-1024\n\t" \
    "global_load_lds_dwordx4 v2, s[40:41] offset:0\n\t" \
    "global_load_lds_dwordx4 v3, s[40:41] offset:1024\n\t" \
    "s_cmp_eq_u32 s46, s52\n\t" \
    "s_cselect_b32 s46, s51, s46\n\t" \
    "s_add_u32 s40, s40, s49\n\t" \
    "s_addc_u32 s41, s41, 0\n\t" \
    "s_sub_u32 s54, s54, 1\n\t" \
    "s_cmp_eq_u32 s54, 0\n\t" \
    "s_cselect_b32 s50, s56, 0\n\t" \
    "s_sub_u32 s40, s40, s50\n\t" \
    "s_subb_u32 s41, s41, 0\n\t" \
    "s_add_u32 m0, s47, s53\n\t" \
    "s_nop 0\n\t" \
    "global_load_lds_dwordx4 v5, s[42:43] offset:0\n\t" \
    "global_load_lds_dwordx4 v6, s[42:43] offset:1024\n\t" \
    "s_waitcnt vmcnt(24)\n\t" \
    "s_barrier\n\t" \
    "s_mov_b32 m0, s46\n\t" \
    "s_add_u32 s46, s46, 14336\n\t" \
    "global_load_lds_dwordx4 v0, s[40:41] offset:-2048\n\t" \
    "global_load_lds_dwordx4 v1, s[40:41] offset:-1024\n\t" \
    "global_load_lds_dwordx4 v2, s[40:41] offset:0\n\t" \
    "global_load_lds_dwordx4 v3, s[40:41] offset:1024\n\t" \
    "s_cmp_eq_u32 s46, s52\n\t" \
    "s_cselect_b32 s46, s51, s46\n\t" \
    "s_add_u32 s40, s40, s49\n\t" \
    "s_addc_u32 s41, s41, 0\n\t" \
    "s_sub_u32 s54, s54, 1\n\t" \
    "s_cmp_eq_u32 s54, 0\n\t" \
    "s_cselect_b32 s50, s56, 0\n\t" \
    "s_sub_u32 s40, s40, s50\n\t" \
    "s_subb_u32 s41, s41, 0\n\t" \
    "s_add_u32 m0, s58, s53\n\t" \
    "s_nop 0\n\t" \
    "global_load_lds_dwordx4 v5, s[44:45] offset:0\n\t" \
    "global_load_lds_dwordx4 v6, s[44:45] offset:1024\n\t" \
    "s_add_u32 s53, s53, 16384\n\t" \
    "s_and_b32 s53, s53, 0xffff\n\t" \
    "s_add_u32 s42, s42, 128\n\t" \
    "s_addc_u32 s43, s43, 0\n\t" \
    "s_add_u32 s44, s44, 128\n\t" \
    "s_addc_u32 s45, s45, 0\n\t" \
    "s_sub_u32 s55, s55, 1\n\t" \
    "s_cmp_eq_u32 s55, 0\n\t" \
    "s_cselect_b32 s50, s57, 0\n\t" \
    "s_sub_u32 s42, s42, s50\n\t" \
    "s_subb_u32 s43, s43, 0\n\t" \
    "s_sub_u32 s44, s44, s50\n\t" \
    "s_subb_u32 s45, s45, 0\n\t" \
    "s_sub_u32 s48, s48, 1\n\t" \
    "s_cmp_lg_u32 s48, 0\n\t" \
    "s_waitcnt vmcnt(24)\n\t" \
    "s_barrier\n\t" \
    "s_cbranch_scc1 1b\n\t" \
    "s_mov_b32 m0, s46\n\t" \
    "s_add_u32 s46, s46, 14336\n\t" \
    "global_load_lds_dwordx4 v0, s[40:41] offset:-2048\n\t" \
    "global_load_lds_dwordx4 v1, s[40:41] offset:-1024\n\t" \
    "global_load_lds_dwordx4 v2, s[40:41] offset:0\n\t" \
    "global_load_lds_dwordx4 v3, s[40:41] offset:1024\n\t" \
    "s_cmp_eq_u32 s46, s52\n\t" \
    "s_cselect_b32 s46, s51, s46\n\t" \
    "s_add_u32 s40, s40, s49\n\t" \
    "s_addc_u32 s41, s41, 0\n\t" \
    "s_sub_u32 s54, s54, 1\n\t" \
    "s_cmp_eq_u32 s54, 0\n\t" \
    "s_cselect_b32 s50, s56, 0\n\t" \
    "s_sub_u32 s40, s40, s50\n\t" \
    "s_subb_u32 s41, s41, 0\n\t" \
    "s_waitcnt vmcnt(22)\n\t" \
    "s_barrier\n\t" \
    "s_waitcnt vmcnt(16)\n\t" \
    "s_barrier\n\t" \
    "s_waitcnt vmcnt(10)\n\t" \
    "s_barrier\n\t" \
    "s_waitcnt vmcnt(4)\n\t" \
    "s_barrier\n\t" \
    "s_waitcnt vmcnt(0)\n\t" \
    "s_barrier\n\t" \
    "s_barrier\n\t" \
    "s_barrier\n\t" \
    ""
#define LOADER_LOOPS(nstep) (((nstep) - 8) / 2)
#define LOADER_CLOB "s50", "scc", "memory"

#define STAMP(id) do { } while (0)
#define STAMPC(id) do { } while (0)
#define STARG
#define STPASS
namespace {
constexpr int A_SLOT = 16384, B_SLOT = 14336, LDS_A = 0, LDS_B = 65536;
constexpr int LDS_BYTES = 163840;
constexpr unsigned WS_EPOCH = 0, WS_ARR = 1024, WS_F1 = 4096, WS_FO = 6144, WS_FP = 8192;
constexpr unsigned WS_Q = 16384, WS_K = WS_Q + 2097152, WS_V = WS_K + 2097152, WS_O = WS_V + 2097152, WS_PQ = WS_O + 2097152;
constexpr unsigned WS_END = WS_PQ + 7340032;
constexpr int AUX_SC1 = 16;
constexpr unsigned SPIN_MAX = 1u << 22;

DI unsigned cvtpk(float lo, float hi) { f32x2 v = {lo, hi}; bf16x2 b = __builtin_convertvector(v, bf16x2); return __builtin_bit_cast(unsigned, b); }
DI float rbf(float x) { return __uint_as_float(cvtpk(0.0f, x)); }
DI void glds16(const void* sbase, unsigned voff, unsigned lds_base) {
    asm volatile("s_mov_b32 m0, %2\n\ts_nop 0\n\tglobal_load_lds_dwordx4 %1, %0" :: "s"(sbase), "v"(voff), "s"(lds_base) : "memory"); }
DI void wait_vm0() { asm volatile("s_waitcnt vmcnt(0)" ::: "memory"); }
DI void bar() { asm volatile("s_waitcnt lgkmcnt(0)\n\ts_barrier" ::: "memory"); }
DI s16x4 trread(LAS const char* p) { return __builtin_bit_cast(s16x4, __builtin_amdgcn_ds_read_tr16_b64_v4i16((LAS s16x4*)p)); }
DI v4u ld_sc1(__amdgpu_buffer_rsrc_t rs, unsigned off) { return __builtin_amdgcn_raw_buffer_load_b128(rs, off, 0, AUX_SC1); }
DI void st_sc1(__amdgpu_buffer_rsrc_t rs, unsigned off, v4u v) { __builtin_amdgcn_raw_buffer_store_b128(v, rs, off, 0, AUX_SC1); }
DI void flag_store(char* ws, unsigned off, u64 tag) { __hip_atomic_store((GAS u64*)(ws + off), tag, __ATOMIC_RELAXED, __HIP_MEMORY_SCOPE_AGENT); }
DI void poll(char* ws, unsigned off, bool active, u64 tag) {
    for (unsigned spins = 0; spins < SPIN_MAX; ++spins) {
        u64 v = tag;
        if (active) v = __hip_atomic_load((GAS u64*)(ws + off), __ATOMIC_RELAXED, __HIP_MEMORY_SCOPE_AGENT);
        if (__all(v == tag)) break;
        __builtin_amdgcn_s_sleep(1);
    }
    asm volatile("" ::: "memory");
}

template <int NB, int NA>
DI void g224_loader(unsigned lds0, int w, int lane, const char* a0, const char* a1, unsigned lda, const char* bbase, unsigned ldb, int nstep, int rot) {
    const int b0 = 5 * w, i0 = 3 * w;
    unsigned vo[5], va[3];
#pragma unroll
    for (int i = 0; i < 5; ++i) {
        const unsigned o = (b0 + i) * 1024 + lane * 16;
        const unsigned row = o / 448, pc = (o - row * 448) >> 4;
        vo[i] = (i < NB) ? row * ldb + ((pc ^ (((row >> 3) & 1) << 1)) * 16) + 2048 - i * 1024 : 0u;
    }
    const int rl = lane >> 3, ch = lane & 7;
#pragma unroll
    for (int i = 0; i < 3; ++i) {
        const int ii = i0 + i;
        va[i] = (i < NA) ? (ii * 8 + rl) * lda + ((ch ^ ((((ii & 1) * 8 + rl) >> 1) & 7)) * 16) + 2048 - i * 1024 : 0u;
    }
    const unsigned bdst = lds0 + LDS_B + b0 * 1024 + 2048, adst = lds0 + LDS_A + i0 * 1024;
    const int rb = 2 * rot, ra = rot;
    const char* pb = bbase + (size_t)rb * 32 * ldb; const char* pa0 = a0 + ra * 128 - 2048; const char* pa1 = a1 + ra * 128 - 2048;
    unsigned s46 = bdst, s47 = adst, s48 = LOADER_LOOPS(nstep), s49 = 32 * ldb, s51 = bdst, s52 = bdst + 6 * B_SLOT, s53 = 0, s54 = nstep - rb, s55 = nstep / 2 - ra,
             s56 = nstep * 32 * ldb, s57 = (nstep / 2) * 128, s58 = adst + 8192;
    if (NB == 5) asm volatile(LOADER53 : "+{s[40:41]}"(pb), "+{s[42:43]}"(pa0), "+{s[44:45]}"(pa1), "+{s46}"(s46), "+{s48}"(s48), "+{s53}"(s53), "+{s54}"(s54), "+{s55}"(s55)
         : "{v0}"(vo[0]), "{v1}"(vo[1]), "{v2}"(vo[2]), "{v3}"(vo[3]), "{v4}"(vo[4]), "{v5}"(va[0]), "{v6}"(va[1]), "{v7}"(va[2]),
           "{s47}"(s47), "{s49}"(s49), "{s51}"(s51), "{s52}"(s52), "{s56}"(s56), "{s57}"(s57), "{s58}"(s58) : LOADER_CLOB);
    else asm volatile(LOADER42 : "+{s[40:41]}"(pb), "+{s[42:43]}"(pa0), "+{s[44:45]}"(pa1), "+{s46}"(s46), "+{s48}"(s48), "+{s53}"(s53), "+{s54}"(s54), "+{s55}"(s55)
         : "{v0}"(vo[0]), "{v1}"(vo[1]), "{v2}"(vo[2]), "{v3}"(vo[3]), "{v4}"(vo[4]), "{v5}"(va[0]), "{v6}"(va[1]), "{v7}"(va[2]),
           "{s47}"(s47), "{s49}"(s49), "{s51}"(s51), "{s52}"(s52), "{s56}"(s56), "{s57}"(s57), "{s58}"(s58) : LOADER_CLOB);
}
DI void touch(const char* p, unsigned lds_dummy) {
    asm volatile("s_mov_b32 m0, %1\n\ts_nop 0\n\tglobal_load_lds_dword %0, off" :: "v"(p), "s"(lds_dummy) : "memory"); }
constexpr int LDS_DUMMY = LDS_B + 6 * B_SLOT;
constexpr int LDS_CNT = LDS_BYTES - 64;
constexpr int PF_DIST = 12;
#define WARM_SLEEP 4
template <class PF, class EX, class WM>
DI void g224(unsigned lds0, int wid, int lane, const char* a0, const char* a1, unsigned lda, const char* bbase, unsigned ldb, int nstep, int rot, bool swap, f32x16 (&c)[7], PF pf, EX extra, WM warm) {
    if (wid < 4 || wid == 7) { __builtin_amdgcn_s_sleep(WARM_SLEEP); warm(wid < 4 ? wid : 4); }
    if (wid == 7) {
        for (int st = 6; st < PF_DIST && st < nstep; ++st) pf(st);
        asm volatile("s_barrier\n\ts_barrier" ::: "memory");
        for (int s = 0; s < nstep; ++s) {
            if (s + PF_DIST < nstep) pf(s + PF_DIST);
            extra(s);
            asm volatile("s_barrier" ::: "memory");
        }
    }
    else if (wid == 6) g224_loader<4, 2>(lds0, 2, lane, a0, a1, lda, bbase, ldb, nstep, rot);
    else if (wid >= 4) g224_loader<5, 3>(lds0, wid - 4, lane, a0, a1, lda, bbase, ldb, nstep, rot);
    else {
        const int mh = wid & 1, nh = wid >> 1;
        const int r16 = lane & 15, g = lane >> 4, q = (lane & 15) >> 2, p = lane & 3;
        const unsigned aoff0 = lds0 + LDS_A + (mh * 64 + r16) * 128 + ((g ^ ((r16 >> 1) & 7)) * 16);
        const unsigned brow = lds0 + LDS_B + (8 * g + q) * 448 + p * 8 + nh * 224;
        const unsigned gb = (g & 1) * 32;
        const unsigned bE = nh ? brow - gb : brow + gb, bO = nh ? brow + gb : brow - gb;
        unsigned aslot, bslot, cnt = nstep / 2 - 1;
        if (swap) asm volatile(G224_BODY_SWAP
            : "={v[0:15]}"(c[0]), "={v[16:31]}"(c[1]), "={v[32:47]}"(c[2]), "={v[48:63]}"(c[3]), "={v[64:79]}"(c[4]), "={v[80:95]}"(c[5]), "={v[96:111]}"(c[6]),
              [aslot] "=&s"(aslot), [bslot] "=&s"(bslot), [cnt] "+s"(cnt)
            : "{v200}"(aoff0), "{v201}"(aoff0 ^ 64), "{v202}"(bE), "{v203}"(bO) : G224_CLOB);
        else asm volatile(G224_BODY
            : "={v[0:15]}"(c[0]), "={v[16:31]}"(c[1]), "={v[32:47]}"(c[2]), "={v[48:63]}"(c[3]), "={v[64:79]}"(c[4]), "={v[80:95]}"(c[5]), "={v[96:111]}"(c[6]),
              [aslot] "=&s"(aslot), [bslot] "=&s"(bslot), [cnt] "+s"(cnt)
            : "{v200}"(aoff0), "{v201}"(aoff0 ^ 64), "{v202}"(bE), "{v203}"(bO) : G224_CLOB);
    }
}

DI void final_store(LAS char* L, int wid, int lane, f32x4 (&acc)[7], float sign, char* outp  ) {
    bar();
    const int m = wid & 3;
    if (wid >= 4) {
#pragma unroll
        for (int n = 0; n < 7; ++n) *(LAS f32x4*)(L + (m * 7 + n) * 1024 + lane * 16) = acc[n];
    }
    bar();
    if (wid < 4) {
        LAS char* stg = L + 32768 + m * 4096;
        const int t16 = lane & 15, g = lane >> 4;
        f32x4 o[7];
#pragma unroll
        for (int n = 0; n < 7; ++n) o[n] = *(const LAS f32x4*)(L + (m * 7 + n) * 1024 + lane * 16);
#pragma unroll
        for (int n = 0; n < 7; ++n) {
            v2u w;
            w[0] = cvtpk(acc[n][0] + sign * o[n][0], acc[n][1] + sign * o[n][1]);
            w[1] = cvtpk(acc[n][2] + sign * o[n][2], acc[n][3] + sign * o[n][3]);
            *(LAS v2u*)(stg + t16 * 240 + n * 32 + g * 8) = w;
        }
        asm volatile("s_waitcnt lgkmcnt(0)" ::: "memory");
        v4u v[4];
#pragma unroll
        for (int k = 0; k < 4; ++k) {
            const int idx = min(k * 64 + lane, 223), row = idx / 14, chn = idx - row * 14;
            v[k] = *(const LAS v4u*)(stg + row * 240 + chn * 16);
        }
#pragma unroll
        for (int k = 0; k < 4; ++k) {
            const int idx = k * 64 + lane, row = idx / 14, chn = idx - row * 14;
            if (idx < 224) *(GAS v4u*)(outp + (size_t)(m * 16 + row) * 1792 + chn * 16) = v[k];
        }
    }
}

DI bf16x8 add_frag(v4u a, v4u b) {
    v4u r;
#pragma unroll
    for (int j = 0; j < 4; ++j) {
        const float lo = __uint_as_float(a[j] << 16) + __uint_as_float(b[j] << 16);
        const float hi = __uint_as_float(a[j] & 0xffff0000u) + __uint_as_float(b[j] & 0xffff0000u);
        r[j] = cvtpk(lo, hi);
    }
    return __builtin_bit_cast(bf16x8, r);
}

DI v4u pack_pair(const f32x16& ca, const f32x16& cb, int m) {
    unsigned a0 = cvtpk(ca[4 * m], ca[4 * m + 1]), a1 = cvtpk(ca[4 * m + 2], ca[4 * m + 3]);
    unsigned b0 = cvtpk(cb[4 * m], cb[4 * m + 1]), b1 = cvtpk(cb[4 * m + 2], cb[4 * m + 3]);
    auto s0 = __builtin_amdgcn_permlane32_swap(a0, b0, false, false);
    auto s1 = __builtin_amdgcn_permlane32_swap(a1, b1, false, false);
    auto t0 = __builtin_amdgcn_permlane16_swap(s0[0], s0[1], false, false);
    auto t1 = __builtin_amdgcn_permlane16_swap(s1[0], s1[1], false, false);
    return (v4u){t0[0], t1[0], t0[1], t1[1]};
}
DI v4u pack_single(const f32x16& ca, int m) {
    unsigned a0 = cvtpk(ca[4 * m], ca[4 * m + 1]), a1 = cvtpk(ca[4 * m + 2], ca[4 * m + 3]);
    auto t0 = __builtin_amdgcn_permlane16_swap(a0, a0, false, false);
    auto t1 = __builtin_amdgcn_permlane16_swap(a1, a1, false, false);
    return (v4u){t0[0], t1[0], t0[1], t1[1]};
}

DI void warm_inputs(int idx  , int x, int lane, unsigned dummy, const char* Ax, const char* Wqkv, const char* Wo, const char* Bx,
                    const char* Cre, const char* Cim, const char* Wre, const char* Wim) {
}

DI void role_b(char* lds, int x, int i, int wid, int lane, const char* Ax, const char* Wqkv, const char* Wo, const char* Bx, const char* Cre, const char* Cim, const char* Wre, const char* Wim, char* out, char* ws, u64 tag STARG) {
    const unsigned lds0 = (unsigned)(uintptr_t)lds;
    LAS char* L = (LAS char*)lds;
    const int tb = x * 4 + (i >> 2), cb = i & 3, ch = cb & 1, kh = cb >> 1;
    const __amdgpu_buffer_rsrc_t rs = __builtin_amdgcn_make_buffer_rsrc(ws, 0, WS_END, 0x00020000);
    f32x16 c[7];
    STAMP(0); STAMPC(0);
    {
        const unsigned dummy = lds0 + LDS_DUMMY;
        const char* pfa = (ch ? Cim : Cre) + ((size_t)(tb * 64 + (lane & 31)) * 2048 + kh * 1024) * 2;
        const char* pfb = Bx + ((size_t)kh * 1024 + (i >> 2) * 8 + ((lane & 31) >> 2)) * 896 + (3 * ch + (lane & 3)) * 128;
        const char* pfw = ((i & 1) ? Wim : Wre) + (size_t)((i >> 1) * 392 + lane) * 128;
        auto pf = [&](int st) {
            (void)st; (void)pfa; (void)pfb;
        };
        auto extra = [&](int s) {
            if (s == 0 && lane == 0) flag_store(ws, WS_ARR + blockIdx.x * 8, tag);
            (void)pfw;
        };
        g224(lds0, wid, lane, Cre + ((size_t)tb * 64 * 2048 + kh * 1024) * 2, Cim + ((size_t)tb * 64 * 2048 + kh * 1024) * 2, 4096,
             Bx + ((size_t)kh * 1024 * 448 + ch * 224) * 2, 896, 32, 2 * x, false, c, pf, extra,
             [&](int k) { warm_inputs(i * 5 + k, x, lane, dummy, Ax, Wqkv, Wo, Bx, Cre, Cim, Wre, Wim); });
    }
    STAMP(1); STAMPC(1);
    const unsigned wvo = (lane >> 2) * 896 + (((lane & 3) ^ ((((lane >> 5) ^ (lane >> 4)) & 1) * 2 + (lane >> 5))) * 16);
    auto wchunk = [&](int u, int pq, int slot) {
        const char* src = (pq ? Wim : Wre) + (size_t)cb * 112 * 896 + u * 64;
#pragma unroll
        for (int k = 0; k < 7; ++k) glds16(src + k * 16 * 896, wvo, __builtin_amdgcn_readfirstlane(lds0 + slot * 7168 + k * 1024));
    };
    if (wid < 4) {
        const int mh = wid & 1, nh = wid >> 1, g = lane >> 4;
        const unsigned base = WS_PQ + ((kh * 2 + mh) * 128 + tb * 4) * 14336 + (ch * 7) * 1024 + lane * 16;
#pragma unroll
        for (int m = 0; m < 4; ++m) {
            const unsigned bm = base + m * 14336;
            if (nh == 0) {
                st_sc1(rs, bm, pack_pair(c[0], c[1], m)); st_sc1(rs, bm + 1024, pack_pair(c[2], c[3], m)); st_sc1(rs, bm + 2048, pack_pair(c[4], c[5], m));
                const v4u s = pack_single(c[6], m);
                if ((g & 1) == 0) st_sc1(rs, bm + 3072 - lane * 16 + ((g >> 1) * 16 + (lane & 15)) * 16, s);
            } else {
                const v4u s = pack_single(c[0], m);
                if ((g & 1) == 0) st_sc1(rs, bm + 3072 - lane * 16 + ((2 + (g >> 1)) * 16 + (lane & 15)) * 16, s);
                st_sc1(rs, bm + 4096, pack_pair(c[1], c[2], m)); st_sc1(rs, bm + 5120, pack_pair(c[3], c[4], m)); st_sc1(rs, bm + 6144, pack_pair(c[5], c[6], m));
            }
        }
    }
    STAMP(9);
    asm volatile("s_barrier" ::: "memory");
    if (wid < 4) {
        wait_vm0();
        STAMP(2);
        unsigned old = 0;
        if (lane == 0) old = __hip_atomic_fetch_add((LAS unsigned*)(L + LDS_CNT), 1u, __ATOMIC_RELAXED, __HIP_MEMORY_SCOPE_WORKGROUP);
        if (__builtin_amdgcn_readfirstlane(old) == 3) {
            if (lane == 0) flag_store(ws, WS_FP + (tb * 4 + cb) * 8, tag);
            STAMP(3);
            poll(ws, WS_FP + (tb * 4 + (lane & 3)) * 8, lane < 4, tag);
        }
    } else {
        for (int s = wid - 4; s < 22; s += 4) wchunk(s >> 1, s & 1, s);
        STAMP(2);
        wait_vm0();
    }
    STAMP(4);
    bar();
    STAMP(5);
    const int m = wid & 3, pq = wid >> 2;
    const unsigned pb = WS_PQ + (pq * 128 + tb * 4 + m) * 14336 + lane * 16;
    v4u p0[14], p1[14];
#pragma unroll
    for (int u = 0; u < 14; ++u) { p0[u] = ld_sc1(rs, pb + u * 1024); p1[u] = ld_sc1(rs, pb + 2 * 128 * 14336 + u * 1024); }
    asm volatile("" ::: "memory"); __builtin_amdgcn_sched_barrier(0);
    const int e16 = lane & 15, g = lane >> 4, rq = e16 >> 2;
    const unsigned wro = e16 * 64 + ((g ^ ((((rq >> 1) ^ rq) & 1) * 2 + (rq >> 1))) * 16) + pq * 7168;
    f32x4 acc[7];
#pragma unroll
    for (int n = 0; n < 7; ++n) acc[n] = (f32x4){0.f, 0.f, 0.f, 0.f};
    bf16x8 w[2][7];
#pragma unroll
    for (int n = 0; n < 7; ++n) w[0][n] = *(const LAS bf16x8*)(L + wro + n * 1024);
#pragma unroll
    for (int u = 0; u < 14; ++u) {
        if (u == 1) STAMP(10);
        if (u == 3) STAMP(11);
        if (u == 10) STAMP(12);
        if (u == 3) {
            bar();
            if (wid >= 4) { for (int s = wid - 4; s < 6; s += 4) wchunk(11 + (s >> 1), s & 1, s); }
        }
        if (u == 10) { if (wid >= 4) wait_vm0(); bar(); }
        if (u + 1 < 14) {
            const int sl = (u + 1 < 11 ? u + 1 : u + 1 - 11) * 2;
#pragma unroll
            for (int n = 0; n < 7; ++n) w[(u + 1) & 1][n] = *(const LAS bf16x8*)(L + wro + sl * 7168 + n * 1024);
        }
        const bf16x8 f = add_frag(p0[u], p1[u]);
        __builtin_amdgcn_sched_barrier(0);
#pragma unroll
        for (int n = 0; n < 7; ++n) acc[n] = __builtin_amdgcn_mfma_f32_16x16x32_bf16(w[u & 1][n], f, acc[n], 0, 0, 0);
        __builtin_amdgcn_sched_barrier(0);
    }
    STAMP(6);
    final_store(L, wid, lane, acc, -1.0f, out + ((size_t)tb * 64 * 896 + 448 + cb * 112) * 2);
    STAMP(7);
}

DI void attention(int iq, int head, int lane, __amdgpu_buffer_rsrc_t rs) {
    const float SC = 0.18898223650461362f, L2E = 1.4426950408889634f;
    const int a = lane & 31, hi = lane >> 5;
    const unsigned hb = head * 64 * 2048 + lane * 16;
    const bf16x8 q0 = __builtin_bit_cast(bf16x8, ld_sc1(rs, WS_Q + hb + iq * 2048)), q1 = __builtin_bit_cast(bf16x8, ld_sc1(rs, WS_Q + hb + iq * 2048 + 1024));
    v4u kf[9][2];
#pragma unroll
    for (int j = 0; j < 9; ++j) { const int kt = max(iq - 8 + j, 0); kf[j][0] = ld_sc1(rs, WS_K + hb + kt * 2048); kf[j][1] = ld_sc1(rs, WS_K + hb + kt * 2048 + 1024); }
    v4u vf[9][2];
#pragma unroll
    for (int j = 0; j < 9; ++j) { const int kt = max(iq - 8 + j, 0); vf[j][0] = ld_sc1(rs, WS_V + hb + kt * 2048); vf[j][1] = ld_sc1(rs, WS_V + hb + kt * 2048 + 1024); }
    asm volatile("" ::: "memory"); __builtin_amdgcn_sched_barrier(0);
    const f32x16 zero = {};
    f32x16 S[8];
    {
        f32x16 so = __builtin_amdgcn_mfma_f32_32x32x16_bf16(__builtin_bit_cast(bf16x8, kf[0][0]), q0, zero, 0, 0, 0);
        so = __builtin_amdgcn_mfma_f32_32x32x16_bf16(__builtin_bit_cast(bf16x8, kf[0][1]), q1, so, 0, 0, 0);
        f32x16 sd = __builtin_amdgcn_mfma_f32_32x32x16_bf16(__builtin_bit_cast(bf16x8, kf[8][0]), q0, zero, 0, 0, 0);
        sd = __builtin_amdgcn_mfma_f32_32x32x16_bf16(__builtin_bit_cast(bf16x8, kf[8][1]), q1, sd, 0, 0, 0);
        if (iq < 8) {
            asm volatile("" ::: "memory");
#pragma unroll
            for (int r = 0; r < 16; ++r) so[r] = -INFINITY;
        }
#pragma unroll
        for (int r = 0; r < 16; ++r) { const int b = (r & 3) + 8 * (r >> 2) + 4 * hi; S[0][r] = (b <= a) ? sd[r] : so[r]; }
    }
#pragma unroll
    for (int j = 1; j < 8; ++j) {
        f32x16 s = __builtin_amdgcn_mfma_f32_32x32x16_bf16(__builtin_bit_cast(bf16x8, kf[j][0]), q0, zero, 0, 0, 0);
        s = __builtin_amdgcn_mfma_f32_32x32x16_bf16(__builtin_bit_cast(bf16x8, kf[j][1]), q1, s, 0, 0, 0);
        S[j] = s;
    }
    if (iq < 7) {
        asm volatile("" ::: "memory");
#pragma unroll
        for (int j = 1; j < 8; ++j) if (iq - 8 + j < 0) {
#pragma unroll
            for (int r = 0; r < 16; ++r) S[j][r] = -INFINITY;
        }
    }
    float mx = S[0][0];
#pragma unroll
    for (int j = 0; j < 8; ++j)
#pragma unroll
        for (int r = 0; r < 16; ++r) mx = __builtin_fmaxf(mx, S[j][r]);
    { auto rr = __builtin_amdgcn_permlane32_swap(__float_as_uint(mx), __float_as_uint(mx), false, false);
      mx = __builtin_fmaxf(__uint_as_float(rr[0]), __uint_as_float(rr[1])); }
    const float nml = -(rbf(rbf(mx) * SC) * L2E);
    f32x16 O = zero;
#pragma unroll
    for (int j = 0; j < 8; ++j) {
        v4u pw[2];
#pragma unroll
        for (int k = 0; k < 8; ++k) {
            const float e0 = __builtin_amdgcn_exp2f(__builtin_fmaf(rbf(rbf(S[j][2 * k]) * SC), L2E, nml));
            const float e1 = __builtin_amdgcn_exp2f(__builtin_fmaf(rbf(rbf(S[j][2 * k + 1]) * SC), L2E, nml));
            pw[k >> 2][k & 3] = cvtpk(e0, e1);
        }
        if (j == 0) {
            v4u pd[2], po[2];
#pragma unroll
            for (int k = 0; k < 8; ++k) {
                const int b = ((2 * k) & 3) + 8 * ((2 * k) >> 2) + 4 * hi;
                const unsigned msk = (b <= a ? 0xffffu : 0u) | (b + 1 <= a ? 0xffff0000u : 0u);
                pd[k >> 2][k & 3] = pw[k >> 2][k & 3] & msk; po[k >> 2][k & 3] = pw[k >> 2][k & 3] & ~msk;
            }
            O = __builtin_amdgcn_mfma_f32_32x32x16_bf16(__builtin_bit_cast(bf16x8, vf[8][0]), __builtin_bit_cast(bf16x8, pd[0]), O, 0, 0, 0);
            O = __builtin_amdgcn_mfma_f32_32x32x16_bf16(__builtin_bit_cast(bf16x8, vf[8][1]), __builtin_bit_cast(bf16x8, pd[1]), O, 0, 0, 0);
            O = __builtin_amdgcn_mfma_f32_32x32x16_bf16(__builtin_bit_cast(bf16x8, vf[0][0]), __builtin_bit_cast(bf16x8, po[0]), O, 0, 0, 0);
            O = __builtin_amdgcn_mfma_f32_32x32x16_bf16(__builtin_bit_cast(bf16x8, vf[0][1]), __builtin_bit_cast(bf16x8, po[1]), O, 0, 0, 0);
        } else {
            O = __builtin_amdgcn_mfma_f32_32x32x16_bf16(__builtin_bit_cast(bf16x8, vf[j][0]), __builtin_bit_cast(bf16x8, pw[0]), O, 0, 0, 0);
            O = __builtin_amdgcn_mfma_f32_32x32x16_bf16(__builtin_bit_cast(bf16x8, vf[j][1]), __builtin_bit_cast(bf16x8, pw[1]), O, 0, 0, 0);
        }
    }
    auto lr = __builtin_amdgcn_permlane32_swap(__float_as_uint(O[12]), __float_as_uint(O[12]), false, false);
    const float rl = 1.0f / __uint_as_float(lr[1]);
    v4u o0, o1;
#pragma unroll
    for (int k = 0; k < 4; ++k) { o0[k] = cvtpk(O[2 * k] * rl, O[2 * k + 1] * rl); o1[k] = cvtpk(O[8 + 2 * k] * rl, O[8 + 2 * k + 1] * rl); }
    if (hi) { o1[2] = 0; o1[3] = 0; }
    const unsigned ob = WS_O + (head * 128 + iq * 2 + (a >> 4)) * 1024 + (hi * 16 + (a & 15)) * 16;
    st_sc1(rs, ob, o0); st_sc1(rs, ob + 512, o1);
}

DI void role_a(char* lds, int x, int ai, int wid, int lane, const char* Ax, const char* Wqkv, const char* Wo, const char* Bx, const char* Cre, const char* Cim, const char* Wre, const char* Wim, char* out, char* ws, u64 tag STARG) {
    const unsigned lds0 = (unsigned)(uintptr_t)lds;
    LAS char* L = (LAS char*)lds;
    const __amdgpu_buffer_rsrc_t rs = __builtin_amdgcn_make_buffer_rsrc(ws, 0, WS_END, 0x00020000);
    const int iq = 8 * x + (ai >> 1), hh = ai & 1, cb = (iq & 1) * 2 + hh, rp = iq >> 1;
    if (ai < 12) {
        const int rb = 2 * x + ai / 6, j = ai % 6;
        const bool isv = j >= 4;
        f32x16 c[7];
        STAMP(0); STAMPC(0);
        {
            const unsigned dummy = lds0 + LDS_DUMMY;
            const char* pfa = Ax + (size_t)(rb * 128 + (j & 1) * 32 + (lane & 31)) * 896;
            const char* pfb = Wqkv + (size_t)((ai / 6) * 16 + (lane >> 2)) * 2688 + (((j * 448) >> 7) + (lane & 3)) * 128;
            auto pf = [&](int st) {
                (void)st; (void)pfa; (void)pfb;
            };
            auto extra = [&](int s) { if (s == 0 && lane == 0) flag_store(ws, WS_ARR + blockIdx.x * 8, tag); };
            g224(lds0, wid, lane, Ax + (size_t)rb * 128 * 896, Ax + ((size_t)rb * 128 + 64) * 896, 896, Wqkv + j * 448, 2688, 14, 0, isv, c, pf, extra,
                 [&](int k) { warm_inputs(80 + ai * 5 + k, x, lane, dummy, Ax, Wqkv, Wo, Bx, Cre, Cim, Wre, Wim); });
        }
        STAMP(1); STAMPC(1);
        if (wid < 4) {
            const int mh = wid & 1, nh = wid >> 1, l16 = lane & 15, g = lane >> 4;
#pragma unroll
            for (int n = 0; n < 7; ++n) {
                unsigned off;
                if (!isv) { const int u4 = nh * 28 + 4 * n + g, h8 = u4 / 7, du = u4 - 7 * h8;
                    off = h8 * 8192 + mh * 4096 + (du >> 2) * 1024 + (((du >> 1) & 1) * 32 + l16) * 16 + (du & 1) * 8; }
                else { const int col = nh * 112 + 16 * n + l16, h8 = col / 28, d = col - 28 * h8;
                    off = h8 * 8192 + mh * 4096 + ((g & 1) * 32 + d) * 16 + (g >> 1) * 8; }
#pragma unroll
                for (int m = 0; m < 4; ++m) {
                    v2u w; w[0] = cvtpk(c[n][4 * m], c[n][4 * m + 1]); w[1] = cvtpk(c[n][4 * m + 2], c[n][4 * m + 3]);
                    const unsigned mo = isv ? (m >> 1) * 2048 + (m & 1) * 1024 : (m >> 1) * 2048 + (m & 1) * 256;
                    *(LAS v2u*)(L + off + mo) = w;
                }
            }
        } else {
            const int t = (wid - 4) * 64 + lane;
            if (!isv) {
#pragma unroll
                for (int k = 0; k < 4; ++k) { const int e = t + 256 * k, h8 = e >> 7, tok = e & 127;
                    *(LAS v2u*)(L + h8 * 8192 + (tok >> 5) * 2048 + 1024 + (32 + (tok & 31)) * 16 + 8) = (v2u){0u, 0u}; }
            } else {
#pragma unroll
                for (int k = 0; k < 2; ++k) { const int e = t + 256 * k, dd = e & 3; const unsigned val = dd == 0 ? 0x3f803f80u : 0u;
                    *(LAS v4u*)(L + (e >> 6) * 8192 + ((e >> 4) & 3) * 2048 + ((e >> 3) & 1) * 1024 + (((e >> 2) & 1) * 32 + 28 + dd) * 16) = (v4u){val, val, val, val}; }
            }
        }
        bar();
        const unsigned gb = (j < 2 ? WS_Q : j < 4 ? WS_K : WS_V) + ((j & 1) * 8 * 64 + rb * 4) * 2048 + lane * 16;
#pragma unroll
        for (int k = 0; k < 8; ++k) {
            const v4u v = *(const LAS v4u*)(L + (wid * 8 + k) * 1024 + lane * 16);
            st_sc1(rs, gb + wid * 64 * 2048 + k * 1024, v);
        }
        wait_vm0();
        bar();
        if (wid == 0 && lane == 0) flag_store(ws, WS_F1 + (rb * 8 + j) * 8, tag);
        STAMP(2);
    }
    if (ai >= 12) {
        warm_inputs(140 + (ai - 12) * 8 + wid, x, lane, lds0 + LDS_DUMMY, Ax, Wqkv, Wo, Bx, Cre, Cim, Wre, Wim);
        if (wid == 7 && lane == 0) flag_store(ws, WS_ARR + blockIdx.x * 8, tag);
    }
    {
        const unsigned wb = (cb * 224) & ~63u;
        const int rr = lane >> 4;
#pragma unroll
        for (int k = 0; k < 14; ++k) {
            const int r4 = wid * 14 + k;
            const int row = 4 * r4 + rr;
            const unsigned vo = rr * 896 + (((lane & 15) ^ ((row & 7) << 1)) * 16);
            glds16(Wo + (size_t)r4 * 4 * 896 + wb, vo, __builtin_amdgcn_readfirstlane(lds0 + r4 * 1024));
        }
    }
    {
        const int rq = iq >> 2;
        const int sel = lane == 0 ? 0 : lane < 4 ? 1 : 2;
        const int rb = sel == 0 ? rq : rq - 2 + ((lane - 1) % 3);
        poll(ws, WS_F1 + (max(rb, 0) * 8 + sel * 2 + hh) * 8, lane < 7 && rb >= 0, tag);
        STAMP(3);
        attention(iq, hh * 8 + wid, lane, rs);
        STAMP(4);
        wait_vm0();
        bar();
        STAMP(5);
        if (wid == 0) {
            if (lane == 0) flag_store(ws, WS_FO + (iq * 2 + hh) * 8, tag);
            poll(ws, WS_FO + (rp * 4 + (lane & 3)) * 8, lane < 4, tag);
        }
        bar();
        STAMP(6);
    }
    {
        const int m = wid & 3, k2 = wid >> 2;
        v4u of[8];
#pragma unroll
        for (int h = 0; h < 8; ++h) of[h] = ld_sc1(rs, WS_O + ((k2 * 8 + h) * 128 + rp * 4 + m) * 1024 + lane * 16);
        asm volatile("" ::: "memory"); __builtin_amdgcn_sched_barrier(0);
        const int g = lane >> 4, q = (lane & 15) >> 2, p = lane & 3;
        const int s0 = ((cb * 224) & 63) >> 5;
        unsigned ad[2][2][7];
#pragma unroll
        for (int hp = 0; hp < 2; ++hp)
#pragma unroll
            for (int xx = 0; xx < 2; ++xx) {
                int unit = 4 * (g >> 1) + 2 * xx + (g & 1); if (unit == 7) unit = 6;
                const int rl = 4 * unit + q, sw = (4 * hp + rl) & 7;
#pragma unroll
                for (int n = 0; n < 7; ++n) ad[hp][xx][n] = (k2 * 8 * 28 + hp * 28 + rl) * 256 + (((s0 + n) ^ sw) * 32) + p * 8;
            }
        f32x4 acc[7];
#pragma unroll
        for (int n = 0; n < 7; ++n) acc[n] = (f32x4){0.f, 0.f, 0.f, 0.f};
#pragma unroll
        for (int h = 0; h < 8; ++h) {
#pragma unroll
            for (int n = 0; n < 7; ++n) {
                const s16x4 lo = trread(L + ad[h & 1][0][n] + (h >> 1) * 2 * 7168), hi2 = trread(L + ad[h & 1][1][n] + (h >> 1) * 2 * 7168);
                const bf16x8 w = (bf16x8){lo[0], lo[1], lo[2], lo[3], hi2[0], hi2[1], hi2[2], hi2[3]};
                acc[n] = __builtin_amdgcn_mfma_f32_16x16x32_bf16(w, __builtin_bit_cast(bf16x8, of[h]), acc[n], 0, 0, 0);
            }
        }
        final_store(L, wid, lane, acc, 1.0f, out + ((size_t)rp * 64 * 896 + cb * 112) * 2);
        STAMP(7);
    }
}
}

__global__ void __launch_bounds__(512, 2)
fused_kernel(const char* Ax, const char* Wqkv, const char* Wo, const char* Bx, const char* Cre, const char* Cim, const char* Wre, const char* Wim, char* out, char* ws) {
    extern __shared__ __attribute__((aligned(16))) char lds[];
    const int wid = __builtin_amdgcn_readfirstlane(threadIdx.x >> 6), lane = threadIdx.x & 63;
    const int x = blockIdx.x & 7, i = blockIdx.x >> 3;
    STAMP(8);
    const u64 tag = *(const GAS u64*)(ws + WS_EPOCH) + 1;
    if (threadIdx.x < 4) *(LAS unsigned*)((LAS char*)lds + LDS_CNT + threadIdx.x * 4) = 0u;
    if (i < 16) role_b(lds, x, i, wid, lane, Ax, Wqkv, Wo, Bx, Cre, Cim, Wre, Wim, out, ws, tag STPASS);
    else role_a(lds, x, i - 16, wid, lane, Ax, Wqkv, Wo, Bx, Cre, Cim, Wre, Wim, out, ws, tag STPASS);
    if (blockIdx.x == 0 && wid == 7) {
#pragma unroll
        for (int k = 0; k < 4; ++k) poll(ws, WS_ARR + (k * 64 + lane) * 8, true, tag);
        if (lane == 0) flag_store(ws, WS_EPOCH, tag);
    }
}

extern "C" void kernel_launch(void* const* d_in, const int* in_sizes, int n_in, void* d_out, int out_size, void* d_ws, size_t ws_size, hipStream_t stream) {
    (void)in_sizes; (void)n_in; (void)out_size; (void)ws_size;
    (void)hipFuncSetAttribute((const void*)fused_kernel, hipFuncAttributeMaxDynamicSharedMemorySize, LDS_BYTES);
    fused_kernel<<<256, 512, LDS_BYTES, stream>>>((const char*)d_in[0], (const char*)d_in[1], (const char*)d_in[2], (const char*)d_in[3], (const char*)d_in[4], (const char*)d_in[5],
                                                   (const char*)d_in[6], (const char*)d_in[7], (char*)d_out, (char*)d_ws);
}
```

```cpp
#include <hip/hip_runtime.h>
#include <hip/hip_bf16.h>
#include <stdint.h>

typedef short bf16x8 __attribute__((ext_vector_type(8)));
typedef short s16x4 __attribute__((ext_vector_type(4)));
typedef float f32x2 __attribute__((ext_vector_type(2)));
typedef float f32x4 __attribute__((ext_vector_type(4)));
typedef float f32x16 __attribute__((ext_vector_type(16)));
typedef __bf16 bf16x2 __attribute__((ext_vector_type(2)));
typedef unsigned v4u __attribute__((ext_vector_type(4)));
typedef unsigned v2u __attribute__((ext_vector_type(2)));
typedef unsigned long long u64;
#define LAS __attribute__((address_space(3)))
#define GAS __attribute__((address_space(1)))
#define DI __device__ __forceinline__

#define G224_BODY \
    "v_mov_b32 v0, 0\n\t" \
    "v_mov_b32 v1, 0\n\t" \
    "v_mov_b32 v2, 0\n\t" \
    "v_mov_b32 v3, 0\n\t" \
    "v_mov_b32 v4, 0\n\t" \
    "v_mov_b32 v5, 0\n\t" \
    "v_mov_b32 v6, 0\n\t" \
    "v_mov_b32 v7, 0\n\t" \
    "v_mov_b32 v8, 0\n\t" \
    "v_mov_b32 v9, 0\n\t" \
    "v_mov_b32 v10, 0\n\t" \
    "v_mov_b32 v11, 0\n\t" \
    "v_mov_b32 v12, 0\n\t" \
    "v_mov_b32 v13, 0\n\t" \
    "v_mov_b32 v14, 0\n\t" \
    "v_mov_b32 v15, 0\n\t" \
    "v_mov_b32 v16, 0\n\t" \
    "v_mov_b32 v17, 0\n\t" \
    "v_mov_b32 v18, 0\n\t" \
    "v_mov_b32 v19, 0\n\t" \
    "v_mov_b32 v20, 0\n\t" \
    "v_mov_b32 v21, 0\n\t" \
    "v_mov_b32 v22, 0\n\t" \
    "v_mov_b32 v23, 0\n\t" \
    "v_mov_b32 v24, 0\n\t" \
    "v_mov_b32 v25, 0\n\t" \
    "v_mov_b32 v26, 0\n\t" \
    "v_mov_b32 v27, 0\n\t" \
    "v_mov_b32 v28, 0\n\t" \
    "v_mov_b32 v29, 0\n\t" \
    "v_mov_b32 v30, 0\n\t" \
    "v_mov_b32 v31, 0\n\t" \
    "v_mov_b32 v32, 0\n\t" \
    "v_mov_b32 v33, 0\n\t" \
    "v_mov_b32 v34, 0\n\t" \
    "v_mov_b32 v35, 0\n\t" \
    "v_mov_b32 v36, 0\n\t" \
    "v_mov_b32 v37, 0\n\t" \
    "v_mov_b32 v38, 0\n\t" \
    "v_mov_b32 v39, 0\n\t" \
    "v_mov_b32 v40, 0\n\t" \
    "v_mov_b32 v41, 0\n\t" \
    "v_mov_b32 v42, 0\n\t" \
    "v_mov_b32 v43, 0\n\t" \
    "v_mov_b32 v44, 0\n\t" \
    "v_mov_b32 v45, 0\n\t" \
    "v_mov_b32 v46, 0\n\t" \
    "v_mov_b32 v47, 0\n\t" \
    "v_mov_b32 v48, 0\n\t" \
    "v_mov_b32 v49, 0\n\t" \
    "v_mov_b32 v50, 0\n\t" \
    "v_mov_b32 v51, 0\n\t" \
    "v_mov_b32 v52, 0\n\t" \
    "v_mov_b32 v53, 0\n\t" \
    "v_mov_b32 v54, 0\n\t" \
    "v_mov_b32 v55, 0\n\t" \
    "v_mov_b32 v56, 0\n\t" \
    "v_mov_b32 v57, 0\n\t" \
    "v_mov_b32 v58, 0\n\t" \
    "v_mov_b32 v59, 0\n\t" \
    "v_mov_b32 v60, 0\n\t" \
    "v_mov_b32 v61, 0\n\t" \
    "v_mov_b32 v62, 0\n\t" \
    "v_mov_b32 v63, 0\n\t" \
    "v_mov_b32 v64, 0\n\t" \
    "v_mov_b32 v65, 0\n\t" \
    "v_mov_b32 v66, 0\n\t" \
    "v_mov_b32 v67, 0\n\t" \
    "v_mov_b32 v68, 0\n\t" \
    "v_mov_b32 v69, 0\n\t" \
    "v_mov_b32 v70, 0\n\t" \
    "v_mov_b32 v71, 0\n\t" \
    "v_mov_b32 v72, 0\n\t" \
    "v_mov_b32 v73, 0\n\t" \
    "v_mov_b32 v74, 0\n\t" \
    "v_mov_b32 v75, 0\n\t" \
    "v_mov_b32 v76, 0\n\t" \
    "v_mov_b32 v77, 0\n\t" \
    "v_mov_b32 v78, 0\n\t" \
    "v_mov_b32 v79, 0\n\t" \
    "v_mov_b32 v80, 0\n\t" \
    "v_mov_b32 v81, 0\n\t" \
    "v_mov_b32 v82, 0\n\t" \
    "v_mov_b32 v83, 0\n\t" \
    "v_mov_b32 v84, 0\n\t" \
    "v_mov_b32 v85, 0\n\t" \
    "v_mov_b32 v86, 0\n\t" \
    "v_mov_b32 v87, 0\n\t" \
    "v_mov_b32 v88, 0\n\t" \
    "v_mov_b32 v89, 0\n\t" \
    "v_mov_b32 v90, 0\n\t" \
    "v_mov_b32 v91, 0\n\t" \
    "v_mov_b32 v92, 0\n\t" \
    "v_mov_b32 v93, 0\n\t" \
    "v_mov_b32 v94, 0\n\t" \
    "v_mov_b32 v95, 0\n\t" \
    "v_mov_b32 v96, 0\n\t" \
    "v_mov_b32 v97, 0\n\t" \
    "v_mov_b32 v98, 0\n\t" \
    "v_mov_b32 v99, 0\n\t" \
    "v_mov_b32 v100, 0\n\t" \
    "v_mov_b32 v101, 0\n\t" \
    "v_mov_b32 v102, 0\n\t" \
    "v_mov_b32 v103, 0\n\t" \
    "v_mov_b32 v104, 0\n\t" \
    "v_mov_b32 v105, 0\n\t" \
    "v_mov_b32 v106, 0\n\t" \
    "v_mov_b32 v107, 0\n\t" \
    "v_mov_b32 v108, 0\n\t" \
    "v_mov_b32 v109, 0\n\t" \
    "v_mov_b32 v110, 0\n\t" \
    "v_mov_b32 v111, 0\n\t" \
    "s_waitcnt lgkmcnt(0)\n\t" \
    "s_barrier\n\t" \
    "v_mov_b32 v207, v200\n\t" \
    "v_mov_b32 v208, v202\n\t" \
    "v_mov_b32 v209, v203\n\t" \
    "ds_read_b128 v[112:115], v207 offset:0\n\t" \
    "ds_read_b64_tr_b16 v[128:129], v208 offset:0\n\t" \
    "ds_read_b64_tr_b16 v[130:131], v208 offset:1792\n\t" \
    "ds_read_b128 v[116:119], v207 offset:2048\n\t" \
    "ds_read_b128 v[120:123], v207 offset:4096\n\t" \
    "ds_read_b128 v[124:127], v207 offset:6144\n\t" \
    "ds_read_b64_tr_b16 v[132:133], v209 offset:32\n\t" \
    "ds_read_b64_tr_b16 v[134:135], v209 offset:1824\n\t" \
    "ds_read_b64_tr_b16 v[136:137], v208 offset:64\n\t" \
    "ds_read_b64_tr_b16 v[138:139], v208 offset:1856\n\t" \
    "ds_read_b64_tr_b16 v[140:141], v209 offset:96\n\t" \
    "ds_read_b64_tr_b16 v[142:143], v209 offset:1888\n\t" \
    "ds_read_b64_tr_b16 v[144:145], v208 offset:128\n\t" \
    "ds_read_b64_tr_b16 v[146:147], v208 offset:1920\n\t" \
    "ds_read_b64_tr_b16 v[148:149], v209 offset:160\n\t" \
    "ds_read_b64_tr_b16 v[150:151], v209 offset:1952\n\t" \
    "ds_read_b64_tr_b16 v[152:153], v208 offset:192\n\t" \
    "ds_read_b64_tr_b16 v[154:155], v208 offset:1984\n\t" \
    "s_mov_b32 %[aslot], 0\n\t" \
    "s_mov_b32 %[bslot], 14336\n\t" \
    "v_mov_b32 v204, v201\n\t" \
    "v_add_u32 v205, %[bslot], v202\n\t" \
    "v_add_u32 v206, %[bslot], v203\n\t" \
    "s_waitcnt lgkmcnt(0)\n\t" \
    "s_barrier\n\t" \
    "1:\n\t" \
    "v_mfma_f32_16x16x32_bf16 v[0:3], v[128:131], v[112:115], v[0:3]\n\t" \
    "ds_read_b128 v[156:159], v204 offset:0\n\t" \
    "v_mfma_f32_16x16x32_bf16 v[4:7], v[128:131], v[116:119], v[4:7]\n\t" \
    "ds_read_b64_tr_b16 v[172:173], v205 offset:0\n\t" \
    "v_mfma_f32_16x16x32_bf16 v[8:11], v[128:131], v[120:123], v[8:11]\n\t" \
    "ds_read_b64_tr_b16 v[174:175], v205 offset:1792\n\t" \
    "v_mfma_f32_16x16x32_bf16 v[12:15], v[128:131], v[124:127], v[12:15]\n\t" \
    "ds_read_b128 v[160:163], v204 offset:2048\n\t" \
    "v_mfma_f32_16x16x32_bf16 v[16:19], v[132:135], v[112:115], v[16:19]\n\t" \
    "ds_read_b128 v[164:167], v204 offset:4096\n\t" \
    "v_mfma_f32_16x16x32_bf16 v[20:23], v[132:135], v[116:119], v[20:23]\n\t" \
    "ds_read_b128 v[168:171], v204 offset:6144\n\t" \
    "v_mfma_f32_16x16x32_bf16 v[24:27], v[132:135], v[120:123], v[24:27]\n\t" \
    "ds_read_b64_tr_b16 v[176:177], v206 offset:32\n\t" \
    "v_mfma_f32_16x16x32_bf16 v[28:31], v[132:135], v[124:127], v[28:31]\n\t" \
    "ds_read_b64_tr_b16 v[178:179], v206 offset:1824\n\t" \
    "v_mfma_f32_16x16x32_bf16 v[32:35], v[136:139], v[112:115], v[32:35]\n\t" \
    "ds_read_b64_tr_b16 v[180:181], v205 offset:64\n\t" \
    "v_mfma_f32_16x16x32_bf16 v[36:39], v[136:139], v[116:119], v[36:39]\n\t" \
    "ds_read_b64_tr_b16 v[182:183], v205 offset:1856\n\t" \
    "v_mfma_f32_16x16x32_bf16 v[40:43], v[136:139], v[120:123], v[40:43]\n\t" \
    "ds_read_b64_tr_b16 v[184:185], v206 offset:96\n\t" \
    "v_mfma_f32_16x16x32_bf16 v[44:47], v[136:139], v[124:127], v[44:47]\n\t" \
    "ds_read_b64_tr_b16 v[186:187], v206 offset:1888\n\t" \
    "v_mfma_f32_16x16x32_bf16 v[48:51], v[140:143], v[112:115], v[48:51]\n\t" \
    "ds_read_b64_tr_b16 v[188:189], v205 offset:128\n\t" \
    "v_mfma_f32_16x16x32_bf16 v[52:55], v[140:143], v[116:119], v[52:55]\n\t" \
    "ds_read_b64_tr_b16 v[190:191], v205 offset:1920\n\t" \
    "v_mfma_f32_16x16x32_bf16 v[56:59], v[140:143], v[120:123], v[56:59]\n\t" \
    "ds_read_b64_tr_b16 v[192:193], v206 offset:160\n\t" \
    "v_mfma_f32_16x16x32_bf16 v[60:63], v[140:143], v[124:127], v[60:63]\n\t" \
    "ds_read_b64_tr_b16 v[194:195], v206 offset:1952\n\t" \
    "v_mfma_f32_16x16x32_bf16 v[64:67], v[144:147], v[112:115], v[64:67]\n\t" \
    "ds_read_b64_tr_b16 v[196:197], v205 offset:192\n\t" \
    "v_mfma_f32_16x16x32_bf16 v[68:71], v[144:147], v[116:119], v[68:71]\n\t" \
    "ds_read_b64_tr_b16 v[198:199], v205 offset:1984\n\t" \
    "v_mfma_f32_16x16x32_bf16 v[72:75], v[144:147], v[120:123], v[72:75]\n\t" \
    "v_mfma_f32_16x16x32_bf16 v[76:79], v[144:147], v[124:127], v[76:79]\n\t" \
    "s_add_u32 %[aslot], %[aslot], 16384\n\t" \
    "v_mfma_f32_16x16x32_bf16 v[80:83], v[148:151], v[112:115], v[80:83]\n\t" \
    "s_and_b32 %[aslot], %[aslot], 0xffff\n\t" \
    "v_mfma_f32_16x16x32_bf16 v[84:87], v[148:151], v[116:119], v[84:87]\n\t" \
    "s_add_u32 %[bslot], %[bslot], 14336\n\t" \
    "v_mfma_f32_16x16x32_bf16 v[88:91], v[148:151], v[120:123], v[88:91]\n\t" \
    "s_cmp_eq_u32 %[bslot], 86016\n\t" \
    "v_mfma_f32_16x16x32_bf16 v[92:95], v[148:151], v[124:127], v[92:95]\n\t" \
    "s_cselect_b32 %[bslot], 0, %[bslot]\n\t" \
    "v_mfma_f32_16x16x32_bf16 v[96:99], v[152:155], v[112:115], v[96:99]\n\t" \
    "v_add_u32 v207, %[aslot], v200\n\t" \
    "v_mfma_f32_16x16x32_bf16 v[100:103], v[152:155], v[116:119], v[100:103]\n\t" \
    "v_add_u32 v208, %[bslot], v202\n\t" \
    "v_mfma_f32_16x16x32_bf16 v[104:107], v[152:155], v[120:123], v[104:107]\n\t" \
    "v_add_u32 v209, %[bslot], v203\n\t" \
    "v_mfma_f32_16x16x32_bf16 v[108:111], v[152:155], v[124:127], v[108:111]\n\t" \
    "s_waitcnt lgkmcnt(0)\n\t" \
    "s_barrier\n\t" \
    "v_mfma_f32_16x16x32_bf16 v[0:3], v[172:175], v[156:159], v[0:3]\n\t" \
    "ds_read_b128 v[112:115], v207 offset:0\n\t" \
    "v_mfma_f32_16x16x32_bf16 v[4:7], v[172:175], v[160:163], v[4:7]\n\t" \
    "ds_read_b64_tr_b16 v[128:129], v208 offset:0\n\t" \
    "v_mfma_f32_16x16x32_bf16 v[8:11], v[172:175], v[164:167], v[8:11]\n\t" \
    "ds_read_b64_tr_b16 v[130:131], v208 offset:1792\n\t" \
    "v_mfma_f32_16x16x32_bf16 v[12:15], v[172:175], v[168:171], v[12:15]\n\t" \
    "ds_read_b128 v[116:119], v207 offset:2048\n\t" \
    "v_mfma_f32_16x16x32_bf16 v[16:19], v[176:179], v[156:159], v[16:19]\n\t" \
    "ds_read_b128 v[120:123], v207 offset:4096\n\t" \
    "v_mfma_f32_16x16x32_bf16 v[20:23], v[176:179], v[160:163], v[20:23]\n\t" \
    "ds_read_b128 v[124:127], v207 offset:6144\n\t" \
    "v_mfma_f32_16x16x32_bf16 v[24:27], v[176:179], v[164:167], v[24:27]\n\t" \
    "ds_read_b64_tr_b16 v[132:133], v209 offset:32\n\t" \
    "v_mfma_f32_16x16x32_bf16 v[28:31], v[176:179], v[168:171], v[28:31]\n\t" \
    "ds_read_b64_tr_b16 v[134:135], v209 offset:1824\n\t" \
    "v_mfma_f32_16x16x32_bf16 v[32:35], v[180:183], v[156:159], v[32:35]\n\t" \
    "ds_read_b64_tr_b16 v[136:137], v208 offset:64\n\t" \
    "v_mfma_f32_16x16x32_bf16 v[36:39], v[180:183], v[160:163], v[36:39]\n\t" \
    "ds_read_b64_tr_b16 v[138:139], v208 offset:1856\n\t" \
    "v_mfma_f32_16x16x32_bf16 v[40:43], v[180:183], v[164:167], v[40:43]\n\t" \
    "ds_read_b64_tr_b16 v[140:141], v209 offset:96\n\t" \
    "v_mfma_f32_16x16x32_bf16 v[44:47], v[180:183], v[168:171], v[44:47]\n\t" \
    "ds_read_b64_tr_b16 v[142:143], v209 offset:1888\n\t" \
    "v_mfma_f32_16x16x32_bf16 v[48:51], v[184:187], v[156:159], v[48:51]\n\t" \
    "ds_read_b64_tr_b16 v[144:145], v208 offset:128\n\t" \
    "v_mfma_f32_16x16x32_bf16 v[52:55], v[184:187], v[160:163], v[52:55]\n\t" \
    "ds_read_b64_tr_b16 v[146:147], v208 offset:1920\n\t" \
    "v_mfma_f32_16x16x32_bf16 v[56:59], v[184:187], v[164:167], v[56:59]\n\t" \
    "ds_read_b64_tr_b16 v[148:149], v209 offset:160\n\t" \
    "v_mfma_f32_16x16x32_bf16 v[60:63], v[184:187], v[168:171], v[60:63]\n\t" \
    "ds_read_b64_tr_b16 v[150:151], v209 offset:1952\n\t" \
    "v_mfma_f32_16x16x32_bf16 v[64:67], v[188:191], v[156:159], v[64:67]\n\t" \
    "ds_read_b64_tr_b16 v[152:153], v208 offset:192\n\t" \
    "v_mfma_f32_16x16x32_bf16 v[68:71], v[188:191], v[160:163], v[68:71]\n\t" \
    "ds_read_b64_tr_b16 v[154:155], v208 offset:1984\n\t" \
    "v_mfma_f32_16x16x32_bf16 v[72:75], v[188:191], v[164:167], v[72:75]\n\t" \
    "v_mfma_f32_16x16x32_bf16 v[76:79], v[188:191], v[168:171], v[76:79]\n\t" \
    "s_add_u32 %[bslot], %[bslot], 14336\n\t" \
    "v_mfma_f32_16x16x32_bf16 v[80:83], v[192:195], v[156:159], v[80:83]\n\t" \
    "s_cmp_eq_u32 %[bslot], 86016\n\t" \
    "v_mfma_f32_16x16x32_bf16 v[84:87], v[192:195], v[160:163], v[84:87]\n\t" \
    "s_cselect_b32 %[bslot], 0, %[bslot]\n\t" \
    "v_mfma_f32_16x16x32_bf16 v[88:91], v[192:195], v[164:167], v[88:91]\n\t" \
    "v_add_u32 v204, %[aslot], v201\n\t" \
    "v_mfma_f32_16x16x32_bf16 v[92:95], v[192:195], v[168:171], v[92:95]\n\t" \
    "v_add_u32 v205, %[bslot], v202\n\t" \
    "v_mfma_f32_16x16x32_bf16 v[96:99], v[196:199], v[156:159], v[96:99]\n\t" \
    "v_add_u32 v206, %[bslot], v203\n\t" \
    "v_mfma_f32_16x16x32_bf16 v[100:103], v[196:199], v[160:163], v[100:103]\n\t" \
    "s_sub_u32 %[cnt], %[cnt], 1\n\t" \
    "v_mfma_f32_16x16x32_bf16 v[104:107], v[196:199], v[164:167], v[104:107]\n\t" \
    "s_cmp_lg_u32 %[cnt], 0\n\t" \
    "v_mfma_f32_16x16x32_bf16 v[108:111], v[196:199], v[168:171], v[108:111]\n\t" \
    "s_waitcnt lgkmcnt(0)\n\t" \
    "s_barrier\n\t" \
    "s_cbranch_scc1 1b\n\t" \
    "v_mfma_f32_16x16x32_bf16 v[0:3], v[128:131], v[112:115], v[0:3]\n\t" \
    "ds_read_b128 v[156:159], v204 offset:0\n\t" \
    "v_mfma_f32_16x16x32_bf16 v[4:7], v[128:131], v[116:119], v[4:7]\n\t" \
    "ds_read_b64_tr_b16 v[172:173], v205 offset:0\n\t" \
    "v_mfma_f32_16x16x32_bf16 v[8:11], v[128:131], v[120:123], v[8:11]\n\t" \
    "ds_read_b64_tr_b16 v[174:175], v205 offset:1792\n\t" \
    "v_mfma_f32_16x16x32_bf16 v[12:15], v[128:131], v[124:127], v[12:15]\n\t" \
    "ds_read_b128 v[160:163], v204 offset:2048\n\t" \
    "v_mfma_f32_16x16x32_bf16 v[16:19], v[132:135], v[112:115], v[16:19]\n\t" \
    "ds_read_b128 v[164:167], v204 offset:4096\n\t" \
    "v_mfma_f32_16x16x32_bf16 v[20:23], v[132:135], v[116:119], v[20:23]\n\t" \
    "ds_read_b128 v[168:171], v204 offset:6144\n\t" \
    "v_mfma_f32_16x16x32_bf16 v[24:27], v[132:135], v[120:123], v[24:27]\n\t" \
    "ds_read_b64_tr_b16 v[176:177], v206 offset:32\n\t" \
    "v_mfma_f32_16x16x32_bf16 v[28:31], v[132:135], v[124:127], v[28:31]\n\t" \
    "ds_read_b64_tr_b16 v[178:179], v206 offset:1824\n\t" \
    "v_mfma_f32_16x16x32_bf16 v[32:35], v[136:139], v[112:115], v[32:35]\n\t" \
    "ds_read_b64_tr_b16 v[180:181], v205 offset:64\n\t" \
    "v_mfma_f32_16x16x32_bf16 v[36:39], v[136:139], v[116:119], v[36:39]\n\t" \
    "ds_read_b64_tr_b16 v[182:183], v205 offset:1856\n\t" \
    "v_mfma_f32_16x16x32_bf16 v[40:43], v[136:139], v[120:123], v[40:43]\n\t" \
    "ds_read_b64_tr_b16 v[184:185], v206 offset:96\n\t" \
    "v_mfma_f32_16x16x32_bf16 v[44:47], v[136:139], v[124:127], v[44:47]\n\t" \
    "ds_read_b64_tr_b16 v[186:187], v206 offset:1888\n\t" \
    "v_mfma_f32_16x16x32_bf16 v[48:51], v[140:143], v[112:115], v[48:51]\n\t" \
    "ds_read_b64_tr_b16 v[188:189], v205 offset:128\n\t" \
    "v_mfma_f32_16x16x32_bf16 v[52:55], v[140:143], v[116:119], v[52:55]\n\t" \
    "ds_read_b64_tr_b16 v[190:191], v205 offset:1920\n\t" \
    "v_mfma_f32_16x16x32_bf16 v[56:59], v[140:143], v[120:123], v[56:59]\n\t" \
    "ds_read_b64_tr_b16 v[192:193], v206 offset:160\n\t" \
    "v_mfma_f32_16x16x32_bf16 v[60:63], v[140:143], v[124:127], v[60:63]\n\t" \
    "ds_read_b64_tr_b16 v[194:195], v206 offset:1952\n\t" \
    "v_mfma_f32_16x16x32_bf16 v[64:67], v[144:147], v[112:115], v[64:67]\n\t" \
    "ds_read_b64_tr_b16 v[196:197], v205 offset:192\n\t" \
    "v_mfma_f32_16x16x32_bf16 v[68:71], v[144:147], v[116:119], v[68:71]\n\t" \
    "ds_read_b64_tr_b16 v[198:199], v205 offset:1984\n\t" \
    "v_mfma_f32_16x16x32_bf16 v[72:75], v[144:147], v[120:123], v[72:75]\n\t" \
    "v_mfma_f32_16x16x32_bf16 v[76:79], v[144:147], v[124:127], v[76:79]\n\t" \
    "v_mfma_f32_16x16x32_bf16 v[80:83], v[148:151], v[112:115], v[80:83]\n\t" \
    "v_mfma_f32_16x16x32_bf16 v[84:87], v[148:151], v[116:119], v[84:87]\n\t" \
    "v_mfma_f32_16x16x32_bf16 v[88:91], v[148:151], v[120:123], v[88:91]\n\t" \
    "v_mfma_f32_16x16x32_bf16 v[92:95], v[148:151], v[124:127], v[92:95]\n\t" \
    "v_mfma_f32_16x16x32_bf16 v[96:99], v[152:155], v[112:115], v[96:99]\n\t" \
    "v_mfma_f32_16x16x32_bf16 v[100:103], v[152:155], v[116:119], v[100:103]\n\t" \
    "v_mfma_f32_16x16x32_bf16 v[104:107], v[152:155], v[120:123], v[104:107]\n\t" \
    "v_mfma_f32_16x16x32_bf16 v[108:111], v[152:155], v[124:127], v[108:111]\n\t" \
    "s_waitcnt lgkmcnt(0)\n\t" \
    "s_barrier\n\t" \
    "v_mfma_f32_16x16x32_bf16 v[0:3], v[172:175], v[156:159], v[0:3]\n\t" \
    "v_mfma_f32_16x16x32_bf16 v[4:7], v[172:175], v[160:163], v[4:7]\n\t" \
    "v_mfma_f32_16x16x32_bf16 v[8:11], v[172:175], v[164:167], v[8:11]\n\t" \
    "v_mfma_f32_16x16x32_bf16 v[12:15], v[172:175], v[168:171], v[12:15]\n\t" \
    "v_mfma_f32_16x16x32_bf16 v[16:19], v[176:179], v[156:159], v[16:19]\n\t" \
    "v_mfma_f32_16x16x32_bf16 v[20:23], v[176:179], v[160:163], v[20:23]\n\t" \
    "v_mfma_f32_16x16x32_bf16 v[24:27], v[176:179], v[164:167], v[24:27]\n\t" \
    "v_mfma_f32_16x16x32_bf16 v[28:31], v[176:179], v[168:171], v[28:31]\n\t" \
    "v_mfma_f32_16x16x32_bf16 v[32:35], v[180:183], v[156:159], v[32:35]\n\t" \
    "v_mfma_f32_16x16x32_bf16 v[36:39], v[180:183], v[160:163], v[36:39]\n\t" \
    "v_mfma_f32_16x16x32_bf16 v[40:43], v[180:183], v[164:167], v[40:43]\n\t" \
    "v_mfma_f32_16x16x32_bf16 v[44:47], v[180:183], v[168:171], v[44:47]\n\t" \
    "v_mfma_f32_16x16x32_bf16 v[48:51], v[184:187], v[156:159], v[48:51]\n\t" \
    "v_mfma_f32_16x16x32_bf16 v[52:55], v[184:187], v[160:163], v[52:55]\n\t" \
    "v_mfma_f32_16x16x32_bf16 v[56:59], v[184:187], v[164:167], v[56:59]\n\t" \
    "v_mfma_f32_16x16x32_bf16 v[60:63], v[184:187], v[168:171], v[60:63]\n\t" \
    "v_mfma_f32_16x16x32_bf16 v[64:67], v[188:191], v[156:159], v[64:67]\n\t" \
    "v_mfma_f32_16x16x32_bf16 v[68:71], v[188:191], v[160:163], v[68:71]\n\t" \
    "v_mfma_f32_16x16x32_bf16 v[72:75], v[188:191], v[164:167], v[72:75]\n\t" \
    "v_mfma_f32_16x16x32_bf16 v[76:79], v[188:191], v[168:171], v[76:79]\n\t" \
    "v_mfma_f32_16x16x32_bf16 v[80:83], v[192:195], v[156:159], v[80:83]\n\t" \
    "v_mfma_f32_16x16x32_bf16 v[84:87], v[192:195], v[160:163], v[84:87]\n\t" \
    "v_mfma_f32_16x16x32_bf16 v[88:91], v[192:195], v[164:167], v[88:91]\n\t" \
    "v_mfma_f32_16x16x32_bf16 v[92:95], v[192:195], v[168:171], v[92:95]\n\t" \
    "v_mfma_f32_16x16x32_bf16 v[96:99], v[196:199], v[156:159], v[96:99]\n\t" \
    "v_mfma_f32_16x16x32_bf16 v[100:103], v[196:199], v[160:163], v[100:103]\n\t" \
    "v_mfma_f32_16x16x32_bf16 v[104:107], v[196:199], v[164:167], v[104:107]\n\t" \
    "v_mfma_f32_16x16x32_bf16 v[108:111], v[196:199], v[168:171], v[108:111]\n\t" \
    "s_waitcnt lgkmcnt(0)\n\t" \
    "s_barrier\n\t" \
    "s_nop 7\n\t" \
    "s_nop 7\n\t" \
    ""
#define G224_BODY_SWAP \
    "v_mov_b32 v0, 0\n\t" \
    "v_mov_b32 v1, 0\n\t" \
    "v_mov_b32 v2, 0\n\t" \
    "v_mov_b32 v3, 0\n\t" \
    "v_mov_b32 v4, 0\n\t" \
    "v_mov_b32 v5, 0\n\t" \
    "v_mov_b32 v6, 0\n\t" \
    "v_mov_b32 v7, 0\n\t" \
    "v_mov_b32 v8, 0\n\t" \
    "v_mov_b32 v9, 0\n\t" \
    "v_mov_b32 v10, 0\n\t" \
    "v_mov_b32 v11, 0\n\t" \
    "v_mov_b32 v12, 0\n\t" \
    "v_mov_b32 v13, 0\n\t" \
    "v_mov_b32 v14, 0\n\t" \
    "v_mov_b32 v15, 0\n\t" \
    "v_mov_b32 v16, 0\n\t" \
    "v_mov_b32 v17, 0\n\t" \
    "v_mov_b32 v18, 0\n\t" \
    "v_mov_b32 v19, 0\n\t" \
    "v_mov_b32 v20, 0\n\t" \
    "v_mov_b32 v21, 0\n\t" \
    "v_mov_b32 v22, 0\n\t" \
    "v_mov_b32 v23, 0\n\t" \
    "v_mov_b32 v24, 0\n\t" \
    "v_mov_b32 v25, 0\n\t" \
    "v_mov_b32 v26, 0\n\t" \
    "v_mov_b32 v27, 0\n\t" \
    "v_mov_b32 v28, 0\n\t" \
    "v_mov_b32 v29, 0\n\t" \
    "v_mov_b32 v30, 0\n\t" \
    "v_mov_b32 v31, 0\n\t" \
    "v_mov_b32 v32, 0\n\t" \
    "v_mov_b32 v33, 0\n\t" \
    "v_mov_b32 v34, 0\n\t" \
    "v_mov_b32 v35, 0\n\t" \
    "v_mov_b32 v36, 0\n\t" \
    "v_mov_b32 v37, 0\n\t" \
    "v_mov_b32 v38, 0\n\t" \
    "v_mov_b32 v39, 0\n\t" \
    "v_mov_b32 v40, 0\n\t" \
    "v_mov_b32 v41, 0\n\t" \
    "v_mov_b32 v42, 0\n\t" \
    "v_mov_b32 v43, 0\n\t" \
    "v_mov_b32 v44, 0\n\t" \
    "v_mov_b32 v45, 0\n\t" \
    "v_mov_b32 v46, 0\n\t" \
    "v_mov_b32 v47, 0\n\t" \
    "v_mov_b32 v48, 0\n\t" \
    "v_mov_b32 v49, 0\n\t" \
    "v_mov_b32 v50, 0\n\t" \
    "v_mov_b32 v51, 0\n\t" \
    "v_mov_b32 v52, 0\n\t" \
    "v_mov_b32 v53, 0\n\t" \
    "v_mov_b32 v54, 0\n\t" \
    "v_mov_b32 v55, 0\n\t" \
    "v_mov_b32 v56, 0\n\t" \
    "v_mov_b32 v57, 0\n\t" \
    "v_mov_b32 v58, 0\n\t" \
    "v_mov_b32 v59, 0\n\t" \
    "v_mov_b32 v60, 0\n\t" \
    "v_mov_b32 v61, 0\n\t" \
    "v_mov_b32 v62, 0\n\t" \
    "v_mov_b32 v63, 0\n\t" \
    "v_mov_b32 v64, 0\n\t" \
    "v_mov_b32 v65, 0\n\t" \
    "v_mov_b32 v66, 0\n\t" \
    "v_mov_b32 v67, 0\n\t" \
    "v_mov_b32 v68, 0\n\t" \
    "v_mov_b32 v69, 0\n\t" \
    "v_mov_b32 v70, 0\n\t" \
    "v_mov_b32 v71, 0\n\t" \
    "v_mov_b32 v72, 0\n\t" \
    "v_mov_b32 v73, 0\n\t" \
    "v_mov_b32 v74, 0\n\t" \
    "v_mov_b32 v75, 0\n\t" \
    "v_mov_b32 v76, 0\n\t" \
    "v_mov_b32 v77, 0\n\t" \
    "v_mov_b32 v78, 0\n\t" \
    "v_mov_b32 v79, 0\n\t" \
    "v_mov_b32 v80, 0\n\t" \
    "v_mov_b32 v81, 0\n\t" \
    "v_mov_b32 v82, 0\n\t" \
    "v_mov_b32 v83, 0\n\t" \
    "v_mov_b32 v84, 0\n\t" \
    "v_mov_b32 v85, 0\n\t" \
    "v_mov_b32 v86, 0\n\t" \
    "v_mov_b32 v87, 0\n\t" \
    "v_mov_b32 v88, 0\n\t" \
    "v_mov_b32 v89, 0\n\t" \
    "v_mov_b32 v90, 0\n\t" \
    "v_mov_b32 v91, 0\n\t" \
    "v_mov_b32 v92, 0\n\t" \
    "v_mov_b32 v93, 0\n\t" \
    "v_mov_b32 v94, 0\n\t" \
    "v_mov_b32 v95, 0\n\t" \
    "v_mov_b32 v96, 0\n\t" \
    "v_mov_b32 v97, 0\n\t" \
    "v_mov_b32 v98, 0\n\t" \
    "v_mov_b32 v99, 0\n\t" \
    "v_mov_b32 v100, 0\n\t" \
    "v_mov_b32 v101, 0\n\t" \
    "v_mov_b32 v102, 0\n\t" \
    "v_mov_b32 v103, 0\n\t" \
    "v_mov_b32 v104, 0\n\t" \
    "v_mov_b32 v105, 0\n\t" \
    "v_mov_b32 v106, 0\n\t" \
    "v_mov_b32 v107, 0\n\t" \
    "v_mov_b32 v108, 0\n\t" \
    "v_mov_b32 v109, 0\n\t" \
    "v_mov_b32 v110, 0\n\t" \
    "v_mov_b32 v111, 0\n\t" \
    "s_waitcnt lgkmcnt(0)\n\t" \
    "s_barrier\n\t" \
    "v_mov_b32 v207, v200\n\t" \
    "v_mov_b32 v208, v202\n\t" \
    "v_mov_b32 v209, v203\n\t" \
    "ds_read_b128 v[112:115], v207 offset:0\n\t" \
    "ds_read_b64_tr_b16 v[128:129], v208 offset:0\n\t" \
    "ds_read_b64_tr_b16 v[130:131], v208 offset:1792\n\t" \
    "ds_read_b128 v[116:119], v207 offset:2048\n\t" \
    "ds_read_b128 v[120:123], v207 offset:4096\n\t" \
    "ds_read_b128 v[124:127], v207 offset:6144\n\t" \
    "ds_read_b64_tr_b16 v[132:133], v209 offset:32\n\t" \
    "ds_read_b64_tr_b16 v[134:135], v209 offset:1824\n\t" \
    "ds_read_b64_tr_b16 v[136:137], v208 offset:64\n\t" \
    "ds_read_b64_tr_b16 v[138:139], v208 offset:1856\n\t" \
    "ds_read_b64_tr_b16 v[140:141], v209 offset:96\n\t" \
    "ds_read_b64_tr_b16 v[142:143], v209 offset:1888\n\t" \
    "ds_read_b64_tr_b16 v[144:145], v208 offset:128\n\t" \
    "ds_read_b64_tr_b16 v[146:147], v208 offset:1920\n\t" \
    "ds_read_b64_tr_b16 v[148:149], v209 offset:160\n\t" \
    "ds_read_b64_tr_b16 v[150:151], v209 offset:1952\n\t" \
    "ds_read_b64_tr_b16 v[152:153], v208 offset:192\n\t" \
    "ds_read_b64_tr_b16 v[154:155], v208 offset:1984\n\t" \
    "s_mov_b32 %[aslot], 0\n\t" \
    "s_mov_b32 %[bslot], 14336\n\t" \
    "v_mov_b32 v204, v201\n\t" \
    "v_add_u32 v205, %[bslot], v202\n\t" \
    "v_add_u32 v206, %[bslot], v203\n\t" \
    "s_waitcnt lgkmcnt(0)\n\t" \
    "s_barrier\n\t" \
    "1:\n\t" \
    "v_mfma_f32_16x16x32_bf16 v[0:3], v[112:115], v[128:131], v[0:3]\n\t" \
    "ds_read_b128 v[156:159], v204 offset:0\n\t" \
    "v_mfma_f32_16x16x32_bf16 v[4:7], v[116:119], v[128:131], v[4:7]\n\t" \
    "ds_read_b64_tr_b16 v[172:173], v205 offset:0\n\t" \
    "v_mfma_f32_16x16x32_bf16 v[8:11], v[120:123], v[128:131], v[8:11]\n\t" \
    "ds_read_b64_tr_b16 v[174:175], v205 offset:1792\n\t" \
    "v_mfma_f32_16x16x32_bf16 v[12:15], v[124:127], v[128:131], v[12:15]\n\t" \
    "ds_read_b128 v[160:163], v204 offset:2048\n\t" \
    "v_mfma_f32_16x16x32_bf16 v[16:19], v[112:115], v[132:135], v[16:19]\n\t" \
    "ds_read_b128 v[164:167], v204 offset:4096\n\t" \
    "v_mfma_f32_16x16x32_bf16 v[20:23], v[116:119], v[132:135], v[20:23]\n\t" \
    "ds_read_b128 v[168:171], v204 offset:6144\n\t" \
    "v_mfma_f32_16x16x32_bf16 v[24:27], v[120:123], v[132:135], v[24:27]\n\t" \
    "ds_read_b64_tr_b16 v[176:177], v206 offset:32\n\t" \
    "v_mfma_f32_16x16x32_bf16 v[28:31], v[124:127], v[132:135], v[28:31]\n\t" \
    "ds_read_b64_tr_b16 v[178:179], v206 offset:1824\n\t" \
    "v_mfma_f32_16x16x32_bf16 v[32:35], v[112:115], v[136:139], v[32:35]\n\t" \
    "ds_read_b64_tr_b16 v[180:181], v205 offset:64\n\t" \
    "v_mfma_f32_16x16x32_bf16 v[36:39], v[116:119], v[136:139], v[36:39]\n\t" \
    "ds_read_b64_tr_b16 v[182:183], v205 offset:1856\n\t" \
    "v_mfma_f32_16x16x32_bf16 v[40:43], v[120:123], v[136:139], v[40:43]\n\t" \
    "ds_read_b64_tr_b16 v[184:185], v206 offset:96\n\t" \
    "v_mfma_f32_16x16x32_bf16 v[44:47], v[124:127], v[136:139], v[44:47]\n\t" \
    "ds_read_b64_tr_b16 v[186:187], v206 offset:1888\n\t" \
    "v_mfma_f32_16x16x32_bf16 v[48:51], v[112:115], v[140:143], v[48:51]\n\t" \
    "ds_read_b64_tr_b16 v[188:189], v205 offset:128\n\t" \
    "v_mfma_f32_16x16x32_bf16 v[52:55], v[116:119], v[140:143], v[52:55]\n\t" \
    "ds_read_b64_tr_b16 v[190:191], v205 offset:1920\n\t" \
    "v_mfma_f32_16x16x32_bf16 v[56:59], v[120:123], v[140:143], v[56:59]\n\t" \
    "ds_read_b64_tr_b16 v[192:193], v206 offset:160\n\t" \
    "v_mfma_f32_16x16x32_bf16 v[60:63], v[124:127], v[140:143], v[60:63]\n\t" \
    "ds_read_b64_tr_b16 v[194:195], v206 offset:1952\n\t" \
    "v_mfma_f32_16x16x32_bf16 v[64:67], v[112:115], v[144:147], v[64:67]\n\t" \
    "ds_read_b64_tr_b16 v[196:197], v205 offset:192\n\t" \
    "v_mfma_f32_16x16x32_bf16 v[68:71], v[116:119], v[144:147], v[68:71]\n\t" \
    "ds_read_b64_tr_b16 v[198:199], v205 offset:1984\n\t" \
    "v_mfma_f32_16x16x32_bf16 v[72:75], v[120:123], v[144:147], v[72:75]\n\t" \
    "v_mfma_f32_16x16x32_bf16 v[76:79], v[124:127], v[144:147], v[76:79]\n\t" \
    "s_add_u32 %[aslot], %[aslot], 16384\n\t" \
    "v_mfma_f32_16x16x32_bf16 v[80:83], v[112:115], v[148:151], v[80:83]\n\t" \
    "s_and_b32 %[aslot], %[aslot], 0xffff\n\t" \
    "v_mfma_f32_16x16x32_bf16 v[84:87], v[116:119], v[148:151], v[84:87]\n\t" \
    "s_add_u32 %[bslot], %[bslot], 14336\n\t" \
    "v_mfma_f32_16x16x32_bf16 v[88:91], v[120:123], v[148:151], v[88:91]\n\t" \
    "s_cmp_eq_u32 %[bslot], 86016\n\t" \
    "v_mfma_f32_16x16x32_bf16 v[92:95], v[124:127], v[148:151], v[92:95]\n\t" \
    "s_cselect_b32 %[bslot], 0, %[bslot]\n\t" \
    "v_mfma_f32_16x16x32_bf16 v[96:99], v[112:115], v[152:155], v[96:99]\n\t" \
    "v_add_u32 v207, %[aslot], v200\n\t" \
    "v_mfma_f32_16x16x32_bf16 v[100:103], v[116:119], v[152:155], v[100:103]\n\t" \
    "v_add_u32 v208, %[bslot], v202\n\t" \
    "v_mfma_f32_16x16x32_bf16 v[104:107], v[120:123], v[152:155], v[104:107]\n\t" \
    "v_add_u32 v209, %[bslot], v203\n\t" \
    "v_mfma_f32_16x16x32_bf16 v[108:111], v[124:127], v[152:155], v[108:111]\n\t" \
    "s_waitcnt lgkmcnt(0)\n\t" \
    "s_barrier\n\t" \
    "v_mfma_f32_16x16x32_bf16 v[0:3], v[156:159], v[172:175], v[0:3]\n\t" \
    "ds_read_b128 v[112:115], v207 offset:0\n\t" \
    "v_mfma_f32_16x16x32_bf16 v[4:7], v[160:163], v[172:175], v[4:7]\n\t" \
    "ds_read_b64_tr_b16 v[128:129], v208 offset:0\n\t" \
    "v_mfma_f32_16x16x32_bf16 v[8:11], v[164:167], v[172:175], v[8:11]\n\t" \
    "ds_read_b64_tr_b16 v[130:131], v208 offset:1792\n\t" \
    "v_mfma_f32_16x16x32_bf16 v[12:15], v[168:171], v[172:175], v[12:15]\n\t" \
    "ds_read_b128 v[116:119], v207 offset:2048\n\t" \
    "v_mfma_f32_16x16x32_bf16 v[16:19], v[156:159], v[176:179], v[16:19]\n\t" \
    "ds_read_b128 v[120:123], v207 offset:4096\n\t" \
    "v_mfma_f32_16x16x32_bf16 v[20:23], v[160:163], v[176:179], v[20:23]\n\t" \
    "ds_read_b128 v[124:127], v207 offset:6144\n\t" \
    "v_mfma_f32_16x16x32_bf16 v[24:27], v[164:167], v[176:179], v[24:27]\n\t" \
    "ds_read_b64_tr_b16 v[132:133], v209 offset:32\n\t" \
    "v_mfma_f32_16x16x32_bf16 v[28:31], v[168:171], v[176:179], v[28:31]\n\t" \
    "ds_read_b64_tr_b16 v[134:135], v209 offset:1824\n\t" \
    "v_mfma_f32_16x16x32_bf16 v[32:35], v[156:159], v[180:183], v[32:35]\n\t" \
    "ds_read_b64_tr_b16 v[136:137], v208 offset:64\n\t" \
    "v_mfma_f32_16x16x32_bf16 v[36:39], v[160:163], v[180:183], v[36:39]\n\t" \
    "ds_read_b64_tr_b16 v[138:139], v208 offset:1856\n\t" \
    "v_mfma_f32_16x16x32_bf16 v[40:43], v[164:167], v[180:183], v[40:43]\n\t" \
    "ds_read_b64_tr_b16 v[140:141], v209 offset:96\n\t" \
    "v_mfma_f32_16x16x32_bf16 v[44:47], v[168:171], v[180:183], v[44:47]\n\t" \
    "ds_read_b64_tr_b16 v[142:143], v209 offset:1888\n\t" \
    "v_mfma_f32_16x16x32_bf16 v[48:51], v[156:159], v[184:187], v[48:51]\n\t" \
    "ds_read_b64_tr_b16 v[144:145], v208 offset:128\n\t" \
    "v_mfma_f32_16x16x32_bf16 v[52:55], v[160:163], v[184:187], v[52:55]\n\t" \
    "ds_read_b64_tr_b16 v[146:147], v208 offset:1920\n\t" \
    "v_mfma_f32_16x16x32_bf16 v[56:59], v[164:167], v[184:187], v[56:59]\n\t" \
    "ds_read_b64_tr_b16 v[148:149], v209 offset:160\n\t" \
    "v_mfma_f32_16x16x32_bf16 v[60:63], v[168:171], v[184:187], v[60:63]\n\t" \
    "ds_read_b64_tr_b16 v[150:151], v209 offset:1952\n\t" \
    "v_mfma_f32_16x16x32_bf16 v[64:67], v[156:159], v[188:191], v[64:67]\n\t" \
    "ds_read_b64_tr_b16 v[152:153], v208 offset:192\n\t" \
    "v_mfma_f32_16x16x32_bf16 v[68:71], v[160:163], v[188:191], v[68:71]\n\t" \
    "ds_read_b64_tr_b16 v[154:155], v208 offset:1984\n\t" \
    "v_mfma_f32_16x16x32_bf16 v[72:75], v[164:167], v[188:191], v[72:75]\n\t" \
    "v_mfma_f32_16x16x32_bf16 v[76:79], v[168:171], v[188:191], v[76:79]\n\t" \
    "s_add_u32 %[bslot], %[bslot], 14336\n\t" \
    "v_mfma_f32_16x16x32_bf16 v[80:83], v[156:159], v[192:195], v[80:83]\n\t" \
    "s_cmp_eq_u32 %[bslot], 86016\n\t" \
    "v_mfma_f32_16x16x32_bf16 v[84:87], v[160:163], v[192:195], v[84:87]\n\t" \
    "s_cselect_b32 %[bslot], 0, %[bslot]\n\t" \
    "v_mfma_f32_16x16x32_bf16 v[88:91], v[164:167], v[192:195], v[88:91]\n\t" \
    "v_add_u32 v204, %[aslot], v201\n\t" \
    "v_mfma_f32_16x16x32_bf16 v[92:95], v[168:171], v[192:195], v[92:95]\n\t" \
    "v_add_u32 v205, %[bslot], v202\n\t" \
    "v_mfma_f32_16x16x32_bf16 v[96:99], v[156:159], v[196:199], v[96:99]\n\t" \
    "v_add_u32 v206, %[bslot], v203\n\t" \
    "v_mfma_f32_16x16x32_bf16 v[100:103], v[160:163], v[196:199], v[100:103]\n\t" \
    "s_sub_u32 %[cnt], %[cnt], 1\n\t" \
    "v_mfma_f32_16x16x32_bf16 v[104:107], v[164:167], v[196:199], v[104:107]\n\t" \
    "s_cmp_lg_u32 %[cnt], 0\n\t" \
    "v_mfma_f32_16x16x32_bf16 v[108:111], v[168:171], v[196:199], v[108:111]\n\t" \
    "s_waitcnt lgkmcnt(0)\n\t" \
    "s_barrier\n\t" \
    "s_cbranch_scc1 1b\n\t" \
    "v_mfma_f32_16x16x32_bf16 v[0:3], v[112:115], v[128:131], v[0:3]\n\t" \
    "ds_read_b128 v[156:159], v204 offset:0\n\t" \
    "v_mfma_f32_16x16x32_bf16 v[4:7], v[116:119], v[128:131], v[4:7]\n\t" \
    "ds_read_b64_tr_b16 v[172:173], v205 offset:0\n\t" \
    "v_mfma_f32_16x16x32_bf16 v[8:11], v[120:123], v[128:131], v[8:11]\n\t" \
    "ds_read_b64_tr_b16 v[174:175], v205 offset:1792\n\t" \
    "v_mfma_f32_16x16x32_bf16 v[12:15], v[124:127], v[128:131], v[12:15]\n\t" \
    "ds_read_b128 v[160:163], v204 offset:2048\n\t" \
    "v_mfma_f32_16x16x32_bf16 v[16:19], v[112:115], v[132:135], v[16:19]\n\t" \
    "ds_read_b128 v[164:167], v204 offset:4096\n\t" \
    "v_mfma_f32_16x16x32_bf16 v[20:23], v[116:119], v[132:135], v[20:23]\n\t" \
    "ds_read_b128 v[168:171], v204 offset:6144\n\t" \
    "v_mfma_f32_16x16x32_bf16 v[24:27], v[120:123], v[132:135], v[24:27]\n\t" \
    "ds_read_b64_tr_b16 v[176:177], v206 offset:32\n\t" \
    "v_mfma_f32_16x16x32_bf16 v[28:31], v[124:127], v[132:135], v[28:31]\n\t" \
    "ds_read_b64_tr_b16 v[178:179], v206 offset:1824\n\t" \
    "v_mfma_f32_16x16x32_bf16 v[32:35], v[112:115], v[136:139], v[32:35]\n\t" \
    "ds_read_b64_tr_b16 v[180:181], v205 offset:64\n\t" \
    "v_mfma_f32_16x16x32_bf16 v[36:39], v[116:119], v[136:139], v[36:39]\n\t" \
    "ds_read_b64_tr_b16 v[182:183], v205 offset:1856\n\t" \
    "v_mfma_f32_16x16x32_bf16 v[40:43], v[120:123], v[136:139], v[40:43]\n\t" \
    "ds_read_b64_tr_b16 v[184:185], v206 offset:96\n\t" \
    "v_mfma_f32_16x16x32_bf16 v[44:47], v[124:127], v[136:139], v[44:47]\n\t" \
    "ds_read_b64_tr_b16 v[186:187], v206 offset:1888\n\t" \
    "v_mfma_f32_16x16x32_bf16 v[48:51], v[112:115], v[140:143], v[48:51]\n\t" \
    "ds_read_b64_tr_b16 v[188:189], v205 offset:128\n\t" \
    "v_mfma_f32_16x16x32_bf16 v[52:55], v[116:119], v[140:143], v[52:55]\n\t" \
    "ds_read_b64_tr_b16 v[190:191], v205 offset:1920\n\t" \
    "v_mfma_f32_16x16x32_bf16 v[56:59], v[120:123], v[140:143], v[56:59]\n\t" \
    "ds_read_b64_tr_b16 v[192:193], v206 offset:160\n\t" \
    "v_mfma_f32_16x16x32_bf16 v[60:63], v[124:127], v[140:143], v[60:63]\n\t" \
    "ds_read_b64_tr_b16 v[194:195], v206 offset:1952\n\t" \
    "v_mfma_f32_16x16x32_bf16 v[64:67], v[112:115], v[144:147], v[64:67]\n\t" \
    "ds_read_b64_tr_b16 v[196:197], v205 offset:192\n\t" \
    "v_mfma_f32_16x16x32_bf16 v[68:71], v[116:119], v[144:147], v[68:71]\n\t" \
    "ds_read_b64_tr_b16 v[198:199], v205 offset:1984\n\t" \
    "v_mfma_f32_16x16x32_bf16 v[72:75], v[120:123], v[144:147], v[72:75]\n\t" \
    "v_mfma_f32_16x16x32_bf16 v[76:79], v[124:127], v[144:147], v[76:79]\n\t" \
    "v_mfma_f32_16x16x32_bf16 v[80:83], v[112:115], v[148:151], v[80:83]\n\t" \
    "v_mfma_f32_16x16x32_bf16 v[84:87], v[116:119], v[148:151], v[84:87]\n\t" \
    "v_mfma_f32_16x16x32_bf16 v[88:91], v[120:123], v[148:151], v[88:91]\n\t" \
    "v_mfma_f32_16x16x32_bf16 v[92:95], v[124:127], v[148:151], v[92:95]\n\t" \
    "v_mfma_f32_16x16x32_bf16 v[96:99], v[112:115], v[152:155], v[96:99]\n\t" \
    "v_mfma_f32_16x16x32_bf16 v[100:103], v[116:119], v[152:155], v[100:103]\n\t" \
    "v_mfma_f32_16x16x32_bf16 v[104:107], v[120:123], v[152:155], v[104:107]\n\t" \
    "v_mfma_f32_16x16x32_bf16 v[108:111], v[124:127], v[152:155], v[108:111]\n\t" \
    "s_waitcnt lgkmcnt(0)\n\t" \
    "s_barrier\n\t" \
    "v_mfma_f32_16x16x32_bf16 v[0:3], v[156:159], v[172:175], v[0:3]\n\t" \
    "v_mfma_f32_16x16x32_bf16 v[4:7], v[160:163], v[172:175], v[4:7]\n\t" \
    "v_mfma_f32_16x16x32_bf16 v[8:11], v[164:167], v[172:175], v[8:11]\n\t" \
    "v_mfma_f32_16x16x32_bf16 v[12:15], v[168:171], v[172:175], v[12:15]\n\t" \
    "v_mfma_f32_16x16x32_bf16 v[16:19], v[156:159], v[176:179], v[16:19]\n\t" \
    "v_mfma_f32_16x16x32_bf16 v[20:23], v[160:163], v[176:179], v[20:23]\n\t" \
    "v_mfma_f32_16x16x32_bf16 v[24:27], v[164:167], v[176:179], v[24:27]\n\t" \
    "v_mfma_f32_16x16x32_bf16 v[28:31], v[168:171], v[176:179], v[28:31]\n\t" \
    "v_mfma_f32_16x16x32_bf16 v[32:35], v[156:159], v[180:183], v[32:35]\n\t" \
    "v_mfma_f32_16x16x32_bf16 v[36:39], v[160:163], v[180:183], v[36:39]\n\t" \
    "v_mfma_f32_16x16x32_bf16 v[40:43], v[164:167], v[180:183], v[40:43]\n\t" \
    "v_mfma_f32_16x16x32_bf16 v[44:47], v[168:171], v[180:183], v[44:47]\n\t" \
    "v_mfma_f32_16x16x32_bf16 v[48:51], v[156:159], v[184:187], v[48:51]\n\t" \
    "v_mfma_f32_16x16x32_bf16 v[52:55], v[160:163], v[184:187], v[52:55]\n\t" \
    "v_mfma_f32_16x16x32_bf16 v[56:59], v[164:167], v[184:187], v[56:59]\n\t" \
    "v_mfma_f32_16x16x32_bf16 v[60:63], v[168:171], v[184:187], v[60:63]\n\t" \
    "v_mfma_f32_16x16x32_bf16 v[64:67], v[156:159], v[188:191], v[64:67]\n\t" \
    "v_mfma_f32_16x16x32_bf16 v[68:71], v[160:163], v[188:191], v[68:71]\n\t" \
    "v_mfma_f32_16x16x32_bf16 v[72:75], v[164:167], v[188:191], v[72:75]\n\t" \
    "v_mfma_f32_16x16x32_bf16 v[76:79], v[168:171], v[188:191], v[76:79]\n\t" \
    "v_mfma_f32_16x16x32_bf16 v[80:83], v[156:159], v[192:195], v[80:83]\n\t" \
    "v_mfma_f32_16x16x32_bf16 v[84:87], v[160:163], v[192:195], v[84:87]\n\t" \
    "v_mfma_f32_16x16x32_bf16 v[88:91], v[164:167], v[192:195], v[88:91]\n\t" \
    "v_mfma_f32_16x16x32_bf16 v[92:95], v[168:171], v[192:195], v[92:95]\n\t" \
    "v_mfma_f32_16x16x32_bf16 v[96:99], v[156:159], v[196:199], v[96:99]\n\t" \
    "v_mfma_f32_16x16x32_bf16 v[100:103], v[160:163], v[196:199], v[100:103]\n\t" \
    "v_mfma_f32_16x16x32_bf16 v[104:107], v[164:167], v[196:199], v[104:107]\n\t" \
    "v_mfma_f32_16x16x32_bf16 v[108:111], v[168:171], v[196:199], v[108:111]\n\t" \
    "s_waitcnt lgkmcnt(0)\n\t" \
    "s_barrier\n\t" \
    "s_nop 7\n\t" \
    "s_nop 7\n\t" \
    ""
#define G224_CLOB "v112","v113","v114","v115","v116","v117","v118","v119","v120","v121","v122","v123","v124","v125","v126","v127","v128","v129","v130","v131","v132","v133","v134","v135","v136","v137","v138","v139","v140","v141","v142","v143","v144","v145","v146","v147","v148","v149","v150","v151","v152","v153","v154","v155","v156","v157","v158","v159","v160","v161","v162","v163","v164","v165","v166","v167","v168","v169","v170","v171","v172","v173","v174","v175","v176","v177","v178","v179","v180","v181","v182","v183","v184","v185","v186","v187","v188","v189","v190","v191","v192","v193","v194","v195","v196","v197","v198","v199","v204","v205","v206","v207","v208","v209", "scc", "memory"
#define LOADER53 \
    "s_add_u32 m0, s47, s53\n\t" \
    "s_nop 0\n\t" \
    "global_load_lds_dwordx4 v5, s[42:43] offset:0\n\t" \
    "global_load_lds_dwordx4 v6, s[42:43] offset:1024\n\t" \
    "global_load_lds_dwordx4 v7, s[42:43] offset:2048\n\t" \
    "s_mov_b32 m0, s46\n\t" \
    "s_add_u32 s46, s46, 14336\n\t" \
    "global_load_lds_dwordx4 v0, s[40:41] offset:-2048\n\t" \
    "global_load_lds_dwordx4 v1, s[40:41] offset:-1024\n\t" \
    "global_load_lds_dwordx4 v2, s[40:41] offset:0\n\t" \
    "global_load_lds_dwordx4 v3, s[40:41] offset:1024\n\t" \
    "global_load_lds_dwordx4 v4, s[40:41] offset:2048\n\t" \
    "s_cmp_eq_u32 s46, s52\n\t" \
    "s_cselect_b32 s46, s51, s46\n\t" \
    "s_add_u32 s40, s40, s49\n\t" \
    "s_addc_u32 s41, s41, 0\n\t" \
    "s_sub_u32 s54, s54, 1\n\t" \
    "s_cmp_eq_u32 s54, 0\n\t" \
    "s_cselect_b32 s50, s56, 0\n\t" \
    "s_sub_u32 s40, s40, s50\n\t" \
    "s_subb_u32 s41, s41, 0\n\t" \
    "s_add_u32 m0, s58, s53\n\t" \
    "s_nop 0\n\t" \
    "global_load_lds_dwordx4 v5, s[44:45] offset:0\n\t" \
    "global_load_lds_dwordx4 v6, s[44:45] offset:1024\n\t" \
    "global_load_lds_dwordx4 v7, s[44:45] offset:2048\n\t" \
    "s_add_u32 s53, s53, 16384\n\t" \
    "s_and_b32 s53, s53, 0xffff\n\t" \
    "s_add_u32 s42, s42, 128\n\t" \
    "s_addc_u32 s43, s43, 0\n\t" \
    "s_add_u32 s44, s44, 128\n\t" \
    "s_addc_u32 s45, s45, 0\n\t" \
    "s_sub_u32 s55, s55, 1\n\t" \
    "s_cmp_eq_u32 s55, 0\n\t" \
    "s_cselect_b32 s50, s57, 0\n\t" \
    "s_sub_u32 s42, s42, s50\n\t" \
    "s_subb_u32 s43, s43, 0\n\t" \
    "s_sub_u32 s44, s44, s50\n\t" \
    "s_subb_u32 s45, s45, 0\n\t" \
    "s_mov_b32 m0, s46\n\t" \
    "s_add_u32 s46, s46, 14336\n\t" \
    "global_load_lds_dwordx4 v0, s[40:41] offset:-2048\n\t" \
    "global_load_lds_dwordx4 v1, s[40:41] offset:-1024\n\t" \
    "global_load_lds_dwordx4 v2, s[40:41] offset:0\n\t" \
    "global_load_lds_dwordx4 v3, s[40:41] offset:1024\n\t" \
    "global_load_lds_dwordx4 v4, s[40:41] offset:2048\n\t" \
    "s_cmp_eq_u32 s46, s52\n\t" \
    "s_cselect_b32 s46, s51, s46\n\t" \
    "s_add_u32 s40, s40, s49\n\t" \
    "s_addc_u32 s41, s41, 0\n\t" \
    "s_sub_u32 s54, s54, 1\n\t" \
    "s_cmp_eq_u32 s54, 0\n\t" \
    "s_cselect_b32 s50, s56, 0\n\t" \
    "s_sub_u32 s40, s40, s50\n\t" \
    "s_subb_u32 s41, s41, 0\n\t" \
    "s_add_u32 m0, s47, s53\n\t" \
    "s_nop 0\n\t" \
    "global_load_lds_dwordx4 v5, s[42:43] offset:0\n\t" \
    "global_load_lds_dwordx4 v6, s[42:43] offset:1024\n\t" \
    "global_load_lds_dwordx4 v7, s[42:43] offset:2048\n\t" \
    "s_mov_b32 m0, s46\n\t" \
    "s_add_u32 s46, s46, 14336\n\t" \
    "global_load_lds_dwordx4 v0, s[40:41] offset:-2048\n\t" \
    "global_load_lds_dwordx4 v1, s[40:41] offset:-1024\n\t" \
    "global_load_lds_dwordx4 v2, s[40:41] offset:0\n\t" \
    "global_load_lds_dwordx4 v3, s[40:41] offset:1024\n\t" \
    "global_load_lds_dwordx4 v4, s[40:41] offset:2048\n\t" \
    "s_cmp_eq_u32 s46, s52\n\t" \
    "s_cselect_b32 s46, s51, s46\n\t" \
    "s_add_u32 s40, s40, s49\n\t" \
    "s_addc_u32 s41, s41, 0\n\t" \
    "s_sub_u32 s54, s54, 1\n\t" \
    "s_cmp_eq_u32 s54, 0\n\t" \
    "s_cselect_b32 s50, s56, 0\n\t" \
    "s_sub_u32 s40, s40, s50\n\t" \
    "s_subb_u32 s41, s41, 0\n\t" \
    "s_add_u32 m0, s58, s53\n\t" \
    "s_nop 0\n\t" \
    "global_load_lds_dwordx4 v5, s[44:45] offset:0\n\t" \
    "global_load_lds_dwordx4 v6, s[44:45] offset:1024\n\t" \
    "global_load_lds_dwordx4 v7, s[44:45] offset:2048\n\t" \
    "s_add_u32 s53, s53, 16384\n\t" \
    "s_and_b32 s53, s53, 0xffff\n\t" \
    "s_add_u32 s42, s42, 128\n\t" \
    "s_addc_u32 s43, s43, 0\n\t" \
    "s_add_u32 s44, s44, 128\n\t" \
    "s_addc_u32 s45, s45, 0\n\t" \
    "s_sub_u32 s55, s55, 1\n\t" \
    "s_cmp_eq_u32 s55, 0\n\t" \
    "s_cselect_b32 s50, s57, 0\n\t" \
    "s_sub_u32 s42, s42, s50\n\t" \
    "s_subb_u32 s43, s43, 0\n\t" \
    "s_sub_u32 s44, s44, s50\n\t" \
    "s_subb_u32 s45, s45, 0\n\t" \
    "s_waitcnt vmcnt(16)\n\t" \
    "s_barrier\n\t" \
    "s_waitcnt vmcnt(11)\n\t" \
    "s_barrier\n\t" \
    "s_mov_b32 m0, s46\n\t" \
    "s_add_u32 s46, s46, 14336\n\t" \
    "global_load_lds_dwordx4 v0, s[40:41] offset:-2048\n\t" \
    "global_load_lds_dwordx4 v1, s[40:41] offset:-1024\n\t" \
    "global_load_lds_dwordx4 v2, s[40:41] offset:0\n\t" \
    "global_load_lds_dwordx4 v3, s[40:41] offset:1024\n\t" \
    "global_load_lds_dwordx4 v4, s[40:41] offset:2048\n\t" \
    "s_cmp_eq_u32 s46, s52\n\t" \
    "s_cselect_b32 s46, s51, s46\n\t" \
    "s_add_u32 s40, s40, s49\n\t" \
    "s_addc_u32 s41, s41, 0\n\t" \
    "s_sub_u32 s54, s54, 1\n\t" \
    "s_cmp_eq_u32 s54, 0\n\t" \
    "s_cselect_b32 s50, s56, 0\n\t" \
    "s_sub_u32 s40, s40, s50\n\t" \
    "s_subb_u32 s41, s41, 0\n\t" \
    "s_add_u32 m0, s47, s53\n\t" \
    "s_nop 0\n\t" \
    "global_load_lds_dwordx4 v5, s[42:43] offset:0\n\t" \
    "global_load_lds_dwordx4 v6, s[42:43] offset:1024\n\t" \
    "global_load_lds_dwordx4 v7, s[42:43] offset:2048\n\t" \
    "s_mov_b32 m0, s46\n\t" \
    "s_add_u32 s46, s46, 14336\n\t" \
    "global_load_lds_dwordx4 v0, s[40:41] offset:-2048\n\t" \
    "global_load_lds_dwordx4 v1, s[40:41] offset:-1024\n\t" \
    "global_load_lds_dwordx4 v2, s[40:41] offset:0\n\t" \
    "global_load_lds_dwordx4 v3, s[40:41] offset:1024\n\t" \
    "global_load_lds_dwordx4 v4, s[40:41] offset:2048\n\t" \
    "s_cmp_eq_u32 s46, s52\n\t" \
    "s_cselect_b32 s46, s51, s46\n\t" \
    "s_add_u32 s40, s40, s49\n\t" \
    "s_addc_u32 s41, s41, 0\n\t" \
    "s_sub_u32 s54, s54, 1\n\t" \
    "s_cmp_eq_u32 s54, 0\n\t" \
    "s_cselect_b32 s50, s56, 0\n\t" \
    "s_sub_u32 s40, s40, s50\n\t" \
    "s_subb_u32 s41, s41, 0\n\t" \
    "s_waitcnt vmcnt(13)\n\t" \
    "s_barrier\n\t" \
    "s_add_u32 m0, s58, s53\n\t" \
    "s_nop 0\n\t" \
    "global_load_lds_dwordx4 v5, s[44:45] offset:0\n\t" \
    "global_load_lds_dwordx4 v6, s[44:45] offset:1024\n\t" \
    "global_load_lds_dwordx4 v7, s[44:45] offset:2048\n\t" \
    "s_add_u32 s53, s53, 16384\n\t" \
    "s_and_b32 s53, s53, 0xffff\n\t" \
    "s_add_u32 s42, s42, 128\n\t" \
    "s_addc_u32 s43, s43, 0\n\t" \
    "s_add_u32 s44, s44, 128\n\t" \
    "s_addc_u32 s45, s45, 0\n\t" \
    "s_sub_u32 s55, s55, 1\n\t" \
    "s_cmp_eq_u32 s55, 0\n\t" \
    "s_cselect_b32 s50, s57, 0\n\t" \
    "s_sub_u32 s42, s42, s50\n\t" \
    "s_subb_u32 s43, s43, 0\n\t" \
    "s_sub_u32 s44, s44, s50\n\t" \
    "s_subb_u32 s45, s45, 0\n\t" \
    "s_mov_b32 m0, s46\n\t" \
    "s_add_u32 s46, s46, 14336\n\t" \
    "global_load_lds_dwordx4 v0, s[40:41] offset:-2048\n\t" \
    "global_load_lds_dwordx4 v1, s[40:41] offset:-1024\n\t" \
    "global_load_lds_dwordx4 v2, s[40:41] offset:0\n\t" \
    "global_load_lds_dwordx4 v3, s[40:41] offset:1024\n\t" \
    "global_load_lds_dwordx4 v4, s[40:41] offset:2048\n\t" \
    "s_cmp_eq_u32 s46, s52\n\t" \
    "s_cselect_b32 s46, s51, s46\n\t" \
    "s_add_u32 s40, s40, s49\n\t" \
    "s_addc_u32 s41, s41, 0\n\t" \
    "s_sub_u32 s54, s54, 1\n\t" \
    "s_cmp_eq_u32 s54, 0\n\t" \
    "s_cselect_b32 s50, s56, 0\n\t" \
    "s_sub_u32 s40, s40, s50\n\t" \
    "s_subb_u32 s41, s41, 0\n\t" \
    "s_add_u32 m0, s47, s53\n\t" \
    "s_nop 0\n\t" \
    "global_load_lds_dwordx4 v5, s[42:43] offset:0\n\t" \
    "global_load_lds_dwordx4 v6, s[42:43] offset:1024\n\t" \
    "global_load_lds_dwordx4 v7, s[42:43] offset:2048\n\t" \
    "s_waitcnt vmcnt(19)\n\t" \
    "s_barrier\n\t" \
    "s_mov_b32 m0, s46\n\t" \
    "s_add_u32 s46, s46, 14336\n\t" \
    "global_load_lds_dwordx4 v0, s[40:41] offset:-2048\n\t" \
    "global_load_lds_dwordx4 v1, s[40:41] offset:-1024\n\t" \
    "global_load_lds_dwordx4 v2, s[40:41] offset:0\n\t" \
    "global_load_lds_dwordx4 v3, s[40:41] offset:1024\n\t" \
    "global_load_lds_dwordx4 v4, s[40:41] offset:2048\n\t" \
    "s_cmp_eq_u32 s46, s52\n\t" \
    "s_cselect_b32 s46, s51, s46\n\t" \
    "s_add_u32 s40, s40, s49\n\t" \
    "s_addc_u32 s41, s41, 0\n\t" \
    "s_sub_u32 s54, s54, 1\n\t" \
    "s_cmp_eq_u32 s54, 0\n\t" \
    "s_cselect_b32 s50, s56, 0\n\t" \
    "s_sub_u32 s40, s40, s50\n\t" \
    "s_subb_u32 s41, s41, 0\n\t" \
    "s_add_u32 m0, s58, s53\n\t" \
    "s_nop 0\n\t" \
    "global_load_lds_dwordx4 v5, s[44:45] offset:0\n\t" \
    "global_load_lds_dwordx4 v6, s[44:45] offset:1024\n\t" \
    "global_load_lds_dwordx4 v7, s[44:45] offset:2048\n\t" \
    "s_add_u32 s53, s53, 16384\n\t" \
    "s_and_b32 s53, s53, 0xffff\n\t" \
    "s_add_u32 s42, s42, 128\n\t" \
    "s_addc_u32 s43, s43, 0\n\t" \
    "s_add_u32 s44, s44, 128\n\t" \
    "s_addc_u32 s45, s45, 0\n\t" \
    "s_sub_u32 s55, s55, 1\n\t" \
    "s_cmp_eq_u32 s55, 0\n\t" \
    "s_cselect_b32 s50, s57, 0\n\t" \
    "s_sub_u32 s42, s42, s50\n\t" \
    "s_subb_u32 s43, s43, 0\n\t" \
    "s_sub_u32 s44, s44, s50\n\t" \
    "s_subb_u32 s45, s45, 0\n\t" \
    "s_mov_b32 m0, s46\n\t" \
    "s_add_u32 s46, s46, 14336\n\t" \
    "global_load_lds_dwordx4 v0, s[40:41] offset:-2048\n\t" \
    "global_load_lds_dwordx4 v1, s[40:41] offset:-1024\n\t" \
    "global_load_lds_dwordx4 v2, s[40:41] offset:0\n\t" \
    "global_load_lds_dwordx4 v3, s[40:41] offset:1024\n\t" \
    "global_load_lds_dwordx4 v4, s[40:41] offset:2048\n\t" \
    "s_cmp_eq_u32 s46, s52\n\t" \
    "s_cselect_b32 s46, s51, s46\n\t" \
    "s_add_u32 s40, s40, s49\n\t" \
    "s_addc_u32 s41, s41, 0\n\t" \
    "s_sub_u32 s54, s54, 1\n\t" \
    "s_cmp_eq_u32 s54, 0\n\t" \
    "s_cselect_b32 s50, s56, 0\n\t" \
    "s_sub_u32 s40, s40, s50\n\t" \
    "s_subb_u32 s41, s41, 0\n\t" \
    "s_waitcnt vmcnt(21)\n\t" \
    "s_barrier\n\t" \
    "s_add_u32 m0, s47, s53\n\t" \
    "s_nop 0\n\t" \
    "global_load_lds_dwordx4 v5, s[42:43] offset:0\n\t" \
    "global_load_lds_dwordx4 v6, s[42:43] offset:1024\n\t" \
    "global_load_lds_dwordx4 v7, s[42:43] offset:2048\n\t" \
    "s_mov_b32 m0, s46\n\t" \
    "s_add_u32 s46, s46, 14336\n\t" \
    "global_load_lds_dwordx4 v0, s[40:41] offset:-2048\n\t" \
    "global_load_lds_dwordx4 v1, s[40:41] offset:-1024\n\t" \
    "global_load_lds_dwordx4 v2, s[40:41] offset:0\n\t" \
    "global_load_lds_dwordx4 v3, s[40:41] offset:1024\n\t" \
    "global_load_lds_dwordx4 v4, s[40:41] offset:2048\n\t" \
    "s_cmp_eq_u32 s46, s52\n\t" \
    "s_cselect_b32 s46, s51, s46\n\t" \
    "s_add_u32 s40, s40, s49\n\t" \
    "s_addc_u32 s41, s41, 0\n\t" \
    "s_sub_u32 s54, s54, 1\n\t" \
    "s_cmp_eq_u32 s54, 0\n\t" \
    "s_cselect_b32 s50, s56, 0\n\t" \
    "s_sub_u32 s40, s40, s50\n\t" \
    "s_subb_u32 s41, s41, 0\n\t" \
    "s_add_u32 m0, s58, s53\n\t" \
    "s_nop 0\n\t" \
    "global_load_lds_dwordx4 v5, s[44:45] offset:0\n\t" \
    "global_load_lds_dwordx4 v6, s[44:45] offset:1024\n\t" \
    "global_load_lds_dwordx4 v7, s[44:45] offset:2048\n\t" \
    "s_add_u32 s53, s53, 16384\n\t" \
    "s_and_b32 s53, s53, 0xffff\n\t" \
    "s_add_u32 s42, s42, 128\n\t" \
    "s_addc_u32 s43, s43, 0\n\t" \
    "s_add_u32 s44, s44, 128\n\t" \
    "s_addc_u32 s45, s45, 0\n\t" \
    "s_sub_u32 s55, s55, 1\n\t" \
    "s_cmp_eq_u32 s55, 0\n\t" \
    "s_cselect_b32 s50, s57, 0\n\t" \
    "s_sub_u32 s42, s42, s50\n\t" \
    "s_subb_u32 s43, s43, 0\n\t" \
    "s_sub_u32 s44, s44, s50\n\t" \
    "s_subb_u32 s45, s45, 0\n\t" \
    "s_waitcnt vmcnt(27)\n\t" \
    "s_barrier\n\t" \
    "s_mov_b32 m0, s46\n\t" \
    "s_add_u32 s46, s46, 14336\n\t" \
    "global_load_lds_dwordx4 v0, s[40:41] offset:-2048\n\t" \
    "global_load_lds_dwordx4 v1, s[40:41] offset:-1024\n\t" \
    "global_load_lds_dwordx4 v2, s[40:41] offset:0\n\t" \
    "global_load_lds_dwordx4 v3, s[40:41] offset:1024\n\t" \
    "global_load_lds_dwordx4 v4, s[40:41] offset:2048\n\t" \
    "s_cmp_eq_u32 s46, s52\n\t" \
    "s_cselect_b32 s46, s51, s46\n\t" \
    "s_add_u32 s40, s40, s49\n\t" \
    "s_addc_u32 s41, s41, 0\n\t" \
    "s_sub_u32 s54, s54, 1\n\t" \
    "s_cmp_eq_u32 s54, 0\n\t" \
    "s_cselect_b32 s50, s56, 0\n\t" \
    "s_sub_u32 s40, s40, s50\n\t" \
    "s_subb_u32 s41, s41, 0\n\t" \
    "s_add_u32 m0, s47, s53\n\t" \
    "s_nop 0\n\t" \
    "global_load_lds_dwordx4 v5, s[42:43] offset:0\n\t" \
    "global_load_lds_dwordx4 v6, s[42:43] offset:1024\n\t" \
    "global_load_lds_dwordx4 v7, s[42:43] offset:2048\n\t" \
    "s_mov_b32 m0, s46\n\t" \
    "s_add_u32 s46, s46, 14336\n\t" \
    "global_load_lds_dwordx4 v0, s[40:41] offset:-2048\n\t" \
    "global_load_lds_dwordx4 v1, s[40:41] offset:-1024\n\t" \
    "global_load_lds_dwordx4 v2, s[40:41] offset:0\n\t" \
    "global_load_lds_dwordx4 v3, s[40:41] offset:1024\n\t" \
    "global_load_lds_dwordx4 v4, s[40:41] offset:2048\n\t" \
    "s_cmp_eq_u32 s46, s52\n\t" \
    "s_cselect_b32 s46, s51, s46\n\t" \
    "s_add_u32 s40, s40, s49\n\t" \
    "s_addc_u32 s41, s41, 0\n\t" \
    "s_sub_u32 s54, s54, 1\n\t" \
    "s_cmp_eq_u32 s54, 0\n\t" \
    "s_cselect_b32 s50, s56, 0\n\t" \
    "s_sub_u32 s40, s40, s50\n\t" \
    "s_subb_u32 s41, s41, 0\n\t" \
    "s_waitcnt vmcnt(29)\n\t" \
    "s_barrier\n\t" \
    "s_add_u32 m0, s58, s53\n\t" \
    "s_nop 0\n\t" \
    "global_load_lds_dwordx4 v5, s[44:45] offset:0\n\t" \
    "global_load_lds_dwordx4 v6, s[44:45] offset:1024\n\t" \
    "global_load_lds_dwordx4 v7, s[44:45] offset:2048\n\t" \
    "s_add_u32 s53, s53, 16384\n\t" \
    "s_and_b32 s53, s53, 0xffff\n\t" \
    "s_add_u32 s42, s42, 128\n\t" \
    "s_addc_u32 s43, s43, 0\n\t" \
    "s_add_u32 s44, s44, 128\n\t" \
    "s_addc_u32 s45, s45, 0\n\t" \
    "s_sub_u32 s55, s55, 1\n\t" \
    "s_cmp_eq_u32 s55, 0\n\t" \
    "s_cselect_b32 s50, s57, 0\n\t" \
    "s_sub_u32 s42, s42, s50\n\t" \
    "s_subb_u32 s43, s43, 0\n\t" \
    "s_sub_u32 s44, s44, s50\n\t" \
    "s_subb_u32 s45, s45, 0\n\t" \
    "s_mov_b32 m0, s46\n\t" \
    "s_add_u32 s46, s46, 14336\n\t" \
    "global_load_lds_dwordx4 v0, s[40:41] offset:-2048\n\t" \
    "global_load_lds_dwordx4 v1, s[40:41] offset:-1024\n\t" \
    "global_load_lds_dwordx4 v2, s[40:41] offset:0\n\t" \
    "global_load_lds_dwordx4 v3, s[40:41] offset:1024\n\t" \
    "global_load_lds_dwordx4 v4, s[40:41] offset:2048\n\t" \
    "s_cmp_eq_u32 s46, s52\n\t" \
    "s_cselect_b32 s46, s51, s46\n\t" \
    "s_add_u32 s40, s40, s49\n\t" \
    "s_addc_u32 s41, s41, 0\n\t" \
    "s_sub_u32 s54, s54, 1\n\t" \
    "s_cmp_eq_u32 s54, 0\n\t" \
    "s_cselect_b32 s50, s56, 0\n\t" \
    "s_sub_u32 s40, s40, s50\n\t" \
    "s_subb_u32 s41, s41, 0\n\t" \
    "s_add_u32 m0, s47, s53\n\t" \
    "s_nop 0\n\t" \
    "global_load_lds_dwordx4 v5, s[42:43] offset:0\n\t" \
    "global_load_lds_dwordx4 v6, s[42:43] offset:1024\n\t" \
    "global_load_lds_dwordx4 v7, s[42:43] offset:2048\n\t" \
    "s_waitcnt vmcnt(32)\n\t" \
    "s_barrier\n\t" \
    "s_mov_b32 m0, s46\n\t" \
    "s_add_u32 s46, s46, 14336\n\t" \
    "global_load_lds_dwordx4 v0, s[40:41] offset:-2048\n\t" \
    "global_load_lds_dwordx4 v1, s[40:41] offset:-1024\n\t" \
    "global_load_lds_dwordx4 v2, s[40:41] offset:0\n\t" \
    "global_load_lds_dwordx4 v3, s[40:41] offset:1024\n\t" \
    "global_load_lds_dwordx4 v4, s[40:41] offset:2048\n\t" \
    "s_cmp_eq_u32 s46, s52\n\t" \
    "s_cselect_b32 s46, s51, s46\n\t" \
    "s_add_u32 s40, s40, s49\n\t" \
    "s_addc_u32 s41, s41, 0\n\t" \
    "s_sub_u32 s54, s54, 1\n\t" \
    "s_cmp_eq_u32 s54, 0\n\t" \
    "s_cselect_b32 s50, s56, 0\n\t" \
    "s_sub_u32 s40, s40, s50\n\t" \
    "s_subb_u32 s41, s41, 0\n\t" \
    "s_add_u32 m0, s58, s53\n\t" \
    "s_nop 0\n\t" \
    "global_load_lds_dwordx4 v5, s[44:45] offset:0\n\t" \
    "global_load_lds_dwordx4 v6, s[44:45] offset:1024\n\t" \
    "global_load_lds_dwordx4 v7, s[44:45] offset:2048\n\t" \
    "s_add_u32 s53, s53, 16384\n\t" \
    "s_and_b32 s53, s53, 0xffff\n\t" \
    "s_add_u32 s42, s42, 128\n\t" \
    "s_addc_u32 s43, s43, 0\n\t" \
    "s_add_u32 s44, s44, 128\n\t" \
    "s_addc_u32 s45, s45, 0\n\t" \
    "s_sub_u32 s55, s55, 1\n\t" \
    "s_cmp_eq_u32 s55, 0\n\t" \
    "s_cselect_b32 s50, s57, 0\n\t" \
    "s_sub_u32 s42, s42, s50\n\t" \
    "s_subb_u32 s43, s43, 0\n\t" \
    "s_sub_u32 s44, s44, s50\n\t" \
    "s_subb_u32 s45, s45, 0\n\t" \
    "s_waitcnt vmcnt(32)\n\t" \
    "s_barrier\n\t" \
    "s_cmp_eq_u32 s48, 0\n\t" \
    "s_cbranch_scc1 2f\n\t" \
    "1:\n\t" \
    "s_mov_b32 m0, s46\n\t" \
    "s_add_u32 s46, s46, 14336\n\t" \
    "global_load_lds_dwordx4 v0, s[40:41] offset:-2048\n\t" \
    "global_load_lds_dwordx4 v1, s[40:41] offset:-1024\n\t" \
    "global_load_lds_dwordx4 v2, s[40:41] offset:0\n\t" \
    "global_load_lds_dwordx4 v3, s[40:41] offset:1024\n\t" \
    "global_load_lds_dwordx4 v4, s[40:41] offset:2048\n\t" \
    "s_cmp_eq_u32 s46, s52\n\t" \
    "s_cselect_b32 s46, s51, s46\n\t" \
    "s_add_u32 s40, s40, s49\n\t" \
    "s_addc_u32 s41, s41, 0\n\t" \
    "s_sub_u32 s54, s54, 1\n\t" \
    "s_cmp_eq_u32 s54, 0\n\t" \
    "s_cselect_b32 s50, s56, 0\n\t" \
    "s_sub_u32 s40, s40, s50\n\t" \
    "s_subb_u32 s41, s41, 0\n\t" \
    "s_add_u32 m0, s47, s53\n\t" \
    "s_nop 0\n\t" \
    "global_load_lds_dwordx4 v5, s[42:43] offset:0\n\t" \
    "global_load_lds_dwordx4 v6, s[42:43] offset:1024\n\t" \
    "global_load_lds_dwordx4 v7, s[42:43] offset:2048\n\t" \
    "s_waitcnt vmcnt(32)\n\t" \
    "s_barrier\n\t" \
    "s_mov_b32 m0, s46\n\t" \
    "s_add_u32 s46, s46, 14336\n\t" \
    "global_load_lds_dwordx4 v0, s[40:41] offset:-2048\n\t" \
    "global_load_lds_dwordx4 v1, s[40:41] offset:-1024\n\t" \
    "global_load_lds_dwordx4 v2, s[40:41] offset:0\n\t" \
    "global_load_lds_dwordx4 v3, s[40:41] offset:1024\n\t" \
    "global_load_lds_dwordx4 v4, s[40:41] offset:2048\n\t" \
    "s_cmp_eq_u32 s46, s52\n\t" \
    "s_cselect_b32 s46, s51, s46\n\t" \
    "s_add_u32 s40, s40, s49\n\t" \
    "s_addc_u32 s41, s41, 0\n\t" \
    "s_sub_u32 s54, s54, 1\n\t" \
    "s_cmp_eq_u32 s54, 0\n\t" \
    "s_cselect_b32 s50, s56, 0\n\t" \
    "s_sub_u32 s40, s40, s50\n\t" \
    "s_subb_u32 s41, s41, 0\n\t" \
    "s_add_u32 m0, s58, s53\n\t" \
    "s_nop 0\n\t" \
    "global_load_lds_dwordx4 v5, s[44:45] offset:0\n\t" \
    "global_load_lds_dwordx4 v6, s[44:45] offset:1024\n\t" \
    "global_load_lds_dwordx4 v7, s[44:45] offset:2048\n\t" \
    "s_add_u32 s53, s53, 16384\n\t" \
    "s_and_b32 s53, s53, 0xffff\n\t" \
    "s_add_u32 s42, s42, 128\n\t" \
    "s_addc_u32 s43, s43, 0\n\t" \
    "s_add_u32 s44, s44, 128\n\t" \
    "s_addc_u32 s45, s45, 0\n\t" \
    "s_sub_u32 s55, s55, 1\n\t" \
    "s_cmp_eq_u32 s55, 0\n\t" \
    "s_cselect_b32 s50, s57, 0\n\t" \
    "s_sub_u32 s42, s42, s50\n\t" \
    "s_subb_u32 s43, s43, 0\n\t" \
    "s_sub_u32 s44, s44, s50\n\t" \
    "s_subb_u32 s45, s45, 0\n\t" \
    "s_sub_u32 s48, s48, 1\n\t" \
    "s_cmp_lg_u32 s48, 0\n\t" \
    "s_waitcnt vmcnt(32)\n\t" \
    "s_barrier\n\t" \
    "s_cbranch_scc1 1b\n\t" \
    "2:\n\t" \
    "s_mov_b32 m0, s46\n\t" \
    "s_add_u32 s46, s46, 14336\n\t" \
    "global_load_lds_dwordx4 v0, s[40:41] offset:-2048\n\t" \
    "global_load_lds_dwordx4 v1, s[40:41] offset:-1024\n\t" \
    "global_load_lds_dwordx4 v2, s[40:41] offset:0\n\t" \
    "global_load_lds_dwordx4 v3, s[40:41] offset:1024\n\t" \
    "global_load_lds_dwordx4 v4, s[40:41] offset:2048\n\t" \
    "s_cmp_eq_u32 s46, s52\n\t" \
    "s_cselect_b32 s46, s51, s46\n\t" \
    "s_add_u32 s40, s40, s49\n\t" \
    "s_addc_u32 s41, s41, 0\n\t" \
    "s_sub_u32 s54, s54, 1\n\t" \
    "s_cmp_eq_u32 s54, 0\n\t" \
    "s_cselect_b32 s50, s56, 0\n\t" \
    "s_sub_u32 s40, s40, s50\n\t" \
    "s_subb_u32 s41, s41, 0\n\t" \
    ""
#define LOADER42 \
    "s_add_u32 m0, s47, s53\n\t" \
    "s_nop 0\n\t" \
    "global_load_lds_dwordx4 v5, s[42:43] offset:0\n\t" \
    "global_load_lds_dwordx4 v6, s[42:43] offset:1024\n\t" \
    "s_mov_b32 m0, s46\n\t" \
    "s_add_u32 s46, s46, 14336\n\t" \
    "global_load_lds_dwordx4 v0, s[40:41] offset:-2048\n\t" \
    "global_load_lds_dwordx4 v1, s[40:41] offset:-1024\n\t" \
    "global_load_lds_dwordx4 v2, s[40:41] offset:0\n\t" \
    "global_load_lds_dwordx4 v3, s[40:41] offset:1024\n\t" \
    "s_cmp_eq_u32 s46, s52\n\t" \
    "s_cselect_b32 s46, s51, s46\n\t" \
    "s_add_u32 s40, s40, s49\n\t" \
    "s_addc_u32 s41, s41, 0\n\t" \
    "s_sub_u32 s54, s54, 1\n\t" \
    "s_cmp_eq_u32 s54, 0\n\t" \
    "s_cselect_b32 s50, s56, 0\n\t" \
    "s_sub_u32 s40, s40, s50\n\t" \
    "s_subb_u32 s41, s41, 0\n\t" \
    "s_add_u32 m0, s58, s53\n\t" \
    "s_nop 0\n\t" \
    "global_load_lds_dwordx4 v5, s[44:45] offset:0\n\t" \
    "global_load_lds_dwordx4 v6, s[44:45] offset:1024\n\t" \
    "s_add_u32 s53, s53, 16384\n\t" \
    "s_and_b32 s53, s53, 0xffff\n\t" \
    "s_add_u32 s42, s42, 128\n\t" \
    "s_addc_u32 s43, s43, 0\n\t" \
    "s_add_u32 s44, s44, 128\n\t" \
    "s_addc_u32 s45, s45, 0\n\t" \
    "s_sub_u32 s55, s55, 1\n\t" \
    "s_cmp_eq_u32 s55, 0\n\t" \
    "s_cselect_b32 s50, s57, 0\n\t" \
    "s_sub_u32 s42, s42, s50\n\t" \
    "s_subb_u32 s43, s43, 0\n\t" \
    "s_sub_u32 s44, s44, s50\n\t" \
    "s_subb_u32 s45, s45, 0\n\t" \
    "s_mov_b32 m0, s46\n\t" \
    "s_add_u32 s46, s46, 14336\n\t" \
    "global_load_lds_dwordx4 v0, s[40:41] offset:-2048\n\t" \
    "global_load_lds_dwordx4 v1, s[40:41] offset:-1024\n\t" \
    "global_load_lds_dwordx4 v2, s[40:41] offset:0\n\t" \
    "global_load_lds_dwordx4 v3, s[40:41] offset:1024\n\t" \
    "s_cmp_eq_u32 s46, s52\n\t" \
    "s_cselect_b32 s46, s51, s46\n\t" \
    "s_add_u32 s40, s40, s49\n\t" \
    "s_addc_u32 s41, s41, 0\n\t" \
    "s_sub_u32 s54, s54, 1\n\t" \
    "s_cmp_eq_u32 s54, 0\n\t" \
    "s_cselect_b32 s50, s56, 0\n\t" \
    "s_sub_u32 s40, s40, s50\n\t" \
    "s_subb_u32 s41, s41, 0\n\t" \
    "s_add_u32 m0, s47, s53\n\t" \
    "s_nop 0\n\t" \
    "global_load_lds_dwordx4 v5, s[42:43] offset:0\n\t" \
    "global_load_lds_dwordx4 v6, s[42:43] offset:1024\n\t" \
    "s_mov_b32 m0, s46\n\t" \
    "s_add_u32 s46, s46, 14336\n\t" \
    "global_load_lds_dwordx4 v0, s[40:41] offset:-2048\n\t" \
    "global_load_lds_dwordx4 v1, s[40:41] offset:-1024\n\t" \
    "global_load_lds_dwordx4 v2, s[40:41] offset:0\n\t" \
    "global_load_lds_dwordx4 v3, s[40:41] offset:1024\n\t" \
    "s_cmp_eq_u32 s46, s52\n\t" \
    "s_cselect_b32 s46, s51, s46\n\t" \
    "s_add_u32 s40, s40, s49\n\t" \
    "s_addc_u32 s41, s41, 0\n\t" \
    "s_sub_u32 s54, s54, 1\n\t" \
    "s_cmp_eq_u32 s54, 0\n\t" \
    "s_cselect_b32 s50, s56, 0\n\t" \
    "s_sub_u32 s40, s40, s50\n\t" \
    "s_subb_u32 s41, s41, 0\n\t" \
    "s_add_u32 m0, s58, s53\n\t" \
    "s_nop 0\n\t" \
    "global_load_lds_dwordx4 v5, s[44:45] offset:0\n\t" \
    "global_load_lds_dwordx4 v6, s[44:45] offset:1024\n\t" \
    "s_add_u32 s53, s53, 16384\n\t" \
    "s_and_b32 s53, s53, 0xffff\n\t" \
    "s_add_u32 s42, s42, 128\n\t" \
    "s_addc_u32 s43, s43, 0\n\t" \
    "s_add_u32 s44, s44, 128\n\t" \
    "s_addc_u32 s45, s45, 0\n\t" \
    "s_sub_u32 s55, s55, 1\n\t" \
    "s_cmp_eq_u32 s55, 0\n\t" \
    "s_cselect_b32 s50, s57, 0\n\t" \
    "s_sub_u32 s42, s42, s50\n\t" \
    "s_subb_u32 s43, s43, 0\n\t" \
    "s_sub_u32 s44, s44, s50\n\t" \
    "s_subb_u32 s45, s45, 0\n\t" \
    "s_waitcnt vmcnt(12)\n\t" \
    "s_barrier\n\t" \
    "s_waitcnt vmcnt(8)\n\t" \
    "s_barrier\n\t" \
    "s_mov_b32 m0, s46\n\t" \
    "s_add_u32 s46, s46, 14336\n\t" \
    "global_load_lds_dwordx4 v0, s[40:41] offset:-2048\n\t" \
    "global_load_lds_dwordx4 v1, s[40:41] offset:-1024\n\t" \
    "global_load_lds_dwordx4 v2, s[40:41] offset:0\n\t" \
    "global_load_lds_dwordx4 v3, s[40:41] offset:1024\n\t" \
    "s_cmp_eq_u32 s46, s52\n\t" \
    "s_cselect_b32 s46, s51, s46\n\t" \
    "s_add_u32 s40, s40, s49\n\t" \
    "s_addc_u32 s41, s41, 0\n\t" \
    "s_sub_u32 s54, s54, 1\n\t" \
    "s_cmp_eq_u32 s54, 0\n\t" \
    "s_cselect_b32 s50, s56, 0\n\t" \
    "s_sub_u32 s40, s40, s50\n\t" \
    "s_subb_u32 s41, s41, 0\n\t" \
    "s_add_u32 m0, s47, s53\n\t" \
    "s_nop 0\n\t" \
    "global_load_lds_dwordx4 v5, s[42:43] offset:0\n\t" \
    "global_load_lds_dwordx4 v6, s[42:43] offset:1024\n\t" \
    "s_mov_b32 m0, s46\n\t" \
    "s_add_u32 s46, s46, 14336\n\t" \
    "global_load_lds_dwordx4 v0, s[40:41] offset:-2048\n\t" \
    "global_load_lds_dwordx4 v1, s[40:41] offset:-1024\n\t" \
    "global_load_lds_dwordx4 v2, s[40:41] offset:0\n\t" \
    "global_load_lds_dwordx4 v3, s[40:41] offset:1024\n\t" \
    "s_cmp_eq_u32 s46, s52\n\t" \
    "s_cselect_b32 s46, s51, s46\n\t" \
    "s_add_u32 s40, s40, s49\n\t" \
    "s_addc_u32 s41, s41, 0\n\t" \
    "s_sub_u32 s54, s54, 1\n\t" \
    "s_cmp_eq_u32 s54, 0\n\t" \
    "s_cselect_b32 s50, s56, 0\n\t" \
    "s_sub_u32 s40, s40, s50\n\t" \
    "s_subb_u32 s41, s41, 0\n\t" \
    "s_waitcnt vmcnt(10)\n\t" \
    "s_barrier\n\t" \
    "s_add_u32 m0, s58, s53\n\t" \
    "s_nop 0\n\t" \
    "global_load_lds_dwordx4 v5, s[44:45] offset:0\n\t" \
    "global_load_lds_dwordx4 v6, s[44:45] offset:1024\n\t" \
    "s_add_u32 s53, s53, 16384\n\t" \
    "s_and_b32 s53, s53, 0xffff\n\t" \
    "s_add_u32 s42, s42, 128\n\t" \
    "s_addc_u32 s43, s43, 0\n\t" \
    "s_add_u32 s44, s44, 128\n\t" \
    "s_addc_u32 s45, s45, 0\n\t" \
    "s_sub_u32 s55, s55, 1\n\t" \
    "s_cmp_eq_u32 s55, 0\n\t" \
    "s_cselect_b32 s50, s57, 0\n\t" \
    "s_sub_u32 s42, s42, s50\n\t" \
    "s_subb_u32 s43, s43, 0\n\t" \
    "s_sub_u32 s44, s44, s50\n\t" \
    "s_subb_u32 s45, s45, 0\n\t" \
    "s_mov_b32 m0, s46\n\t" \
    "s_add_u32 s46, s46, 14336\n\t" \
    "global_load_lds_dwordx4 v0, s[40:41] offset:-2048\n\t" \
    "global_load_lds_dwordx4 v1, s[40:41] offset:-1024\n\t" \
    "global_load_lds_dwordx4 v2, s[40:41] offset:0\n\t" \
    "global_load_lds_dwordx4 v3, s[40:41] offset:1024\n\t" \
    "s_cmp_eq_u32 s46, s52\n\t" \
    "s_cselect_b32 s46, s51, s46\n\t" \
    "s_add_u32 s40, s40, s49\n\t" \
    "s_addc_u32 s41, s41, 0\n\t" \
    "s_sub_u32 s54, s54, 1\n\t" \
    "s_cmp_eq_u32 s54, 0\n\t" \
    "s_cselect_b32 s50, s56, 0\n\t" \
    "s_sub_u32 s40, s40, s50\n\t" \
    "s_subb_u32 s41, s41, 0\n\t" \
    "s_add_u32 m0, s47, s53\n\t" \
    "s_nop 0\n\t" \
    "global_load_lds_dwordx4 v5, s[42:43] offset:0\n\t" \
    "global_load_lds_dwordx4 v6, s[42:43] offset:1024\n\t" \
    "s_waitcnt vmcnt(14)\n\t" \
    "s_barrier\n\t" \
    "s_mov_b32 m0, s46\n\t" \
    "s_add_u32 s46, s46, 14336\n\t" \
    "global_load_lds_dwordx4 v0, s[40:41] offset:-2048\n\t" \
    "global_load_lds_dwordx4 v1, s[40:41] offset:-1024\n\t" \
    "global_load_lds_dwordx4 v2, s[40:41] offset:0\n\t" \
    "global_load_lds_dwordx4 v3, s[40:41] offset:1024\n\t" \
    "s_cmp_eq_u32 s46, s52\n\t" \
    "s_cselect_b32 s46, s51, s46\n\t" \
    "s_add_u32 s40, s40, s49\n\t" \
    "s_addc_u32 s41, s41, 0\n\t" \
    "s_sub_u32 s54, s54, 1\n\t" \
    "s_cmp_eq_u32 s54, 0\n\t" \
    "s_cselect_b32 s50, s56, 0\n\t" \
    "s_sub_u32 s40, s40, s50\n\t" \
    "s_subb_u32 s41, s41, 0\n\t" \
    "s_add_u32 m0, s58, s53\n\t" \
    "s_nop 0\n\t" \
    "global_load_lds_dwordx4 v5, s[44:45] offset:0\n\t" \
    "global_load_lds_dwordx4 v6, s[44:45] offset:1024\n\t" \
    "s_add_u32 s53, s53, 16384\n\t" \
    "s_and_b32 s53, s53, 0xffff\n\t" \
    "s_add_u32 s42, s42, 128\n\t" \
    "s_addc_u32 s43, s43, 0\n\t" \
    "s_add_u32 s44, s44, 128\n\t" \
    "s_addc_u32 s45, s45, 0\n\t" \
    "s_sub_u32 s55, s55, 1\n\t" \
    "s_cmp_eq_u32 s55, 0\n\t" \
    "s_cselect_b32 s50, s57, 0\n\t" \
    "s_sub_u32 s42, s42, s50\n\t" \
    "s_subb_u32 s43, s43, 0\n\t" \
    "s_sub_u32 s44, s44, s50\n\t" \
    "s_subb_u32 s45, s45, 0\n\t" \
    "s_mov_b32 m0, s46\n\t" \
    "s_add_u32 s46, s46, 14336\n\t" \
    "global_load_lds_dwordx4 v0, s[40:41] offset:-2048\n\t" \
    "global_load_lds_dwordx4 v1, s[40:41] offset:-1024\n\t" \
    "global_load_lds_dwordx4 v2, s[40:41] offset:0\n\t" \
    "global_load_lds_dwordx4 v3, s[40:41] offset:1024\n\t" \
    "s_cmp_eq_u32 s46, s52\n\t" \
    "s_cselect_b32 s46, s51, s46\n\t" \
    "s_add_u32 s40, s40, s49\n\t" \
    "s_addc_u32 s41, s41, 0\n\t" \
    "s_sub_u32 s54, s54, 1\n\t" \
    "s_cmp_eq_u32 s54, 0\n\t" \
    "s_cselect_b32 s50, s56, 0\n\t" \
    "s_sub_u32 s40, s40, s50\n\t" \
    "s_subb_u32 s41, s41, 0\n\t" \
    "s_waitcnt vmcnt(16)\n\t" \
    "s_barrier\n\t" \
    "s_add_u32 m0, s47, s53\n\t" \
    "s_nop 0\n\t" \
    "global_load_lds_dwordx4 v5, s[42:43] offset:0\n\t" \
    "global_load_lds_dwordx4 v6, s[42:43] offset:1024\n\t" \
    "s_mov_b32 m0, s46\n\t" \
    "s_add_u32 s46, s46, 14336\n\t" \
    "global_load_lds_dwordx4 v0, s[40:41] offset:-2048\n\t" \
    "global_load_lds_dwordx4 v1, s[40:41] offset:-1024\n\t" \
    "global_load_lds_dwordx4 v2, s[40:41] offset:0\n\t" \
    "global_load_lds_dwordx4 v3, s[40:41] offset:1024\n\t" \
    "s_cmp_eq_u32 s46, s52\n\t" \
    "s_cselect_b32 s46, s51, s46\n\t" \
    "s_add_u32 s40, s40, s49\n\t" \
    "s_addc_u32 s41, s41, 0\n\t" \
    "s_sub_u32 s54, s54, 1\n\t" \
    "s_cmp_eq_u32 s54, 0\n\t" \
    "s_cselect_b32 s50, s56, 0\n\t" \
    "s_sub_u32 s40, s40, s50\n\t" \
    "s_subb_u32 s41, s41, 0\n\t" \
    "s_add_u32 m0, s58, s53\n\t" \
    "s_nop 0\n\t" \
    "global_load_lds_dwordx4 v5, s[44:45] offset:0\n\t" \
    "global_load_lds_dwordx4 v6, s[44:45] offset:1024\n\t" \
    "s_add_u32 s53, s53, 16384\n\t" \
    "s_and_b32 s53, s53, 0xffff\n\t" \
    "s_add_u32 s42, s42, 128\n\t" \
    "s_addc_u32 s43, s43, 0\n\t" \
    "s_add_u32 s44, s44, 128\n\t" \
    "s_addc_u32 s45, s45, 0\n\t" \
    "s_sub_u32 s55, s55, 1\n\t" \
    "s_cmp_eq_u32 s55, 0\n\t" \
    "s_cselect_b32 s50, s57, 0\n\t" \
    "s_sub_u32 s42, s42, s50\n\t" \
    "s_subb_u32 s43, s43, 0\n\t" \
    "s_sub_u32 s44, s44, s50\n\t" \
    "s_subb_u32 s45, s45, 0\n\t" \
    "s_waitcnt vmcnt(20)\n\t" \
    "s_barrier\n\t" \
    "s_mov_b32 m0, s46\n\t" \
    "s_add_u32 s46, s46, 14336\n\t" \
    "global_load_lds_dwordx4 v0, s[40:41] offset:-2048\n\t" \
    "global_load_lds_dwordx4 v1, s[40:41] offset:-1024\n\t" \
    "global_load_lds_dwordx4 v2, s[40:41] offset:0\n\t" \
    "global_load_lds_dwordx4 v3, s[40:41] offset:1024\n\t" \
    "s_cmp_eq_u32 s46, s52\n\t" \
    "s_cselect_b32 s46, s51, s46\n\t" \
    "s_add_u32 s40, s40, s49\n\t" \
    "s_addc_u32 s41, s41, 0\n\t" \
    "s_sub_u32 s54, s54, 1\n\t" \
    "s_cmp_eq_u32 s54, 0\n\t" \
    "s_cselect_b32 s50, s56, 0\n\t" \
    "s_sub_u32 s40, s40, s50\n\t" \
    "s_subb_u32 s41, s41, 0\n\t" \
    "s_add_u32 m0, s47, s53\n\t" \
    "s_nop 0\n\t" \
    "global_load_lds_dwordx4 v5, s[42:43] offset:0\n\t" \
    "global_load_lds_dwordx4 v6, s[42:43] offset:1024\n\t" \
    "s_mov_b32 m0, s46\n\t" \
    "s_add_u32 s46, s46, 14336\n\t" \
    "global_load_lds_dwordx4 v0, s[40:41] offset:-2048\n\t" \
    "global_load_lds_dwordx4 v1, s[40:41] offset:-1024\n\t" \
    "global_load_lds_dwordx4 v2, s[40:41] offset:0\n\t" \
    "global_load_lds_dwordx4 v3, s[40:41] offset:1024\n\t" \
    "s_cmp_eq_u32 s46, s52\n\t" \
    "s_cselect_b32 s46, s51, s46\n\t" \
    "s_add_u32 s40, s40, s49\n\t" \
    "s_addc_u32 s41, s41, 0\n\t" \
    "s_sub_u32 s54, s54, 1\n\t" \
    "s_cmp_eq_u32 s54, 0\n\t" \
    "s_cselect_b32 s50, s56, 0\n\t" \
    "s_sub_u32 s40, s40, s50\n\t" \
    "s_subb_u32 s41, s41, 0\n\t" \
    "s_waitcnt vmcnt(22)\n\t" \
    "s_barrier\n\t" \
    "s_add_u32 m0, s58, s53\n\t" \
    "s_nop 0\n\t" \
    "global_load_lds_dwordx4 v5, s[44:45] offset:0\n\t" \
    "global_load_lds_dwordx4 v6, s[44:45] offset:1024\n\t" \
    "s_add_u32 s53, s53, 16384\n\t" \
    "s_and_b32 s53, s53, 0xffff\n\t" \
    "s_add_u32 s42, s42, 128\n\t" \
    "s_addc_u32 s43, s43, 0\n\t" \
    "s_add_u32 s44, s44, 128\n\t" \
    "s_addc_u32 s45, s45, 0\n\t" \
    "s_sub_u32 s55, s55, 1\n\t" \
    "s_cmp_eq_u32 s55, 0\n\t" \
    "s_cselect_b32 s50, s57, 0\n\t" \
    "s_sub_u32 s42, s42, s50\n\t" \
    "s_subb_u32 s43, s43, 0\n\t" \
    "s_sub_u32 s44, s44, s50\n\t" \
    "s_subb_u32 s45, s45, 0\n\t" \
    "s_mov_b32 m0, s46\n\t" \
    "s_add_u32 s46, s46, 14336\n\t" \
    "global_load_lds_dwordx4 v0, s[40:41] offset:-2048\n\t" \
    "global_load_lds_dwordx4 v1, s[40:41] offset:-1024\n\t" \
    "global_load_lds_dwordx4 v2, s[40:41] offset:0\n\t" \
    "global_load_lds_dwordx4 v3, s[40:41] offset:1024\n\t" \
    "s_cmp_eq_u32 s46, s52\n\t" \
    "s_cselect_b32 s46, s51, s46\n\t" \
    "s_add_u32 s40, s40, s49\n\t" \
    "s_addc_u32 s41, s41, 0\n\t" \
    "s_sub_u32 s54, s54, 1\n\t" \
    "s_cmp_eq_u32 s54, 0\n\t" \
    "s_cselect_b32 s50, s56, 0\n\t" \
    "s_sub_u32 s40, s40, s50\n\t" \
    "s_subb_u32 s41, s41, 0\n\t" \
    "s_add_u32 m0, s47, s53\n\t" \
    "s_nop 0\n\t" \
    "global_load_lds_dwordx4 v5, s[42:43] offset:0\n\t" \
    "global_load_lds_dwordx4 v6, s[42:43] offset:1024\n\t" \
    "s_waitcnt vmcnt(24)\n\t" \
    "s_barrier\n\t" \
    "s_mov_b32 m0, s46\n\t" \
    "s_add_u32 s46, s46, 14336\n\t" \
    "global_load_lds_dwordx4 v0, s[40:41] offset:-2048\n\t" \
    "global_load_lds_dwordx4 v1, s[40:41] offset:-1024\n\t" \
    "global_load_lds_dwordx4 v2, s[40:41] offset:0\n\t" \
    "global_load_lds_dwordx4 v3, s[40:41] offset:1024\n\t" \
    "s_cmp_eq_u32 s46, s52\n\t" \
    "s_cselect_b32 s46, s51, s46\n\t" \
    "s_add_u32 s40, s40, s49\n\t" \
    "s_addc_u32 s41, s41, 0\n\t" \
    "s_sub_u32 s54, s54, 1\n\t" \
    "s_cmp_eq_u32 s54, 0\n\t" \
    "s_cselect_b32 s50, s56, 0\n\t" \
    "s_sub_u32 s40, s40, s50\n\t" \
    "s_subb_u32 s41, s41, 0\n\t" \
    "s_add_u32 m0, s58, s53\n\t" \
    "s_nop 0\n\t" \
    "global_load_lds_dwordx4 v5, s[44:45] offset:0\n\t" \
    "global_load_lds_dwordx4 v6, s[44:45] offset:1024\n\t" \
    "s_add_u32 s53, s53, 16384\n\t" \
    "s_and_b32 s53, s53, 0xffff\n\t" \
    "s_add_u32 s42, s42, 128\n\t" \
    "s_addc_u32 s43, s43, 0\n\t" \
    "s_add_u32 s44, s44, 128\n\t" \
    "s_addc_u32 s45, s45, 0\n\t" \
    "s_sub_u32 s55, s55, 1\n\t" \
    "s_cmp_eq_u32 s55, 0\n\t" \
    "s_cselect_b32 s50, s57, 0\n\t" \
    "s_sub_u32 s42, s42, s50\n\t" \
    "s_subb_u32 s43, s43, 0\n\t" \
    "s_sub_u32 s44, s44, s50\n\t" \
    "s_subb_u32 s45, s45, 0\n\t" \
    "s_waitcnt vmcnt(24)\n\t" \
    "s_barrier\n\t" \
    "s_cmp_eq_u32 s48, 0\n\t" \
    "s_cbranch_scc1 2f\n\t" \
    "1:\n\t" \
    "s_mov_b32 m0, s46\n\t" \
    "s_add_u32 s46, s46, 14336\n\t" \
    "global_load_lds_dwordx4 v0, s[40:41] offset:-2048\n\t" \
    "global_load_lds_dwordx4 v1, s[40:41] offset:-1024\n\t" \
    "global_load_lds_dwordx4 v2, s[40:41] offset:0\n\t" \
    "global_load_lds_dwordx4 v3, s[40:41] offset:1024\n\t" \
    "s_cmp_eq_u32 s46, s52\n\t" \
    "s_cselect_b32 s46, s51, s46\n\t" \
    "s_add_u32 s40, s40, s49\n\t" \
    "s_addc_u32 s41, s41, 0\n\t" \
    "s_sub_u32 s54, s54, 1\n\t" \
    "s_cmp_eq_u32 s54, 0\n\t" \
    "s_cselect_b32 s50, s56, 0\n\t" \
    "s_sub_u32 s40, s40, s50\n\t" \
    "s_subb_u32 s41, s41, 0\n\t" \
    "s_add_u32 m0, s47, s53\n\t" \
    "s_nop 0\n\t" \
    "global_load_lds_dwordx4 v5, s[42:43] offset:0\n\t" \
    "global_load_lds_dwordx4 v6, s[42:43] offset:1024\n\t" \
    "s_waitcnt vmcnt(24)\n\t" \
    "s_barrier\n\t" \
    "s_mov_b32 m0, s46\n\t" \
    "s_add_u32 s46, s46, 14336\n\t" \
    "global_load_lds_dwordx4 v0, s[40:41] offset:-2048\n\t" \
    "global_load_lds_dwordx4 v1, s[40:41] offset:-1024\n\t" \
    "global_load_lds_dwordx4 v2, s[40:41] offset:0\n\t" \
    "global_load_lds_dwordx4 v3, s[40:41] offset:1024\n\t" \
    "s_cmp_eq_u32 s46, s52\n\t" \
    "s_cselect_b32 s46, s51, s46\n\t" \
    "s_add_u32 s40, s40, s49\n\t" \
    "s_addc_u32 s41, s41, 0\n\t" \
    "s_sub_u32 s54, s54, 1\n\t" \
    "s_cmp_eq_u32 s54, 0\n\t" \
    "s_cselect_b32 s50, s56, 0\n\t" \
    "s_sub_u32 s40, s40, s50\n\t" \
    "s_subb_u32 s41, s41, 0\n\t" \
    "s_add_u32 m0, s58, s53\n\t" \
    "s_nop 0\n\t" \
    "global_load_lds_dwordx4 v5, s[44:45] offset:0\n\t" \
    "global_load_lds_dwordx4 v6, s[44:45] offset:1024\n\t" \
    "s_add_u32 s53, s53, 16384\n\t" \
    "s_and_b32 s53, s53, 0xffff\n\t" \
    "s_add_u32 s42, s42, 128\n\t" \
    "s_addc_u32 s43, s43, 0\n\t" \
    "s_add_u32 s44, s44, 128\n\t" \
    "s_addc_u32 s45, s45, 0\n\t" \
    "s_sub_u32 s55, s55, 1\n\t" \
    "s_cmp_eq_u32 s55, 0\n\t" \
    "s_cselect_b32 s50, s57, 0\n\t" \
    "s_sub_u32 s42, s42, s50\n\t" \
    "s_subb_u32 s43, s43, 0\n\t" \
    "s_sub_u32 s44, s44, s50\n\t" \
    "s_subb_u32 s45, s45, 0\n\t" \
    "s_sub_u32 s48, s48, 1\n\t" \
    "s_cmp_lg_u32 s48, 0\n\t" \
    "s_waitcnt vmcnt(24)\n\t" \
    "s_barrier\n\t" \
    "s_cbranch_scc1 1b\n\t" \
    "2:\n\t" \
    "s_mov_b32 m0, s46\n\t" \
    "s_add_u32 s46, s46, 14336\n\t" \
    "global_load_lds_dwordx4 v0, s[40:41] offset:-2048\n\t" \
    "global_load_lds_dwordx4 v1, s[40:41] offset:-1024\n\t" \
    "global_load_lds_dwordx4 v2, s[40:41] offset:0\n\t" \
    "global_load_lds_dwordx4 v3, s[40:41] offset:1024\n\t" \
    "s_cmp_eq_u32 s46, s52\n\t" \
    "s_cselect_b32 s46, s51, s46\n\t" \
    "s_add_u32 s40, s40, s49\n\t" \
    "s_addc_u32 s41, s41, 0\n\t" \
    "s_sub_u32 s54, s54, 1\n\t" \
    "s_cmp_eq_u32 s54, 0\n\t" \
    "s_cselect_b32 s50, s56, 0\n\t" \
    "s_sub_u32 s40, s40, s50\n\t" \
    "s_subb_u32 s41, s41, 0\n\t" \
    ""
#define LOADER_LOOPS(nstep) (((nstep) - 14) / 2)
#define LOADER_CLOB "s50", "scc", "memory"

#define STAMP(id) do { } while (0)
#define STAMPC(id) do { } while (0)
#define STARG
#define STPASS
namespace {
constexpr int A_SLOT = 16384, B_SLOT = 14336, LDS_A = 0, LDS_B = 65536;
constexpr int LDS_BYTES = 163840;
constexpr unsigned WS_EPOCH = 0, WS_ARR = 1024, WS_F1 = 4096, WS_FO = 6144, WS_FP = 8192;
constexpr unsigned WS_Q = 16384, WS_K = WS_Q + 2097152, WS_V = WS_K + 2097152, WS_O = WS_V + 2097152, WS_PQ = WS_O + 2097152;
constexpr unsigned WS_END = WS_PQ + 7340032;
constexpr int AUX_SC1 = 16;
constexpr unsigned SPIN_MAX = 1u << 22;

DI unsigned cvtpk(float lo, float hi) { f32x2 v = {lo, hi}; bf16x2 b = __builtin_convertvector(v, bf16x2); return __builtin_bit_cast(unsigned, b); }
DI float rbf(float x) { return __uint_as_float(cvtpk(0.0f, x)); }
DI void glds16(const void* sbase, unsigned voff, unsigned lds_base) {
    asm volatile("s_mov_b32 m0, %2\n\ts_nop 0\n\tglobal_load_lds_dwordx4 %1, %0" :: "s"(sbase), "v"(voff), "s"(lds_base) : "memory"); }
DI void wait_vm0() { asm volatile("s_waitcnt vmcnt(0)" ::: "memory"); }
DI void bar() { asm volatile("s_waitcnt lgkmcnt(0)\n\ts_barrier" ::: "memory"); }
DI s16x4 trread(LAS const char* p) { return __builtin_bit_cast(s16x4, __builtin_amdgcn_ds_read_tr16_b64_v4i16((LAS s16x4*)p)); }
DI v4u ld_sc1(__amdgpu_buffer_rsrc_t rs, unsigned off) { return __builtin_amdgcn_raw_buffer_load_b128(rs, off, 0, AUX_SC1); }
DI void st_sc1(__amdgpu_buffer_rsrc_t rs, unsigned off, v4u v) { __builtin_amdgcn_raw_buffer_store_b128(v, rs, off, 0, AUX_SC1); }
DI void flag_store(char* ws, unsigned off, u64 tag) { __hip_atomic_store((GAS u64*)(ws + off), tag, __ATOMIC_RELAXED, __HIP_MEMORY_SCOPE_AGENT); }
DI void poll(char* ws, unsigned off, bool active, u64 tag) {
    for (unsigned spins = 0; spins < SPIN_MAX; ++spins) {
        u64 v = tag;
        if (active) v = __hip_atomic_load((GAS u64*)(ws + off), __ATOMIC_RELAXED, __HIP_MEMORY_SCOPE_AGENT);
        if (__all(v == tag)) break;
        __builtin_amdgcn_s_sleep(1);
    }
    asm volatile("" ::: "memory");
}

template <int N> DI void wait_vm() { asm volatile("s_waitcnt vmcnt(%0)" :: "i"(N) : "memory"); }
struct NoTail { static constexpr int count(int, int) { return 0; } template <int W, int K> DI void issue() const {} };
template <class TAIL> constexpr int tail_sum(int w, int k) { int t = 0; for (int i = 0; i <= k; ++i) t += TAIL::count(w, i); return t; }
template <int W, int NB, int NA, class TAIL>
DI void g224_tail(const TAIL& t) {
    constexpr int n = NB + NA;
#define G224_TS(K, RING) t.template issue<W, K>(); if (RING >= 0) wait_vm<(RING >= 0 ? RING + tail_sum<TAIL>(W, K) : 0)>(); asm volatile("s_barrier" ::: "memory");
    G224_TS(0, 3 * n + NB) G224_TS(1, 2 * n + NB) G224_TS(2, n + NB) G224_TS(3, NB) G224_TS(4, 0) G224_TS(5, -1)
    t.template issue<W, 6>(); wait_vm<tail_sum<TAIL>(W, 6) - tail_sum<TAIL>(W, 2)>();
    asm volatile("s_barrier" ::: "memory");
#undef G224_TS
}
template <int NB, int NA>
DI void g224_loader(unsigned lds0, int w, int lane, const char* a0, const char* a1, unsigned lda, const char* bbase, unsigned ldb, int nstep, int rot) {
    const int b0 = 5 * w, i0 = 3 * w;
    unsigned vo[5], va[3];
#pragma unroll
    for (int i = 0; i < 5; ++i) {
        const unsigned o = (b0 + i) * 1024 + lane * 16;
        const unsigned row = o / 448, pc = (o - row * 448) >> 4;
        vo[i] = (i < NB) ? row * ldb + ((pc ^ (((row >> 3) & 1) << 1)) * 16) + 2048 - i * 1024 : 0u;
    }
    const int rl = lane >> 3, ch = lane & 7;
#pragma unroll
    for (int i = 0; i < 3; ++i) {
        const int ii = i0 + i;
        va[i] = (i < NA) ? (ii * 8 + rl) * lda + ((ch ^ ((((ii & 1) * 8 + rl) >> 1) & 7)) * 16) + 2048 - i * 1024 : 0u;
    }
    const unsigned bdst = lds0 + LDS_B + b0 * 1024 + 2048, adst = lds0 + LDS_A + i0 * 1024;
    const int rb = 2 * rot, ra = rot;
    const char* pb = bbase + (size_t)rb * 32 * ldb; const char* pa0 = a0 + ra * 128 - 2048; const char* pa1 = a1 + ra * 128 - 2048;
    unsigned s46 = bdst, s47 = adst, s48 = LOADER_LOOPS(nstep), s49 = 32 * ldb, s51 = bdst, s52 = bdst + 6 * B_SLOT, s53 = 0, s54 = nstep - rb, s55 = nstep / 2 - ra,
             s56 = nstep * 32 * ldb, s57 = (nstep / 2) * 128, s58 = adst + 8192;
    if (NB == 5) asm volatile(LOADER53 : "+{s[40:41]}"(pb), "+{s[42:43]}"(pa0), "+{s[44:45]}"(pa1), "+{s46}"(s46), "+{s48}"(s48), "+{s53}"(s53), "+{s54}"(s54), "+{s55}"(s55)
         : "{v0}"(vo[0]), "{v1}"(vo[1]), "{v2}"(vo[2]), "{v3}"(vo[3]), "{v4}"(vo[4]), "{v5}"(va[0]), "{v6}"(va[1]), "{v7}"(va[2]),
           "{s47}"(s47), "{s49}"(s49), "{s51}"(s51), "{s52}"(s52), "{s56}"(s56), "{s57}"(s57), "{s58}"(s58) : LOADER_CLOB);
    else asm volatile(LOADER42 : "+{s[40:41]}"(pb), "+{s[42:43]}"(pa0), "+{s[44:45]}"(pa1), "+{s46}"(s46), "+{s48}"(s48), "+{s53}"(s53), "+{s54}"(s54), "+{s55}"(s55)
         : "{v0}"(vo[0]), "{v1}"(vo[1]), "{v2}"(vo[2]), "{v3}"(vo[3]), "{v4}"(vo[4]), "{v5}"(va[0]), "{v6}"(va[1]), "{v7}"(va[2]),
           "{s47}"(s47), "{s49}"(s49), "{s51}"(s51), "{s52}"(s52), "{s56}"(s56), "{s57}"(s57), "{s58}"(s58) : LOADER_CLOB);
}
DI void touch(const char* p, unsigned lds_dummy) {
    asm volatile("s_mov_b32 m0, %1\n\ts_nop 0\n\tglobal_load_lds_dword %0, off" :: "v"(p), "s"(lds_dummy) : "memory"); }
constexpr int LDS_DUMMY = LDS_B + 6 * B_SLOT;
constexpr int LDS_CNT = LDS_BYTES - 64;
#define B2_PD 5
#define ATT_PRIO 0
#define ENG_FIRST 2
#define B1_ROT(x, tbl) (2 * (x))
template <class EX, class TAIL>
DI void g224(unsigned lds0, int wid, int lane, const char* a0, const char* a1, unsigned lda, const char* bbase, unsigned ldb, int nstep, int rot, bool swap, f32x16 (&c)[7], EX extra, const TAIL& tail) {
    if (wid == 7) {
        asm volatile("s_barrier\n\ts_barrier" ::: "memory");
        for (int s = 0; s < nstep - 7; ++s) {
            extra(s);
            asm volatile("s_barrier" ::: "memory");
        }
        g224_tail<3, 0, 0>(tail);
    }
    else if (wid == 6) { g224_loader<4, 2>(lds0, 2, lane, a0, a1, lda, bbase, ldb, nstep, rot); g224_tail<2, 4, 2>(tail); }
    else if (wid == 5) { g224_loader<5, 3>(lds0, 1, lane, a0, a1, lda, bbase, ldb, nstep, rot); g224_tail<1, 5, 3>(tail); }
    else if (wid == 4) { g224_loader<5, 3>(lds0, 0, lane, a0, a1, lda, bbase, ldb, nstep, rot); g224_tail<0, 5, 3>(tail); }
    else {
        const int mh = wid & 1, nh = wid >> 1;
        const int r16 = lane & 15, g = lane >> 4, q = (lane & 15) >> 2, p = lane & 3;
        const unsigned aoff0 = lds0 + LDS_A + (mh * 64 + r16) * 128 + ((g ^ ((r16 >> 1) & 7)) * 16);
        const unsigned brow = lds0 + LDS_B + (8 * g + q) * 448 + p * 8 + nh * 224;
        const unsigned gb = (g & 1) * 32;
        const unsigned bE = nh ? brow - gb : brow + gb, bO = nh ? brow + gb : brow - gb;
        unsigned aslot, bslot, cnt = nstep / 2 - 1;
        if (swap) asm volatile(G224_BODY_SWAP
            : "={v[0:15]}"(c[0]), "={v[16:31]}"(c[1]), "={v[32:47]}"(c[2]), "={v[48:63]}"(c[3]), "={v[64:79]}"(c[4]), "={v[80:95]}"(c[5]), "={v[96:111]}"(c[6]),
              [aslot] "=&s"(aslot), [bslot] "=&s"(bslot), [cnt] "+s"(cnt)
            : "{v200}"(aoff0), "{v201}"(aoff0 ^ 64), "{v202}"(bE), "{v203}"(bO) : G224_CLOB);
        else asm volatile(G224_BODY
            : "={v[0:15]}"(c[0]), "={v[16:31]}"(c[1]), "={v[32:47]}"(c[2]), "={v[48:63]}"(c[3]), "={v[64:79]}"(c[4]), "={v[80:95]}"(c[5]), "={v[96:111]}"(c[6]),
              [aslot] "=&s"(aslot), [bslot] "=&s"(bslot), [cnt] "+s"(cnt)
            : "{v200}"(aoff0), "{v201}"(aoff0 ^ 64), "{v202}"(bE), "{v203}"(bO) : G224_CLOB);
    }
}

DI bool last_arriver(LAS char* L, int k, unsigned n, int lane) {
    unsigned old = 0;
    if (lane == 0) old = __hip_atomic_fetch_add((LAS unsigned*)(L + LDS_CNT + k * 4), 1u, __ATOMIC_RELAXED, __HIP_MEMORY_SCOPE_WORKGROUP);
    return __builtin_amdgcn_readfirstlane(old) == n - 1;
}
struct FinalAddr { unsigned so[4]; char* gp[4]; };
DI FinalAddr final_addr(int lane, int m, char* outp  ) {
    FinalAddr f;
#pragma unroll
    for (int k = 0; k < 4; ++k) {
        const int idx = min(k * 64 + lane, 223), row = idx / 14, chn = idx - row * 14;
        f.so[k] = 32768 + m * 4096 + row * 240 + chn * 16;
        f.gp[k] = outp + (size_t)(m * 16 + row) * 1792 + chn * 16;
        asm volatile("" : "+v"(f.so[k]), "+v"(f.gp[k]));
    }
    return f;
}
DI void final_store(LAS char* L, int wid, int lane, f32x4 (&acc)[7], float sign, const FinalAddr& fa) {
    bar();
    const int m = wid & 3;
    if (wid >= 4) {
#pragma unroll
        for (int n = 0; n < 7; ++n) *(LAS f32x4*)(L + (m * 7 + n) * 1024 + lane * 16) = acc[n];
    }
    bar();
    if (wid < 4) {
        LAS char* stg = L + 32768 + m * 4096;
        const int t16 = lane & 15, g = lane >> 4;
        f32x4 o[7];
#pragma unroll
        for (int n = 0; n < 7; ++n) o[n] = *(const LAS f32x4*)(L + (m * 7 + n) * 1024 + lane * 16);
#pragma unroll
        for (int n = 0; n < 7; ++n) {
            v2u w;
            w[0] = cvtpk(acc[n][0] + sign * o[n][0], acc[n][1] + sign * o[n][1]);
            w[1] = cvtpk(acc[n][2] + sign * o[n][2], acc[n][3] + sign * o[n][3]);
            *(LAS v2u*)(stg + t16 * 240 + n * 32 + g * 8) = w;
        }
        asm volatile("s_waitcnt lgkmcnt(0)" ::: "memory");
        v4u v[4];
#pragma unroll
        for (int k = 0; k < 4; ++k) v[k] = *(const LAS v4u*)(L + fa.so[k]);
#pragma unroll
        for (int k = 0; k < 3; ++k) *(GAS v4u*)fa.gp[k] = v[k];
        if (lane < 32) *(GAS v4u*)fa.gp[3] = v[3];
    }
}

DI bf16x8 add_frag(v4u a, v4u b) {
    v4u r;
#pragma unroll
    for (int j = 0; j < 4; ++j) {
        const float lo = __uint_as_float(a[j] << 16) + __uint_as_float(b[j] << 16);
        const float hi = __uint_as_float(a[j] & 0xffff0000u) + __uint_as_float(b[j] & 0xffff0000u);
        r[j] = cvtpk(lo, hi);
    }
    return __builtin_bit_cast(bf16x8, r);
}

DI v4u pack_pair(const f32x16& ca, const f32x16& cb, int m) {
    unsigned a0 = cvtpk(ca[4 * m], ca[4 * m + 1]), a1 = cvtpk(ca[4 * m + 2], ca[4 * m + 3]);
    unsigned b0 = cvtpk(cb[4 * m], cb[4 * m + 1]), b1 = cvtpk(cb[4 * m + 2], cb[4 * m + 3]);
    auto s0 = __builtin_amdgcn_permlane32_swap(a0, b0, false, false);
    auto s1 = __builtin_amdgcn_permlane32_swap(a1, b1, false, false);
    auto t0 = __builtin_amdgcn_permlane16_swap(s0[0], s0[1], false, false);
    auto t1 = __builtin_amdgcn_permlane16_swap(s1[0], s1[1], false, false);
    return (v4u){t0[0], t1[0], t0[1], t1[1]};
}
DI v4u pack_single(const f32x16& ca, int m) {
    unsigned a0 = cvtpk(ca[4 * m], ca[4 * m + 1]), a1 = cvtpk(ca[4 * m + 2], ca[4 * m + 3]);
    auto t0 = __builtin_amdgcn_permlane16_swap(a0, a0, false, false);
    auto t1 = __builtin_amdgcn_permlane16_swap(a1, a1, false, false);
    return (v4u){t0[0], t1[0], t0[1], t1[1]};
}

constexpr unsigned W_POS[22] = {151552, 0, 7168, 94208, 101376, 14336, 21504, 108544, 115712, 122880, 130048, 28672, 35840, 137216, 144384, 65536, 72704,
                                43008, 50176, 57344, 79872, 87040};
constexpr int W_STEP[22] = {0, 0, 0, 1, 1, 2, 2, 2, 2, 3, 3, 4, 4, 4, 4, 5, 5, 6, 6, 6, 6, 6};
struct WTail {
    const char* wre; const char* wim; unsigned wvo, lds0;
    static constexpr int count(int w, int k) { int t = 0; for (int c = 0; c < 22; ++c) if ((c & 3) == w && W_STEP[c] == k) t += 7; return t; }
    DI void chunk(int u, int pq, unsigned pos) const {
        const char* src = (pq ? wim : wre) + u * 64;
#pragma unroll
        for (int k = 0; k < 7; ++k) glds16(src + k * 16 * 896, wvo, __builtin_amdgcn_readfirstlane(lds0 + pos + k * 1024));
    }
    template <int W, int K> DI void issue() const {
#pragma unroll
        for (int c = 0; c < 22; ++c) if ((c & 3) == W && W_STEP[c] == K) chunk(c >> 1, c & 1, W_POS[c]);
    }
};
DI void role_b(char* lds, int x, int i, int wid, int lane, const char* Ax, const char* Wqkv, const char* Wo, const char* Bx, const char* Cre, const char* Cim, const char* Wre, const char* Wim, char* out, char* ws, u64 tag STARG) {
    const unsigned lds0 = (unsigned)(uintptr_t)lds;
    LAS char* L = (LAS char*)lds;
    const int kh = x & 1, ch = i & 1, tb = 8 * (x >> 1) + (i >> 1), cb = 2 * kh + ch;
    const __amdgpu_buffer_rsrc_t rs = __builtin_amdgcn_make_buffer_rsrc(ws, 0, WS_END, 0x00020000);
    f32x16 c[7];
    STAMP(0); STAMPC(0);
    WTail wt;
    wt.wre = Wre + (size_t)cb * 112 * 896; wt.wim = Wim + (size_t)cb * 112 * 896; wt.lds0 = lds0;
    wt.wvo = (lane >> 2) * 896 + (((lane & 3) ^ ((((lane >> 5) ^ (lane >> 4)) & 1) * 2 + (lane >> 5))) * 16);
    g224(lds0, wid, lane, Cre + ((size_t)tb * 64 * 2048 + kh * 1024) * 2, Cim + ((size_t)tb * 64 * 2048 + kh * 1024) * 2, 4096,
         Bx + ((size_t)kh * 1024 * 448 + ch * 224) * 2, 896, 32, B1_ROT(x, i >> 2), false, c,
         [&](int s) { if (s == 0 && lane == 0) flag_store(ws, WS_ARR + blockIdx.x * 8, tag);
         }, wt);
    STAMP(1); STAMPC(1);
    if (wid < 4) {
        const int mh = wid & 1, nh = wid >> 1, g = lane >> 4;
        const unsigned base = WS_PQ + ((kh * 2 + mh) * 128 + tb * 4) * 14336 + (ch * 7) * 1024 + lane * 16;
#pragma unroll
        for (int m = 0; m < 4; ++m) {
            const unsigned bm = base + m * 14336;
            if (nh == 0) {
                st_sc1(rs, bm, pack_pair(c[0], c[1], m)); st_sc1(rs, bm + 1024, pack_pair(c[2], c[3], m)); st_sc1(rs, bm + 2048, pack_pair(c[4], c[5], m));
                const v4u s = pack_single(c[6], m);
                if ((g & 1) == 0) st_sc1(rs, bm + 3072 - lane * 16 + ((g >> 1) * 16 + (lane & 15)) * 16, s);
            } else {
                const v4u s = pack_single(c[0], m);
                if ((g & 1) == 0) st_sc1(rs, bm + 3072 - lane * 16 + ((2 + (g >> 1)) * 16 + (lane & 15)) * 16, s);
                st_sc1(rs, bm + 4096, pack_pair(c[1], c[2], m)); st_sc1(rs, bm + 5120, pack_pair(c[3], c[4], m)); st_sc1(rs, bm + 6144, pack_pair(c[5], c[6], m));
            }
        }
    }
    const int m = wid & 3, pq = wid >> 2;
    const int e16 = lane & 15, g = lane >> 4, rq = e16 >> 2;
    const unsigned wro = e16 * 64 + ((g ^ ((((rq >> 1) ^ rq) & 1) * 2 + (rq >> 1))) * 16);
    auto wpos = [&](int u) { const int uu = u < 11 ? u : u - 11; return pq ? W_POS[2 * uu + 1] : W_POS[2 * uu]; };
    bf16x8 w[3][7];
#pragma unroll
    for (int u = 0; u < 3; ++u)
#pragma unroll
        for (int n = 0; n < 7; ++n) w[u][n] = *(const LAS bf16x8*)(L + wro + wpos(u) + n * 1024);
    STAMP(9);
    bar();
    if (wid >= 4) {
        for (int s = wid - 4; s < 6; s += 4) wt.chunk(11 + (s >> 1), s & 1, s == 0 ? W_POS[0] : s == 1 ? W_POS[1] : s == 2 ? W_POS[2] : s == 3 ? W_POS[3] : s == 4 ? W_POS[4] : W_POS[5]);
        wait_vm0();
    } else {
        wait_vm0();
        STAMP(2);
        if (last_arriver(L, 0, 4, lane)) {
            if (lane == 0) flag_store(ws, WS_FP + (tb * 4 + cb) * 8, tag);
            STAMP(3);
            poll(ws, WS_FP + (tb * 4 + (lane & 3)) * 8, lane < 4, tag);
        }
    }
    STAMP(4);
    bar();
    STAMP(5);
    const unsigned pb = WS_PQ + (pq * 128 + tb * 4 + m) * 14336 + lane * 16;
    constexpr int PD = B2_PD;
    v4u p0[14], p1[14];
#pragma unroll
    for (int u = 0; u < PD; ++u) { p0[u] = ld_sc1(rs, pb + u * 1024); p1[u] = ld_sc1(rs, pb + 2 * 128 * 14336 + u * 1024); }
    asm volatile("" ::: "memory"); __builtin_amdgcn_sched_barrier(0);
    const FinalAddr fa = final_addr(lane, m, out + ((size_t)tb * 64 * 896 + 448 + cb * 112) * 2);
    f32x4 acc[7];
#pragma unroll
    for (int n = 0; n < 7; ++n) acc[n] = (f32x4){0.f, 0.f, 0.f, 0.f};
#pragma unroll
    for (int u = 0; u < 14; ++u) {
        if (u == 1) STAMP(10);
        if (u == 3) STAMP(11);
        if (u == 8) STAMP(12);
        if (u + PD < 14) { p0[u + PD] = ld_sc1(rs, pb + (u + PD) * 1024); p1[u + PD] = ld_sc1(rs, pb + 2 * 128 * 14336 + (u + PD) * 1024); __builtin_amdgcn_sched_barrier(0); }
        const bf16x8 f = add_frag(p0[u], p1[u]);
        __builtin_amdgcn_sched_barrier(0);
#pragma unroll
        for (int n = 0; n < 7; ++n) acc[n] = __builtin_amdgcn_mfma_f32_16x16x32_bf16(w[u % 3][n], f, acc[n], 0, 0, 0);
        if (u + 3 < 14) {
#pragma unroll
            for (int n = 0; n < 7; ++n) w[u % 3][n] = *(const LAS bf16x8*)(L + wro + wpos(u + 3) + n * 1024);
        }
        __builtin_amdgcn_sched_barrier(0);
    }
    STAMP(6);
    final_store(L, wid, lane, acc, -1.0f, fa);
    STAMP(7);
}

#define ASTAMP(id) do { } while (0)
template <class HOOK>
DI void attention(int iq, int head, int lane, __amdgpu_buffer_rsrc_t rs, HOOK after_loads STARG) {
    float SC = 0.18898223650461362f, L2E = 1.4426950408889634f;
    asm volatile("" : "+v"(SC), "+v"(L2E));
    const int a = lane & 31, hi = lane >> 5;
    const unsigned hb = head * 64 * 2048 + lane * 16;
    v4u kf[9][2], vf[9][2];
    const bf16x8 q0 = __builtin_bit_cast(bf16x8, ld_sc1(rs, WS_Q + hb + iq * 2048)), q1 = __builtin_bit_cast(bf16x8, ld_sc1(rs, WS_Q + hb + iq * 2048 + 1024));
#pragma unroll
    for (int jj = 0; jj < 9; ++jj) { const int j = jj == 0 ? 0 : jj == 1 ? 8 : jj - 1; const int kt = max(iq - 8 + j, 0);
        kf[j][0] = ld_sc1(rs, WS_K + hb + kt * 2048); kf[j][1] = ld_sc1(rs, WS_K + hb + kt * 2048 + 1024); }
    asm volatile("" ::: "memory"); __builtin_amdgcn_sched_barrier(0);
    after_loads();
    ASTAMP(9);
    __builtin_amdgcn_sched_barrier(0);
    const f32x16 zero = {};
    f32x16 S[8];
    {
        f32x16 so = __builtin_amdgcn_mfma_f32_32x32x16_bf16(__builtin_bit_cast(bf16x8, kf[0][0]), q0, zero, 0, 0, 0);
        so = __builtin_amdgcn_mfma_f32_32x32x16_bf16(__builtin_bit_cast(bf16x8, kf[0][1]), q1, so, 0, 0, 0);
        f32x16 sd = __builtin_amdgcn_mfma_f32_32x32x16_bf16(__builtin_bit_cast(bf16x8, kf[8][0]), q0, zero, 0, 0, 0);
        sd = __builtin_amdgcn_mfma_f32_32x32x16_bf16(__builtin_bit_cast(bf16x8, kf[8][1]), q1, sd, 0, 0, 0);
        if (iq < 8) {
            asm volatile("s_nop 7\n\ts_nop 7\n\ts_nop 7");
#pragma unroll
            for (int r = 0; r < 16; ++r) { float t; asm volatile("v_mov_b32 %0, 0xff800000" : "=v"(t)); so[r] = t; }
        }
#pragma unroll
        for (int r = 0; r < 16; ++r) { const int b = (r & 3) + 8 * (r >> 2) + 4 * hi; S[0][r] = (b <= a) ? sd[r] : so[r]; }
    }
    asm volatile("" :: "v"(S[0][0])); ASTAMP(10);
#pragma unroll
    for (int j = 1; j < 8; ++j) {
        f32x16 s = __builtin_amdgcn_mfma_f32_32x32x16_bf16(__builtin_bit_cast(bf16x8, kf[j][0]), q0, zero, 0, 0, 0);
        s = __builtin_amdgcn_mfma_f32_32x32x16_bf16(__builtin_bit_cast(bf16x8, kf[j][1]), q1, s, 0, 0, 0);
        if (iq - 8 + j < 0) {
            asm volatile("s_nop 7\n\ts_nop 7\n\ts_nop 7");
#pragma unroll
            for (int r = 0; r < 16; ++r) { float t; asm volatile("v_mov_b32 %0, 0xff800000" : "=v"(t)); s[r] = t; }
        }
        S[j] = s;
    }
    __builtin_amdgcn_sched_barrier(0);
#pragma unroll
    for (int jj = 0; jj < 9; ++jj) { const int j = jj == 0 ? 8 : jj - 1; const int kt = max(iq - 8 + j, 0);
        vf[j][0] = ld_sc1(rs, WS_V + hb + kt * 2048); vf[j][1] = ld_sc1(rs, WS_V + hb + kt * 2048 + 1024); }
    asm volatile("" ::: "memory"); __builtin_amdgcn_sched_barrier(0);
    float mx = S[0][0];
#pragma unroll
    for (int j = 0; j < 8; ++j)
#pragma unroll
        for (int r = 0; r < 16; ++r) mx = __builtin_fmaxf(mx, S[j][r]);
    { auto rr = __builtin_amdgcn_permlane32_swap(__float_as_uint(mx), __float_as_uint(mx), false, false);
      mx = __builtin_fmaxf(__uint_as_float(rr[0]), __uint_as_float(rr[1])); }
    const float nml = -(rbf(rbf(mx) * SC) * L2E);
    asm volatile("" :: "v"(nml)); ASTAMP(11);
    f32x16 O = zero;
#pragma unroll
    for (int j = 0; j < 8; ++j) {
        if (j == 4) { asm volatile("" :: "v"(O[0])); ASTAMP(12); }
        v4u pw[2];
#pragma unroll
        for (int k = 0; k < 8; ++k) {
            const float e0 = __builtin_amdgcn_exp2f(__builtin_fmaf(rbf(rbf(S[j][2 * k]) * SC), L2E, nml));
            const float e1 = __builtin_amdgcn_exp2f(__builtin_fmaf(rbf(rbf(S[j][2 * k + 1]) * SC), L2E, nml));
            pw[k >> 2][k & 3] = cvtpk(e0, e1);
        }
        if (j == 0) {
            v4u pd[2], po[2];
#pragma unroll
            for (int k = 0; k < 8; ++k) {
                const int b = ((2 * k) & 3) + 8 * ((2 * k) >> 2) + 4 * hi;
                const unsigned msk = (b <= a ? 0xffffu : 0u) | (b + 1 <= a ? 0xffff0000u : 0u);
                pd[k >> 2][k & 3] = pw[k >> 2][k & 3] & msk; po[k >> 2][k & 3] = pw[k >> 2][k & 3] & ~msk;
            }
            O = __builtin_amdgcn_mfma_f32_32x32x16_bf16(__builtin_bit_cast(bf16x8, vf[8][0]), __builtin_bit_cast(bf16x8, pd[0]), O, 0, 0, 0);
            O = __builtin_amdgcn_mfma_f32_32x32x16_bf16(__builtin_bit_cast(bf16x8, vf[8][1]), __builtin_bit_cast(bf16x8, pd[1]), O, 0, 0, 0);
            O = __builtin_amdgcn_mfma_f32_32x32x16_bf16(__builtin_bit_cast(bf16x8, vf[0][0]), __builtin_bit_cast(bf16x8, po[0]), O, 0, 0, 0);
            O = __builtin_amdgcn_mfma_f32_32x32x16_bf16(__builtin_bit_cast(bf16x8, vf[0][1]), __builtin_bit_cast(bf16x8, po[1]), O, 0, 0, 0);
        } else {
            O = __builtin_amdgcn_mfma_f32_32x32x16_bf16(__builtin_bit_cast(bf16x8, vf[j][0]), __builtin_bit_cast(bf16x8, pw[0]), O, 0, 0, 0);
            O = __builtin_amdgcn_mfma_f32_32x32x16_bf16(__builtin_bit_cast(bf16x8, vf[j][1]), __builtin_bit_cast(bf16x8, pw[1]), O, 0, 0, 0);
        }
    }
    auto lr = __builtin_amdgcn_permlane32_swap(__float_as_uint(O[12]), __float_as_uint(O[12]), false, false);
    const float rl = 1.0f / __uint_as_float(lr[1]);
    v4u o0, o1;
#pragma unroll
    for (int k = 0; k < 4; ++k) { o0[k] = cvtpk(O[2 * k] * rl, O[2 * k + 1] * rl); o1[k] = cvtpk(O[8 + 2 * k] * rl, O[8 + 2 * k + 1] * rl); }
    if (hi) { o1[2] = 0; o1[3] = 0; }
    const unsigned ob = WS_O + (head * 128 + iq * 2 + (a >> 4)) * 1024 + (hi * 16 + (a & 15)) * 16;
    st_sc1(rs, ob, o0); st_sc1(rs, ob + 512, o1);
}

DI void role_a(char* lds, int x, int ai, int wid, int lane, const char* Ax, const char* Wqkv, const char* Wo, const char* Bx, const char* Cre, const char* Cim, const char* Wre, const char* Wim, char* out, char* ws, u64 tag STARG) {
    const unsigned lds0 = (unsigned)(uintptr_t)lds;
    LAS char* L = (LAS char*)lds;
    const __amdgpu_buffer_rsrc_t rs = __builtin_amdgcn_make_buffer_rsrc(ws, 0, WS_END, 0x00020000);
    const int iq = 8 * x + (ai >> 1), hh = ai & 1, cb = (iq & 1) * 2 + hh, rp = iq >> 1;
    if (ai < 12) {
        const int rb = 4 * (x >> 1) + ai / 3, j = 3 * (x & 1) + ai % 3;
        const bool isv = j >= 4;
        f32x16 c[7];
        STAMP(0); STAMPC(0);
        unsigned ioff[7] = {};
        if (wid < 4) {
            const int mh = wid & 1, nh = (wid >> 1) & 1, l16 = lane & 15, g = lane >> 4;
#pragma unroll
            for (int n = 0; n < 7; ++n) {
                const int u4 = nh * 28 + 4 * n + g, h8 = u4 / 7, du = u4 - 7 * h8;
                const int col = nh * 112 + 16 * n + l16, hv = col / 28, d = col - 28 * hv;
                ioff[n] = lds0 + mh * 4096 + (isv ? hv * 8192 + ((g & 1) * 32 + d) * 16 + (g >> 1) * 8
                                                  : h8 * 8192 + (du >> 2) * 1024 + (((du >> 1) & 1) * 32 + l16) * 16 + (du & 1) * 8);
                asm volatile("" : "+v"(ioff[n]));
            }
        }
        g224(lds0, wid, lane, Ax + (size_t)rb * 128 * 896, Ax + ((size_t)rb * 128 + 64) * 896, 896, Wqkv + j * 448, 2688, 14, 0, isv, c,
             [&](int s) { if (s == 0 && lane == 0) flag_store(ws, WS_ARR + blockIdx.x * 8, tag);
             }, NoTail());
        STAMP(1); STAMPC(1);
        if (wid < 4) {
            if (isv) {
#pragma unroll
                for (int n = 0; n < 7; ++n)
#pragma unroll
                    for (int m = 0; m < 4; ++m) {
                        v2u w; w[0] = cvtpk(c[n][4 * m], c[n][4 * m + 1]); w[1] = cvtpk(c[n][4 * m + 2], c[n][4 * m + 3]);
                        *(LAS v2u*)(uintptr_t)(ioff[n] + (m >> 1) * 2048 + (m & 1) * 1024) = w;
                    }
            } else {
#pragma unroll
                for (int n = 0; n < 7; ++n)
#pragma unroll
                    for (int m = 0; m < 4; ++m) {
                        v2u w; w[0] = cvtpk(c[n][4 * m], c[n][4 * m + 1]); w[1] = cvtpk(c[n][4 * m + 2], c[n][4 * m + 3]);
                        *(LAS v2u*)(uintptr_t)(ioff[n] + (m >> 1) * 2048 + (m & 1) * 256) = w;
                    }
            }
        } else {
            const int t = (wid - 4) * 64 + lane;
            if (!isv) {
#pragma unroll
                for (int k = 0; k < 4; ++k) { const int e = t + 256 * k, h8 = e >> 7, tok = e & 127;
                    *(LAS v2u*)(L + h8 * 8192 + (tok >> 5) * 2048 + 1024 + (32 + (tok & 31)) * 16 + 8) = (v2u){0u, 0u}; }
            } else {
#pragma unroll
                for (int k = 0; k < 2; ++k) { const int e = t + 256 * k, dd = e & 3; const unsigned val = dd == 0 ? 0x3f803f80u : 0u;
                    *(LAS v4u*)(L + (e >> 6) * 8192 + ((e >> 4) & 3) * 2048 + ((e >> 3) & 1) * 1024 + (((e >> 2) & 1) * 32 + 28 + dd) * 16) = (v4u){val, val, val, val}; }
            }
        }
        STAMP(9);
        bar();
        STAMP(10);
        const unsigned gb = (j < 2 ? WS_Q : j < 4 ? WS_K : WS_V) + ((j & 1) * 8 * 64 + rb * 4) * 2048 + lane * 16;
        {
            v4u v[8];
#pragma unroll
            for (int k = 0; k < 8; ++k) v[k] = *(const LAS v4u*)(L + (wid * 8 + k) * 1024 + lane * 16);
#pragma unroll
            for (int k = 0; k < 8; ++k) st_sc1(rs, gb + wid * 64 * 2048 + k * 1024, v[k]);
        }
        STAMP(11);
        wait_vm0();
        STAMP(12);
        asm volatile("s_waitcnt lgkmcnt(0)" ::: "memory");
        if (last_arriver(L, 0, 8, lane) && lane == 0) flag_store(ws, WS_F1 + (rb * 8 + j) * 8, tag);
        bar();
        STAMP(2);
    }
    if (ai >= 12) {
        bar();
        const unsigned dummy = lds0 + LDS_DUMMY;
        const int gw = (ai - 12) * 8 + wid;
        const int Lr = (gw & 15) * 64 + lane, tbl = Lr >> 7;
        const char* p = (((Lr >> 6) & 1) ? Cim : Cre) + (size_t)((8 * (x >> 1) + tbl) * 64 + (Lr & 63)) * 4096 + (x & 1) * 2048;
        for (int a = ENG_FIRST + (gw >> 4); a < 16; a += 2) touch(p + ((a + B1_ROT(x, tbl)) & 15) * 128, dummy);
        if (wid == 7 && lane == 0) flag_store(ws, WS_ARR + blockIdx.x * 8, tag);
    }
    const int m = wid & 3, k2 = wid >> 2;
    unsigned ad[2][2][7];
    {
        auto wo_stage = [&]() {
            const unsigned wb = (cb * 224) & ~63u;
            const int rr = lane >> 4;
#pragma unroll
            for (int k = 0; k < 14; ++k) {
                const int r4 = wid * 14 + k;
                const int row = 4 * r4 + rr;
                const unsigned vo = rr * 896 + (((lane & 15) ^ ((row & 7) << 1)) * 16);
                glds16(Wo + (size_t)r4 * 4 * 896 + wb, vo, __builtin_amdgcn_readfirstlane(lds0 + r4 * 1024));
            }
        };
        wo_stage();
        const int rq = iq >> 2;
        const int sel = lane == 0 ? 0 : lane < 4 ? 1 : 2;
        const int rb = sel == 0 ? rq : rq - 2 + ((lane - 1) % 3);
        if (wid == 0) poll(ws, WS_F1 + (max(rb, 0) * 8 + sel * 2 + hh) * 8, lane < 7 && rb >= 0, tag);
        asm volatile("s_barrier" ::: "memory");
        STAMP(3);
        if (wid >= 4) __builtin_amdgcn_s_setprio(ATT_PRIO);
        attention(iq, hh * 8 + wid, lane, rs, [] {} STPASS);
        __builtin_amdgcn_s_setprio(0);
        STAMP(4);
        {
            const int g = lane >> 4, q = (lane & 15) >> 2, p = lane & 3;
            const int s0 = ((cb * 224) & 63) >> 5;
#pragma unroll
            for (int hp = 0; hp < 2; ++hp)
#pragma unroll
                for (int xx = 0; xx < 2; ++xx) {
                    int unit = 4 * (g >> 1) + 2 * xx + (g & 1); if (unit == 7) unit = 6;
                    const int rl = 4 * unit + q, sw = (4 * hp + rl) & 7;
#pragma unroll
                    for (int n = 0; n < 7; ++n) { ad[hp][xx][n] = lds0 + (k2 * 8 * 28 + hp * 28 + rl) * 256 + (((s0 + n) ^ sw) * 32) + p * 8; asm volatile("" : "+v"(ad[hp][xx][n])); }
                }
        }
        wait_vm0();
        if (last_arriver(L, 1, 8, lane)) {
            if (lane == 0) flag_store(ws, WS_FO + (iq * 2 + hh) * 8, tag);
            STAMP(5);
            poll(ws, WS_FO + (rp * 4 + (lane & 3)) * 8, lane < 4, tag);
        }
        bar();
        STAMP(6);
    }
    {
        v4u of[8];
#pragma unroll
        for (int h = 0; h < 8; ++h) of[h] = ld_sc1(rs, WS_O + ((k2 * 8 + h) * 128 + rp * 4 + m) * 1024 + lane * 16);
        asm volatile("" ::: "memory"); __builtin_amdgcn_sched_barrier(0);
        const FinalAddr fa = final_addr(lane, m, out + ((size_t)rp * 64 * 896 + cb * 112) * 2);
        f32x4 acc[7];
#pragma unroll
        for (int n = 0; n < 7; ++n) acc[n] = (f32x4){0.f, 0.f, 0.f, 0.f};
        auto wfrag = [&](int h, int n) {
            const s16x4 lo = trread((LAS const char*)(uintptr_t)(ad[h & 1][0][n] + (h >> 1) * 2 * 7168)), hi2 = trread((LAS const char*)(uintptr_t)(ad[h & 1][1][n] + (h >> 1) * 2 * 7168));
            return (bf16x8){lo[0], lo[1], lo[2], lo[3], hi2[0], hi2[1], hi2[2], hi2[3]};
        };
        bf16x8 w[2][7];
#pragma unroll
        for (int n = 0; n < 7; ++n) w[0][n] = wfrag(0, n);
#pragma unroll
        for (int h = 0; h < 8; ++h) {
            if (h + 1 < 8) {
#pragma unroll
                for (int n = 0; n < 7; ++n) w[(h + 1) & 1][n] = wfrag(h + 1, n);
            }
            __builtin_amdgcn_sched_barrier(0);
#pragma unroll
            for (int n = 0; n < 7; ++n) acc[n] = __builtin_amdgcn_mfma_f32_16x16x32_bf16(w[h & 1][n], __builtin_bit_cast(bf16x8, of[h]), acc[n], 0, 0, 0);
            __builtin_amdgcn_sched_barrier(0);
        }
        final_store(L, wid, lane, acc, 1.0f, fa);
        STAMP(7);
    }
}
}

__global__ void __launch_bounds__(512, 2)
fused_kernel(const char* Ax, const char* Wqkv, const char* Wo, const char* Bx, const char* Cre, const char* Cim, const char* Wre, const char* Wim, char* out, char* ws) {
    extern __shared__ __attribute__((aligned(16))) char lds[];
    const int wid = __builtin_amdgcn_readfirstlane(threadIdx.x >> 6), lane = threadIdx.x & 63;
    const int x = blockIdx.x & 7, i = blockIdx.x >> 3;
    STAMP(8);
    const u64 tag = *(const GAS u64*)(ws + WS_EPOCH) + 1;
    if (threadIdx.x < 4) *(LAS unsigned*)((LAS char*)lds + LDS_CNT + threadIdx.x * 4) = 0u;
    if (i < 16) role_b(lds, x, i, wid, lane, Ax, Wqkv, Wo, Bx, Cre, Cim, Wre, Wim, out, ws, tag STPASS);
    else role_a(lds, x, i - 16, wid, lane, Ax, Wqkv, Wo, Bx, Cre, Cim, Wre, Wim, out, ws, tag STPASS);
    if (blockIdx.x == 0 && wid == 7) {
#pragma unroll
        for (int k = 0; k < 4; ++k) poll(ws, WS_ARR + (k * 64 + lane) * 8, true, tag);
        if (lane == 0) flag_store(ws, WS_EPOCH, tag);
    }
}

extern "C" void kernel_launch(void* const* d_in, const int* in_sizes, int n_in, void* d_out, int out_size, void* d_ws, size_t ws_size, hipStream_t stream) {
    (void)in_sizes; (void)n_in; (void)out_size; (void)ws_size;
    (void)hipFuncSetAttribute((const void*)fused_kernel, hipFuncAttributeMaxDynamicSharedMemorySize, LDS_BYTES);
    fused_kernel<<<256, 512, LDS_BYTES, stream>>>((const char*)d_in[0], (const char*)d_in[1], (const char*)d_in[2], (const char*)d_in[3], (const char*)d_in[4], (const char*)d_in[5],
                                                   (const char*)d_in[6], (const char*)d_in[7], (char*)d_out, (char*)d_ws);
}
```

```cpp
#include <hip/hip_runtime.h>
#include <hip/hip_bf16.h>
#include <stdint.h>
#define DIAG 5

typedef short bf16x8 __attribute__((ext_vector_type(8)));
typedef short s16x4 __attribute__((ext_vector_type(4)));
typedef float f32x2 __attribute__((ext_vector_type(2)));
typedef float f32x4 __attribute__((ext_vector_type(4)));
typedef float f32x16 __attribute__((ext_vector_type(16)));
typedef __bf16 bf16x2 __attribute__((ext_vector_type(2)));
typedef unsigned v4u __attribute__((ext_vector_type(4)));
typedef unsigned v2u __attribute__((ext_vector_type(2)));
typedef unsigned long long u64;
#define LAS __attribute__((address_space(3)))
#define GAS __attribute__((address_space(1)))
#define DI __device__ __forceinline__

#define G224_BODY \
    "v_mov_b32 v0, 0\n\t" \
    "v_mov_b32 v1, 0\n\t" \
    "v_mov_b32 v2, 0\n\t" \
    "v_mov_b32 v3, 0\n\t" \
    "v_mov_b32 v4, 0\n\t" \
    "v_mov_b32 v5, 0\n\t" \
    "v_mov_b32 v6, 0\n\t" \
    "v_mov_b32 v7, 0\n\t" \
    "v_mov_b32 v8, 0\n\t" \
    "v_mov_b32 v9, 0\n\t" \
    "v_mov_b32 v10, 0\n\t" \
    "v_mov_b32 v11, 0\n\t" \
    "v_mov_b32 v12, 0\n\t" \
    "v_mov_b32 v13, 0\n\t" \
    "v_mov_b32 v14, 0\n\t" \
    "v_mov_b32 v15, 0\n\t" \
    "v_mov_b32 v16, 0\n\t" \
    "v_mov_b32 v17, 0\n\t" \
    "v_mov_b32 v18, 0\n\t" \
    "v_mov_b32 v19, 0\n\t" \
    "v_mov_b32 v20, 0\n\t" \
    "v_mov_b32 v21, 0\n\t" \
    "v_mov_b32 v22, 0\n\t" \
    "v_mov_b32 v23, 0\n\t" \
    "v_mov_b32 v24, 0\n\t" \
    "v_mov_b32 v25, 0\n\t" \
    "v_mov_b32 v26, 0\n\t" \
    "v_mov_b32 v27, 0\n\t" \
    "v_mov_b32 v28, 0\n\t" \
    "v_mov_b32 v29, 0\n\t" \
    "v_mov_b32 v30, 0\n\t" \
    "v_mov_b32 v31, 0\n\t" \
    "v_mov_b32 v32, 0\n\t" \
    "v_mov_b32 v33, 0\n\t" \
    "v_mov_b32 v34, 0\n\t" \
    "v_mov_b32 v35, 0\n\t" \
    "v_mov_b32 v36, 0\n\t" \
    "v_mov_b32 v37, 0\n\t" \
    "v_mov_b32 v38, 0\n\t" \
    "v_mov_b32 v39, 0\n\t" \
    "v_mov_b32 v40, 0\n\t" \
    "v_mov_b32 v41, 0\n\t" \
    "v_mov_b32 v42, 0\n\t" \
    "v_mov_b32 v43, 0\n\t" \
    "v_mov_b32 v44, 0\n\t" \
    "v_mov_b32 v45, 0\n\t" \
    "v_mov_b32 v46, 0\n\t" \
    "v_mov_b32 v47, 0\n\t" \
    "v_mov_b32 v48, 0\n\t" \
    "v_mov_b32 v49, 0\n\t" \
    "v_mov_b32 v50, 0\n\t" \
    "v_mov_b32 v51, 0\n\t" \
    "v_mov_b32 v52, 0\n\t" \
    "v_mov_b32 v53, 0\n\t" \
    "v_mov_b32 v54, 0\n\t" \
    "v_mov_b32 v55, 0\n\t" \
    "v_mov_b32 v56, 0\n\t" \
    "v_mov_b32 v57, 0\n\t" \
    "v_mov_b32 v58, 0\n\t" \
    "v_mov_b32 v59, 0\n\t" \
    "v_mov_b32 v60, 0\n\t" \
    "v_mov_b32 v61, 0\n\t" \
    "v_mov_b32 v62, 0\n\t" \
    "v_mov_b32 v63, 0\n\t" \
    "v_mov_b32 v64, 0\n\t" \
    "v_mov_b32 v65, 0\n\t" \
    "v_mov_b32 v66, 0\n\t" \
    "v_mov_b32 v67, 0\n\t" \
    "v_mov_b32 v68, 0\n\t" \
    "v_mov_b32 v69, 0\n\t" \
    "v_mov_b32 v70, 0\n\t" \
    "v_mov_b32 v71, 0\n\t" \
    "v_mov_b32 v72, 0\n\t" \
    "v_mov_b32 v73, 0\n\t" \
    "v_mov_b32 v74, 0\n\t" \
    "v_mov_b32 v75, 0\n\t" \
    "v_mov_b32 v76, 0\n\t" \
    "v_mov_b32 v77, 0\n\t" \
    "v_mov_b32 v78, 0\n\t" \
    "v_mov_b32 v79, 0\n\t" \
    "v_mov_b32 v80, 0\n\t" \
    "v_mov_b32 v81, 0\n\t" \
    "v_mov_b32 v82, 0\n\t" \
    "v_mov_b32 v83, 0\n\t" \
    "v_mov_b32 v84, 0\n\t" \
    "v_mov_b32 v85, 0\n\t" \
    "v_mov_b32 v86, 0\n\t" \
    "v_mov_b32 v87, 0\n\t" \
    "v_mov_b32 v88, 0\n\t" \
    "v_mov_b32 v89, 0\n\t" \
    "v_mov_b32 v90, 0\n\t" \
    "v_mov_b32 v91, 0\n\t" \
    "v_mov_b32 v92, 0\n\t" \
    "v_mov_b32 v93, 0\n\t" \
    "v_mov_b32 v94, 0\n\t" \
    "v_mov_b32 v95, 0\n\t" \
    "v_mov_b32 v96, 0\n\t" \
    "v_mov_b32 v97, 0\n\t" \
    "v_mov_b32 v98, 0\n\t" \
    "v_mov_b32 v99, 0\n\t" \
    "v_mov_b32 v100, 0\n\t" \
    "v_mov_b32 v101, 0\n\t" \
    "v_mov_b32 v102, 0\n\t" \
    "v_mov_b32 v103, 0\n\t" \
    "v_mov_b32 v104, 0\n\t" \
    "v_mov_b32 v105, 0\n\t" \
    "v_mov_b32 v106, 0\n\t" \
    "v_mov_b32 v107, 0\n\t" \
    "v_mov_b32 v108, 0\n\t" \
    "v_mov_b32 v109, 0\n\t" \
    "v_mov_b32 v110, 0\n\t" \
    "v_mov_b32 v111, 0\n\t" \
    "s_waitcnt lgkmcnt(0)\n\t" \
    "s_barrier\n\t" \
    "v_mov_b32 v207, v200\n\t" \
    "v_mov_b32 v208, v202\n\t" \
    "v_mov_b32 v209, v203\n\t" \
    "ds_read_b128 v[112:115], v207 offset:0\n\t" \
    "ds_read_b64_tr_b16 v[128:129], v208 offset:0\n\t" \
    "ds_read_b64_tr_b16 v[130:131], v208 offset:1792\n\t" \
    "ds_read_b128 v[116:119], v207 offset:2048\n\t" \
    "ds_read_b128 v[120:123], v207 offset:4096\n\t" \
    "ds_read_b128 v[124:127], v207 offset:6144\n\t" \
    "ds_read_b64_tr_b16 v[132:133], v209 offset:32\n\t" \
    "ds_read_b64_tr_b16 v[134:135], v209 offset:1824\n\t" \
    "ds_read_b64_tr_b16 v[136:137], v208 offset:64\n\t" \
    "ds_read_b64_tr_b16 v[138:139], v208 offset:1856\n\t" \
    "ds_read_b64_tr_b16 v[140:141], v209 offset:96\n\t" \
    "ds_read_b64_tr_b16 v[142:143], v209 offset:1888\n\t" \
    "ds_read_b64_tr_b16 v[144:145], v208 offset:128\n\t" \
    "ds_read_b64_tr_b16 v[146:147], v208 offset:1920\n\t" \
    "ds_read_b64_tr_b16 v[148:149], v209 offset:160\n\t" \
    "ds_read_b64_tr_b16 v[150:151], v209 offset:1952\n\t" \
    "ds_read_b64_tr_b16 v[152:153], v208 offset:192\n\t" \
    "ds_read_b64_tr_b16 v[154:155], v208 offset:1984\n\t" \
    "s_mov_b32 %[aslot], 0\n\t" \
    "s_mov_b32 %[bslot], 14336\n\t" \
    "v_mov_b32 v204, v201\n\t" \
    "v_add_u32 v205, %[bslot], v202\n\t" \
    "v_add_u32 v206, %[bslot], v203\n\t" \
    "s_waitcnt lgkmcnt(0)\n\t" \
    "s_barrier\n\t" \
    "1:\n\t" \
    "v_mfma_f32_16x16x32_bf16 v[0:3], v[128:131], v[112:115], v[0:3]\n\t" \
    "ds_read_b128 v[156:159], v204 offset:0\n\t" \
    "v_mfma_f32_16x16x32_bf16 v[4:7], v[128:131], v[116:119], v[4:7]\n\t" \
    "ds_read_b64_tr_b16 v[172:173], v205 offset:0\n\t" \
    "v_mfma_f32_16x16x32_bf16 v[8:11], v[128:131], v[120:123], v[8:11]\n\t" \
    "ds_read_b64_tr_b16 v[174:175], v205 offset:1792\n\t" \
    "v_mfma_f32_16x16x32_bf16 v[12:15], v[128:131], v[124:127], v[12:15]\n\t" \
    "ds_read_b128 v[160:163], v204 offset:2048\n\t" \
    "v_mfma_f32_16x16x32_bf16 v[16:19], v[132:135], v[112:115], v[16:19]\n\t" \
    "ds_read_b128 v[164:167], v204 offset:4096\n\t" \
    "v_mfma_f32_16x16x32_bf16 v[20:23], v[132:135], v[116:119], v[20:23]\n\t" \
    "ds_read_b128 v[168:171], v204 offset:6144\n\t" \
    "v_mfma_f32_16x16x32_bf16 v[24:27], v[132:135], v[120:123], v[24:27]\n\t" \
    "ds_read_b64_tr_b16 v[176:177], v206 offset:32\n\t" \
    "v_mfma_f32_16x16x32_bf16 v[28:31], v[132:135], v[124:127], v[28:31]\n\t" \
    "ds_read_b64_tr_b16 v[178:179], v206 offset:1824\n\t" \
    "v_mfma_f32_16x16x32_bf16 v[32:35], v[136:139], v[112:115], v[32:35]\n\t" \
    "ds_read_b64_tr_b16 v[180:181], v205 offset:64\n\t" \
    "v_mfma_f32_16x16x32_bf16 v[36:39], v[136:139], v[116:119], v[36:39]\n\t" \
    "ds_read_b64_tr_b16 v[182:183], v205 offset:1856\n\t" \
    "v_mfma_f32_16x16x32_bf16 v[40:43], v[136:139], v[120:123], v[40:43]\n\t" \
    "ds_read_b64_tr_b16 v[184:185], v206 offset:96\n\t" \
    "v_mfma_f32_16x16x32_bf16 v[44:47], v[136:139], v[124:127], v[44:47]\n\t" \
    "ds_read_b64_tr_b16 v[186:187], v206 offset:1888\n\t" \
    "v_mfma_f32_16x16x32_bf16 v[48:51], v[140:143], v[112:115], v[48:51]\n\t" \
    "ds_read_b64_tr_b16 v[188:189], v205 offset:128\n\t" \
    "v_mfma_f32_16x16x32_bf16 v[52:55], v[140:143], v[116:119], v[52:55]\n\t" \
    "ds_read_b64_tr_b16 v[190:191], v205 offset:1920\n\t" \
    "v_mfma_f32_16x16x32_bf16 v[56:59], v[140:143], v[120:123], v[56:59]\n\t" \
    "ds_read_b64_tr_b16 v[192:193], v206 offset:160\n\t" \
    "v_mfma_f32_16x16x32_bf16 v[60:63], v[140:143], v[124:127], v[60:63]\n\t" \
    "ds_read_b64_tr_b16 v[194:195], v206 offset:1952\n\t" \
    "v_mfma_f32_16x16x32_bf16 v[64:67], v[144:147], v[112:115], v[64:67]\n\t" \
    "ds_read_b64_tr_b16 v[196:197], v205 offset:192\n\t" \
    "v_mfma_f32_16x16x32_bf16 v[68:71], v[144:147], v[116:119], v[68:71]\n\t" \
    "ds_read_b64_tr_b16 v[198:199], v205 offset:1984\n\t" \
    "v_mfma_f32_16x16x32_bf16 v[72:75], v[144:147], v[120:123], v[72:75]\n\t" \
    "v_mfma_f32_16x16x32_bf16 v[76:79], v[144:147], v[124:127], v[76:79]\n\t" \
    "s_add_u32 %[aslot], %[aslot], 16384\n\t" \
    "v_mfma_f32_16x16x32_bf16 v[80:83], v[148:151], v[112:115], v[80:83]\n\t" \
    "s_and_b32 %[aslot], %[aslot], 0xffff\n\t" \
    "v_mfma_f32_16x16x32_bf16 v[84:87], v[148:151], v[116:119], v[84:87]\n\t" \
    "s_add_u32 %[bslot], %[bslot], 14336\n\t" \
    "v_mfma_f32_16x16x32_bf16 v[88:91], v[148:151], v[120:123], v[88:91]\n\t" \
    "s_cmp_eq_u32 %[bslot], 86016\n\t" \
    "v_mfma_f32_16x16x32_bf16 v[92:95], v[148:151], v[124:127], v[92:95]\n\t" \
    "s_cselect_b32 %[bslot], 0, %[bslot]\n\t" \
    "v_mfma_f32_16x16x32_bf16 v[96:99], v[152:155], v[112:115], v[96:99]\n\t" \
    "v_add_u32 v207, %[aslot], v200\n\t" \
    "v_mfma_f32_16x16x32_bf16 v[100:103], v[152:155], v[116:119], v[100:103]\n\t" \
    "v_add_u32 v208, %[bslot], v202\n\t" \
    "v_mfma_f32_16x16x32_bf16 v[104:107], v[152:155], v[120:123], v[104:107]\n\t" \
    "v_add_u32 v209, %[bslot], v203\n\t" \
    "v_mfma_f32_16x16x32_bf16 v[108:111], v[152:155], v[124:127], v[108:111]\n\t" \
    "s_waitcnt lgkmcnt(0)\n\t" \
    "s_barrier\n\t" \
    "v_mfma_f32_16x16x32_bf16 v[0:3], v[172:175], v[156:159], v[0:3]\n\t" \
    "ds_read_b128 v[112:115], v207 offset:0\n\t" \
    "v_mfma_f32_16x16x32_bf16 v[4:7], v[172:175], v[160:163], v[4:7]\n\t" \
    "ds_read_b64_tr_b16 v[128:129], v208 offset:0\n\t" \
    "v_mfma_f32_16x16x32_bf16 v[8:11], v[172:175], v[164:167], v[8:11]\n\t" \
    "ds_read_b64_tr_b16 v[130:131], v208 offset:1792\n\t" \
    "v_mfma_f32_16x16x32_bf16 v[12:15], v[172:175], v[168:171], v[12:15]\n\t" \
    "ds_read_b128 v[116:119], v207 offset:2048\n\t" \
    "v_mfma_f32_16x16x32_bf16 v[16:19], v[176:179], v[156:159], v[16:19]\n\t" \
    "ds_read_b128 v[120:123], v207 offset:4096\n\t" \
    "v_mfma_f32_16x16x32_bf16 v[20:23], v[176:179], v[160:163], v[20:23]\n\t" \
    "ds_read_b128 v[124:127], v207 offset:6144\n\t" \
    "v_mfma_f32_16x16x32_bf16 v[24:27], v[176:179], v[164:167], v[24:27]\n\t" \
    "ds_read_b64_tr_b16 v[132:133], v209 offset:32\n\t" \
    "v_mfma_f32_16x16x32_bf16 v[28:31], v[176:179], v[168:171], v[28:31]\n\t" \
    "ds_read_b64_tr_b16 v[134:135], v209 offset:1824\n\t" \
    "v_mfma_f32_16x16x32_bf16 v[32:35], v[180:183], v[156:159], v[32:35]\n\t" \
    "ds_read_b64_tr_b16 v[136:137], v208 offset:64\n\t" \
    "v_mfma_f32_16x16x32_bf16 v[36:39], v[180:183], v[160:163], v[36:39]\n\t" \
    "ds_read_b64_tr_b16 v[138:139], v208 offset:1856\n\t" \
    "v_mfma_f32_16x16x32_bf16 v[40:43], v[180:183], v[164:167], v[40:43]\n\t" \
    "ds_read_b64_tr_b16 v[140:141], v209 offset:96\n\t" \
    "v_mfma_f32_16x16x32_bf16 v[44:47], v[180:183], v[168:171], v[44:47]\n\t" \
    "ds_read_b64_tr_b16 v[142:143], v209 offset:1888\n\t" \
    "v_mfma_f32_16x16x32_bf16 v[48:51], v[184:187], v[156:159], v[48:51]\n\t" \
    "ds_read_b64_tr_b16 v[144:145], v208 offset:128\n\t" \
    "v_mfma_f32_16x16x32_bf16 v[52:55], v[184:187], v[160:163], v[52:55]\n\t" \
    "ds_read_b64_tr_b16 v[146:147], v208 offset:1920\n\t" \
    "v_mfma_f32_16x16x32_bf16 v[56:59], v[184:187], v[164:167], v[56:59]\n\t" \
    "ds_read_b64_tr_b16 v[148:149], v209 offset:160\n\t" \
    "v_mfma_f32_16x16x32_bf16 v[60:63], v[184:187], v[168:171], v[60:63]\n\t" \
    "ds_read_b64_tr_b16 v[150:151], v209 offset:1952\n\t" \
    "v_mfma_f32_16x16x32_bf16 v[64:67], v[188:191], v[156:159], v[64:67]\n\t" \
    "ds_read_b64_tr_b16 v[152:153], v208 offset:192\n\t" \
    "v_mfma_f32_16x16x32_bf16 v[68:71], v[188:191], v[160:163], v[68:71]\n\t" \
    "ds_read_b64_tr_b16 v[154:155], v208 offset:1984\n\t" \
    "v_mfma_f32_16x16x32_bf16 v[72:75], v[188:191], v[164:167], v[72:75]\n\t" \
    "v_mfma_f32_16x16x32_bf16 v[76:79], v[188:191], v[168:171], v[76:79]\n\t" \
    "s_add_u32 %[bslot], %[bslot], 14336\n\t" \
    "v_mfma_f32_16x16x32_bf16 v[80:83], v[192:195], v[156:159], v[80:83]\n\t" \
    "s_cmp_eq_u32 %[bslot], 86016\n\t" \
    "v_mfma_f32_16x16x32_bf16 v[84:87], v[192:195], v[160:163], v[84:87]\n\t" \
    "s_cselect_b32 %[bslot], 0, %[bslot]\n\t" \
    "v_mfma_f32_16x16x32_bf16 v[88:91], v[192:195], v[164:167], v[88:91]\n\t" \
    "v_add_u32 v204, %[aslot], v201\n\t" \
    "v_mfma_f32_16x16x32_bf16 v[92:95], v[192:195], v[168:171], v[92:95]\n\t" \
    "v_add_u32 v205, %[bslot], v202\n\t" \
    "v_mfma_f32_16x16x32_bf16 v[96:99], v[196:199], v[156:159], v[96:99]\n\t" \
    "v_add_u32 v206, %[bslot], v203\n\t" \
    "v_mfma_f32_16x16x32_bf16 v[100:103], v[196:199], v[160:163], v[100:103]\n\t" \
    "s_sub_u32 %[cnt], %[cnt], 1\n\t" \
    "v_mfma_f32_16x16x32_bf16 v[104:107], v[196:199], v[164:167], v[104:107]\n\t" \
    "s_cmp_lg_u32 %[cnt], 0\n\t" \
    "v_mfma_f32_16x16x32_bf16 v[108:111], v[196:199], v[168:171], v[108:111]\n\t" \
    "s_waitcnt lgkmcnt(0)\n\t" \
    "s_barrier\n\t" \
    "s_cbranch_scc1 1b\n\t" \
    "v_mfma_f32_16x16x32_bf16 v[0:3], v[128:131], v[112:115], v[0:3]\n\t" \
    "ds_read_b128 v[156:159], v204 offset:0\n\t" \
    "v_mfma_f32_16x16x32_bf16 v[4:7], v[128:131], v[116:119], v[4:7]\n\t" \
    "ds_read_b64_tr_b16 v[172:173], v205 offset:0\n\t" \
    "v_mfma_f32_16x16x32_bf16 v[8:11], v[128:131], v[120:123], v[8:11]\n\t" \
    "ds_read_b64_tr_b16 v[174:175], v205 offset:1792\n\t" \
    "v_mfma_f32_16x16x32_bf16 v[12:15], v[128:131], v[124:127], v[12:15]\n\t" \
    "ds_read_b128 v[160:163], v204 offset:2048\n\t" \
    "v_mfma_f32_16x16x32_bf16 v[16:19], v[132:135], v[112:115], v[16:19]\n\t" \
    "ds_read_b128 v[164:167], v204 offset:4096\n\t" \
    "v_mfma_f32_16x16x32_bf16 v[20:23], v[132:135], v[116:119], v[20:23]\n\t" \
    "ds_read_b128 v[168:171], v204 offset:6144\n\t" \
    "v_mfma_f32_16x16x32_bf16 v[24:27], v[132:135], v[120:123], v[24:27]\n\t" \
    "ds_read_b64_tr_b16 v[176:177], v206 offset:32\n\t" \
    "v_mfma_f32_16x16x32_bf16 v[28:31], v[132:135], v[124:127], v[28:31]\n\t" \
    "ds_read_b64_tr_b16 v[178:179], v206 offset:1824\n\t" \
    "v_mfma_f32_16x16x32_bf16 v[32:35], v[136:139], v[112:115], v[32:35]\n\t" \
    "ds_read_b64_tr_b16 v[180:181], v205 offset:64\n\t" \
    "v_mfma_f32_16x16x32_bf16 v[36:39], v[136:139], v[116:119], v[36:39]\n\t" \
    "ds_read_b64_tr_b16 v[182:183], v205 offset:1856\n\t" \
    "v_mfma_f32_16x16x32_bf16 v[40:43], v[136:139], v[120:123], v[40:43]\n\t" \
    "ds_read_b64_tr_b16 v[184:185], v206 offset:96\n\t" \
    "v_mfma_f32_16x16x32_bf16 v[44:47], v[136:139], v[124:127], v[44:47]\n\t" \
    "ds_read_b64_tr_b16 v[186:187], v206 offset:1888\n\t" \
    "v_mfma_f32_16x16x32_bf16 v[48:51], v[140:143], v[112:115], v[48:51]\n\t" \
    "ds_read_b64_tr_b16 v[188:189], v205 offset:128\n\t" \
    "v_mfma_f32_16x16x32_bf16 v[52:55], v[140:143], v[116:119], v[52:55]\n\t" \
    "ds_read_b64_tr_b16 v[190:191], v205 offset:1920\n\t" \
    "v_mfma_f32_16x16x32_bf16 v[56:59], v[140:143], v[120:123], v[56:59]\n\t" \
    "ds_read_b64_tr_b16 v[192:193], v206 offset:160\n\t" \
    "v_mfma_f32_16x16x32_bf16 v[60:63], v[140:143], v[124:127], v[60:63]\n\t" \
    "ds_read_b64_tr_b16 v[194:195], v206 offset:1952\n\t" \
    "v_mfma_f32_16x16x32_bf16 v[64:67], v[144:147], v[112:115], v[64:67]\n\t" \
    "ds_read_b64_tr_b16 v[196:197], v205 offset:192\n\t" \
    "v_mfma_f32_16x16x32_bf16 v[68:71], v[144:147], v[116:119], v[68:71]\n\t" \
    "ds_read_b64_tr_b16 v[198:199], v205 offset:1984\n\t" \
    "v_mfma_f32_16x16x32_bf16 v[72:75], v[144:147], v[120:123], v[72:75]\n\t" \
    "v_mfma_f32_16x16x32_bf16 v[76:79], v[144:147], v[124:127], v[76:79]\n\t" \
    "v_mfma_f32_16x16x32_bf16 v[80:83], v[148:151], v[112:115], v[80:83]\n\t" \
    "v_mfma_f32_16x16x32_bf16 v[84:87], v[148:151], v[116:119], v[84:87]\n\t" \
    "v_mfma_f32_16x16x32_bf16 v[88:91], v[148:151], v[120:123], v[88:91]\n\t" \
    "v_mfma_f32_16x16x32_bf16 v[92:95], v[148:151], v[124:127], v[92:95]\n\t" \
    "v_mfma_f32_16x16x32_bf16 v[96:99], v[152:155], v[112:115], v[96:99]\n\t" \
    "v_mfma_f32_16x16x32_bf16 v[100:103], v[152:155], v[116:119], v[100:103]\n\t" \
    "v_mfma_f32_16x16x32_bf16 v[104:107], v[152:155], v[120:123], v[104:107]\n\t" \
    "v_mfma_f32_16x16x32_bf16 v[108:111], v[152:155], v[124:127], v[108:111]\n\t" \
    "s_waitcnt lgkmcnt(0)\n\t" \
    "s_barrier\n\t" \
    "v_mfma_f32_16x16x32_bf16 v[0:3], v[172:175], v[156:159], v[0:3]\n\t" \
    "v_mfma_f32_16x16x32_bf16 v[4:7], v[172:175], v[160:163], v[4:7]\n\t" \
    "v_mfma_f32_16x16x32_bf16 v[8:11], v[172:175], v[164:167], v[8:11]\n\t" \
    "v_mfma_f32_16x16x32_bf16 v[12:15], v[172:175], v[168:171], v[12:15]\n\t" \
    "v_mfma_f32_16x16x32_bf16 v[16:19], v[176:179], v[156:159], v[16:19]\n\t" \
    "v_mfma_f32_16x16x32_bf16 v[20:23], v[176:179], v[160:163], v[20:23]\n\t" \
    "v_mfma_f32_16x16x32_bf16 v[24:27], v[176:179], v[164:167], v[24:27]\n\t" \
    "v_mfma_f32_16x16x32_bf16 v[28:31], v[176:179], v[168:171], v[28:31]\n\t" \
    "v_mfma_f32_16x16x32_bf16 v[32:35], v[180:183], v[156:159], v[32:35]\n\t" \
    "v_mfma_f32_16x16x32_bf16 v[36:39], v[180:183], v[160:163], v[36:39]\n\t" \
    "v_mfma_f32_16x16x32_bf16 v[40:43], v[180:183], v[164:167], v[40:43]\n\t" \
    "v_mfma_f32_16x16x32_bf16 v[44:47], v[180:183], v[168:171], v[44:47]\n\t" \
    "v_mfma_f32_16x16x32_bf16 v[48:51], v[184:187], v[156:159], v[48:51]\n\t" \
    "v_mfma_f32_16x16x32_bf16 v[52:55], v[184:187], v[160:163], v[52:55]\n\t" \
    "v_mfma_f32_16x16x32_bf16 v[56:59], v[184:187], v[164:167], v[56:59]\n\t" \
    "v_mfma_f32_16x16x32_bf16 v[60:63], v[184:187], v[168:171], v[60:63]\n\t" \
    "v_mfma_f32_16x16x32_bf16 v[64:67], v[188:191], v[156:159], v[64:67]\n\t" \
    "v_mfma_f32_16x16x32_bf16 v[68:71], v[188:191], v[160:163], v[68:71]\n\t" \
    "v_mfma_f32_16x16x32_bf16 v[72:75], v[188:191], v[164:167], v[72:75]\n\t" \
    "v_mfma_f32_16x16x32_bf16 v[76:79], v[188:191], v[168:171], v[76:79]\n\t" \
    "v_mfma_f32_16x16x32_bf16 v[80:83], v[192:195], v[156:159], v[80:83]\n\t" \
    "v_mfma_f32_16x16x32_bf16 v[84:87], v[192:195], v[160:163], v[84:87]\n\t" \
    "v_mfma_f32_16x16x32_bf16 v[88:91], v[192:195], v[164:167], v[88:91]\n\t" \
    "v_mfma_f32_16x16x32_bf16 v[92:95], v[192:195], v[168:171], v[92:95]\n\t" \
    "v_mfma_f32_16x16x32_bf16 v[96:99], v[196:199], v[156:159], v[96:99]\n\t" \
    "v_mfma_f32_16x16x32_bf16 v[100:103], v[196:199], v[160:163], v[100:103]\n\t" \
    "v_mfma_f32_16x16x32_bf16 v[104:107], v[196:199], v[164:167], v[104:107]\n\t" \
    "v_mfma_f32_16x16x32_bf16 v[108:111], v[196:199], v[168:171], v[108:111]\n\t" \
    "s_waitcnt lgkmcnt(0)\n\t" \
    "s_barrier\n\t" \
    "s_nop 7\n\t" \
    "s_nop 7\n\t" \
    ""
#define G224_BODY_SWAP \
    "v_mov_b32 v0, 0\n\t" \
    "v_mov_b32 v1, 0\n\t" \
    "v_mov_b32 v2, 0\n\t" \
    "v_mov_b32 v3, 0\n\t" \
    "v_mov_b32 v4, 0\n\t" \
    "v_mov_b32 v5, 0\n\t" \
    "v_mov_b32 v6, 0\n\t" \
    "v_mov_b32 v7, 0\n\t" \
    "v_mov_b32 v8, 0\n\t" \
    "v_mov_b32 v9, 0\n\t" \
    "v_mov_b32 v10, 0\n\t" \
    "v_mov_b32 v11, 0\n\t" \
    "v_mov_b32 v12, 0\n\t" \
    "v_mov_b32 v13, 0\n\t" \
    "v_mov_b32 v14, 0\n\t" \
    "v_mov_b32 v15, 0\n\t" \
    "v_mov_b32 v16, 0\n\t" \
    "v_mov_b32 v17, 0\n\t" \
    "v_mov_b32 v18, 0\n\t" \
    "v_mov_b32 v19, 0\n\t" \
    "v_mov_b32 v20, 0\n\t" \
    "v_mov_b32 v21, 0\n\t" \
    "v_mov_b32 v22, 0\n\t" \
    "v_mov_b32 v23, 0\n\t" \
    "v_mov_b32 v24, 0\n\t" \
    "v_mov_b32 v25, 0\n\t" \
    "v_mov_b32 v26, 0\n\t" \
    "v_mov_b32 v27, 0\n\t" \
    "v_mov_b32 v28, 0\n\t" \
    "v_mov_b32 v29, 0\n\t" \
    "v_mov_b32 v30, 0\n\t" \
    "v_mov_b32 v31, 0\n\t" \
    "v_mov_b32 v32, 0\n\t" \
    "v_mov_b32 v33, 0\n\t" \
    "v_mov_b32 v34, 0\n\t" \
    "v_mov_b32 v35, 0\n\t" \
    "v_mov_b32 v36, 0\n\t" \
    "v_mov_b32 v37, 0\n\t" \
    "v_mov_b32 v38, 0\n\t" \
    "v_mov_b32 v39, 0\n\t" \
    "v_mov_b32 v40, 0\n\t" \
    "v_mov_b32 v41, 0\n\t" \
    "v_mov_b32 v42, 0\n\t" \
    "v_mov_b32 v43, 0\n\t" \
    "v_mov_b32 v44, 0\n\t" \
    "v_mov_b32 v45, 0\n\t" \
    "v_mov_b32 v46, 0\n\t" \
    "v_mov_b32 v47, 0\n\t" \
    "v_mov_b32 v48, 0\n\t" \
    "v_mov_b32 v49, 0\n\t" \
    "v_mov_b32 v50, 0\n\t" \
    "v_mov_b32 v51, 0\n\t" \
    "v_mov_b32 v52, 0\n\t" \
    "v_mov_b32 v53, 0\n\t" \
    "v_mov_b32 v54, 0\n\t" \
    "v_mov_b32 v55, 0\n\t" \
    "v_mov_b32 v56, 0\n\t" \
    "v_mov_b32 v57, 0\n\t" \
    "v_mov_b32 v58, 0\n\t" \
    "v_mov_b32 v59, 0\n\t" \
    "v_mov_b32 v60, 0\n\t" \
    "v_mov_b32 v61, 0\n\t" \
    "v_mov_b32 v62, 0\n\t" \
    "v_mov_b32 v63, 0\n\t" \
    "v_mov_b32 v64, 0\n\t" \
    "v_mov_b32 v65, 0\n\t" \
    "v_mov_b32 v66, 0\n\t" \
    "v_mov_b32 v67, 0\n\t" \
    "v_mov_b32 v68, 0\n\t" \
    "v_mov_b32 v69, 0\n\t" \
    "v_mov_b32 v70, 0\n\t" \
    "v_mov_b32 v71, 0\n\t" \
    "v_mov_b32 v72, 0\n\t" \
    "v_mov_b32 v73, 0\n\t" \
    "v_mov_b32 v74, 0\n\t" \
    "v_mov_b32 v75, 0\n\t" \
    "v_mov_b32 v76, 0\n\t" \
    "v_mov_b32 v77, 0\n\t" \
    "v_mov_b32 v78, 0\n\t" \
    "v_mov_b32 v79, 0\n\t" \
    "v_mov_b32 v80, 0\n\t" \
    "v_mov_b32 v81, 0\n\t" \
    "v_mov_b32 v82, 0\n\t" \
    "v_mov_b32 v83, 0\n\t" \
    "v_mov_b32 v84, 0\n\t" \
    "v_mov_b32 v85, 0\n\t" \
    "v_mov_b32 v86, 0\n\t" \
    "v_mov_b32 v87, 0\n\t" \
    "v_mov_b32 v88, 0\n\t" \
    "v_mov_b32 v89, 0\n\t" \
    "v_mov_b32 v90, 0\n\t" \
    "v_mov_b32 v91, 0\n\t" \
    "v_mov_b32 v92, 0\n\t" \
    "v_mov_b32 v93, 0\n\t" \
    "v_mov_b32 v94, 0\n\t" \
    "v_mov_b32 v95, 0\n\t" \
    "v_mov_b32 v96, 0\n\t" \
    "v_mov_b32 v97, 0\n\t" \
    "v_mov_b32 v98, 0\n\t" \
    "v_mov_b32 v99, 0\n\t" \
    "v_mov_b32 v100, 0\n\t" \
    "v_mov_b32 v101, 0\n\t" \
    "v_mov_b32 v102, 0\n\t" \
    "v_mov_b32 v103, 0\n\t" \
    "v_mov_b32 v104, 0\n\t" \
    "v_mov_b32 v105, 0\n\t" \
    "v_mov_b32 v106, 0\n\t" \
    "v_mov_b32 v107, 0\n\t" \
    "v_mov_b32 v108, 0\n\t" \
    "v_mov_b32 v109, 0\n\t" \
    "v_mov_b32 v110, 0\n\t" \
    "v_mov_b32 v111, 0\n\t" \
    "s_waitcnt lgkmcnt(0)\n\t" \
    "s_barrier\n\t" \
    "v_mov_b32 v207, v200\n\t" \
    "v_mov_b32 v208, v202\n\t" \
    "v_mov_b32 v209, v203\n\t" \
    "ds_read_b128 v[112:115], v207 offset:0\n\t" \
    "ds_read_b64_tr_b16 v[128:129], v208 offset:0\n\t" \
    "ds_read_b64_tr_b16 v[130:131], v208 offset:1792\n\t" \
    "ds_read_b128 v[116:119], v207 offset:2048\n\t" \
    "ds_read_b128 v[120:123], v207 offset:4096\n\t" \
    "ds_read_b128 v[124:127], v207 offset:6144\n\t" \
    "ds_read_b64_tr_b16 v[132:133], v209 offset:32\n\t" \
    "ds_read_b64_tr_b16 v[134:135], v209 offset:1824\n\t" \
    "ds_read_b64_tr_b16 v[136:137], v208 offset:64\n\t" \
    "ds_read_b64_tr_b16 v[138:139], v208 offset:1856\n\t" \
    "ds_read_b64_tr_b16 v[140:141], v209 offset:96\n\t" \
    "ds_read_b64_tr_b16 v[142:143], v209 offset:1888\n\t" \
    "ds_read_b64_tr_b16 v[144:145], v208 offset:128\n\t" \
    "ds_read_b64_tr_b16 v[146:147], v208 offset:1920\n\t" \
    "ds_read_b64_tr_b16 v[148:149], v209 offset:160\n\t" \
    "ds_read_b64_tr_b16 v[150:151], v209 offset:1952\n\t" \
    "ds_read_b64_tr_b16 v[152:153], v208 offset:192\n\t" \
    "ds_read_b64_tr_b16 v[154:155], v208 offset:1984\n\t" \
    "s_mov_b32 %[aslot], 0\n\t" \
    "s_mov_b32 %[bslot], 14336\n\t" \
    "v_mov_b32 v204, v201\n\t" \
    "v_add_u32 v205, %[bslot], v202\n\t" \
    "v_add_u32 v206, %[bslot], v203\n\t" \
    "s_waitcnt lgkmcnt(0)\n\t" \
    "s_barrier\n\t" \
    "1:\n\t" \
    "v_mfma_f32_16x16x32_bf16 v[0:3], v[112:115], v[128:131], v[0:3]\n\t" \
    "ds_read_b128 v[156:159], v204 offset:0\n\t" \
    "v_mfma_f32_16x16x32_bf16 v[4:7], v[116:119], v[128:131], v[4:7]\n\t" \
    "ds_read_b64_tr_b16 v[172:173], v205 offset:0\n\t" \
    "v_mfma_f32_16x16x32_bf16 v[8:11], v[120:123], v[128:131], v[8:11]\n\t" \
    "ds_read_b64_tr_b16 v[174:175], v205 offset:1792\n\t" \
    "v_mfma_f32_16x16x32_bf16 v[12:15], v[124:127], v[128:131], v[12:15]\n\t" \
    "ds_read_b128 v[160:163], v204 offset:2048\n\t" \
    "v_mfma_f32_16x16x32_bf16 v[16:19], v[112:115], v[132:135], v[16:19]\n\t" \
    "ds_read_b128 v[164:167], v204 offset:4096\n\t" \
    "v_mfma_f32_16x16x32_bf16 v[20:23], v[116:119], v[132:135], v[20:23]\n\t" \
    "ds_read_b128 v[168:171], v204 offset:6144\n\t" \
    "v_mfma_f32_16x16x32_bf16 v[24:27], v[120:123], v[132:135], v[24:27]\n\t" \
    "ds_read_b64_tr_b16 v[176:177], v206 offset:32\n\t" \
    "v_mfma_f32_16x16x32_bf16 v[28:31], v[124:127], v[132:135], v[28:31]\n\t" \
    "ds_read_b64_tr_b16 v[178:179], v206 offset:1824\n\t" \
    "v_mfma_f32_16x16x32_bf16 v[32:35], v[112:115], v[136:139], v[32:35]\n\t" \
    "ds_read_b64_tr_b16 v[180:181], v205 offset:64\n\t" \
    "v_mfma_f32_16x16x32_bf16 v[36:39], v[116:119], v[136:139], v[36:39]\n\t" \
    "ds_read_b64_tr_b16 v[182:183], v205 offset:1856\n\t" \
    "v_mfma_f32_16x16x32_bf16 v[40:43], v[120:123], v[136:139], v[40:43]\n\t" \
    "ds_read_b64_tr_b16 v[184:185], v206 offset:96\n\t" \
    "v_mfma_f32_16x16x32_bf16 v[44:47], v[124:127], v[136:139], v[44:47]\n\t" \
    "ds_read_b64_tr_b16 v[186:187], v206 offset:1888\n\t" \
    "v_mfma_f32_16x16x32_bf16 v[48:51], v[112:115], v[140:143], v[48:51]\n\t" \
    "ds_read_b64_tr_b16 v[188:189], v205 offset:128\n\t" \
    "v_mfma_f32_16x16x32_bf16 v[52:55], v[116:119], v[140:143], v[52:55]\n\t" \
    "ds_read_b64_tr_b16 v[190:191], v205 offset:1920\n\t" \
    "v_mfma_f32_16x16x32_bf16 v[56:59], v[120:123], v[140:143], v[56:59]\n\t" \
    "ds_read_b64_tr_b16 v[192:193], v206 offset:160\n\t" \
    "v_mfma_f32_16x16x32_bf16 v[60:63], v[124:127], v[140:143], v[60:63]\n\t" \
    "ds_read_b64_tr_b16 v[194:195], v206 offset:1952\n\t" \
    "v_mfma_f32_16x16x32_bf16 v[64:67], v[112:115], v[144:147], v[64:67]\n\t" \
    "ds_read_b64_tr_b16 v[196:197], v205 offset:192\n\t" \
    "v_mfma_f32_16x16x32_bf16 v[68:71], v[116:119], v[144:147], v[68:71]\n\t" \
    "ds_read_b64_tr_b16 v[198:199], v205 offset:1984\n\t" \
    "v_mfma_f32_16x16x32_bf16 v[72:75], v[120:123], v[144:147], v[72:75]\n\t" \
    "v_mfma_f32_16x16x32_bf16 v[76:79], v[124:127], v[144:147], v[76:79]\n\t" \
    "s_add_u32 %[aslot], %[aslot], 16384\n\t" \
    "v_mfma_f32_16x16x32_bf16 v[80:83], v[112:115], v[148:151], v[80:83]\n\t" \
    "s_and_b32 %[aslot], %[aslot], 0xffff\n\t" \
    "v_mfma_f32_16x16x32_bf16 v[84:87], v[116:119], v[148:151], v[84:87]\n\t" \
    "s_add_u32 %[bslot], %[bslot], 14336\n\t" \
    "v_mfma_f32_16x16x32_bf16 v[88:91], v[120:123], v[148:151], v[88:91]\n\t" \
    "s_cmp_eq_u32 %[bslot], 86016\n\t" \
    "v_mfma_f32_16x16x32_bf16 v[92:95], v[124:127], v[148:151], v[92:95]\n\t" \
    "s_cselect_b32 %[bslot], 0, %[bslot]\n\t" \
    "v_mfma_f32_16x16x32_bf16 v[96:99], v[112:115], v[152:155], v[96:99]\n\t" \
    "v_add_u32 v207, %[aslot], v200\n\t" \
    "v_mfma_f32_16x16x32_bf16 v[100:103], v[116:119], v[152:155], v[100:103]\n\t" \
    "v_add_u32 v208, %[bslot], v202\n\t" \
    "v_mfma_f32_16x16x32_bf16 v[104:107], v[120:123], v[152:155], v[104:107]\n\t" \
    "v_add_u32 v209, %[bslot], v203\n\t" \
    "v_mfma_f32_16x16x32_bf16 v[108:111], v[124:127], v[152:155], v[108:111]\n\t" \
    "s_waitcnt lgkmcnt(0)\n\t" \
    "s_barrier\n\t" \
    "v_mfma_f32_16x16x32_bf16 v[0:3], v[156:159], v[172:175], v[0:3]\n\t" \
    "ds_read_b128 v[112:115], v207 offset:0\n\t" \
    "v_mfma_f32_16x16x32_bf16 v[4:7], v[160:163], v[172:175], v[4:7]\n\t" \
    "ds_read_b64_tr_b16 v[128:129], v208 offset:0\n\t" \
    "v_mfma_f32_16x16x32_bf16 v[8:11], v[164:167], v[172:175], v[8:11]\n\t" \
    "ds_read_b64_tr_b16 v[130:131], v208 offset:1792\n\t" \
    "v_mfma_f32_16x16x32_bf16 v[12:15], v[168:171], v[172:175], v[12:15]\n\t" \
    "ds_read_b128 v[116:119], v207 offset:2048\n\t" \
    "v_mfma_f32_16x16x32_bf16 v[16:19], v[156:159], v[176:179], v[16:19]\n\t" \
    "ds_read_b128 v[120:123], v207 offset:4096\n\t" \
    "v_mfma_f32_16x16x32_bf16 v[20:23], v[160:163], v[176:179], v[20:23]\n\t" \
    "ds_read_b128 v[124:127], v207 offset:6144\n\t" \
    "v_mfma_f32_16x16x32_bf16 v[24:27], v[164:167], v[176:179], v[24:27]\n\t" \
    "ds_read_b64_tr_b16 v[132:133], v209 offset:32\n\t" \
    "v_mfma_f32_16x16x32_bf16 v[28:31], v[168:171], v[176:179], v[28:31]\n\t" \
    "ds_read_b64_tr_b16 v[134:135], v209 offset:1824\n\t" \
    "v_mfma_f32_16x16x32_bf16 v[32:35], v[156:159], v[180:183], v[32:35]\n\t" \
    "ds_read_b64_tr_b16 v[136:137], v208 offset:64\n\t" \
    "v_mfma_f32_16x16x32_bf16 v[36:39], v[160:163], v[180:183], v[36:39]\n\t" \
    "ds_read_b64_tr_b16 v[138:139], v208 offset:1856\n\t" \
    "v_mfma_f32_16x16x32_bf16 v[40:43], v[164:167], v[180:183], v[40:43]\n\t" \
    "ds_read_b64_tr_b16 v[140:141], v209 offset:96\n\t" \
    "v_mfma_f32_16x16x32_bf16 v[44:47], v[168:171], v[180:183], v[44:47]\n\t" \
    "ds_read_b64_tr_b16 v[142:143], v209 offset:1888\n\t" \
    "v_mfma_f32_16x16x32_bf16 v[48:51], v[156:159], v[184:187], v[48:51]\n\t" \
    "ds_read_b64_tr_b16 v[144:145], v208 offset:128\n\t" \
    "v_mfma_f32_16x16x32_bf16 v[52:55], v[160:163], v[184:187], v[52:55]\n\t" \
    "ds_read_b64_tr_b16 v[146:147], v208 offset:1920\n\t" \
    "v_mfma_f32_16x16x32_bf16 v[56:59], v[164:167], v[184:187], v[56:59]\n\t" \
    "ds_read_b64_tr_b16 v[148:149], v209 offset:160\n\t" \
    "v_mfma_f32_16x16x32_bf16 v[60:63], v[168:171], v[184:187], v[60:63]\n\t" \
    "ds_read_b64_tr_b16 v[150:151], v209 offset:1952\n\t" \
    "v_mfma_f32_16x16x32_bf16 v[64:67], v[156:159], v[188:191], v[64:67]\n\t" \
    "ds_read_b64_tr_b16 v[152:153], v208 offset:192\n\t" \
    "v_mfma_f32_16x16x32_bf16 v[68:71], v[160:163], v[188:191], v[68:71]\n\t" \
    "ds_read_b64_tr_b16 v[154:155], v208 offset:1984\n\t" \
    "v_mfma_f32_16x16x32_bf16 v[72:75], v[164:167], v[188:191], v[72:75]\n\t" \
    "v_mfma_f32_16x16x32_bf16 v[76:79], v[168:171], v[188:191], v[76:79]\n\t" \
    "s_add_u32 %[bslot], %[bslot], 14336\n\t" \
    "v_mfma_f32_16x16x32_bf16 v[80:83], v[156:159], v[192:195], v[80:83]\n\t" \
    "s_cmp_eq_u32 %[bslot], 86016\n\t" \
    "v_mfma_f32_16x16x32_bf16 v[84:87], v[160:163], v[192:195], v[84:87]\n\t" \
    "s_cselect_b32 %[bslot], 0, %[bslot]\n\t" \
    "v_mfma_f32_16x16x32_bf16 v[88:91], v[164:167], v[192:195], v[88:91]\n\t" \
    "v_add_u32 v204, %[aslot], v201\n\t" \
    "v_mfma_f32_16x16x32_bf16 v[92:95], v[168:171], v[192:195], v[92:95]\n\t" \
    "v_add_u32 v205, %[bslot], v202\n\t" \
    "v_mfma_f32_16x16x32_bf16 v[96:99], v[156:159], v[196:199], v[96:99]\n\t" \
    "v_add_u32 v206, %[bslot], v203\n\t" \
    "v_mfma_f32_16x16x32_bf16 v[100:103], v[160:163], v[196:199], v[100:103]\n\t" \
    "s_sub_u32 %[cnt], %[cnt], 1\n\t" \
    "v_mfma_f32_16x16x32_bf16 v[104:107], v[164:167], v[196:199], v[104:107]\n\t" \
    "s_cmp_lg_u32 %[cnt], 0\n\t" \
    "v_mfma_f32_16x16x32_bf16 v[108:111], v[168:171], v[196:199], v[108:111]\n\t" \
    "s_waitcnt lgkmcnt(0)\n\t" \
    "s_barrier\n\t" \
    "s_cbranch_scc1 1b\n\t" \
    "v_mfma_f32_16x16x32_bf16 v[0:3], v[112:115], v[128:131], v[0:3]\n\t" \
    "ds_read_b128 v[156:159], v204 offset:0\n\t" \
    "v_mfma_f32_16x16x32_bf16 v[4:7], v[116:119], v[128:131], v[4:7]\n\t" \
    "ds_read_b64_tr_b16 v[172:173], v205 offset:0\n\t" \
    "v_mfma_f32_16x16x32_bf16 v[8:11], v[120:123], v[128:131], v[8:11]\n\t" \
    "ds_read_b64_tr_b16 v[174:175], v205 offset:1792\n\t" \
    "v_mfma_f32_16x16x32_bf16 v[12:15], v[124:127], v[128:131], v[12:15]\n\t" \
    "ds_read_b128 v[160:163], v204 offset:2048\n\t" \
    "v_mfma_f32_16x16x32_bf16 v[16:19], v[112:115], v[132:135], v[16:19]\n\t" \
    "ds_read_b128 v[164:167], v204 offset:4096\n\t" \
    "v_mfma_f32_16x16x32_bf16 v[20:23], v[116:119], v[132:135], v[20:23]\n\t" \
    "ds_read_b128 v[168:171], v204 offset:6144\n\t" \
    "v_mfma_f32_16x16x32_bf16 v[24:27], v[120:123], v[132:135], v[24:27]\n\t" \
    "ds_read_b64_tr_b16 v[176:177], v206 offset:32\n\t" \
    "v_mfma_f32_16x16x32_bf16 v[28:31], v[124:127], v[132:135], v[28:31]\n\t" \
    "ds_read_b64_tr_b16 v[178:179], v206 offset:1824\n\t" \
    "v_mfma_f32_16x16x32_bf16 v[32:35], v[112:115], v[136:139], v[32:35]\n\t" \
    "ds_read_b64_tr_b16 v[180:181], v205 offset:64\n\t" \
    "v_mfma_f32_16x16x32_bf16 v[36:39], v[116:119], v[136:139], v[36:39]\n\t" \
    "ds_read_b64_tr_b16 v[182:183], v205 offset:1856\n\t" \
    "v_mfma_f32_16x16x32_bf16 v[40:43], v[120:123], v[136:139], v[40:43]\n\t" \
    "ds_read_b64_tr_b16 v[184:185], v206 offset:96\n\t" \
    "v_mfma_f32_16x16x32_bf16 v[44:47], v[124:127], v[136:139], v[44:47]\n\t" \
    "ds_read_b64_tr_b16 v[186:187], v206 offset:1888\n\t" \
    "v_mfma_f32_16x16x32_bf16 v[48:51], v[112:115], v[140:143], v[48:51]\n\t" \
    "ds_read_b64_tr_b16 v[188:189], v205 offset:128\n\t" \
    "v_mfma_f32_16x16x32_bf16 v[52:55], v[116:119], v[140:143], v[52:55]\n\t" \
    "ds_read_b64_tr_b16 v[190:191], v205 offset:1920\n\t" \
    "v_mfma_f32_16x16x32_bf16 v[56:59], v[120:123], v[140:143], v[56:59]\n\t" \
    "ds_read_b64_tr_b16 v[192:193], v206 offset:160\n\t" \
    "v_mfma_f32_16x16x32_bf16 v[60:63], v[124:127], v[140:143], v[60:63]\n\t" \
    "ds_read_b64_tr_b16 v[194:195], v206 offset:1952\n\t" \
    "v_mfma_f32_16x16x32_bf16 v[64:67], v[112:115], v[144:147], v[64:67]\n\t" \
    "ds_read_b64_tr_b16 v[196:197], v205 offset:192\n\t" \
    "v_mfma_f32_16x16x32_bf16 v[68:71], v[116:119], v[144:147], v[68:71]\n\t" \
    "ds_read_b64_tr_b16 v[198:199], v205 offset:1984\n\t" \
    "v_mfma_f32_16x16x32_bf16 v[72:75], v[120:123], v[144:147], v[72:75]\n\t" \
    "v_mfma_f32_16x16x32_bf16 v[76:79], v[124:127], v[144:147], v[76:79]\n\t" \
    "v_mfma_f32_16x16x32_bf16 v[80:83], v[112:115], v[148:151], v[80:83]\n\t" \
    "v_mfma_f32_16x16x32_bf16 v[84:87], v[116:119], v[148:151], v[84:87]\n\t" \
    "v_mfma_f32_16x16x32_bf16 v[88:91], v[120:123], v[148:151], v[88:91]\n\t" \
    "v_mfma_f32_16x16x32_bf16 v[92:95], v[124:127], v[148:151], v[92:95]\n\t" \
    "v_mfma_f32_16x16x32_bf16 v[96:99], v[112:115], v[152:155], v[96:99]\n\t" \
    "v_mfma_f32_16x16x32_bf16 v[100:103], v[116:119], v[152:155], v[100:103]\n\t" \
    "v_mfma_f32_16x16x32_bf16 v[104:107], v[120:123], v[152:155], v[104:107]\n\t" \
    "v_mfma_f32_16x16x32_bf16 v[108:111], v[124:127], v[152:155], v[108:111]\n\t" \
    "s_waitcnt lgkmcnt(0)\n\t" \
    "s_barrier\n\t" \
    "v_mfma_f32_16x16x32_bf16 v[0:3], v[156:159], v[172:175], v[0:3]\n\t" \
    "v_mfma_f32_16x16x32_bf16 v[4:7], v[160:163], v[172:175], v[4:7]\n\t" \
    "v_mfma_f32_16x16x32_bf16 v[8:11], v[164:167], v[172:175], v[8:11]\n\t" \
    "v_mfma_f32_16x16x32_bf16 v[12:15], v[168:171], v[172:175], v[12:15]\n\t" \
    "v_mfma_f32_16x16x32_bf16 v[16:19], v[156:159], v[176:179], v[16:19]\n\t" \
    "v_mfma_f32_16x16x32_bf16 v[20:23], v[160:163], v[176:179], v[20:23]\n\t" \
    "v_mfma_f32_16x16x32_bf16 v[24:27], v[164:167], v[176:179], v[24:27]\n\t" \
    "v_mfma_f32_16x16x32_bf16 v[28:31], v[168:171], v[176:179], v[28:31]\n\t" \
    "v_mfma_f32_16x16x32_bf16 v[32:35], v[156:159], v[180:183], v[32:35]\n\t" \
    "v_mfma_f32_16x16x32_bf16 v[36:39], v[160:163], v[180:183], v[36:39]\n\t" \
    "v_mfma_f32_16x16x32_bf16 v[40:43], v[164:167], v[180:183], v[40:43]\n\t" \
    "v_mfma_f32_16x16x32_bf16 v[44:47], v[168:171], v[180:183], v[44:47]\n\t" \
    "v_mfma_f32_16x16x32_bf16 v[48:51], v[156:159], v[184:187], v[48:51]\n\t" \
    "v_mfma_f32_16x16x32_bf16 v[52:55], v[160:163], v[184:187], v[52:55]\n\t" \
    "v_mfma_f32_16x16x32_bf16 v[56:59], v[164:167], v[184:187], v[56:59]\n\t" \
    "v_mfma_f32_16x16x32_bf16 v[60:63], v[168:171], v[184:187], v[60:63]\n\t" \
    "v_mfma_f32_16x16x32_bf16 v[64:67], v[156:159], v[188:191], v[64:67]\n\t" \
    "v_mfma_f32_16x16x32_bf16 v[68:71], v[160:163], v[188:191], v[68:71]\n\t" \
    "v_mfma_f32_16x16x32_bf16 v[72:75], v[164:167], v[188:191], v[72:75]\n\t" \
    "v_mfma_f32_16x16x32_bf16 v[76:79], v[168:171], v[188:191], v[76:79]\n\t" \
    "v_mfma_f32_16x16x32_bf16 v[80:83], v[156:159], v[192:195], v[80:83]\n\t" \
    "v_mfma_f32_16x16x32_bf16 v[84:87], v[160:163], v[192:195], v[84:87]\n\t" \
    "v_mfma_f32_16x16x32_bf16 v[88:91], v[164:167], v[192:195], v[88:91]\n\t" \
    "v_mfma_f32_16x16x32_bf16 v[92:95], v[168:171], v[192:195], v[92:95]\n\t" \
    "v_mfma_f32_16x16x32_bf16 v[96:99], v[156:159], v[196:199], v[96:99]\n\t" \
    "v_mfma_f32_16x16x32_bf16 v[100:103], v[160:163], v[196:199], v[100:103]\n\t" \
    "v_mfma_f32_16x16x32_bf16 v[104:107], v[164:167], v[196:199], v[104:107]\n\t" \
    "v_mfma_f32_16x16x32_bf16 v[108:111], v[168:171], v[196:199], v[108:111]\n\t" \
    "s_waitcnt lgkmcnt(0)\n\t" \
    "s_barrier\n\t" \
    "s_nop 7\n\t" \
    "s_nop 7\n\t" \
    ""
#define G224_CLOB "v112","v113","v114","v115","v116","v117","v118","v119","v120","v121","v122","v123","v124","v125","v126","v127","v128","v129","v130","v131","v132","v133","v134","v135","v136","v137","v138","v139","v140","v141","v142","v143","v144","v145","v146","v147","v148","v149","v150","v151","v152","v153","v154","v155","v156","v157","v158","v159","v160","v161","v162","v163","v164","v165","v166","v167","v168","v169","v170","v171","v172","v173","v174","v175","v176","v177","v178","v179","v180","v181","v182","v183","v184","v185","v186","v187","v188","v189","v190","v191","v192","v193","v194","v195","v196","v197","v198","v199","v204","v205","v206","v207","v208","v209", "scc", "memory"
#define LOADER53 \
    "s_add_u32 m0, s47, s53\n\t" \
    "s_nop 0\n\t" \
    "global_load_lds_dwordx4 v5, s[42:43] offset:0\n\t" \
    "global_load_lds_dwordx4 v6, s[42:43] offset:1024\n\t" \
    "global_load_lds_dwordx4 v7, s[42:43] offset:2048\n\t" \
    "s_mov_b32 m0, s46\n\t" \
    "s_add_u32 s46, s46, 14336\n\t" \
    "global_load_lds_dwordx4 v0, s[40:41] offset:-2048\n\t" \
    "global_load_lds_dwordx4 v1, s[40:41] offset:-1024\n\t" \
    "global_load_lds_dwordx4 v2, s[40:41] offset:0\n\t" \
    "global_load_lds_dwordx4 v3, s[40:41] offset:1024\n\t" \
    "global_load_lds_dwordx4 v4, s[40:41] offset:2048\n\t" \
    "s_cmp_eq_u32 s46, s52\n\t" \
    "s_cselect_b32 s46, s51, s46\n\t" \
    "s_add_u32 s40, s40, s49\n\t" \
    "s_addc_u32 s41, s41, 0\n\t" \
    "s_sub_u32 s54, s54, 1\n\t" \
    "s_cmp_eq_u32 s54, 0\n\t" \
    "s_cselect_b32 s50, s56, 0\n\t" \
    "s_sub_u32 s40, s40, s50\n\t" \
    "s_subb_u32 s41, s41, 0\n\t" \
    "s_add_u32 m0, s58, s53\n\t" \
    "s_nop 0\n\t" \
    "global_load_lds_dwordx4 v5, s[44:45] offset:0\n\t" \
    "global_load_lds_dwordx4 v6, s[44:45] offset:1024\n\t" \
    "global_load_lds_dwordx4 v7, s[44:45] offset:2048\n\t" \
    "s_add_u32 s53, s53, 16384\n\t" \
    "s_and_b32 s53, s53, 0xffff\n\t" \
    "s_add_u32 s42, s42, 128\n\t" \
    "s_addc_u32 s43, s43, 0\n\t" \
    "s_add_u32 s44, s44, 128\n\t" \
    "s_addc_u32 s45, s45, 0\n\t" \
    "s_sub_u32 s55, s55, 1\n\t" \
    "s_cmp_eq_u32 s55, 0\n\t" \
    "s_cselect_b32 s50, s57, 0\n\t" \
    "s_sub_u32 s42, s42, s50\n\t" \
    "s_subb_u32 s43, s43, 0\n\t" \
    "s_sub_u32 s44, s44, s50\n\t" \
    "s_subb_u32 s45, s45, 0\n\t" \
    "s_mov_b32 m0, s46\n\t" \
    "s_add_u32 s46, s46, 14336\n\t" \
    "global_load_lds_dwordx4 v0, s[40:41] offset:-2048\n\t" \
    "global_load_lds_dwordx4 v1, s[40:41] offset:-1024\n\t" \
    "global_load_lds_dwordx4 v2, s[40:41] offset:0\n\t" \
    "global_load_lds_dwordx4 v3, s[40:41] offset:1024\n\t" \
    "global_load_lds_dwordx4 v4, s[40:41] offset:2048\n\t" \
    "s_cmp_eq_u32 s46, s52\n\t" \
    "s_cselect_b32 s46, s51, s46\n\t" \
    "s_add_u32 s40, s40, s49\n\t" \
    "s_addc_u32 s41, s41, 0\n\t" \
    "s_sub_u32 s54, s54, 1\n\t" \
    "s_cmp_eq_u32 s54, 0\n\t" \
    "s_cselect_b32 s50, s56, 0\n\t" \
    "s_sub_u32 s40, s40, s50\n\t" \
    "s_subb_u32 s41, s41, 0\n\t" \
    "s_add_u32 m0, s47, s53\n\t" \
    "s_nop 0\n\t" \
    "global_load_lds_dwordx4 v5, s[42:43] offset:0\n\t" \
    "global_load_lds_dwordx4 v6, s[42:43] offset:1024\n\t" \
    "global_load_lds_dwordx4 v7, s[42:43] offset:2048\n\t" \
    "s_mov_b32 m0, s46\n\t" \
    "s_add_u32 s46, s46, 14336\n\t" \
    "global_load_lds_dwordx4 v0, s[40:41] offset:-2048\n\t" \
    "global_load_lds_dwordx4 v1, s[40:41] offset:-1024\n\t" \
    "global_load_lds_dwordx4 v2, s[40:41] offset:0\n\t" \
    "global_load_lds_dwordx4 v3, s[40:41] offset:1024\n\t" \
    "global_load_lds_dwordx4 v4, s[40:41] offset:2048\n\t" \
    "s_cmp_eq_u32 s46, s52\n\t" \
    "s_cselect_b32 s46, s51, s46\n\t" \
    "s_add_u32 s40, s40, s49\n\t" \
    "s_addc_u32 s41, s41, 0\n\t" \
    "s_sub_u32 s54, s54, 1\n\t" \
    "s_cmp_eq_u32 s54, 0\n\t" \
    "s_cselect_b32 s50, s56, 0\n\t" \
    "s_sub_u32 s40, s40, s50\n\t" \
    "s_subb_u32 s41, s41, 0\n\t" \
    "s_add_u32 m0, s58, s53\n\t" \
    "s_nop 0\n\t" \
    "global_load_lds_dwordx4 v5, s[44:45] offset:0\n\t" \
    "global_load_lds_dwordx4 v6, s[44:45] offset:1024\n\t" \
    "global_load_lds_dwordx4 v7, s[44:45] offset:2048\n\t" \
    "s_add_u32 s53, s53, 16384\n\t" \
    "s_and_b32 s53, s53, 0xffff\n\t" \
    "s_add_u32 s42, s42, 128\n\t" \
    "s_addc_u32 s43, s43, 0\n\t" \
    "s_add_u32 s44, s44, 128\n\t" \
    "s_addc_u32 s45, s45, 0\n\t" \
    "s_sub_u32 s55, s55, 1\n\t" \
    "s_cmp_eq_u32 s55, 0\n\t" \
    "s_cselect_b32 s50, s57, 0\n\t" \
    "s_sub_u32 s42, s42, s50\n\t" \
    "s_subb_u32 s43, s43, 0\n\t" \
    "s_sub_u32 s44, s44, s50\n\t" \
    "s_subb_u32 s45, s45, 0\n\t" \
    "s_waitcnt vmcnt(16)\n\t" \
    "s_barrier\n\t" \
    "s_waitcnt vmcnt(11)\n\t" \
    "s_barrier\n\t" \
    "s_mov_b32 m0, s46\n\t" \
    "s_add_u32 s46, s46, 14336\n\t" \
    "global_load_lds_dwordx4 v0, s[40:41] offset:-2048\n\t" \
    "global_load_lds_dwordx4 v1, s[40:41] offset:-1024\n\t" \
    "global_load_lds_dwordx4 v2, s[40:41] offset:0\n\t" \
    "global_load_lds_dwordx4 v3, s[40:41] offset:1024\n\t" \
    "global_load_lds_dwordx4 v4, s[40:41] offset:2048\n\t" \
    "s_cmp_eq_u32 s46, s52\n\t" \
    "s_cselect_b32 s46, s51, s46\n\t" \
    "s_add_u32 s40, s40, s49\n\t" \
    "s_addc_u32 s41, s41, 0\n\t" \
    "s_sub_u32 s54, s54, 1\n\t" \
    "s_cmp_eq_u32 s54, 0\n\t" \
    "s_cselect_b32 s50, s56, 0\n\t" \
    "s_sub_u32 s40, s40, s50\n\t" \
    "s_subb_u32 s41, s41, 0\n\t" \
    "s_add_u32 m0, s47, s53\n\t" \
    "s_nop 0\n\t" \
    "global_load_lds_dwordx4 v5, s[42:43] offset:0\n\t" \
    "global_load_lds_dwordx4 v6, s[42:43] offset:1024\n\t" \
    "global_load_lds_dwordx4 v7, s[42:43] offset:2048\n\t" \
    "s_mov_b32 m0, s46\n\t" \
    "s_add_u32 s46, s46, 14336\n\t" \
    "global_load_lds_dwordx4 v0, s[40:41] offset:-2048\n\t" \
    "global_load_lds_dwordx4 v1, s[40:41] offset:-1024\n\t" \
    "global_load_lds_dwordx4 v2, s[40:41] offset:0\n\t" \
    "global_load_lds_dwordx4 v3, s[40:41] offset:1024\n\t" \
    "global_load_lds_dwordx4 v4, s[40:41] offset:2048\n\t" \
    "s_cmp_eq_u32 s46, s52\n\t" \
    "s_cselect_b32 s46, s51, s46\n\t" \
    "s_add_u32 s40, s40, s49\n\t" \
    "s_addc_u32 s41, s41, 0\n\t" \
    "s_sub_u32 s54, s54, 1\n\t" \
    "s_cmp_eq_u32 s54, 0\n\t" \
    "s_cselect_b32 s50, s56, 0\n\t" \
    "s_sub_u32 s40, s40, s50\n\t" \
    "s_subb_u32 s41, s41, 0\n\t" \
    "s_waitcnt vmcnt(13)\n\t" \
    "s_barrier\n\t" \
    "s_add_u32 m0, s58, s53\n\t" \
    "s_nop 0\n\t" \
    "global_load_lds_dwordx4 v5, s[44:45] offset:0\n\t" \
    "global_load_lds_dwordx4 v6, s[44:45] offset:1024\n\t" \
    "global_load_lds_dwordx4 v7, s[44:45] offset:2048\n\t" \
    "s_add_u32 s53, s53, 16384\n\t" \
    "s_and_b32 s53, s53, 0xffff\n\t" \
    "s_add_u32 s42, s42, 128\n\t" \
    "s_addc_u32 s43, s43, 0\n\t" \
    "s_add_u32 s44, s44, 128\n\t" \
    "s_addc_u32 s45, s45, 0\n\t" \
    "s_sub_u32 s55, s55, 1\n\t" \
    "s_cmp_eq_u32 s55, 0\n\t" \
    "s_cselect_b32 s50, s57, 0\n\t" \
    "s_sub_u32 s42, s42, s50\n\t" \
    "s_subb_u32 s43, s43, 0\n\t" \
    "s_sub_u32 s44, s44, s50\n\t" \
    "s_subb_u32 s45, s45, 0\n\t" \
    "s_mov_b32 m0, s46\n\t" \
    "s_add_u32 s46, s46, 14336\n\t" \
    "global_load_lds_dwordx4 v0, s[40:41] offset:-2048\n\t" \
    "global_load_lds_dwordx4 v1, s[40:41] offset:-1024\n\t" \
    "global_load_lds_dwordx4 v2, s[40:41] offset:0\n\t" \
    "global_load_lds_dwordx4 v3, s[40:41] offset:1024\n\t" \
    "global_load_lds_dwordx4 v4, s[40:41] offset:2048\n\t" \
    "s_cmp_eq_u32 s46, s52\n\t" \
    "s_cselect_b32 s46, s51, s46\n\t" \
    "s_add_u32 s40, s40, s49\n\t" \
    "s_addc_u32 s41, s41, 0\n\t" \
    "s_sub_u32 s54, s54, 1\n\t" \
    "s_cmp_eq_u32 s54, 0\n\t" \
    "s_cselect_b32 s50, s56, 0\n\t" \
    "s_sub_u32 s40, s40, s50\n\t" \
    "s_subb_u32 s41, s41, 0\n\t" \
    "s_add_u32 m0, s47, s53\n\t" \
    "s_nop 0\n\t" \
    "global_load_lds_dwordx4 v5, s[42:43] offset:0\n\t" \
    "global_load_lds_dwordx4 v6, s[42:43] offset:1024\n\t" \
    "global_load_lds_dwordx4 v7, s[42:43] offset:2048\n\t" \
    "s_waitcnt vmcnt(19)\n\t" \
    "s_barrier\n\t" \
    "s_mov_b32 m0, s46\n\t" \
    "s_add_u32 s46, s46, 14336\n\t" \
    "global_load_lds_dwordx4 v0, s[40:41] offset:-2048\n\t" \
    "global_load_lds_dwordx4 v1, s[40:41] offset:-1024\n\t" \
    "global_load_lds_dwordx4 v2, s[40:41] offset:0\n\t" \
    "global_load_lds_dwordx4 v3, s[40:41] offset:1024\n\t" \
    "global_load_lds_dwordx4 v4, s[40:41] offset:2048\n\t" \
    "s_cmp_eq_u32 s46, s52\n\t" \
    "s_cselect_b32 s46, s51, s46\n\t" \
    "s_add_u32 s40, s40, s49\n\t" \
    "s_addc_u32 s41, s41, 0\n\t" \
    "s_sub_u32 s54, s54, 1\n\t" \
    "s_cmp_eq_u32 s54, 0\n\t" \
    "s_cselect_b32 s50, s56, 0\n\t" \
    "s_sub_u32 s40, s40, s50\n\t" \
    "s_subb_u32 s41, s41, 0\n\t" \
    "s_add_u32 m0, s58, s53\n\t" \
    "s_nop 0\n\t" \
    "global_load_lds_dwordx4 v5, s[44:45] offset:0\n\t" \
    "global_load_lds_dwordx4 v6, s[44:45] offset:1024\n\t" \
    "global_load_lds_dwordx4 v7, s[44:45] offset:2048\n\t" \
    "s_add_u32 s53, s53, 16384\n\t" \
    "s_and_b32 s53, s53, 0xffff\n\t" \
    "s_add_u32 s42, s42, 128\n\t" \
    "s_addc_u32 s43, s43, 0\n\t" \
    "s_add_u32 s44, s44, 128\n\t" \
    "s_addc_u32 s45, s45, 0\n\t" \
    "s_sub_u32 s55, s55, 1\n\t" \
    "s_cmp_eq_u32 s55, 0\n\t" \
    "s_cselect_b32 s50, s57, 0\n\t" \
    "s_sub_u32 s42, s42, s50\n\t" \
    "s_subb_u32 s43, s43, 0\n\t" \
    "s_sub_u32 s44, s44, s50\n\t" \
    "s_subb_u32 s45, s45, 0\n\t" \
    "s_mov_b32 m0, s46\n\t" \
    "s_add_u32 s46, s46, 14336\n\t" \
    "global_load_lds_dwordx4 v0, s[40:41] offset:-2048\n\t" \
    "global_load_lds_dwordx4 v1, s[40:41] offset:-1024\n\t" \
    "global_load_lds_dwordx4 v2, s[40:41] offset:0\n\t" \
    "global_load_lds_dwordx4 v3, s[40:41] offset:1024\n\t" \
    "global_load_lds_dwordx4 v4, s[40:41] offset:2048\n\t" \
    "s_cmp_eq_u32 s46, s52\n\t" \
    "s_cselect_b32 s46, s51, s46\n\t" \
    "s_add_u32 s40, s40, s49\n\t" \
    "s_addc_u32 s41, s41, 0\n\t" \
    "s_sub_u32 s54, s54, 1\n\t" \
    "s_cmp_eq_u32 s54, 0\n\t" \
    "s_cselect_b32 s50, s56, 0\n\t" \
    "s_sub_u32 s40, s40, s50\n\t" \
    "s_subb_u32 s41, s41, 0\n\t" \
    "s_waitcnt vmcnt(21)\n\t" \
    "s_barrier\n\t" \
    "s_add_u32 m0, s47, s53\n\t" \
    "s_nop 0\n\t" \
    "global_load_lds_dwordx4 v5, s[42:43] offset:0\n\t" \
    "global_load_lds_dwordx4 v6, s[42:43] offset:1024\n\t" \
    "global_load_lds_dwordx4 v7, s[42:43] offset:2048\n\t" \
    "s_mov_b32 m0, s46\n\t" \
    "s_add_u32 s46, s46, 14336\n\t" \
    "global_load_lds_dwordx4 v0, s[40:41] offset:-2048\n\t" \
    "global_load_lds_dwordx4 v1, s[40:41] offset:-1024\n\t" \
    "global_load_lds_dwordx4 v2, s[40:41] offset:0\n\t" \
    "global_load_lds_dwordx4 v3, s[40:41] offset:1024\n\t" \
    "global_load_lds_dwordx4 v4, s[40:41] offset:2048\n\t" \
    "s_cmp_eq_u32 s46, s52\n\t" \
    "s_cselect_b32 s46, s51, s46\n\t" \
    "s_add_u32 s40, s40, s49\n\t" \
    "s_addc_u32 s41, s41, 0\n\t" \
    "s_sub_u32 s54, s54, 1\n\t" \
    "s_cmp_eq_u32 s54, 0\n\t" \
    "s_cselect_b32 s50, s56, 0\n\t" \
    "s_sub_u32 s40, s40, s50\n\t" \
    "s_subb_u32 s41, s41, 0\n\t" \
    "s_add_u32 m0, s58, s53\n\t" \
    "s_nop 0\n\t" \
    "global_load_lds_dwordx4 v5, s[44:45] offset:0\n\t" \
    "global_load_lds_dwordx4 v6, s[44:45] offset:1024\n\t" \
    "global_load_lds_dwordx4 v7, s[44:45] offset:2048\n\t" \
    "s_add_u32 s53, s53, 16384\n\t" \
    "s_and_b32 s53, s53, 0xffff\n\t" \
    "s_add_u32 s42, s42, 128\n\t" \
    "s_addc_u32 s43, s43, 0\n\t" \
    "s_add_u32 s44, s44, 128\n\t" \
    "s_addc_u32 s45, s45, 0\n\t" \
    "s_sub_u32 s55, s55, 1\n\t" \
    "s_cmp_eq_u32 s55, 0\n\t" \
    "s_cselect_b32 s50, s57, 0\n\t" \
    "s_sub_u32 s42, s42, s50\n\t" \
    "s_subb_u32 s43, s43, 0\n\t" \
    "s_sub_u32 s44, s44, s50\n\t" \
    "s_subb_u32 s45, s45, 0\n\t" \
    "s_waitcnt vmcnt(27)\n\t" \
    "s_barrier\n\t" \
    "s_mov_b32 m0, s46\n\t" \
    "s_add_u32 s46, s46, 14336\n\t" \
    "global_load_lds_dwordx4 v0, s[40:41] offset:-2048\n\t" \
    "global_load_lds_dwordx4 v1, s[40:41] offset:-1024\n\t" \
    "global_load_lds_dwordx4 v2, s[40:41] offset:0\n\t" \
    "global_load_lds_dwordx4 v3, s[40:41] offset:1024\n\t" \
    "global_load_lds_dwordx4 v4, s[40:41] offset:2048\n\t" \
    "s_cmp_eq_u32 s46, s52\n\t" \
    "s_cselect_b32 s46, s51, s46\n\t" \
    "s_add_u32 s40, s40, s49\n\t" \
    "s_addc_u32 s41, s41, 0\n\t" \
    "s_sub_u32 s54, s54, 1\n\t" \
    "s_cmp_eq_u32 s54, 0\n\t" \
    "s_cselect_b32 s50, s56, 0\n\t" \
    "s_sub_u32 s40, s40, s50\n\t" \
    "s_subb_u32 s41, s41, 0\n\t" \
    "s_add_u32 m0, s47, s53\n\t" \
    "s_nop 0\n\t" \
    "global_load_lds_dwordx4 v5, s[42:43] offset:0\n\t" \
    "global_load_lds_dwordx4 v6, s[42:43] offset:1024\n\t" \
    "global_load_lds_dwordx4 v7, s[42:43] offset:2048\n\t" \
    "s_mov_b32 m0, s46\n\t" \
    "s_add_u32 s46, s46, 14336\n\t" \
    "global_load_lds_dwordx4 v0, s[40:41] offset:-2048\n\t" \
    "global_load_lds_dwordx4 v1, s[40:41] offset:-1024\n\t" \
    "global_load_lds_dwordx4 v2, s[40:41] offset:0\n\t" \
    "global_load_lds_dwordx4 v3, s[40:41] offset:1024\n\t" \
    "global_load_lds_dwordx4 v4, s[40:41] offset:2048\n\t" \
    "s_cmp_eq_u32 s46, s52\n\t" \
    "s_cselect_b32 s46, s51, s46\n\t" \
    "s_add_u32 s40, s40, s49\n\t" \
    "s_addc_u32 s41, s41, 0\n\t" \
    "s_sub_u32 s54, s54, 1\n\t" \
    "s_cmp_eq_u32 s54, 0\n\t" \
    "s_cselect_b32 s50, s56, 0\n\t" \
    "s_sub_u32 s40, s40, s50\n\t" \
    "s_subb_u32 s41, s41, 0\n\t" \
    "s_waitcnt vmcnt(29)\n\t" \
    "s_barrier\n\t" \
    "s_add_u32 m0, s58, s53\n\t" \
    "s_nop 0\n\t" \
    "global_load_lds_dwordx4 v5, s[44:45] offset:0\n\t" \
    "global_load_lds_dwordx4 v6, s[44:45] offset:1024\n\t" \
    "global_load_lds_dwordx4 v7, s[44:45] offset:2048\n\t" \
    "s_add_u32 s53, s53, 16384\n\t" \
    "s_and_b32 s53, s53, 0xffff\n\t" \
    "s_add_u32 s42, s42, 128\n\t" \
    "s_addc_u32 s43, s43, 0\n\t" \
    "s_add_u32 s44, s44, 128\n\t" \
    "s_addc_u32 s45, s45, 0\n\t" \
    "s_sub_u32 s55, s55, 1\n\t" \
    "s_cmp_eq_u32 s55, 0\n\t" \
    "s_cselect_b32 s50, s57, 0\n\t" \
    "s_sub_u32 s42, s42, s50\n\t" \
    "s_subb_u32 s43, s43, 0\n\t" \
    "s_sub_u32 s44, s44, s50\n\t" \
    "s_subb_u32 s45, s45, 0\n\t" \
    "s_mov_b32 m0, s46\n\t" \
    "s_add_u32 s46, s46, 14336\n\t" \
    "global_load_lds_dwordx4 v0, s[40:41] offset:-2048\n\t" \
    "global_load_lds_dwordx4 v1, s[40:41] offset:-1024\n\t" \
    "global_load_lds_dwordx4 v2, s[40:41] offset:0\n\t" \
    "global_load_lds_dwordx4 v3, s[40:41] offset:1024\n\t" \
    "global_load_lds_dwordx4 v4, s[40:41] offset:2048\n\t" \
    "s_cmp_eq_u32 s46, s52\n\t" \
    "s_cselect_b32 s46, s51, s46\n\t" \
    "s_add_u32 s40, s40, s49\n\t" \
    "s_addc_u32 s41, s41, 0\n\t" \
    "s_sub_u32 s54, s54, 1\n\t" \
    "s_cmp_eq_u32 s54, 0\n\t" \
    "s_cselect_b32 s50, s56, 0\n\t" \
    "s_sub_u32 s40, s40, s50\n\t" \
    "s_subb_u32 s41, s41, 0\n\t" \
    "s_add_u32 m0, s47, s53\n\t" \
    "s_nop 0\n\t" \
    "global_load_lds_dwordx4 v5, s[42:43] offset:0\n\t" \
    "global_load_lds_dwordx4 v6, s[42:43] offset:1024\n\t" \
    "global_load_lds_dwordx4 v7, s[42:43] offset:2048\n\t" \
    "s_waitcnt vmcnt(32)\n\t" \
    "s_barrier\n\t" \
    "s_mov_b32 m0, s46\n\t" \
    "s_add_u32 s46, s46, 14336\n\t" \
    "global_load_lds_dwordx4 v0, s[40:41] offset:-2048\n\t" \
    "global_load_lds_dwordx4 v1, s[40:41] offset:-1024\n\t" \
    "global_load_lds_dwordx4 v2, s[40:41] offset:0\n\t" \
    "global_load_lds_dwordx4 v3, s[40:41] offset:1024\n\t" \
    "global_load_lds_dwordx4 v4, s[40:41] offset:2048\n\t" \
    "s_cmp_eq_u32 s46, s52\n\t" \
    "s_cselect_b32 s46, s51, s46\n\t" \
    "s_add_u32 s40, s40, s49\n\t" \
    "s_addc_u32 s41, s41, 0\n\t" \
    "s_sub_u32 s54, s54, 1\n\t" \
    "s_cmp_eq_u32 s54, 0\n\t" \
    "s_cselect_b32 s50, s56, 0\n\t" \
    "s_sub_u32 s40, s40, s50\n\t" \
    "s_subb_u32 s41, s41, 0\n\t" \
    "s_add_u32 m0, s58, s53\n\t" \
    "s_nop 0\n\t" \
    "global_load_lds_dwordx4 v5, s[44:45] offset:0\n\t" \
    "global_load_lds_dwordx4 v6, s[44:45] offset:1024\n\t" \
    "global_load_lds_dwordx4 v7, s[44:45] offset:2048\n\t" \
    "s_add_u32 s53, s53, 16384\n\t" \
    "s_and_b32 s53, s53, 0xffff\n\t" \
    "s_add_u32 s42, s42, 128\n\t" \
    "s_addc_u32 s43, s43, 0\n\t" \
    "s_add_u32 s44, s44, 128\n\t" \
    "s_addc_u32 s45, s45, 0\n\t" \
    "s_sub_u32 s55, s55, 1\n\t" \
    "s_cmp_eq_u32 s55, 0\n\t" \
    "s_cselect_b32 s50, s57, 0\n\t" \
    "s_sub_u32 s42, s42, s50\n\t" \
    "s_subb_u32 s43, s43, 0\n\t" \
    "s_sub_u32 s44, s44, s50\n\t" \
    "s_subb_u32 s45, s45, 0\n\t" \
    "s_waitcnt vmcnt(32)\n\t" \
    "s_barrier\n\t" \
    "s_cmp_eq_u32 s48, 0\n\t" \
    "s_cbranch_scc1 2f\n\t" \
    "1:\n\t" \
    "s_mov_b32 m0, s46\n\t" \
    "s_add_u32 s46, s46, 14336\n\t" \
    "global_load_lds_dwordx4 v0, s[40:41] offset:-2048\n\t" \
    "global_load_lds_dwordx4 v1, s[40:41] offset:-1024\n\t" \
    "global_load_lds_dwordx4 v2, s[40:41] offset:0\n\t" \
    "global_load_lds_dwordx4 v3, s[40:41] offset:1024\n\t" \
    "global_load_lds_dwordx4 v4, s[40:41] offset:2048\n\t" \
    "s_cmp_eq_u32 s46, s52\n\t" \
    "s_cselect_b32 s46, s51, s46\n\t" \
    "s_add_u32 s40, s40, s49\n\t" \
    "s_addc_u32 s41, s41, 0\n\t" \
    "s_sub_u32 s54, s54, 1\n\t" \
    "s_cmp_eq_u32 s54, 0\n\t" \
    "s_cselect_b32 s50, s56, 0\n\t" \
    "s_sub_u32 s40, s40, s50\n\t" \
    "s_subb_u32 s41, s41, 0\n\t" \
    "s_add_u32 m0, s47, s53\n\t" \
    "s_nop 0\n\t" \
    "global_load_lds_dwordx4 v5, s[42:43] offset:0\n\t" \
    "global_load_lds_dwordx4 v6, s[42:43] offset:1024\n\t" \
    "global_load_lds_dwordx4 v7, s[42:43] offset:2048\n\t" \
    "s_waitcnt vmcnt(32)\n\t" \
    "s_barrier\n\t" \
    "s_mov_b32 m0, s46\n\t" \
    "s_add_u32 s46, s46, 14336\n\t" \
    "global_load_lds_dwordx4 v0, s[40:41] offset:-2048\n\t" \
    "global_load_lds_dwordx4 v1, s[40:41] offset:-1024\n\t" \
    "global_load_lds_dwordx4 v2, s[40:41] offset:0\n\t" \
    "global_load_lds_dwordx4 v3, s[40:41] offset:1024\n\t" \
    "global_load_lds_dwordx4 v4, s[40:41] offset:2048\n\t" \
    "s_cmp_eq_u32 s46, s52\n\t" \
    "s_cselect_b32 s46, s51, s46\n\t" \
    "s_add_u32 s40, s40, s49\n\t" \
    "s_addc_u32 s41, s41, 0\n\t" \
    "s_sub_u32 s54, s54, 1\n\t" \
    "s_cmp_eq_u32 s54, 0\n\t" \
    "s_cselect_b32 s50, s56, 0\n\t" \
    "s_sub_u32 s40, s40, s50\n\t" \
    "s_subb_u32 s41, s41, 0\n\t" \
    "s_add_u32 m0, s58, s53\n\t" \
    "s_nop 0\n\t" \
    "global_load_lds_dwordx4 v5, s[44:45] offset:0\n\t" \
    "global_load_lds_dwordx4 v6, s[44:45] offset:1024\n\t" \
    "global_load_lds_dwordx4 v7, s[44:45] offset:2048\n\t" \
    "s_add_u32 s53, s53, 16384\n\t" \
    "s_and_b32 s53, s53, 0xffff\n\t" \
    "s_add_u32 s42, s42, 128\n\t" \
    "s_addc_u32 s43, s43, 0\n\t" \
    "s_add_u32 s44, s44, 128\n\t" \
    "s_addc_u32 s45, s45, 0\n\t" \
    "s_sub_u32 s55, s55, 1\n\t" \
    "s_cmp_eq_u32 s55, 0\n\t" \
    "s_cselect_b32 s50, s57, 0\n\t" \
    "s_sub_u32 s42, s42, s50\n\t" \
    "s_subb_u32 s43, s43, 0\n\t" \
    "s_sub_u32 s44, s44, s50\n\t" \
    "s_subb_u32 s45, s45, 0\n\t" \
    "s_sub_u32 s48, s48, 1\n\t" \
    "s_cmp_lg_u32 s48, 0\n\t" \
    "s_waitcnt vmcnt(32)\n\t" \
    "s_barrier\n\t" \
    "s_cbranch_scc1 1b\n\t" \
    "2:\n\t" \
    "s_mov_b32 m0, s46\n\t" \
    "s_add_u32 s46, s46, 14336\n\t" \
    "global_load_lds_dwordx4 v0, s[40:41] offset:-2048\n\t" \
    "global_load_lds_dwordx4 v1, s[40:41] offset:-1024\n\t" \
    "global_load_lds_dwordx4 v2, s[40:41] offset:0\n\t" \
    "global_load_lds_dwordx4 v3, s[40:41] offset:1024\n\t" \
    "global_load_lds_dwordx4 v4, s[40:41] offset:2048\n\t" \
    "s_cmp_eq_u32 s46, s52\n\t" \
    "s_cselect_b32 s46, s51, s46\n\t" \
    "s_add_u32 s40, s40, s49\n\t" \
    "s_addc_u32 s41, s41, 0\n\t" \
    "s_sub_u32 s54, s54, 1\n\t" \
    "s_cmp_eq_u32 s54, 0\n\t" \
    "s_cselect_b32 s50, s56, 0\n\t" \
    "s_sub_u32 s40, s40, s50\n\t" \
    "s_subb_u32 s41, s41, 0\n\t" \
    ""
#define LOADER42 \
    "s_add_u32 m0, s47, s53\n\t" \
    "s_nop 0\n\t" \
    "global_load_lds_dwordx4 v5, s[42:43] offset:0\n\t" \
    "global_load_lds_dwordx4 v6, s[42:43] offset:1024\n\t" \
    "s_mov_b32 m0, s46\n\t" \
    "s_add_u32 s46, s46, 14336\n\t" \
    "global_load_lds_dwordx4 v0, s[40:41] offset:-2048\n\t" \
    "global_load_lds_dwordx4 v1, s[40:41] offset:-1024\n\t" \
    "global_load_lds_dwordx4 v2, s[40:41] offset:0\n\t" \
    "global_load_lds_dwordx4 v3, s[40:41] offset:1024\n\t" \
    "s_cmp_eq_u32 s46, s52\n\t" \
    "s_cselect_b32 s46, s51, s46\n\t" \
    "s_add_u32 s40, s40, s49\n\t" \
    "s_addc_u32 s41, s41, 0\n\t" \
    "s_sub_u32 s54, s54, 1\n\t" \
    "s_cmp_eq_u32 s54, 0\n\t" \
    "s_cselect_b32 s50, s56, 0\n\t" \
    "s_sub_u32 s40, s40, s50\n\t" \
    "s_subb_u32 s41, s41, 0\n\t" \
    "s_add_u32 m0, s58, s53\n\t" \
    "s_nop 0\n\t" \
    "global_load_lds_dwordx4 v5, s[44:45] offset:0\n\t" \
    "global_load_lds_dwordx4 v6, s[44:45] offset:1024\n\t" \
    "s_add_u32 s53, s53, 16384\n\t" \
    "s_and_b32 s53, s53, 0xffff\n\t" \
    "s_add_u32 s42, s42, 128\n\t" \
    "s_addc_u32 s43, s43, 0\n\t" \
    "s_add_u32 s44, s44, 128\n\t" \
    "s_addc_u32 s45, s45, 0\n\t" \
    "s_sub_u32 s55, s55, 1\n\t" \
    "s_cmp_eq_u32 s55, 0\n\t" \
    "s_cselect_b32 s50, s57, 0\n\t" \
    "s_sub_u32 s42, s42, s50\n\t" \
    "s_subb_u32 s43, s43, 0\n\t" \
    "s_sub_u32 s44, s44, s50\n\t" \
    "s_subb_u32 s45, s45, 0\n\t" \
    "s_mov_b32 m0, s46\n\t" \
    "s_add_u32 s46, s46, 14336\n\t" \
    "global_load_lds_dwordx4 v0, s[40:41] offset:-2048\n\t" \
    "global_load_lds_dwordx4 v1, s[40:41] offset:-1024\n\t" \
    "global_load_lds_dwordx4 v2, s[40:41] offset:0\n\t" \
    "global_load_lds_dwordx4 v3, s[40:41] offset:1024\n\t" \
    "s_cmp_eq_u32 s46, s52\n\t" \
    "s_cselect_b32 s46, s51, s46\n\t" \
    "s_add_u32 s40, s40, s49\n\t" \
    "s_addc_u32 s41, s41, 0\n\t" \
    "s_sub_u32 s54, s54, 1\n\t" \
    "s_cmp_eq_u32 s54, 0\n\t" \
    "s_cselect_b32 s50, s56, 0\n\t" \
    "s_sub_u32 s40, s40, s50\n\t" \
    "s_subb_u32 s41, s41, 0\n\t" \
    "s_add_u32 m0, s47, s53\n\t" \
    "s_nop 0\n\t" \
    "global_load_lds_dwordx4 v5, s[42:43] offset:0\n\t" \
    "global_load_lds_dwordx4 v6, s[42:43] offset:1024\n\t" \
    "s_mov_b32 m0, s46\n\t" \
    "s_add_u32 s46, s46, 14336\n\t" \
    "global_load_lds_dwordx4 v0, s[40:41] offset:-2048\n\t" \
    "global_load_lds_dwordx4 v1, s[40:41] offset:-1024\n\t" \
    "global_load_lds_dwordx4 v2, s[40:41] offset:0\n\t" \
    "global_load_lds_dwordx4 v3, s[40:41] offset:1024\n\t" \
    "s_cmp_eq_u32 s46, s52\n\t" \
    "s_cselect_b32 s46, s51, s46\n\t" \
    "s_add_u32 s40, s40, s49\n\t" \
    "s_addc_u32 s41, s41, 0\n\t" \
    "s_sub_u32 s54, s54, 1\n\t" \
    "s_cmp_eq_u32 s54, 0\n\t" \
    "s_cselect_b32 s50, s56, 0\n\t" \
    "s_sub_u32 s40, s40, s50\n\t" \
    "s_subb_u32 s41, s41, 0\n\t" \
    "s_add_u32 m0, s58, s53\n\t" \
    "s_nop 0\n\t" \
    "global_load_lds_dwordx4 v5, s[44:45] offset:0\n\t" \
    "global_load_lds_dwordx4 v6, s[44:45] offset:1024\n\t" \
    "s_add_u32 s53, s53, 16384\n\t" \
    "s_and_b32 s53, s53, 0xffff\n\t" \
    "s_add_u32 s42, s42, 128\n\t" \
    "s_addc_u32 s43, s43, 0\n\t" \
    "s_add_u32 s44, s44, 128\n\t" \
    "s_addc_u32 s45, s45, 0\n\t" \
    "s_sub_u32 s55, s55, 1\n\t" \
    "s_cmp_eq_u32 s55, 0\n\t" \
    "s_cselect_b32 s50, s57, 0\n\t" \
    "s_sub_u32 s42, s42, s50\n\t" \
    "s_subb_u32 s43, s43, 0\n\t" \
    "s_sub_u32 s44, s44, s50\n\t" \
    "s_subb_u32 s45, s45, 0\n\t" \
    "s_waitcnt vmcnt(12)\n\t" \
    "s_barrier\n\t" \
    "s_waitcnt vmcnt(8)\n\t" \
    "s_barrier\n\t" \
    "s_mov_b32 m0, s46\n\t" \
    "s_add_u32 s46, s46, 14336\n\t" \
    "global_load_lds_dwordx4 v0, s[40:41] offset:-2048\n\t" \
    "global_load_lds_dwordx4 v1, s[40:41] offset:-1024\n\t" \
    "global_load_lds_dwordx4 v2, s[40:41] offset:0\n\t" \
    "global_load_lds_dwordx4 v3, s[40:41] offset:1024\n\t" \
    "s_cmp_eq_u32 s46, s52\n\t" \
    "s_cselect_b32 s46, s51, s46\n\t" \
    "s_add_u32 s40, s40, s49\n\t" \
    "s_addc_u32 s41, s41, 0\n\t" \
    "s_sub_u32 s54, s54, 1\n\t" \
    "s_cmp_eq_u32 s54, 0\n\t" \
    "s_cselect_b32 s50, s56, 0\n\t" \
    "s_sub_u32 s40, s40, s50\n\t" \
    "s_subb_u32 s41, s41, 0\n\t" \
    "s_add_u32 m0, s47, s53\n\t" \
    "s_nop 0\n\t" \
    "global_load_lds_dwordx4 v5, s[42:43] offset:0\n\t" \
    "global_load_lds_dwordx4 v6, s[42:43] offset:1024\n\t" \
    "s_mov_b32 m0, s46\n\t" \
    "s_add_u32 s46, s46, 14336\n\t" \
    "global_load_lds_dwordx4 v0, s[40:41] offset:-2048\n\t" \
    "global_load_lds_dwordx4 v1, s[40:41] offset:-1024\n\t" \
    "global_load_lds_dwordx4 v2, s[40:41] offset:0\n\t" \
    "global_load_lds_dwordx4 v3, s[40:41] offset:1024\n\t" \
    "s_cmp_eq_u32 s46, s52\n\t" \
    "s_cselect_b32 s46, s51, s46\n\t" \
    "s_add_u32 s40, s40, s49\n\t" \
    "s_addc_u32 s41, s41, 0\n\t" \
    "s_sub_u32 s54, s54, 1\n\t" \
    "s_cmp_eq_u32 s54, 0\n\t" \
    "s_cselect_b32 s50, s56, 0\n\t" \
    "s_sub_u32 s40, s40, s50\n\t" \
    "s_subb_u32 s41, s41, 0\n\t" \
    "s_waitcnt vmcnt(10)\n\t" \
    "s_barrier\n\t" \
    "s_add_u32 m0, s58, s53\n\t" \
    "s_nop 0\n\t" \
    "global_load_lds_dwordx4 v5, s[44:45] offset:0\n\t" \
    "global_load_lds_dwordx4 v6, s[44:45] offset:1024\n\t" \
    "s_add_u32 s53, s53, 16384\n\t" \
    "s_and_b32 s53, s53, 0xffff\n\t" \
    "s_add_u32 s42, s42, 128\n\t" \
    "s_addc_u32 s43, s43, 0\n\t" \
    "s_add_u32 s44, s44, 128\n\t" \
    "s_addc_u32 s45, s45, 0\n\t" \
    "s_sub_u32 s55, s55, 1\n\t" \
    "s_cmp_eq_u32 s55, 0\n\t" \
    "s_cselect_b32 s50, s57, 0\n\t" \
    "s_sub_u32 s42, s42, s50\n\t" \
    "s_subb_u32 s43, s43, 0\n\t" \
    "s_sub_u32 s44, s44, s50\n\t" \
    "s_subb_u32 s45, s45, 0\n\t" \
    "s_mov_b32 m0, s46\n\t" \
    "s_add_u32 s46, s46, 14336\n\t" \
    "global_load_lds_dwordx4 v0, s[40:41] offset:-2048\n\t" \
    "global_load_lds_dwordx4 v1, s[40:41] offset:-1024\n\t" \
    "global_load_lds_dwordx4 v2, s[40:41] offset:0\n\t" \
    "global_load_lds_dwordx4 v3, s[40:41] offset:1024\n\t" \
    "s_cmp_eq_u32 s46, s52\n\t" \
    "s_cselect_b32 s46, s51, s46\n\t" \
    "s_add_u32 s40, s40, s49\n\t" \
    "s_addc_u32 s41, s41, 0\n\t" \
    "s_sub_u32 s54, s54, 1\n\t" \
    "s_cmp_eq_u32 s54, 0\n\t" \
    "s_cselect_b32 s50, s56, 0\n\t" \
    "s_sub_u32 s40, s40, s50\n\t" \
    "s_subb_u32 s41, s41, 0\n\t" \
    "s_add_u32 m0, s47, s53\n\t" \
    "s_nop 0\n\t" \
    "global_load_lds_dwordx4 v5, s[42:43] offset:0\n\t" \
    "global_load_lds_dwordx4 v6, s[42:43] offset:1024\n\t" \
    "s_waitcnt vmcnt(14)\n\t" \
    "s_barrier\n\t" \
    "s_mov_b32 m0, s46\n\t" \
    "s_add_u32 s46, s46, 14336\n\t" \
    "global_load_lds_dwordx4 v0, s[40:41] offset:-2048\n\t" \
    "global_load_lds_dwordx4 v1, s[40:41] offset:-1024\n\t" \
    "global_load_lds_dwordx4 v2, s[40:41] offset:0\n\t" \
    "global_load_lds_dwordx4 v3, s[40:41] offset:1024\n\t" \
    "s_cmp_eq_u32 s46, s52\n\t" \
    "s_cselect_b32 s46, s51, s46\n\t" \
    "s_add_u32 s40, s40, s49\n\t" \
    "s_addc_u32 s41, s41, 0\n\t" \
    "s_sub_u32 s54, s54, 1\n\t" \
    "s_cmp_eq_u32 s54, 0\n\t" \
    "s_cselect_b32 s50, s56, 0\n\t" \
    "s_sub_u32 s40, s40, s50\n\t" \
    "s_subb_u32 s41, s41, 0\n\t" \
    "s_add_u32 m0, s58, s53\n\t" \
    "s_nop 0\n\t" \
    "global_load_lds_dwordx4 v5, s[44:45] offset:0\n\t" \
    "global_load_lds_dwordx4 v6, s[44:45] offset:1024\n\t" \
    "s_add_u32 s53, s53, 16384\n\t" \
    "s_and_b32 s53, s53, 0xffff\n\t" \
    "s_add_u32 s42, s42, 128\n\t" \
    "s_addc_u32 s43, s43, 0\n\t" \
    "s_add_u32 s44, s44, 128\n\t" \
    "s_addc_u32 s45, s45, 0\n\t" \
    "s_sub_u32 s55, s55, 1\n\t" \
    "s_cmp_eq_u32 s55, 0\n\t" \
    "s_cselect_b32 s50, s57, 0\n\t" \
    "s_sub_u32 s42, s42, s50\n\t" \
    "s_subb_u32 s43, s43, 0\n\t" \
    "s_sub_u32 s44, s44, s50\n\t" \
    "s_subb_u32 s45, s45, 0\n\t" \
    "s_mov_b32 m0, s46\n\t" \
    "s_add_u32 s46, s46, 14336\n\t" \
    "global_load_lds_dwordx4 v0, s[40:41] offset:-2048\n\t" \
    "global_load_lds_dwordx4 v1, s[40:41] offset:-1024\n\t" \
    "global_load_lds_dwordx4 v2, s[40:41] offset:0\n\t" \
    "global_load_lds_dwordx4 v3, s[40:41] offset:1024\n\t" \
    "s_cmp_eq_u32 s46, s52\n\t" \
    "s_cselect_b32 s46, s51, s46\n\t" \
    "s_add_u32 s40, s40, s49\n\t" \
    "s_addc_u32 s41, s41, 0\n\t" \
    "s_sub_u32 s54, s54, 1\n\t" \
    "s_cmp_eq_u32 s54, 0\n\t" \
    "s_cselect_b32 s50, s56, 0\n\t" \
    "s_sub_u32 s40, s40, s50\n\t" \
    "s_subb_u32 s41, s41, 0\n\t" \
    "s_waitcnt vmcnt(16)\n\t" \
    "s_barrier\n\t" \
    "s_add_u32 m0, s47, s53\n\t" \
    "s_nop 0\n\t" \
    "global_load_lds_dwordx4 v5, s[42:43] offset:0\n\t" \
    "global_load_lds_dwordx4 v6, s[42:43] offset:1024\n\t" \
    "s_mov_b32 m0, s46\n\t" \
    "s_add_u32 s46, s46, 14336\n\t" \
    "global_load_lds_dwordx4 v0, s[40:41] offset:-2048\n\t" \
    "global_load_lds_dwordx4 v1, s[40:41] offset:-1024\n\t" \
    "global_load_lds_dwordx4 v2, s[40:41] offset:0\n\t" \
    "global_load_lds_dwordx4 v3, s[40:41] offset:1024\n\t" \
    "s_cmp_eq_u32 s46, s52\n\t" \
    "s_cselect_b32 s46, s51, s46\n\t" \
    "s_add_u32 s40, s40, s49\n\t" \
    "s_addc_u32 s41, s41, 0\n\t" \
    "s_sub_u32 s54, s54, 1\n\t" \
    "s_cmp_eq_u32 s54, 0\n\t" \
    "s_cselect_b32 s50, s56, 0\n\t" \
    "s_sub_u32 s40, s40, s50\n\t" \
    "s_subb_u32 s41, s41, 0\n\t" \
    "s_add_u32 m0, s58, s53\n\t" \
    "s_nop 0\n\t" \
    "global_load_lds_dwordx4 v5, s[44:45] offset:0\n\t" \
    "global_load_lds_dwordx4 v6, s[44:45] offset:1024\n\t" \
    "s_add_u32 s53, s53, 16384\n\t" \
    "s_and_b32 s53, s53, 0xffff\n\t" \
    "s_add_u32 s42, s42, 128\n\t" \
    "s_addc_u32 s43, s43, 0\n\t" \
    "s_add_u32 s44, s44, 128\n\t" \
    "s_addc_u32 s45, s45, 0\n\t" \
    "s_sub_u32 s55, s55, 1\n\t" \
    "s_cmp_eq_u32 s55, 0\n\t" \
    "s_cselect_b32 s50, s57, 0\n\t" \
    "s_sub_u32 s42, s42, s50\n\t" \
    "s_subb_u32 s43, s43, 0\n\t" \
    "s_sub_u32 s44, s44, s50\n\t" \
    "s_subb_u32 s45, s45, 0\n\t" \
    "s_waitcnt vmcnt(20)\n\t" \
    "s_barrier\n\t" \
    "s_mov_b32 m0, s46\n\t" \
    "s_add_u32 s46, s46, 14336\n\t" \
    "global_load_lds_dwordx4 v0, s[40:41] offset:-2048\n\t" \
    "global_load_lds_dwordx4 v1, s[40:41] offset:-1024\n\t" \
    "global_load_lds_dwordx4 v2, s[40:41] offset:0\n\t" \
    "global_load_lds_dwordx4 v3, s[40:41] offset:1024\n\t" \
    "s_cmp_eq_u32 s46, s52\n\t" \
    "s_cselect_b32 s46, s51, s46\n\t" \
    "s_add_u32 s40, s40, s49\n\t" \
    "s_addc_u32 s41, s41, 0\n\t" \
    "s_sub_u32 s54, s54, 1\n\t" \
    "s_cmp_eq_u32 s54, 0\n\t" \
    "s_cselect_b32 s50, s56, 0\n\t" \
    "s_sub_u32 s40, s40, s50\n\t" \
    "s_subb_u32 s41, s41, 0\n\t" \
    "s_add_u32 m0, s47, s53\n\t" \
    "s_nop 0\n\t" \
    "global_load_lds_dwordx4 v5, s[42:43] offset:0\n\t" \
    "global_load_lds_dwordx4 v6, s[42:43] offset:1024\n\t" \
    "s_mov_b32 m0, s46\n\t" \
    "s_add_u32 s46, s46, 14336\n\t" \
    "global_load_lds_dwordx4 v0, s[40:41] offset:-2048\n\t" \
    "global_load_lds_dwordx4 v1, s[40:41] offset:-1024\n\t" \
    "global_load_lds_dwordx4 v2, s[40:41] offset:0\n\t" \
    "global_load_lds_dwordx4 v3, s[40:41] offset:1024\n\t" \
    "s_cmp_eq_u32 s46, s52\n\t" \
    "s_cselect_b32 s46, s51, s46\n\t" \
    "s_add_u32 s40, s40, s49\n\t" \
    "s_addc_u32 s41, s41, 0\n\t" \
    "s_sub_u32 s54, s54, 1\n\t" \
    "s_cmp_eq_u32 s54, 0\n\t" \
    "s_cselect_b32 s50, s56, 0\n\t" \
    "s_sub_u32 s40, s40, s50\n\t" \
    "s_subb_u32 s41, s41, 0\n\t" \
    "s_waitcnt vmcnt(22)\n\t" \
    "s_barrier\n\t" \
    "s_add_u32 m0, s58, s53\n\t" \
    "s_nop 0\n\t" \
    "global_load_lds_dwordx4 v5, s[44:45] offset:0\n\t" \
    "global_load_lds_dwordx4 v6, s[44:45] offset:1024\n\t" \
    "s_add_u32 s53, s53, 16384\n\t" \
    "s_and_b32 s53, s53, 0xffff\n\t" \
    "s_add_u32 s42, s42, 128\n\t" \
    "s_addc_u32 s43, s43, 0\n\t" \
    "s_add_u32 s44, s44, 128\n\t" \
    "s_addc_u32 s45, s45, 0\n\t" \
    "s_sub_u32 s55, s55, 1\n\t" \
    "s_cmp_eq_u32 s55, 0\n\t" \
    "s_cselect_b32 s50, s57, 0\n\t" \
    "s_sub_u32 s42, s42, s50\n\t" \
    "s_subb_u32 s43, s43, 0\n\t" \
    "s_sub_u32 s44, s44, s50\n\t" \
    "s_subb_u32 s45, s45, 0\n\t" \
    "s_mov_b32 m0, s46\n\t" \
    "s_add_u32 s46, s46, 14336\n\t" \
    "global_load_lds_dwordx4 v0, s[40:41] offset:-2048\n\t" \
    "global_load_lds_dwordx4 v1, s[40:41] offset:-1024\n\t" \
    "global_load_lds_dwordx4 v2, s[40:41] offset:0\n\t" \
    "global_load_lds_dwordx4 v3, s[40:41] offset:1024\n\t" \
    "s_cmp_eq_u32 s46, s52\n\t" \
    "s_cselect_b32 s46, s51, s46\n\t" \
    "s_add_u32 s40, s40, s49\n\t" \
    "s_addc_u32 s41, s41, 0\n\t" \
    "s_sub_u32 s54, s54, 1\n\t" \
    "s_cmp_eq_u32 s54, 0\n\t" \
    "s_cselect_b32 s50, s56, 0\n\t" \
    "s_sub_u32 s40, s40, s50\n\t" \
    "s_subb_u32 s41, s41, 0\n\t" \
    "s_add_u32 m0, s47, s53\n\t" \
    "s_nop 0\n\t" \
    "global_load_lds_dwordx4 v5, s[42:43] offset:0\n\t" \
    "global_load_lds_dwordx4 v6, s[42:43] offset:1024\n\t" \
    "s_waitcnt vmcnt(24)\n\t" \
    "s_barrier\n\t" \
    "s_mov_b32 m0, s46\n\t" \
    "s_add_u32 s46, s46, 14336\n\t" \
    "global_load_lds_dwordx4 v0, s[40:41] offset:-2048\n\t" \
    "global_load_lds_dwordx4 v1, s[40:41] offset:-1024\n\t" \
    "global_load_lds_dwordx4 v2, s[40:41] offset:0\n\t" \
    "global_load_lds_dwordx4 v3, s[40:41] offset:1024\n\t" \
    "s_cmp_eq_u32 s46, s52\n\t" \
    "s_cselect_b32 s46, s51, s46\n\t" \
    "s_add_u32 s40, s40, s49\n\t" \
    "s_addc_u32 s41, s41, 0\n\t" \
    "s_sub_u32 s54, s54, 1\n\t" \
    "s_cmp_eq_u32 s54, 0\n\t" \
    "s_cselect_b32 s50, s56, 0\n\t" \
    "s_sub_u32 s40, s40, s50\n\t" \
    "s_subb_u32 s41, s41, 0\n\t" \
    "s_add_u32 m0, s58, s53\n\t" \
    "s_nop 0\n\t" \
    "global_load_lds_dwordx4 v5, s[44:45] offset:0\n\t" \
    "global_load_lds_dwordx4 v6, s[44:45] offset:1024\n\t" \
    "s_add_u32 s53, s53, 16384\n\t" \
    "s_and_b32 s53, s53, 0xffff\n\t" \
    "s_add_u32 s42, s42, 128\n\t" \
    "s_addc_u32 s43, s43, 0\n\t" \
    "s_add_u32 s44, s44, 128\n\t" \
    "s_addc_u32 s45, s45, 0\n\t" \
    "s_sub_u32 s55, s55, 1\n\t" \
    "s_cmp_eq_u32 s55, 0\n\t" \
    "s_cselect_b32 s50, s57, 0\n\t" \
    "s_sub_u32 s42, s42, s50\n\t" \
    "s_subb_u32 s43, s43, 0\n\t" \
    "s_sub_u32 s44, s44, s50\n\t" \
    "s_subb_u32 s45, s45, 0\n\t" \
    "s_waitcnt vmcnt(24)\n\t" \
    "s_barrier\n\t" \
    "s_cmp_eq_u32 s48, 0\n\t" \
    "s_cbranch_scc1 2f\n\t" \
    "1:\n\t" \
    "s_mov_b32 m0, s46\n\t" \
    "s_add_u32 s46, s46, 14336\n\t" \
    "global_load_lds_dwordx4 v0, s[40:41] offset:-2048\n\t" \
    "global_load_lds_dwordx4 v1, s[40:41] offset:-1024\n\t" \
    "global_load_lds_dwordx4 v2, s[40:41] offset:0\n\t" \
    "global_load_lds_dwordx4 v3, s[40:41] offset:1024\n\t" \
    "s_cmp_eq_u32 s46, s52\n\t" \
    "s_cselect_b32 s46, s51, s46\n\t" \
    "s_add_u32 s40, s40, s49\n\t" \
    "s_addc_u32 s41, s41, 0\n\t" \
    "s_sub_u32 s54, s54, 1\n\t" \
    "s_cmp_eq_u32 s54, 0\n\t" \
    "s_cselect_b32 s50, s56, 0\n\t" \
    "s_sub_u32 s40, s40, s50\n\t" \
    "s_subb_u32 s41, s41, 0\n\t" \
    "s_add_u32 m0, s47, s53\n\t" \
    "s_nop 0\n\t" \
    "global_load_lds_dwordx4 v5, s[42:43] offset:0\n\t" \
    "global_load_lds_dwordx4 v6, s[42:43] offset:1024\n\t" \
    "s_waitcnt vmcnt(24)\n\t" \
    "s_barrier\n\t" \
    "s_mov_b32 m0, s46\n\t" \
    "s_add_u32 s46, s46, 14336\n\t" \
    "global_load_lds_dwordx4 v0, s[40:41] offset:-2048\n\t" \
    "global_load_lds_dwordx4 v1, s[40:41] offset:-1024\n\t" \
    "global_load_lds_dwordx4 v2, s[40:41] offset:0\n\t" \
    "global_load_lds_dwordx4 v3, s[40:41] offset:1024\n\t" \
    "s_cmp_eq_u32 s46, s52\n\t" \
    "s_cselect_b32 s46, s51, s46\n\t" \
    "s_add_u32 s40, s40, s49\n\t" \
    "s_addc_u32 s41, s41, 0\n\t" \
    "s_sub_u32 s54, s54, 1\n\t" \
    "s_cmp_eq_u32 s54, 0\n\t" \
    "s_cselect_b32 s50, s56, 0\n\t" \
    "s_sub_u32 s40, s40, s50\n\t" \
    "s_subb_u32 s41, s41, 0\n\t" \
    "s_add_u32 m0, s58, s53\n\t" \
    "s_nop 0\n\t" \
    "global_load_lds_dwordx4 v5, s[44:45] offset:0\n\t" \
    "global_load_lds_dwordx4 v6, s[44:45] offset:1024\n\t" \
    "s_add_u32 s53, s53, 16384\n\t" \
    "s_and_b32 s53, s53, 0xffff\n\t" \
    "s_add_u32 s42, s42, 128\n\t" \
    "s_addc_u32 s43, s43, 0\n\t" \
    "s_add_u32 s44, s44, 128\n\t" \
    "s_addc_u32 s45, s45, 0\n\t" \
    "s_sub_u32 s55, s55, 1\n\t" \
    "s_cmp_eq_u32 s55, 0\n\t" \
    "s_cselect_b32 s50, s57, 0\n\t" \
    "s_sub_u32 s42, s42, s50\n\t" \
    "s_subb_u32 s43, s43, 0\n\t" \
    "s_sub_u32 s44, s44, s50\n\t" \
    "s_subb_u32 s45, s45, 0\n\t" \
    "s_sub_u32 s48, s48, 1\n\t" \
    "s_cmp_lg_u32 s48, 0\n\t" \
    "s_waitcnt vmcnt(24)\n\t" \
    "s_barrier\n\t" \
    "s_cbranch_scc1 1b\n\t" \
    "2:\n\t" \
    "s_mov_b32 m0, s46\n\t" \
    "s_add_u32 s46, s46, 14336\n\t" \
    "global_load_lds_dwordx4 v0, s[40:41] offset:-2048\n\t" \
    "global_load_lds_dwordx4 v1, s[40:41] offset:-1024\n\t" \
    "global_load_lds_dwordx4 v2, s[40:41] offset:0\n\t" \
    "global_load_lds_dwordx4 v3, s[40:41] offset:1024\n\t" \
    "s_cmp_eq_u32 s46, s52\n\t" \
    "s_cselect_b32 s46, s51, s46\n\t" \
    "s_add_u32 s40, s40, s49\n\t" \
    "s_addc_u32 s41, s41, 0\n\t" \
    "s_sub_u32 s54, s54, 1\n\t" \
    "s_cmp_eq_u32 s54, 0\n\t" \
    "s_cselect_b32 s50, s56, 0\n\t" \
    "s_sub_u32 s40, s40, s50\n\t" \
    "s_subb_u32 s41, s41, 0\n\t" \
    ""
#define LOADER_LOOPS(nstep) (((nstep) - 14) / 2)
#define LOADER_CLOB "s50", "scc", "memory"

#define STAMP(id) do { } while (0)
#define STAMPC(id) do { } while (0)
#define STARG
#define STPASS
namespace {
constexpr int A_SLOT = 16384, B_SLOT = 14336, LDS_A = 0, LDS_B = 65536;
constexpr int LDS_BYTES = 163840;
constexpr unsigned WS_EPOCH = 0, WS_ARR = 1024, WS_F1 = 4096, WS_FO = 6144, WS_FP = 8192;
constexpr unsigned WS_Q = 16384, WS_K = WS_Q + 2097152, WS_V = WS_K + 2097152, WS_O = WS_V + 2097152, WS_PQ = WS_O + 2097152;
constexpr unsigned WS_END = WS_PQ + 7340032;
constexpr int AUX_SC1 = 16;
constexpr unsigned SPIN_MAX = 1u << 22;

DI unsigned cvtpk(float lo, float hi) { f32x2 v = {lo, hi}; bf16x2 b = __builtin_convertvector(v, bf16x2); return __builtin_bit_cast(unsigned, b); }
DI float rbf(float x) { return __uint_as_float(cvtpk(0.0f, x)); }
DI void glds16(const void* sbase, unsigned voff, unsigned lds_base) {
    asm volatile("s_mov_b32 m0, %2\n\ts_nop 0\n\tglobal_load_lds_dwordx4 %1, %0" :: "s"(sbase), "v"(voff), "s"(lds_base) : "memory"); }
DI void wait_vm0() { asm volatile("s_waitcnt vmcnt(0)" ::: "memory"); }
DI void bar() { asm volatile("s_waitcnt lgkmcnt(0)\n\ts_barrier" ::: "memory"); }
DI s16x4 trread(LAS const char* p) { return __builtin_bit_cast(s16x4, __builtin_amdgcn_ds_read_tr16_b64_v4i16((LAS s16x4*)p)); }
DI v4u ld_sc1(__amdgpu_buffer_rsrc_t rs, unsigned off) { return __builtin_amdgcn_raw_buffer_load_b128(rs, off, 0, AUX_SC1); }
DI void st_sc1(__amdgpu_buffer_rsrc_t rs, unsigned off, v4u v) { __builtin_amdgcn_raw_buffer_store_b128(v, rs, off, 0, AUX_SC1); }
DI void flag_store(char* ws, unsigned off, u64 tag) { __hip_atomic_store((GAS u64*)(ws + off), tag, __ATOMIC_RELAXED, __HIP_MEMORY_SCOPE_AGENT); }
DI void poll(char* ws, unsigned off, bool active, u64 tag) {
    for (unsigned spins = 0; spins < SPIN_MAX; ++spins) {
        u64 v = tag;
        if (active) v = __hip_atomic_load((GAS u64*)(ws + off), __ATOMIC_RELAXED, __HIP_MEMORY_SCOPE_AGENT);
        if (__all(v == tag)) break;
        __builtin_amdgcn_s_sleep(1);
    }
    asm volatile("" ::: "memory");
}

template <int N> DI void wait_vm() { asm volatile("s_waitcnt vmcnt(%0)" :: "i"(N) : "memory"); }
struct NoTail { static constexpr int count(int, int) { return 0; } template <int W, int K> DI void issue() const {} };
template <class TAIL> constexpr int tail_sum(int w, int k) { int t = 0; for (int i = 0; i <= k; ++i) t += TAIL::count(w, i); return t; }
template <int W, int NB, int NA, class TAIL>
DI void g224_tail(const TAIL& t) {
    constexpr int n = NB + NA;
#define G224_TS(K, RING) t.template issue<W, K>(); if (RING >= 0) wait_vm<(RING >= 0 ? RING + tail_sum<TAIL>(W, K) : 0)>(); asm volatile("s_barrier" ::: "memory");
    G224_TS(0, 3 * n + NB) G224_TS(1, 2 * n + NB) G224_TS(2, n + NB) G224_TS(3, NB) G224_TS(4, 0) G224_TS(5, -1)
    t.template issue<W, 6>(); wait_vm<tail_sum<TAIL>(W, 6) - tail_sum<TAIL>(W, 2)>();
    asm volatile("s_barrier" ::: "memory");
#undef G224_TS
}
template <int NB, int NA>
DI void g224_loader(unsigned lds0, int w, int lane, const char* a0, const char* a1, unsigned lda, const char* bbase, unsigned ldb, int nstep, int rot) {
    const int b0 = 5 * w, i0 = 3 * w;
    unsigned vo[5], va[3];
#pragma unroll
    for (int i = 0; i < 5; ++i) {
        const unsigned o = (b0 + i) * 1024 + lane * 16;
        const unsigned row = o / 448, pc = (o - row * 448) >> 4;
        vo[i] = (i < NB) ? row * ldb + ((pc ^ (((row >> 3) & 1) << 1)) * 16) + 2048 - i * 1024 : 0u;
    }
    const int rl = lane >> 3, ch = lane & 7;
#pragma unroll
    for (int i = 0; i < 3; ++i) {
        const int ii = i0 + i;
        va[i] = (i < NA) ? (ii * 8 + rl) * lda + ((ch ^ ((((ii & 1) * 8 + rl) >> 1) & 7)) * 16) + 2048 - i * 1024 : 0u;
    }
    const unsigned bdst = lds0 + LDS_B + b0 * 1024 + 2048, adst = lds0 + LDS_A + i0 * 1024;
    const int rb = 2 * rot, ra = rot;
    const char* pb = bbase + (size_t)rb * 32 * ldb; const char* pa0 = a0 + ra * 128 - 2048; const char* pa1 = a1 + ra * 128 - 2048;
    unsigned s46 = bdst, s47 = adst, s48 = LOADER_LOOPS(nstep), s49 = 32 * ldb, s51 = bdst, s52 = bdst + 6 * B_SLOT, s53 = 0, s54 = nstep - rb, s55 = nstep / 2 - ra,
             s56 = nstep * 32 * ldb, s57 = (nstep / 2) * 128, s58 = adst + 8192;
    if (NB == 5) asm volatile(LOADER53 : "+{s[40:41]}"(pb), "+{s[42:43]}"(pa0), "+{s[44:45]}"(pa1), "+{s46}"(s46), "+{s48}"(s48), "+{s53}"(s53), "+{s54}"(s54), "+{s55}"(s55)
         : "{v0}"(vo[0]), "{v1}"(vo[1]), "{v2}"(vo[2]), "{v3}"(vo[3]), "{v4}"(vo[4]), "{v5}"(va[0]), "{v6}"(va[1]), "{v7}"(va[2]),
           "{s47}"(s47), "{s49}"(s49), "{s51}"(s51), "{s52}"(s52), "{s56}"(s56), "{s57}"(s57), "{s58}"(s58) : LOADER_CLOB);
    else asm volatile(LOADER42 : "+{s[40:41]}"(pb), "+{s[42:43]}"(pa0), "+{s[44:45]}"(pa1), "+{s46}"(s46), "+{s48}"(s48), "+{s53}"(s53), "+{s54}"(s54), "+{s55}"(s55)
         : "{v0}"(vo[0]), "{v1}"(vo[1]), "{v2}"(vo[2]), "{v3}"(vo[3]), "{v4}"(vo[4]), "{v5}"(va[0]), "{v6}"(va[1]), "{v7}"(va[2]),
           "{s47}"(s47), "{s49}"(s49), "{s51}"(s51), "{s52}"(s52), "{s56}"(s56), "{s57}"(s57), "{s58}"(s58) : LOADER_CLOB);
}
DI void touch(const char* p, unsigned lds_dummy) {
    asm volatile("s_mov_b32 m0, %1\n\ts_nop 0\n\tglobal_load_lds_dword %0, off" :: "v"(p), "s"(lds_dummy) : "memory"); }
constexpr int LDS_DUMMY = LDS_B + 6 * B_SLOT;
constexpr int LDS_CNT = LDS_BYTES - 64;
#define B2_PD 5
#define ATT_PRIO 0
#define ENG_FIRST 2
#define B1_ROT(x, tbl) (2 * (x))
template <class EX, class TAIL>
DI void g224(unsigned lds0, int wid, int lane, const char* a0, const char* a1, unsigned lda, const char* bbase, unsigned ldb, int nstep, int rot, bool swap, f32x16 (&c)[7], EX extra, const TAIL& tail) {
    if (wid == 7) {
        asm volatile("s_barrier\n\ts_barrier" ::: "memory");
        for (int s = 0; s < nstep - 7; ++s) {
            extra(s);
            asm volatile("s_barrier" ::: "memory");
        }
        g224_tail<3, 0, 0>(tail);
    }
    else if (wid == 6) { g224_loader<4, 2>(lds0, 2, lane, a0, a1, lda, bbase, ldb, nstep, rot); g224_tail<2, 4, 2>(tail); }
    else if (wid == 5) { g224_loader<5, 3>(lds0, 1, lane, a0, a1, lda, bbase, ldb, nstep, rot); g224_tail<1, 5, 3>(tail); }
    else if (wid == 4) { g224_loader<5, 3>(lds0, 0, lane, a0, a1, lda, bbase, ldb, nstep, rot); g224_tail<0, 5, 3>(tail); }
    else {
        const int mh = wid & 1, nh = wid >> 1;
        const int r16 = lane & 15, g = lane >> 4, q = (lane & 15) >> 2, p = lane & 3;
        const unsigned aoff0 = lds0 + LDS_A + (mh * 64 + r16) * 128 + ((g ^ ((r16 >> 1) & 7)) * 16);
        const unsigned brow = lds0 + LDS_B + (8 * g + q) * 448 + p * 8 + nh * 224;
        const unsigned gb = (g & 1) * 32;
        const unsigned bE = nh ? brow - gb : brow + gb, bO = nh ? brow + gb : brow - gb;
        unsigned aslot, bslot, cnt = nstep / 2 - 1;
        if (swap) asm volatile(G224_BODY_SWAP
            : "={v[0:15]}"(c[0]), "={v[16:31]}"(c[1]), "={v[32:47]}"(c[2]), "={v[48:63]}"(c[3]), "={v[64:79]}"(c[4]), "={v[80:95]}"(c[5]), "={v[96:111]}"(c[6]),
              [aslot] "=&s"(aslot), [bslot] "=&s"(bslot), [cnt] "+s"(cnt)
            : "{v200}"(aoff0), "{v201}"(aoff0 ^ 64), "{v202}"(bE), "{v203}"(bO) : G224_CLOB);
        else asm volatile(G224_BODY
            : "={v[0:15]}"(c[0]), "={v[16:31]}"(c[1]), "={v[32:47]}"(c[2]), "={v[48:63]}"(c[3]), "={v[64:79]}"(c[4]), "={v[80:95]}"(c[5]), "={v[96:111]}"(c[6]),
              [aslot] "=&s"(aslot), [bslot] "=&s"(bslot), [cnt] "+s"(cnt)
            : "{v200}"(aoff0), "{v201}"(aoff0 ^ 64), "{v202}"(bE), "{v203}"(bO) : G224_CLOB);
    }
}

DI bool last_arriver(LAS char* L, int k, unsigned n, int lane) {
    unsigned old = 0;
    if (lane == 0) old = __hip_atomic_fetch_add((LAS unsigned*)(L + LDS_CNT + k * 4), 1u, __ATOMIC_RELAXED, __HIP_MEMORY_SCOPE_WORKGROUP);
    return __builtin_amdgcn_readfirstlane(old) == n - 1;
}
struct FinalAddr { unsigned so[4]; char* gp[4]; };
DI FinalAddr final_addr(int lane, int m, char* outp  ) {
    FinalAddr f;
#pragma unroll
    for (int k = 0; k < 4; ++k) {
        const int idx = min(k * 64 + lane, 223), row = idx / 14, chn = idx - row * 14;
        f.so[k] = 32768 + m * 4096 + row * 240 + chn * 16;
        f.gp[k] = outp + (size_t)(m * 16 + row) * 1792 + chn * 16;
        asm volatile("" : "+v"(f.so[k]), "+v"(f.gp[k]));
    }
    return f;
}
DI void final_store(LAS char* L, int wid, int lane, f32x4 (&acc)[7], float sign, const FinalAddr& fa) {
    bar();
    const int m = wid & 3;
    if (wid >= 4) {
#pragma unroll
        for (int n = 0; n < 7; ++n) *(LAS f32x4*)(L + (m * 7 + n) * 1024 + lane * 16) = acc[n];
    }
    bar();
    if (wid < 4) {
        LAS char* stg = L + 32768 + m * 4096;
        const int t16 = lane & 15, g = lane >> 4;
        f32x4 o[7];
#pragma unroll
        for (int n = 0; n < 7; ++n) o[n] = *(const LAS f32x4*)(L + (m * 7 + n) * 1024 + lane * 16);
#pragma unroll
        for (int n = 0; n < 7; ++n) {
            v2u w;
            w[0] = cvtpk(acc[n][0] + sign * o[n][0], acc[n][1] + sign * o[n][1]);
            w[1] = cvtpk(acc[n][2] + sign * o[n][2], acc[n][3] + sign * o[n][3]);
            *(LAS v2u*)(stg + t16 * 240 + n * 32 + g * 8) = w;
        }
        asm volatile("s_waitcnt lgkmcnt(0)" ::: "memory");
        v4u v[4];
#pragma unroll
        for (int k = 0; k < 4; ++k) v[k] = *(const LAS v4u*)(L + fa.so[k]);
#pragma unroll
        for (int k = 0; k < 3; ++k) *(GAS v4u*)fa.gp[k] = v[k];
        if (lane < 32) *(GAS v4u*)fa.gp[3] = v[3];
    }
}

DI bf16x8 add_frag(v4u a, v4u b) {
    v4u r;
#pragma unroll
    for (int j = 0; j < 4; ++j) {
        const float lo = __uint_as_float(a[j] << 16) + __uint_as_float(b[j] << 16);
        const float hi = __uint_as_float(a[j] & 0xffff0000u) + __uint_as_float(b[j] & 0xffff0000u);
        r[j] = cvtpk(lo, hi);
    }
    return __builtin_bit_cast(bf16x8, r);
}

DI v4u pack_pair(const f32x16& ca, const f32x16& cb, int m) {
    unsigned a0 = cvtpk(ca[4 * m], ca[4 * m + 1]), a1 = cvtpk(ca[4 * m + 2], ca[4 * m + 3]);
    unsigned b0 = cvtpk(cb[4 * m], cb[4 * m + 1]), b1 = cvtpk(cb[4 * m + 2], cb[4 * m + 3]);
    auto s0 = __builtin_amdgcn_permlane32_swap(a0, b0, false, false);
    auto s1 = __builtin_amdgcn_permlane32_swap(a1, b1, false, false);
    auto t0 = __builtin_amdgcn_permlane16_swap(s0[0], s0[1], false, false);
    auto t1 = __builtin_amdgcn_permlane16_swap(s1[0], s1[1], false, false);
    return (v4u){t0[0], t1[0], t0[1], t1[1]};
}
DI v4u pack_single(const f32x16& ca, int m) {
    unsigned a0 = cvtpk(ca[4 * m], ca[4 * m + 1]), a1 = cvtpk(ca[4 * m + 2], ca[4 * m + 3]);
    auto t0 = __builtin_amdgcn_permlane16_swap(a0, a0, false, false);
    auto t1 = __builtin_amdgcn_permlane16_swap(a1, a1, false, false);
    return (v4u){t0[0], t1[0], t0[1], t1[1]};
}

constexpr unsigned W_POS[22] = {151552, 0, 7168, 94208, 101376, 14336, 21504, 108544, 115712, 122880, 130048, 28672, 35840, 137216, 144384, 65536, 72704,
                                43008, 50176, 57344, 79872, 87040};
constexpr int W_STEP[22] = {0, 0, 0, 1, 1, 2, 2, 2, 2, 3, 3, 4, 4, 4, 4, 5, 5, 6, 6, 6, 6, 6};
struct WTail {
    const char* wre; const char* wim; unsigned wvo, lds0;
    static constexpr int count(int w, int k) { int t = 0; for (int c = 0; c < 22; ++c) if ((c & 3) == w && W_STEP[c] == k) t += 7; return t; }
    DI void chunk(int u, int pq, unsigned pos) const {
        const char* src = (pq ? wim : wre) + u * 64;
#pragma unroll
        for (int k = 0; k < 7; ++k) glds16(src + k * 16 * 896, wvo, __builtin_amdgcn_readfirstlane(lds0 + pos + k * 1024));
    }
    template <int W, int K> DI void issue() const {
#pragma unroll
        for (int c = 0; c < 22; ++c) if ((c & 3) == W && W_STEP[c] == K) chunk(c >> 1, c & 1, W_POS[c]);
    }
};
DI void role_b(char* lds, int x, int i, int wid, int lane, const char* Ax, const char* Wqkv, const char* Wo, const char* Bx, const char* Cre, const char* Cim, const char* Wre, const char* Wim, char* out, char* ws, u64 tag STARG) {
    const unsigned lds0 = (unsigned)(uintptr_t)lds;
    LAS char* L = (LAS char*)lds;
    const int kh = x & 1, ch = i & 1, tb = 8 * (x >> 1) + (i >> 1), cb = 2 * kh + ch;
    const __amdgpu_buffer_rsrc_t rs = __builtin_amdgcn_make_buffer_rsrc(ws, 0, WS_END, 0x00020000);
    f32x16 c[7];
    STAMP(0); STAMPC(0);
    WTail wt;
    wt.wre = Wre + (size_t)cb * 112 * 896; wt.wim = Wim + (size_t)cb * 112 * 896; wt.lds0 = lds0;
    wt.wvo = (lane >> 2) * 896 + (((lane & 3) ^ ((((lane >> 5) ^ (lane >> 4)) & 1) * 2 + (lane >> 5))) * 16);
    g224(lds0, wid, lane, Cre + ((size_t)tb * 64 * 2048 + kh * 1024) * 2, Cim + ((size_t)tb * 64 * 2048 + kh * 1024) * 2, 4096,
         Bx + ((size_t)kh * 1024 * 448 + ch * 224) * 2, 896, 32, B1_ROT(x, i >> 2), false, c,
         [&](int s) { if (s == 0 && lane == 0) flag_store(ws, WS_ARR + blockIdx.x * 8, tag);
         }, wt);
    STAMP(1); STAMPC(1);
    if (wid < 4) {
        const int mh = wid & 1, nh = wid >> 1, g = lane >> 4;
        const unsigned base = WS_PQ + ((kh * 2 + mh) * 128 + tb * 4) * 14336 + (ch * 7) * 1024 + lane * 16;
#pragma unroll
        for (int m = 0; m < 4; ++m) {
            const unsigned bm = base + m * 14336;
            if (nh == 0) {
                st_sc1(rs, bm, pack_pair(c[0], c[1], m)); st_sc1(rs, bm + 1024, pack_pair(c[2], c[3], m)); st_sc1(rs, bm + 2048, pack_pair(c[4], c[5], m));
                const v4u s = pack_single(c[6], m);
                if ((g & 1) == 0) st_sc1(rs, bm + 3072 - lane * 16 + ((g >> 1) * 16 + (lane & 15)) * 16, s);
            } else {
                const v4u s = pack_single(c[0], m);
                if ((g & 1) == 0) st_sc1(rs, bm + 3072 - lane * 16 + ((2 + (g >> 1)) * 16 + (lane & 15)) * 16, s);
                st_sc1(rs, bm + 4096, pack_pair(c[1], c[2], m)); st_sc1(rs, bm + 5120, pack_pair(c[3], c[4], m)); st_sc1(rs, bm + 6144, pack_pair(c[5], c[6], m));
            }
        }
    }
    const int m = wid & 3, pq = wid >> 2;
    const int e16 = lane & 15, g = lane >> 4, rq = e16 >> 2;
    const unsigned wro = e16 * 64 + ((g ^ ((((rq >> 1) ^ rq) & 1) * 2 + (rq >> 1))) * 16);
    auto wpos = [&](int u) { const int uu = u < 11 ? u : u - 11; return pq ? W_POS[2 * uu + 1] : W_POS[2 * uu]; };
    bf16x8 w[3][7];
#pragma unroll
    for (int u = 0; u < 3; ++u)
#pragma unroll
        for (int n = 0; n < 7; ++n) w[u][n] = *(const LAS bf16x8*)(L + wro + wpos(u) + n * 1024);
    STAMP(9);
    bar();
    if (wid >= 4) {
        for (int s = wid - 4; s < 6; s += 4) wt.chunk(11 + (s >> 1), s & 1, s == 0 ? W_POS[0] : s == 1 ? W_POS[1] : s == 2 ? W_POS[2] : s == 3 ? W_POS[3] : s == 4 ? W_POS[4] : W_POS[5]);
        wait_vm0();
    } else {
        wait_vm0();
        STAMP(2);
        if (last_arriver(L, 0, 4, lane)) {
            if (lane == 0) flag_store(ws, WS_FP + (tb * 4 + cb) * 8, tag);
            STAMP(3);
            poll(ws, WS_FP + (tb * 4 + (lane & 3)) * 8, lane < 4, tag);
        }
    }
    STAMP(4);
    bar();
    STAMP(5);
    const unsigned pb = WS_PQ + (pq * 128 + tb * 4 + m) * 14336 + lane * 16;
    constexpr int PD = B2_PD;
    v4u p0[14], p1[14];
#pragma unroll
    for (int u = 0; u < PD; ++u) { p0[u] = ld_sc1(rs, pb + u * 1024); p1[u] = ld_sc1(rs, pb + 2 * 128 * 14336 + u * 1024); }
    asm volatile("" ::: "memory"); __builtin_amdgcn_sched_barrier(0);
    const FinalAddr fa = final_addr(lane, m, out + ((size_t)tb * 64 * 896 + 448 + cb * 112) * 2);
    f32x4 acc[7];
#pragma unroll
    for (int n = 0; n < 7; ++n) acc[n] = (f32x4){0.f, 0.f, 0.f, 0.f};
#pragma unroll
    for (int u = 0; u < 14; ++u) {
        if (u == 1) STAMP(10);
        if (u == 3) STAMP(11);
        if (u == 8) STAMP(12);
        if (u + PD < 14) { p0[u + PD] = ld_sc1(rs, pb + (u + PD) * 1024); p1[u + PD] = ld_sc1(rs, pb + 2 * 128 * 14336 + (u + PD) * 1024); __builtin_amdgcn_sched_barrier(0); }
        const bf16x8 f = add_frag(p0[u], p1[u]);
        __builtin_amdgcn_sched_barrier(0);
#pragma unroll
        for (int n = 0; n < 7; ++n) acc[n] = __builtin_amdgcn_mfma_f32_16x16x32_bf16(w[u % 3][n], f, acc[n], 0, 0, 0);
        if (u + 3 < 14) {
#pragma unroll
            for (int n = 0; n < 7; ++n) w[u % 3][n] = *(const LAS bf16x8*)(L + wro + wpos(u + 3) + n * 1024);
        }
        __builtin_amdgcn_sched_barrier(0);
    }
    STAMP(6);
    final_store(L, wid, lane, acc, -1.0f, fa);
    STAMP(7);
}

#define ASTAMP(id) do { } while (0)
template <class HOOK>
DI void attention(int iq, int head, int lane, __amdgpu_buffer_rsrc_t rs, HOOK after_loads STARG) {
    float SC = 0.18898223650461362f, L2E = 1.4426950408889634f;
    asm volatile("" : "+v"(SC), "+v"(L2E));
    const int a = lane & 31, hi = lane >> 5;
    const unsigned hb = head * 64 * 2048 + lane * 16;
    v4u kf[9][2], vf[9][2];
    const bf16x8 q0 = __builtin_bit_cast(bf16x8, ld_sc1(rs, WS_Q + hb + iq * 2048)), q1 = __builtin_bit_cast(bf16x8, ld_sc1(rs, WS_Q + hb + iq * 2048 + 1024));
#pragma unroll
    for (int jj = 0; jj < 9; ++jj) { const int j = jj == 0 ? 0 : jj == 1 ? 8 : jj - 1; const int kt = max(iq - 8 + j, 0);
        kf[j][0] = ld_sc1(rs, WS_K + hb + kt * 2048); kf[j][1] = ld_sc1(rs, WS_K + hb + kt * 2048 + 1024); }
    asm volatile("" ::: "memory"); __builtin_amdgcn_sched_barrier(0);
    after_loads();
    ASTAMP(9);
    __builtin_amdgcn_sched_barrier(0);
    const f32x16 zero = {};
    f32x16 S[8];
    {
        f32x16 so = __builtin_amdgcn_mfma_f32_32x32x16_bf16(__builtin_bit_cast(bf16x8, kf[0][0]), q0, zero, 0, 0, 0);
        so = __builtin_amdgcn_mfma_f32_32x32x16_bf16(__builtin_bit_cast(bf16x8, kf[0][1]), q1, so, 0, 0, 0);
        f32x16 sd = __builtin_amdgcn_mfma_f32_32x32x16_bf16(__builtin_bit_cast(bf16x8, kf[8][0]), q0, zero, 0, 0, 0);
        sd = __builtin_amdgcn_mfma_f32_32x32x16_bf16(__builtin_bit_cast(bf16x8, kf[8][1]), q1, sd, 0, 0, 0);
        if (iq < 8) {
            asm volatile("s_nop 7\n\ts_nop 7\n\ts_nop 7");
#pragma unroll
            for (int r = 0; r < 16; ++r) { float t; asm volatile("v_mov_b32 %0, 0xff800000" : "=v"(t)); so[r] = t; }
        }
#pragma unroll
        for (int r = 0; r < 16; ++r) { const int b = (r & 3) + 8 * (r >> 2) + 4 * hi; S[0][r] = (b <= a) ? sd[r] : so[r]; }
    }
    asm volatile("" :: "v"(S[0][0])); ASTAMP(10);
#pragma unroll
    for (int j = 1; j < 8; ++j) {
        f32x16 s = __builtin_amdgcn_mfma_f32_32x32x16_bf16(__builtin_bit_cast(bf16x8, kf[j][0]), q0, zero, 0, 0, 0);
        s = __builtin_amdgcn_mfma_f32_32x32x16_bf16(__builtin_bit_cast(bf16x8, kf[j][1]), q1, s, 0, 0, 0);
        if (iq - 8 + j < 0) {
            asm volatile("s_nop 7\n\ts_nop 7\n\ts_nop 7");
#pragma unroll
            for (int r = 0; r < 16; ++r) { float t; asm volatile("v_mov_b32 %0, 0xff800000" : "=v"(t)); s[r] = t; }
        }
        S[j] = s;
    }
    __builtin_amdgcn_sched_barrier(0);
#pragma unroll
    for (int jj = 0; jj < 9; ++jj) { const int j = jj == 0 ? 8 : jj - 1; const int kt = max(iq - 8 + j, 0);
        vf[j][0] = ld_sc1(rs, WS_V + hb + kt * 2048); vf[j][1] = ld_sc1(rs, WS_V + hb + kt * 2048 + 1024); }
    asm volatile("" ::: "memory"); __builtin_amdgcn_sched_barrier(0);
    float mx = S[0][0];
#pragma unroll
    for (int j = 0; j < 8; ++j)
#pragma unroll
        for (int r = 0; r < 16; ++r) mx = __builtin_fmaxf(mx, S[j][r]);
    { auto rr = __builtin_amdgcn_permlane32_swap(__float_as_uint(mx), __float_as_uint(mx), false, false);
      mx = __builtin_fmaxf(__uint_as_float(rr[0]), __uint_as_float(rr[1])); }
    const float nml = -(rbf(rbf(mx) * SC) * L2E);
    asm volatile("" :: "v"(nml)); ASTAMP(11);
    f32x16 O = zero;
#pragma unroll
    for (int j = 0; j < 8; ++j) {
        if (j == 4) { asm volatile("" :: "v"(O[0])); ASTAMP(12); }
        v4u pw[2];
#pragma unroll
        for (int k = 0; k < 8; ++k) {
            const float e0 = __builtin_amdgcn_exp2f(__builtin_fmaf(rbf(rbf(S[j][2 * k]) * SC), L2E, nml));
            const float e1 = __builtin_amdgcn_exp2f(__builtin_fmaf(rbf(rbf(S[j][2 * k + 1]) * SC), L2E, nml));
            pw[k >> 2][k & 3] = cvtpk(e0, e1);
        }
        if (j == 0) {
            v4u pd[2], po[2];
#pragma unroll
            for (int k = 0; k < 8; ++k) {
                const int b = ((2 * k) & 3) + 8 * ((2 * k) >> 2) + 4 * hi;
                const unsigned msk = (b <= a ? 0xffffu : 0u) | (b + 1 <= a ? 0xffff0000u : 0u);
                pd[k >> 2][k & 3] = pw[k >> 2][k & 3] & msk; po[k >> 2][k & 3] = pw[k >> 2][k & 3] & ~msk;
            }
            O = __builtin_amdgcn_mfma_f32_32x32x16_bf16(__builtin_bit_cast(bf16x8, vf[8][0]), __builtin_bit_cast(bf16x8, pd[0]), O, 0, 0, 0);
            O = __builtin_amdgcn_mfma_f32_32x32x16_bf16(__builtin_bit_cast(bf16x8, vf[8][1]), __builtin_bit_cast(bf16x8, pd[1]), O, 0, 0, 0);
            O = __builtin_amdgcn_mfma_f32_32x32x16_bf16(__builtin_bit_cast(bf16x8, vf[0][0]), __builtin_bit_cast(bf16x8, po[0]), O, 0, 0, 0);
            O = __builtin_amdgcn_mfma_f32_32x32x16_bf16(__builtin_bit_cast(bf16x8, vf[0][1]), __builtin_bit_cast(bf16x8, po[1]), O, 0, 0, 0);
        } else {
            O = __builtin_amdgcn_mfma_f32_32x32x16_bf16(__builtin_bit_cast(bf16x8, vf[j][0]), __builtin_bit_cast(bf16x8, pw[0]), O, 0, 0, 0);
            O = __builtin_amdgcn_mfma_f32_32x32x16_bf16(__builtin_bit_cast(bf16x8, vf[j][1]), __builtin_bit_cast(bf16x8, pw[1]), O, 0, 0, 0);
        }
    }
    auto lr = __builtin_amdgcn_permlane32_swap(__float_as_uint(O[12]), __float_as_uint(O[12]), false, false);
    const float rl = 1.0f / __uint_as_float(lr[1]);
    v4u o0, o1;
#pragma unroll
    for (int k = 0; k < 4; ++k) { o0[k] = cvtpk(O[2 * k] * rl, O[2 * k + 1] * rl); o1[k] = cvtpk(O[8 + 2 * k] * rl, O[8 + 2 * k + 1] * rl); }
    if (hi) { o1[2] = 0; o1[3] = 0; }
    const unsigned ob = WS_O + (head * 128 + iq * 2 + (a >> 4)) * 1024 + (hi * 16 + (a & 15)) * 16;
    st_sc1(rs, ob, o0); st_sc1(rs, ob + 512, o1);
}

DI void role_a(char* lds, int x, int ai, int wid, int lane, const char* Ax, const char* Wqkv, const char* Wo, const char* Bx, const char* Cre, const char* Cim, const char* Wre, const char* Wim, char* out, char* ws, u64 tag STARG) {
    const unsigned lds0 = (unsigned)(uintptr_t)lds;
    LAS char* L = (LAS char*)lds;
    const __amdgpu_buffer_rsrc_t rs = __builtin_amdgcn_make_buffer_rsrc(ws, 0, WS_END, 0x00020000);
    const int iq = 8 * x + (ai >> 1), hh = ai & 1, cb = (iq & 1) * 2 + hh, rp = iq >> 1;
    if (ai < 12) {
        const int rb = 4 * (x >> 1) + ai / 3, j = 3 * (x & 1) + ai % 3;
        const bool isv = j >= 4;
        f32x16 c[7];
        STAMP(0); STAMPC(0);
        unsigned ioff[7] = {};
        if (wid < 4) {
            const int mh = wid & 1, nh = (wid >> 1) & 1, l16 = lane & 15, g = lane >> 4;
#pragma unroll
            for (int n = 0; n < 7; ++n) {
                const int u4 = nh * 28 + 4 * n + g, h8 = u4 / 7, du = u4 - 7 * h8;
                const int col = nh * 112 + 16 * n + l16, hv = col / 28, d = col - 28 * hv;
                ioff[n] = lds0 + mh * 4096 + (isv ? hv * 8192 + ((g & 1) * 32 + d) * 16 + (g >> 1) * 8
                                                  : h8 * 8192 + (du >> 2) * 1024 + (((du >> 1) & 1) * 32 + l16) * 16 + (du & 1) * 8);
                asm volatile("" : "+v"(ioff[n]));
            }
        }
        g224(lds0, wid, lane, Ax + (size_t)rb * 128 * 896, Ax + ((size_t)rb * 128 + 64) * 896, 896, Wqkv + j * 448, 2688, 14, 0, isv, c,
             [&](int s) { if (s == 0 && lane == 0) flag_store(ws, WS_ARR + blockIdx.x * 8, tag);
             }, NoTail());
        STAMP(1); STAMPC(1);
        if (wid < 4) {
            if (isv) {
#pragma unroll
                for (int n = 0; n < 7; ++n)
#pragma unroll
                    for (int m = 0; m < 4; ++m) {
                        v2u w; w[0] = cvtpk(c[n][4 * m], c[n][4 * m + 1]); w[1] = cvtpk(c[n][4 * m + 2], c[n][4 * m + 3]);
                        *(LAS v2u*)(uintptr_t)(ioff[n] + (m >> 1) * 2048 + (m & 1) * 1024) = w;
                    }
            } else {
#pragma unroll
                for (int n = 0; n < 7; ++n)
#pragma unroll
                    for (int m = 0; m < 4; ++m) {
                        v2u w; w[0] = cvtpk(c[n][4 * m], c[n][4 * m + 1]); w[1] = cvtpk(c[n][4 * m + 2], c[n][4 * m + 3]);
                        *(LAS v2u*)(uintptr_t)(ioff[n] + (m >> 1) * 2048 + (m & 1) * 256) = w;
                    }
            }
        } else {
            const int t = (wid - 4) * 64 + lane;
            if (!isv) {
#pragma unroll
                for (int k = 0; k < 4; ++k) { const int e = t + 256 * k, h8 = e >> 7, tok = e & 127;
                    *(LAS v2u*)(L + h8 * 8192 + (tok >> 5) * 2048 + 1024 + (32 + (tok & 31)) * 16 + 8) = (v2u){0u, 0u}; }
            } else {
#pragma unroll
                for (int k = 0; k < 2; ++k) { const int e = t + 256 * k, dd = e & 3; const unsigned val = dd == 0 ? 0x3f803f80u : 0u;
                    *(LAS v4u*)(L + (e >> 6) * 8192 + ((e >> 4) & 3) * 2048 + ((e >> 3) & 1) * 1024 + (((e >> 2) & 1) * 32 + 28 + dd) * 16) = (v4u){val, val, val, val}; }
            }
        }
        STAMP(9);
        bar();
        STAMP(10);
        const unsigned gb = (j < 2 ? WS_Q : j < 4 ? WS_K : WS_V) + ((j & 1) * 8 * 64 + rb * 4) * 2048 + lane * 16;
        {
            v4u v[8];
#pragma unroll
            for (int k = 0; k < 8; ++k) v[k] = *(const LAS v4u*)(L + (wid * 8 + k) * 1024 + lane * 16);
#pragma unroll
            for (int k = 0; k < 8; ++k) st_sc1(rs, gb + wid * 64 * 2048 + k * 1024, v[k]);
        }
        STAMP(11);
        wait_vm0();
        STAMP(12);
        asm volatile("s_waitcnt lgkmcnt(0)" ::: "memory");
        if (last_arriver(L, 0, 8, lane) && lane == 0) flag_store(ws, WS_F1 + (rb * 8 + j) * 8, tag);
        bar();
        STAMP(2);
    }
    if (ai >= 12) {
        bar();
        const unsigned dummy = lds0 + LDS_DUMMY;
        const int gw = (ai - 12) * 8 + wid;
        const int Lr = (gw & 15) * 64 + lane, tbl = Lr >> 7;
        const char* p = (((Lr >> 6) & 1) ? Cim : Cre) + (size_t)((8 * (x >> 1) + tbl) * 64 + (Lr & 63)) * 4096 + (x & 1) * 2048;
        for (int a = ENG_FIRST + (gw >> 4); a < 16; a += 2) touch(p + ((a + B1_ROT(x, tbl)) & 15) * 128, dummy);
        if (wid == 7 && lane == 0) flag_store(ws, WS_ARR + blockIdx.x * 8, tag);
    }
    const int m = wid & 3, k2 = wid >> 2;
    unsigned ad[2][2][7];
    {
        auto wo_stage = [&]() {
            const unsigned wb = (cb * 224) & ~63u;
            const int rr = lane >> 4;
#pragma unroll
            for (int k = 0; k < 14; ++k) {
                const int r4 = wid * 14 + k;
                const int row = 4 * r4 + rr;
                const unsigned vo = rr * 896 + (((lane & 15) ^ ((row & 7) << 1)) * 16);
                glds16(Wo + (size_t)r4 * 4 * 896 + wb, vo, __builtin_amdgcn_readfirstlane(lds0 + r4 * 1024));
            }
        };
        wo_stage();
        const int rq = iq >> 2;
        const int sel = lane == 0 ? 0 : lane < 4 ? 1 : 2;
        const int rb = sel == 0 ? rq : rq - 2 + ((lane - 1) % 3);
        if (wid == 0) poll(ws, WS_F1 + (max(rb, 0) * 8 + sel * 2 + hh) * 8, lane < 7 && rb >= 0, tag);
        asm volatile("s_barrier" ::: "memory");
        STAMP(3);
        if (wid >= 4) __builtin_amdgcn_s_setprio(ATT_PRIO);
        attention(iq, hh * 8 + wid, lane, rs, [] {} STPASS);
        __builtin_amdgcn_s_setprio(0);
        STAMP(4);
        {
            const int g = lane >> 4, q = (lane & 15) >> 2, p = lane & 3;
            const int s0 = ((cb * 224) & 63) >> 5;
#pragma unroll
            for (int hp = 0; hp < 2; ++hp)
#pragma unroll
                for (int xx = 0; xx < 2; ++xx) {
                    int unit = 4 * (g >> 1) + 2 * xx + (g & 1); if (unit == 7) unit = 6;
                    const int rl = 4 * unit + q, sw = (4 * hp + rl) & 7;
#pragma unroll
                    for (int n = 0; n < 7; ++n) { ad[hp][xx][n] = lds0 + (k2 * 8 * 28 + hp * 28 + rl) * 256 + (((s0 + n) ^ sw) * 32) + p * 8; asm volatile("" : "+v"(ad[hp][xx][n])); }
                }
        }
        wait_vm0();
        if (last_arriver(L, 1, 8, lane)) {
            if (lane == 0) flag_store(ws, WS_FO + (iq * 2 + hh) * 8, tag);
            STAMP(5);
            poll(ws, WS_FO + (rp * 4 + (lane & 3)) * 8, lane < 4, tag);
        }
        bar();
        STAMP(6);
    }
    {
        v4u of[8];
#pragma unroll
        for (int h = 0; h < 8; ++h) of[h] = ld_sc1(rs, WS_O + ((k2 * 8 + h) * 128 + rp * 4 + m) * 1024 + lane * 16);
        asm volatile("" ::: "memory"); __builtin_amdgcn_sched_barrier(0);
        const FinalAddr fa = final_addr(lane, m, out + ((size_t)rp * 64 * 896 + cb * 112) * 2);
        f32x4 acc[7];
#pragma unroll
        for (int n = 0; n < 7; ++n) acc[n] = (f32x4){0.f, 0.f, 0.f, 0.f};
        auto wfrag = [&](int h, int n) {
            const s16x4 lo = trread((LAS const char*)(uintptr_t)(ad[h & 1][0][n] + (h >> 1) * 2 * 7168)), hi2 = trread((LAS const char*)(uintptr_t)(ad[h & 1][1][n] + (h >> 1) * 2 * 7168));
            return (bf16x8){lo[0], lo[1], lo[2], lo[3], hi2[0], hi2[1], hi2[2], hi2[3]};
        };
        bf16x8 w[2][7];
#pragma unroll
        for (int n = 0; n < 7; ++n) w[0][n] = wfrag(0, n);
#pragma unroll
        for (int h = 0; h < 8; ++h) {
            if (h + 1 < 8) {
#pragma unroll
                for (int n = 0; n < 7; ++n) w[(h + 1) & 1][n] = wfrag(h + 1, n);
            }
            __builtin_amdgcn_sched_barrier(0);
#pragma unroll
            for (int n = 0; n < 7; ++n) acc[n] = __builtin_amdgcn_mfma_f32_16x16x32_bf16(w[h & 1][n], __builtin_bit_cast(bf16x8, of[h]), acc[n], 0, 0, 0);
            __builtin_amdgcn_sched_barrier(0);
        }
        final_store(L, wid, lane, acc, 1.0f, fa);
        STAMP(7);
    }
}
}

__global__ void __launch_bounds__(512, 2)
fused_kernel(const char* Ax, const char* Wqkv, const char* Wo, const char* Bx, const char* Cre, const char* Cim, const char* Wre, const char* Wim, char* out, char* ws) {
    extern __shared__ __attribute__((aligned(16))) char lds[];
    const int wid = __builtin_amdgcn_readfirstlane(threadIdx.x >> 6), lane = threadIdx.x & 63;
    const int x = blockIdx.x & 7, i = blockIdx.x >> 3;
    STAMP(8);
    {
        const u64 t_start = __builtin_amdgcn_s_memrealtime();
        const unsigned dummy = (unsigned)(uintptr_t)lds + LDS_DUMMY;
        const int gw = blockIdx.x * 8 + wid;
        if (wid == 6) { u64 pc; asm volatile("s_getpc_b64 %0" : "=s"(pc)); const char* c0 = (const char*)(pc & ~127ull);
            for (int k = 0; k < 6; ++k) touch(c0 + (size_t)(k * 64 + lane) * 128, dummy); }
        if (wid == 7) {
            for (int k = 0; k < 3; ++k) { const int pg = k * 64 + lane;
                if (pg < 128) touch(Cre + (size_t)pg * 65536, dummy); if (pg < 128) touch(Cim + (size_t)pg * 65536, dummy); }
            if (lane < 28) touch(Ax + (size_t)lane * 65536, dummy); if (lane < 28) touch(Bx + (size_t)lane * 65536, dummy);
            if (lane < 19) touch(Wqkv + (size_t)lane * 65536, dummy); if (lane < 7) { touch(Wo + (size_t)lane * 65536, dummy); touch(Wre + (size_t)lane * 65536, dummy); touch(Wim + (size_t)lane * 65536, dummy); }
            for (int k = 0; k < 4; ++k) { const int pg = k * 64 + lane; if (pg < 240) touch(ws + (size_t)pg * 65536, dummy); }
            if (lane < 56) touch(out + (size_t)lane * 65536, dummy);
        }
        for (int t = gw; t < 2790; t += 2048) { const int Ln = t * 64 + lane;
            const char* p = Ln < 65536 ? Cre + (size_t)Ln * 128 : Ln < 131072 ? Cim + (size_t)(Ln - 65536) * 128 : Ln < 145408 ? Ax + (size_t)(Ln - 131072) * 128
              : Ln < 159744 ? Bx + (size_t)(Ln - 145408) * 128 : Ln < 169152 ? Wqkv + (size_t)(Ln - 159744) * 128 : Ln < 172288 ? Wo + (size_t)(Ln - 169152) * 128
              : Ln < 175424 ? Wre + (size_t)(Ln - 172288) * 128 : Wim + (size_t)min(Ln - 175424, 3135) * 128;
            touch(p, dummy); }
        (void)gw;
        asm volatile("s_waitcnt vmcnt(0)" ::: "memory");
        while (__builtin_amdgcn_s_memrealtime() - t_start < 1000ull) __builtin_amdgcn_s_sleep(2);
    }
    const u64 tag = *(const GAS u64*)(ws + WS_EPOCH) + 1;
    if (threadIdx.x < 4) *(LAS unsigned*)((LAS char*)lds + LDS_CNT + threadIdx.x * 4) = 0u;
    if (i < 16) role_b(lds, x, i, wid, lane, Ax, Wqkv, Wo, Bx, Cre, Cim, Wre, Wim, out, ws, tag STPASS);
    else role_a(lds, x, i - 16, wid, lane, Ax, Wqkv, Wo, Bx, Cre, Cim, Wre, Wim, out, ws, tag STPASS);
    if (blockIdx.x == 0 && wid == 7) {
#pragma unroll
        for (int k = 0; k < 4; ++k) poll(ws, WS_ARR + (k * 64 + lane) * 8, true, tag);
        if (lane == 0) flag_store(ws, WS_EPOCH, tag);
    }
}

extern "C" void kernel_launch(void* const* d_in, const int* in_sizes, int n_in, void* d_out, int out_size, void* d_ws, size_t ws_size, hipStream_t stream) {
    (void)in_sizes; (void)n_in; (void)out_size; (void)ws_size;
    (void)hipFuncSetAttribute((const void*)fused_kernel, hipFuncAttributeMaxDynamicSharedMemorySize, LDS_BYTES);
    fused_kernel<<<256, 512, LDS_BYTES, stream>>>((const char*)d_in[0], (const char*)d_in[1], (const char*)d_in[2], (const char*)d_in[3], (const char*)d_in[4], (const char*)d_in[5],
                                                   (const char*)d_in[6], (const char*)d_in[7], (char*)d_out, (char*)d_ws);
}
```

```cpp
#include <hip/hip_runtime.h>
#include <hip/hip_bf16.h>
#include <stdint.h>

typedef short bf16x8 __attribute__((ext_vector_type(8)));
typedef short s16x4 __attribute__((ext_vector_type(4)));
typedef float f32x2 __attribute__((ext_vector_type(2)));
typedef float f32x4 __attribute__((ext_vector_type(4)));
typedef float f32x16 __attribute__((ext_vector_type(16)));
typedef __bf16 bf16x2 __attribute__((ext_vector_type(2)));
typedef unsigned v4u __attribute__((ext_vector_type(4)));
typedef unsigned v2u __attribute__((ext_vector_type(2)));
typedef unsigned long long u64;
#define LAS __attribute__((address_space(3)))
#define GAS __attribute__((address_space(1)))
#define DI __device__ __forceinline__

#define G224_BODY \
    "v_mov_b32 v0, 0\n\t" \
    "v_mov_b32 v1, 0\n\t" \
    "v_mov_b32 v2, 0\n\t" \
    "v_mov_b32 v3, 0\n\t" \
    "v_mov_b32 v4, 0\n\t" \
    "v_mov_b32 v5, 0\n\t" \
    "v_mov_b32 v6, 0\n\t" \
    "v_mov_b32 v7, 0\n\t" \
    "v_mov_b32 v8, 0\n\t" \
    "v_mov_b32 v9, 0\n\t" \
    "v_mov_b32 v10, 0\n\t" \
    "v_mov_b32 v11, 0\n\t" \
    "v_mov_b32 v12, 0\n\t" \
    "v_mov_b32 v13, 0\n\t" \
    "v_mov_b32 v14, 0\n\t" \
    "v_mov_b32 v15, 0\n\t" \
    "v_mov_b32 v16, 0\n\t" \
    "v_mov_b32 v17, 0\n\t" \
    "v_mov_b32 v18, 0\n\t" \
    "v_mov_b32 v19, 0\n\t" \
    "v_mov_b32 v20, 0\n\t" \
    "v_mov_b32 v21, 0\n\t" \
    "v_mov_b32 v22, 0\n\t" \
    "v_mov_b32 v23, 0\n\t" \
    "v_mov_b32 v24, 0\n\t" \
    "v_mov_b32 v25, 0\n\t" \
    "v_mov_b32 v26, 0\n\t" \
    "v_mov_b32 v27, 0\n\t" \
    "v_mov_b32 v28, 0\n\t" \
    "v_mov_b32 v29, 0\n\t" \
    "v_mov_b32 v30, 0\n\t" \
    "v_mov_b32 v31, 0\n\t" \
    "v_mov_b32 v32, 0\n\t" \
    "v_mov_b32 v33, 0\n\t" \
    "v_mov_b32 v34, 0\n\t" \
    "v_mov_b32 v35, 0\n\t" \
    "v_mov_b32 v36, 0\n\t" \
    "v_mov_b32 v37, 0\n\t" \
    "v_mov_b32 v38, 0\n\t" \
    "v_mov_b32 v39, 0\n\t" \
    "v_mov_b32 v40, 0\n\t" \
    "v_mov_b32 v41, 0\n\t" \
    "v_mov_b32 v42, 0\n\t" \
    "v_mov_b32 v43, 0\n\t" \
    "v_mov_b32 v44, 0\n\t" \
    "v_mov_b32 v45, 0\n\t" \
    "v_mov_b32 v46, 0\n\t" \
    "v_mov_b32 v47, 0\n\t" \
    "v_mov_b32 v48, 0\n\t" \
    "v_mov_b32 v49, 0\n\t" \
    "v_mov_b32 v50, 0\n\t" \
    "v_mov_b32 v51, 0\n\t" \
    "v_mov_b32 v52, 0\n\t" \
    "v_mov_b32 v53, 0\n\t" \
    "v_mov_b32 v54, 0\n\t" \
    "v_mov_b32 v55, 0\n\t" \
    "v_mov_b32 v56, 0\n\t" \
    "v_mov_b32 v57, 0\n\t" \
    "v_mov_b32 v58, 0\n\t" \
    "v_mov_b32 v59, 0\n\t" \
    "v_mov_b32 v60, 0\n\t" \
    "v_mov_b32 v61, 0\n\t" \
    "v_mov_b32 v62, 0\n\t" \
    "v_mov_b32 v63, 0\n\t" \
    "v_mov_b32 v64, 0\n\t" \
    "v_mov_b32 v65, 0\n\t" \
    "v_mov_b32 v66, 0\n\t" \
    "v_mov_b32 v67, 0\n\t" \
    "v_mov_b32 v68, 0\n\t" \
    "v_mov_b32 v69, 0\n\t" \
    "v_mov_b32 v70, 0\n\t" \
    "v_mov_b32 v71, 0\n\t" \
    "v_mov_b32 v72, 0\n\t" \
    "v_mov_b32 v73, 0\n\t" \
    "v_mov_b32 v74, 0\n\t" \
    "v_mov_b32 v75, 0\n\t" \
    "v_mov_b32 v76, 0\n\t" \
    "v_mov_b32 v77, 0\n\t" \
    "v_mov_b32 v78, 0\n\t" \
    "v_mov_b32 v79, 0\n\t" \
    "v_mov_b32 v80, 0\n\t" \
    "v_mov_b32 v81, 0\n\t" \
    "v_mov_b32 v82, 0\n\t" \
    "v_mov_b32 v83, 0\n\t" \
    "v_mov_b32 v84, 0\n\t" \
    "v_mov_b32 v85, 0\n\t" \
    "v_mov_b32 v86, 0\n\t" \
    "v_mov_b32 v87, 0\n\t" \
    "v_mov_b32 v88, 0\n\t" \
    "v_mov_b32 v89, 0\n\t" \
    "v_mov_b32 v90, 0\n\t" \
    "v_mov_b32 v91, 0\n\t" \
    "v_mov_b32 v92, 0\n\t" \
    "v_mov_b32 v93, 0\n\t" \
    "v_mov_b32 v94, 0\n\t" \
    "v_mov_b32 v95, 0\n\t" \
    "v_mov_b32 v96, 0\n\t" \
    "v_mov_b32 v97, 0\n\t" \
    "v_mov_b32 v98, 0\n\t" \
    "v_mov_b32 v99, 0\n\t" \
    "v_mov_b32 v100, 0\n\t" \
    "v_mov_b32 v101, 0\n\t" \
    "v_mov_b32 v102, 0\n\t" \
    "v_mov_b32 v103, 0\n\t" \
    "v_mov_b32 v104, 0\n\t" \
    "v_mov_b32 v105, 0\n\t" \
    "v_mov_b32 v106, 0\n\t" \
    "v_mov_b32 v107, 0\n\t" \
    "v_mov_b32 v108, 0\n\t" \
    "v_mov_b32 v109, 0\n\t" \
    "v_mov_b32 v110, 0\n\t" \
    "v_mov_b32 v111, 0\n\t" \
    "s_waitcnt lgkmcnt(0)\n\t" \
    "s_barrier\n\t" \
    "v_mov_b32 v207, v200\n\t" \
    "v_mov_b32 v208, v202\n\t" \
    "v_mov_b32 v209, v203\n\t" \
    "ds_read_b128 v[112:115], v207 offset:0\n\t" \
    "ds_read_b64_tr_b16 v[128:129], v208 offset:0\n\t" \
    "ds_read_b64_tr_b16 v[130:131], v208 offset:1792\n\t" \
    "ds_read_b128 v[116:119], v207 offset:2048\n\t" \
    "ds_read_b128 v[120:123], v207 offset:4096\n\t" \
    "ds_read_b128 v[124:127], v207 offset:6144\n\t" \
    "ds_read_b64_tr_b16 v[132:133], v209 offset:32\n\t" \
    "ds_read_b64_tr_b16 v[134:135], v209 offset:1824\n\t" \
    "ds_read_b64_tr_b16 v[136:137], v208 offset:64\n\t" \
    "ds_read_b64_tr_b16 v[138:139], v208 offset:1856\n\t" \
    "ds_read_b64_tr_b16 v[140:141], v209 offset:96\n\t" \
    "ds_read_b64_tr_b16 v[142:143], v209 offset:1888\n\t" \
    "ds_read_b64_tr_b16 v[144:145], v208 offset:128\n\t" \
    "ds_read_b64_tr_b16 v[146:147], v208 offset:1920\n\t" \
    "ds_read_b64_tr_b16 v[148:149], v209 offset:160\n\t" \
    "ds_read_b64_tr_b16 v[150:151], v209 offset:1952\n\t" \
    "ds_read_b64_tr_b16 v[152:153], v208 offset:192\n\t" \
    "ds_read_b64_tr_b16 v[154:155], v208 offset:1984\n\t" \
    "s_mov_b32 %[aslot], 0\n\t" \
    "s_mov_b32 %[bslot], 14336\n\t" \
    "v_mov_b32 v204, v201\n\t" \
    "v_add_u32 v205, %[bslot], v202\n\t" \
    "v_add_u32 v206, %[bslot], v203\n\t" \
    "s_waitcnt lgkmcnt(0)\n\t" \
    "s_barrier\n\t" \
    "1:\n\t" \
    "v_mfma_f32_16x16x32_bf16 v[0:3], v[128:131], v[112:115], v[0:3]\n\t" \
    "ds_read_b128 v[156:159], v204 offset:0\n\t" \
    "v_mfma_f32_16x16x32_bf16 v[4:7], v[128:131], v[116:119], v[4:7]\n\t" \
    "ds_read_b64_tr_b16 v[172:173], v205 offset:0\n\t" \
    "v_mfma_f32_16x16x32_bf16 v[8:11], v[128:131], v[120:123], v[8:11]\n\t" \
    "ds_read_b64_tr_b16 v[174:175], v205 offset:1792\n\t" \
    "v_mfma_f32_16x16x32_bf16 v[12:15], v[128:131], v[124:127], v[12:15]\n\t" \
    "ds_read_b128 v[160:163], v204 offset:2048\n\t" \
    "v_mfma_f32_16x16x32_bf16 v[16:19], v[132:135], v[112:115], v[16:19]\n\t" \
    "ds_read_b128 v[164:167], v204 offset:4096\n\t" \
    "v_mfma_f32_16x16x32_bf16 v[20:23], v[132:135], v[116:119], v[20:23]\n\t" \
    "ds_read_b128 v[168:171], v204 offset:6144\n\t" \
    "v_mfma_f32_16x16x32_bf16 v[24:27], v[132:135], v[120:123], v[24:27]\n\t" \
    "ds_read_b64_tr_b16 v[176:177], v206 offset:32\n\t" \
    "v_mfma_f32_16x16x32_bf16 v[28:31], v[132:135], v[124:127], v[28:31]\n\t" \
    "ds_read_b64_tr_b16 v[178:179], v206 offset:1824\n\t" \
    "v_mfma_f32_16x16x32_bf16 v[32:35], v[136:139], v[112:115], v[32:35]\n\t" \
    "ds_read_b64_tr_b16 v[180:181], v205 offset:64\n\t" \
    "v_mfma_f32_16x16x32_bf16 v[36:39], v[136:139], v[116:119], v[36:39]\n\t" \
    "ds_read_b64_tr_b16 v[182:183], v205 offset:1856\n\t" \
    "v_mfma_f32_16x16x32_bf16 v[40:43], v[136:139], v[120:123], v[40:43]\n\t" \
    "ds_read_b64_tr_b16 v[184:185], v206 offset:96\n\t" \
    "v_mfma_f32_16x16x32_bf16 v[44:47], v[136:139], v[124:127], v[44:47]\n\t" \
    "ds_read_b64_tr_b16 v[186:187], v206 offset:1888\n\t" \
    "v_mfma_f32_16x16x32_bf16 v[48:51], v[140:143], v[112:115], v[48:51]\n\t" \
    "ds_read_b64_tr_b16 v[188:189], v205 offset:128\n\t" \
    "v_mfma_f32_16x16x32_bf16 v[52:55], v[140:143], v[116:119], v[52:55]\n\t" \
    "ds_read_b64_tr_b16 v[190:191], v205 offset:1920\n\t" \
    "v_mfma_f32_16x16x32_bf16 v[56:59], v[140:143], v[120:123], v[56:59]\n\t" \
    "ds_read_b64_tr_b16 v[192:193], v206 offset:160\n\t" \
    "v_mfma_f32_16x16x32_bf16 v[60:63], v[140:143], v[124:127], v[60:63]\n\t" \
    "ds_read_b64_tr_b16 v[194:195], v206 offset:1952\n\t" \
    "v_mfma_f32_16x16x32_bf16 v[64:67], v[144:147], v[112:115], v[64:67]\n\t" \
    "ds_read_b64_tr_b16 v[196:197], v205 offset:192\n\t" \
    "v_mfma_f32_16x16x32_bf16 v[68:71], v[144:147], v[116:119], v[68:71]\n\t" \
    "ds_read_b64_tr_b16 v[198:199], v205 offset:1984\n\t" \
    "v_mfma_f32_16x16x32_bf16 v[72:75], v[144:147], v[120:123], v[72:75]\n\t" \
    "v_mfma_f32_16x16x32_bf16 v[76:79], v[144:147], v[124:127], v[76:79]\n\t" \
    "s_add_u32 %[aslot], %[aslot], 16384\n\t" \
    "v_mfma_f32_16x16x32_bf16 v[80:83], v[148:151], v[112:115], v[80:83]\n\t" \
    "s_and_b32 %[aslot], %[aslot], 0xffff\n\t" \
    "v_mfma_f32_16x16x32_bf16 v[84:87], v[148:151], v[116:119], v[84:87]\n\t" \
    "s_add_u32 %[bslot], %[bslot], 14336\n\t" \
    "v_mfma_f32_16x16x32_bf16 v[88:91], v[148:151], v[120:123], v[88:91]\n\t" \
    "s_cmp_eq_u32 %[bslot], 86016\n\t" \
    "v_mfma_f32_16x16x32_bf16 v[92:95], v[148:151], v[124:127], v[92:95]\n\t" \
    "s_cselect_b32 %[bslot], 0, %[bslot]\n\t" \
    "v_mfma_f32_16x16x32_bf16 v[96:99], v[152:155], v[112:115], v[96:99]\n\t" \
    "v_add_u32 v207, %[aslot], v200\n\t" \
    "v_mfma_f32_16x16x32_bf16 v[100:103], v[152:155], v[116:119], v[100:103]\n\t" \
    "v_add_u32 v208, %[bslot], v202\n\t" \
    "v_mfma_f32_16x16x32_bf16 v[104:107], v[152:155], v[120:123], v[104:107]\n\t" \
    "v_add_u32 v209, %[bslot], v203\n\t" \
    "v_mfma_f32_16x16x32_bf16 v[108:111], v[152:155], v[124:127], v[108:111]\n\t" \
    "s_waitcnt lgkmcnt(0)\n\t" \
    "s_barrier\n\t" \
    "v_mfma_f32_16x16x32_bf16 v[0:3], v[172:175], v[156:159], v[0:3]\n\t" \
    "ds_read_b128 v[112:115], v207 offset:0\n\t" \
    "v_mfma_f32_16x16x32_bf16 v[4:7], v[172:175], v[160:163], v[4:7]\n\t" \
    "ds_read_b64_tr_b16 v[128:129], v208 offset:0\n\t" \
    "v_mfma_f32_16x16x32_bf16 v[8:11], v[172:175], v[164:167], v[8:11]\n\t" \
    "ds_read_b64_tr_b16 v[130:131], v208 offset:1792\n\t" \
    "v_mfma_f32_16x16x32_bf16 v[12:15], v[172:175], v[168:171], v[12:15]\n\t" \
    "ds_read_b128 v[116:119], v207 offset:2048\n\t" \
    "v_mfma_f32_16x16x32_bf16 v[16:19], v[176:179], v[156:159], v[16:19]\n\t" \
    "ds_read_b128 v[120:123], v207 offset:4096\n\t" \
    "v_mfma_f32_16x16x32_bf16 v[20:23], v[176:179], v[160:163], v[20:23]\n\t" \
    "ds_read_b128 v[124:127], v207 offset:6144\n\t" \
    "v_mfma_f32_16x16x32_bf16 v[24:27], v[176:179], v[164:167], v[24:27]\n\t" \
    "ds_read_b64_tr_b16 v[132:133], v209 offset:32\n\t" \
    "v_mfma_f32_16x16x32_bf16 v[28:31], v[176:179], v[168:171], v[28:31]\n\t" \
    "ds_read_b64_tr_b16 v[134:135], v209 offset:1824\n\t" \
    "v_mfma_f32_16x16x32_bf16 v[32:35], v[180:183], v[156:159], v[32:35]\n\t" \
    "ds_read_b64_tr_b16 v[136:137], v208 offset:64\n\t" \
    "v_mfma_f32_16x16x32_bf16 v[36:39], v[180:183], v[160:163], v[36:39]\n\t" \
    "ds_read_b64_tr_b16 v[138:139], v208 offset:1856\n\t" \
    "v_mfma_f32_16x16x32_bf16 v[40:43], v[180:183], v[164:167], v[40:43]\n\t" \
    "ds_read_b64_tr_b16 v[140:141], v209 offset:96\n\t" \
    "v_mfma_f32_16x16x32_bf16 v[44:47], v[180:183], v[168:171], v[44:47]\n\t" \
    "ds_read_b64_tr_b16 v[142:143], v209 offset:1888\n\t" \
    "v_mfma_f32_16x16x32_bf16 v[48:51], v[184:187], v[156:159], v[48:51]\n\t" \
    "ds_read_b64_tr_b16 v[144:145], v208 offset:128\n\t" \
    "v_mfma_f32_16x16x32_bf16 v[52:55], v[184:187], v[160:163], v[52:55]\n\t" \
    "ds_read_b64_tr_b16 v[146:147], v208 offset:1920\n\t" \
    "v_mfma_f32_16x16x32_bf16 v[56:59], v[184:187], v[164:167], v[56:59]\n\t" \
    "ds_read_b64_tr_b16 v[148:149], v209 offset:160\n\t" \
    "v_mfma_f32_16x16x32_bf16 v[60:63], v[184:187], v[168:171], v[60:63]\n\t" \
    "ds_read_b64_tr_b16 v[150:151], v209 offset:1952\n\t" \
    "v_mfma_f32_16x16x32_bf16 v[64:67], v[188:191], v[156:159], v[64:67]\n\t" \
    "ds_read_b64_tr_b16 v[152:153], v208 offset:192\n\t" \
    "v_mfma_f32_16x16x32_bf16 v[68:71], v[188:191], v[160:163], v[68:71]\n\t" \
    "ds_read_b64_tr_b16 v[154:155], v208 offset:1984\n\t" \
    "v_mfma_f32_16x16x32_bf16 v[72:75], v[188:191], v[164:167], v[72:75]\n\t" \
    "v_mfma_f32_16x16x32_bf16 v[76:79], v[188:191], v[168:171], v[76:79]\n\t" \
    "s_add_u32 %[bslot], %[bslot], 14336\n\t" \
    "v_mfma_f32_16x16x32_bf16 v[80:83], v[192:195], v[156:159], v[80:83]\n\t" \
    "s_cmp_eq_u32 %[bslot], 86016\n\t" \
    "v_mfma_f32_16x16x32_bf16 v[84:87], v[192:195], v[160:163], v[84:87]\n\t" \
    "s_cselect_b32 %[bslot], 0, %[bslot]\n\t" \
    "v_mfma_f32_16x16x32_bf16 v[88:91], v[192:195], v[164:167], v[88:91]\n\t" \
    "v_add_u32 v204, %[aslot], v201\n\t" \
    "v_mfma_f32_16x16x32_bf16 v[92:95], v[192:195], v[168:171], v[92:95]\n\t" \
    "v_add_u32 v205, %[bslot], v202\n\t" \
    "v_mfma_f32_16x16x32_bf16 v[96:99], v[196:199], v[156:159], v[96:99]\n\t" \
    "v_add_u32 v206, %[bslot], v203\n\t" \
    "v_mfma_f32_16x16x32_bf16 v[100:103], v[196:199], v[160:163], v[100:103]\n\t" \
    "s_sub_u32 %[cnt], %[cnt], 1\n\t" \
    "v_mfma_f32_16x16x32_bf16 v[104:107], v[196:199], v[164:167], v[104:107]\n\t" \
    "s_cmp_lg_u32 %[cnt], 0\n\t" \
    "v_mfma_f32_16x16x32_bf16 v[108:111], v[196:199], v[168:171], v[108:111]\n\t" \
    "s_waitcnt lgkmcnt(0)\n\t" \
    "s_barrier\n\t" \
    "s_cbranch_scc1 1b\n\t" \
    "v_mfma_f32_16x16x32_bf16 v[0:3], v[128:131], v[112:115], v[0:3]\n\t" \
    "ds_read_b128 v[156:159], v204 offset:0\n\t" \
    "v_mfma_f32_16x16x32_bf16 v[4:7], v[128:131], v[116:119], v[4:7]\n\t" \
    "ds_read_b64_tr_b16 v[172:173], v205 offset:0\n\t" \
    "v_mfma_f32_16x16x32_bf16 v[8:11], v[128:131], v[120:123], v[8:11]\n\t" \
    "ds_read_b64_tr_b16 v[174:175], v205 offset:1792\n\t" \
    "v_mfma_f32_16x16x32_bf16 v[12:15], v[128:131], v[124:127], v[12:15]\n\t" \
    "ds_read_b128 v[160:163], v204 offset:2048\n\t" \
    "v_mfma_f32_16x16x32_bf16 v[16:19], v[132:135], v[112:115], v[16:19]\n\t" \
    "ds_read_b128 v[164:167], v204 offset:4096\n\t" \
    "v_mfma_f32_16x16x32_bf16 v[20:23], v[132:135], v[116:119], v[20:23]\n\t" \
    "ds_read_b128 v[168:171], v204 offset:6144\n\t" \
    "v_mfma_f32_16x16x32_bf16 v[24:27], v[132:135], v[120:123], v[24:27]\n\t" \
    "ds_read_b64_tr_b16 v[176:177], v206 offset:32\n\t" \
    "v_mfma_f32_16x16x32_bf16 v[28:31], v[132:135], v[124:127], v[28:31]\n\t" \
    "ds_read_b64_tr_b16 v[178:179], v206 offset:1824\n\t" \
    "v_mfma_f32_16x16x32_bf16 v[32:35], v[136:139], v[112:115], v[32:35]\n\t" \
    "ds_read_b64_tr_b16 v[180:181], v205 offset:64\n\t" \
    "v_mfma_f32_16x16x32_bf16 v[36:39], v[136:139], v[116:119], v[36:39]\n\t" \
    "ds_read_b64_tr_b16 v[182:183], v205 offset:1856\n\t" \
    "v_mfma_f32_16x16x32_bf16 v[40:43], v[136:139], v[120:123], v[40:43]\n\t" \
    "ds_read_b64_tr_b16 v[184:185], v206 offset:96\n\t" \
    "v_mfma_f32_16x16x32_bf16 v[44:47], v[136:139], v[124:127], v[44:47]\n\t" \
    "ds_read_b64_tr_b16 v[186:187], v206 offset:1888\n\t" \
    "v_mfma_f32_16x16x32_bf16 v[48:51], v[140:143], v[112:115], v[48:51]\n\t" \
    "ds_read_b64_tr_b16 v[188:189], v205 offset:128\n\t" \
    "v_mfma_f32_16x16x32_bf16 v[52:55], v[140:143], v[116:119], v[52:55]\n\t" \
    "ds_read_b64_tr_b16 v[190:191], v205 offset:1920\n\t" \
    "v_mfma_f32_16x16x32_bf16 v[56:59], v[140:143], v[120:123], v[56:59]\n\t" \
    "ds_read_b64_tr_b16 v[192:193], v206 offset:160\n\t" \
    "v_mfma_f32_16x16x32_bf16 v[60:63], v[140:143], v[124:127], v[60:63]\n\t" \
    "ds_read_b64_tr_b16 v[194:195], v206 offset:1952\n\t" \
    "v_mfma_f32_16x16x32_bf16 v[64:67], v[144:147], v[112:115], v[64:67]\n\t" \
    "ds_read_b64_tr_b16 v[196:197], v205 offset:192\n\t" \
    "v_mfma_f32_16x16x32_bf16 v[68:71], v[144:147], v[116:119], v[68:71]\n\t" \
    "ds_read_b64_tr_b16 v[198:199], v205 offset:1984\n\t" \
    "v_mfma_f32_16x16x32_bf16 v[72:75], v[144:147], v[120:123], v[72:75]\n\t" \
    "v_mfma_f32_16x16x32_bf16 v[76:79], v[144:147], v[124:127], v[76:79]\n\t" \
    "v_mfma_f32_16x16x32_bf16 v[80:83], v[148:151], v[112:115], v[80:83]\n\t" \
    "v_mfma_f32_16x16x32_bf16 v[84:87], v[148:151], v[116:119], v[84:87]\n\t" \
    "v_mfma_f32_16x16x32_bf16 v[88:91], v[148:151], v[120:123], v[88:91]\n\t" \
    "v_mfma_f32_16x16x32_bf16 v[92:95], v[148:151], v[124:127], v[92:95]\n\t" \
    "v_mfma_f32_16x16x32_bf16 v[96:99], v[152:155], v[112:115], v[96:99]\n\t" \
    "v_mfma_f32_16x16x32_bf16 v[100:103], v[152:155], v[116:119], v[100:103]\n\t" \
    "v_mfma_f32_16x16x32_bf16 v[104:107], v[152:155], v[120:123], v[104:107]\n\t" \
    "v_mfma_f32_16x16x32_bf16 v[108:111], v[152:155], v[124:127], v[108:111]\n\t" \
    "s_waitcnt lgkmcnt(0)\n\t" \
    "s_barrier\n\t" \
    "v_mfma_f32_16x16x32_bf16 v[0:3], v[172:175], v[156:159], v[0:3]\n\t" \
    "v_mfma_f32_16x16x32_bf16 v[4:7], v[172:175], v[160:163], v[4:7]\n\t" \
    "v_mfma_f32_16x16x32_bf16 v[8:11], v[172:175], v[164:167], v[8:11]\n\t" \
    "v_mfma_f32_16x16x32_bf16 v[12:15], v[172:175], v[168:171], v[12:15]\n\t" \
    "v_mfma_f32_16x16x32_bf16 v[16:19], v[176:179], v[156:159], v[16:19]\n\t" \
    "v_mfma_f32_16x16x32_bf16 v[20:23], v[176:179], v[160:163], v[20:23]\n\t" \
    "v_mfma_f32_16x16x32_bf16 v[24:27], v[176:179], v[164:167], v[24:27]\n\t" \
    "v_mfma_f32_16x16x32_bf16 v[28:31], v[176:179], v[168:171], v[28:31]\n\t" \
    "v_mfma_f32_16x16x32_bf16 v[32:35], v[180:183], v[156:159], v[32:35]\n\t" \
    "v_mfma_f32_16x16x32_bf16 v[36:39], v[180:183], v[160:163], v[36:39]\n\t" \
    "v_mfma_f32_16x16x32_bf16 v[40:43], v[180:183], v[164:167], v[40:43]\n\t" \
    "v_mfma_f32_16x16x32_bf16 v[44:47], v[180:183], v[168:171], v[44:47]\n\t" \
    "v_mfma_f32_16x16x32_bf16 v[48:51], v[184:187], v[156:159], v[48:51]\n\t" \
    "v_mfma_f32_16x16x32_bf16 v[52:55], v[184:187], v[160:163], v[52:55]\n\t" \
    "v_mfma_f32_16x16x32_bf16 v[56:59], v[184:187], v[164:167], v[56:59]\n\t" \
    "v_mfma_f32_16x16x32_bf16 v[60:63], v[184:187], v[168:171], v[60:63]\n\t" \
    "v_mfma_f32_16x16x32_bf16 v[64:67], v[188:191], v[156:159], v[64:67]\n\t" \
    "v_mfma_f32_16x16x32_bf16 v[68:71], v[188:191], v[160:163], v[68:71]\n\t" \
    "v_mfma_f32_16x16x32_bf16 v[72:75], v[188:191], v[164:167], v[72:75]\n\t" \
    "v_mfma_f32_16x16x32_bf16 v[76:79], v[188:191], v[168:171], v[76:79]\n\t" \
    "v_mfma_f32_16x16x32_bf16 v[80:83], v[192:195], v[156:159], v[80:83]\n\t" \
    "v_mfma_f32_16x16x32_bf16 v[84:87], v[192:195], v[160:163], v[84:87]\n\t" \
    "v_mfma_f32_16x16x32_bf16 v[88:91], v[192:195], v[164:167], v[88:91]\n\t" \
    "v_mfma_f32_16x16x32_bf16 v[92:95], v[192:195], v[168:171], v[92:95]\n\t" \
    "v_mfma_f32_16x16x32_bf16 v[96:99], v[196:199], v[156:159], v[96:99]\n\t" \
    "v_mfma_f32_16x16x32_bf16 v[100:103], v[196:199], v[160:163], v[100:103]\n\t" \
    "v_mfma_f32_16x16x32_bf16 v[104:107], v[196:199], v[164:167], v[104:107]\n\t" \
    "v_mfma_f32_16x16x32_bf16 v[108:111], v[196:199], v[168:171], v[108:111]\n\t" \
    "s_waitcnt lgkmcnt(0)\n\t" \
    "s_barrier\n\t" \
    "s_nop 7\n\t" \
    "s_nop 7\n\t" \
    ""
#define G224_BODY_SWAP \
    "v_mov_b32 v0, 0\n\t" \
    "v_mov_b32 v1, 0\n\t" \
    "v_mov_b32 v2, 0\n\t" \
    "v_mov_b32 v3, 0\n\t" \
    "v_mov_b32 v4, 0\n\t" \
    "v_mov_b32 v5, 0\n\t" \
    "v_mov_b32 v6, 0\n\t" \
    "v_mov_b32 v7, 0\n\t" \
    "v_mov_b32 v8, 0\n\t" \
    "v_mov_b32 v9, 0\n\t" \
    "v_mov_b32 v10, 0\n\t" \
    "v_mov_b32 v11, 0\n\t" \
    "v_mov_b32 v12, 0\n\t" \
    "v_mov_b32 v13, 0\n\t" \
    "v_mov_b32 v14, 0\n\t" \
    "v_mov_b32 v15, 0\n\t" \
    "v_mov_b32 v16, 0\n\t" \
    "v_mov_b32 v17, 0\n\t" \
    "v_mov_b32 v18, 0\n\t" \
    "v_mov_b32 v19, 0\n\t" \
    "v_mov_b32 v20, 0\n\t" \
    "v_mov_b32 v21, 0\n\t" \
    "v_mov_b32 v22, 0\n\t" \
    "v_mov_b32 v23, 0\n\t" \
    "v_mov_b32 v24, 0\n\t" \
    "v_mov_b32 v25, 0\n\t" \
    "v_mov_b32 v26, 0\n\t" \
    "v_mov_b32 v27, 0\n\t" \
    "v_mov_b32 v28, 0\n\t" \
    "v_mov_b32 v29, 0\n\t" \
    "v_mov_b32 v30, 0\n\t" \
    "v_mov_b32 v31, 0\n\t" \
    "v_mov_b32 v32, 0\n\t" \
    "v_mov_b32 v33, 0\n\t" \
    "v_mov_b32 v34, 0\n\t" \
    "v_mov_b32 v35, 0\n\t" \
    "v_mov_b32 v36, 0\n\t" \
    "v_mov_b32 v37, 0\n\t" \
    "v_mov_b32 v38, 0\n\t" \
    "v_mov_b32 v39, 0\n\t" \
    "v_mov_b32 v40, 0\n\t" \
    "v_mov_b32 v41, 0\n\t" \
    "v_mov_b32 v42, 0\n\t" \
    "v_mov_b32 v43, 0\n\t" \
    "v_mov_b32 v44, 0\n\t" \
    "v_mov_b32 v45, 0\n\t" \
    "v_mov_b32 v46, 0\n\t" \
    "v_mov_b32 v47, 0\n\t" \
    "v_mov_b32 v48, 0\n\t" \
    "v_mov_b32 v49, 0\n\t" \
    "v_mov_b32 v50, 0\n\t" \
    "v_mov_b32 v51, 0\n\t" \
    "v_mov_b32 v52, 0\n\t" \
    "v_mov_b32 v53, 0\n\t" \
    "v_mov_b32 v54, 0\n\t" \
    "v_mov_b32 v55, 0\n\t" \
    "v_mov_b32 v56, 0\n\t" \
    "v_mov_b32 v57, 0\n\t" \
    "v_mov_b32 v58, 0\n\t" \
    "v_mov_b32 v59, 0\n\t" \
    "v_mov_b32 v60, 0\n\t" \
    "v_mov_b32 v61, 0\n\t" \
    "v_mov_b32 v62, 0\n\t" \
    "v_mov_b32 v63, 0\n\t" \
    "v_mov_b32 v64, 0\n\t" \
    "v_mov_b32 v65, 0\n\t" \
    "v_mov_b32 v66, 0\n\t" \
    "v_mov_b32 v67, 0\n\t" \
    "v_mov_b32 v68, 0\n\t" \
    "v_mov_b32 v69, 0\n\t" \
    "v_mov_b32 v70, 0\n\t" \
    "v_mov_b32 v71, 0\n\t" \
    "v_mov_b32 v72, 0\n\t" \
    "v_mov_b32 v73, 0\n\t" \
    "v_mov_b32 v74, 0\n\t" \
    "v_mov_b32 v75, 0\n\t" \
    "v_mov_b32 v76, 0\n\t" \
    "v_mov_b32 v77, 0\n\t" \
    "v_mov_b32 v78, 0\n\t" \
    "v_mov_b32 v79, 0\n\t" \
    "v_mov_b32 v80, 0\n\t" \
    "v_mov_b32 v81, 0\n\t" \
    "v_mov_b32 v82, 0\n\t" \
    "v_mov_b32 v83, 0\n\t" \
    "v_mov_b32 v84, 0\n\t" \
    "v_mov_b32 v85, 0\n\t" \
    "v_mov_b32 v86, 0\n\t" \
    "v_mov_b32 v87, 0\n\t" \
    "v_mov_b32 v88, 0\n\t" \
    "v_mov_b32 v89, 0\n\t" \
    "v_mov_b32 v90, 0\n\t" \
    "v_mov_b32 v91, 0\n\t" \
    "v_mov_b32 v92, 0\n\t" \
    "v_mov_b32 v93, 0\n\t" \
    "v_mov_b32 v94, 0\n\t" \
    "v_mov_b32 v95, 0\n\t" \
    "v_mov_b32 v96, 0\n\t" \
    "v_mov_b32 v97, 0\n\t" \
    "v_mov_b32 v98, 0\n\t" \
    "v_mov_b32 v99, 0\n\t" \
    "v_mov_b32 v100, 0\n\t" \
    "v_mov_b32 v101, 0\n\t" \
    "v_mov_b32 v102, 0\n\t" \
    "v_mov_b32 v103, 0\n\t" \
    "v_mov_b32 v104, 0\n\t" \
    "v_mov_b32 v105, 0\n\t" \
    "v_mov_b32 v106, 0\n\t" \
    "v_mov_b32 v107, 0\n\t" \
    "v_mov_b32 v108, 0\n\t" \
    "v_mov_b32 v109, 0\n\t" \
    "v_mov_b32 v110, 0\n\t" \
    "v_mov_b32 v111, 0\n\t" \
    "s_waitcnt lgkmcnt(0)\n\t" \
    "s_barrier\n\t" \
    "v_mov_b32 v207, v200\n\t" \
    "v_mov_b32 v208, v202\n\t" \
    "v_mov_b32 v209, v203\n\t" \
    "ds_read_b128 v[112:115], v207 offset:0\n\t" \
    "ds_read_b64_tr_b16 v[128:129], v208 offset:0\n\t" \
    "ds_read_b64_tr_b16 v[130:131], v208 offset:1792\n\t" \
    "ds_read_b128 v[116:119], v207 offset:2048\n\t" \
    "ds_read_b128 v[120:123], v207 offset:4096\n\t" \
    "ds_read_b128 v[124:127], v207 offset:6144\n\t" \
    "ds_read_b64_tr_b16 v[132:133], v209 offset:32\n\t" \
    "ds_read_b64_tr_b16 v[134:135], v209 offset:1824\n\t" \
    "ds_read_b64_tr_b16 v[136:137], v208 offset:64\n\t" \
    "ds_read_b64_tr_b16 v[138:139], v208 offset:1856\n\t" \
    "ds_read_b64_tr_b16 v[140:141], v209 offset:96\n\t" \
    "ds_read_b64_tr_b16 v[142:143], v209 offset:1888\n\t" \
    "ds_read_b64_tr_b16 v[144:145], v208 offset:128\n\t" \
    "ds_read_b64_tr_b16 v[146:147], v208 offset:1920\n\t" \
    "ds_read_b64_tr_b16 v[148:149], v209 offset:160\n\t" \
    "ds_read_b64_tr_b16 v[150:151], v209 offset:1952\n\t" \
    "ds_read_b64_tr_b16 v[152:153], v208 offset:192\n\t" \
    "ds_read_b64_tr_b16 v[154:155], v208 offset:1984\n\t" \
    "s_mov_b32 %[aslot], 0\n\t" \
    "s_mov_b32 %[bslot], 14336\n\t" \
    "v_mov_b32 v204, v201\n\t" \
    "v_add_u32 v205, %[bslot], v202\n\t" \
    "v_add_u32 v206, %[bslot], v203\n\t" \
    "s_waitcnt lgkmcnt(0)\n\t" \
    "s_barrier\n\t" \
    "1:\n\t" \
    "v_mfma_f32_16x16x32_bf16 v[0:3], v[112:115], v[128:131], v[0:3]\n\t" \
    "ds_read_b128 v[156:159], v204 offset:0\n\t" \
    "v_mfma_f32_16x16x32_bf16 v[4:7], v[116:119], v[128:131], v[4:7]\n\t" \
    "ds_read_b64_tr_b16 v[172:173], v205 offset:0\n\t" \
    "v_mfma_f32_16x16x32_bf16 v[8:11], v[120:123], v[128:131], v[8:11]\n\t" \
    "ds_read_b64_tr_b16 v[174:175], v205 offset:1792\n\t" \
    "v_mfma_f32_16x16x32_bf16 v[12:15], v[124:127], v[128:131], v[12:15]\n\t" \
    "ds_read_b128 v[160:163], v204 offset:2048\n\t" \
    "v_mfma_f32_16x16x32_bf16 v[16:19], v[112:115], v[132:135], v[16:19]\n\t" \
    "ds_read_b128 v[164:167], v204 offset:4096\n\t" \
    "v_mfma_f32_16x16x32_bf16 v[20:23], v[116:119], v[132:135], v[20:23]\n\t" \
    "ds_read_b128 v[168:171], v204 offset:6144\n\t" \
    "v_mfma_f32_16x16x32_bf16 v[24:27], v[120:123], v[132:135], v[24:27]\n\t" \
    "ds_read_b64_tr_b16 v[176:177], v206 offset:32\n\t" \
    "v_mfma_f32_16x16x32_bf16 v[28:31], v[124:127], v[132:135], v[28:31]\n\t" \
    "ds_read_b64_tr_b16 v[178:179], v206 offset:1824\n\t" \
    "v_mfma_f32_16x16x32_bf16 v[32:35], v[112:115], v[136:139], v[32:35]\n\t" \
    "ds_read_b64_tr_b16 v[180:181], v205 offset:64\n\t" \
    "v_mfma_f32_16x16x32_bf16 v[36:39], v[116:119], v[136:139], v[36:39]\n\t" \
    "ds_read_b64_tr_b16 v[182:183], v205 offset:1856\n\t" \
    "v_mfma_f32_16x16x32_bf16 v[40:43], v[120:123], v[136:139], v[40:43]\n\t" \
    "ds_read_b64_tr_b16 v[184:185], v206 offset:96\n\t" \
    "v_mfma_f32_16x16x32_bf16 v[44:47], v[124:127], v[136:139], v[44:47]\n\t" \
    "ds_read_b64_tr_b16 v[186:187], v206 offset:1888\n\t" \
    "v_mfma_f32_16x16x32_bf16 v[48:51], v[112:115], v[140:143], v[48:51]\n\t" \
    "ds_read_b64_tr_b16 v[188:189], v205 offset:128\n\t" \
    "v_mfma_f32_16x16x32_bf16 v[52:55], v[116:119], v[140:143], v[52:55]\n\t" \
    "ds_read_b64_tr_b16 v[190:191], v205 offset:1920\n\t" \
    "v_mfma_f32_16x16x32_bf16 v[56:59], v[120:123], v[140:143], v[56:59]\n\t" \
    "ds_read_b64_tr_b16 v[192:193], v206 offset:160\n\t" \
    "v_mfma_f32_16x16x32_bf16 v[60:63], v[124:127], v[140:143], v[60:63]\n\t" \
    "ds_read_b64_tr_b16 v[194:195], v206 offset:1952\n\t" \
    "v_mfma_f32_16x16x32_bf16 v[64:67], v[112:115], v[144:147], v[64:67]\n\t" \
    "ds_read_b64_tr_b16 v[196:197], v205 offset:192\n\t" \
    "v_mfma_f32_16x16x32_bf16 v[68:71], v[116:119], v[144:147], v[68:71]\n\t" \
    "ds_read_b64_tr_b16 v[198:199], v205 offset:1984\n\t" \
    "v_mfma_f32_16x16x32_bf16 v[72:75], v[120:123], v[144:147], v[72:75]\n\t" \
    "v_mfma_f32_16x16x32_bf16 v[76:79], v[124:127], v[144:147], v[76:79]\n\t" \
    "s_add_u32 %[aslot], %[aslot], 16384\n\t" \
    "v_mfma_f32_16x16x32_bf16 v[80:83], v[112:115], v[148:151], v[80:83]\n\t" \
    "s_and_b32 %[aslot], %[aslot], 0xffff\n\t" \
    "v_mfma_f32_16x16x32_bf16 v[84:87], v[116:119], v[148:151], v[84:87]\n\t" \
    "s_add_u32 %[bslot], %[bslot], 14336\n\t" \
    "v_mfma_f32_16x16x32_bf16 v[88:91], v[120:123], v[148:151], v[88:91]\n\t" \
    "s_cmp_eq_u32 %[bslot], 86016\n\t" \
    "v_mfma_f32_16x16x32_bf16 v[92:95], v[124:127], v[148:151], v[92:95]\n\t" \
    "s_cselect_b32 %[bslot], 0, %[bslot]\n\t" \
    "v_mfma_f32_16x16x32_bf16 v[96:99], v[112:115], v[152:155], v[96:99]\n\t" \
    "v_add_u32 v207, %[aslot], v200\n\t" \
    "v_mfma_f32_16x16x32_bf16 v[100:103], v[116:119], v[152:155], v[100:103]\n\t" \
    "v_add_u32 v208, %[bslot], v202\n\t" \
    "v_mfma_f32_16x16x32_bf16 v[104:107], v[120:123], v[152:155], v[104:107]\n\t" \
    "v_add_u32 v209, %[bslot], v203\n\t" \
    "v_mfma_f32_16x16x32_bf16 v[108:111], v[124:127], v[152:155], v[108:111]\n\t" \
    "s_waitcnt lgkmcnt(0)\n\t" \
    "s_barrier\n\t" \
    "v_mfma_f32_16x16x32_bf16 v[0:3], v[156:159], v[172:175], v[0:3]\n\t" \
    "ds_read_b128 v[112:115], v207 offset:0\n\t" \
    "v_mfma_f32_16x16x32_bf16 v[4:7], v[160:163], v[172:175], v[4:7]\n\t" \
    "ds_read_b64_tr_b16 v[128:129], v208 offset:0\n\t" \
    "v_mfma_f32_16x16x32_bf16 v[8:11], v[164:167], v[172:175], v[8:11]\n\t" \
    "ds_read_b64_tr_b16 v[130:131], v208 offset:1792\n\t" \
    "v_mfma_f32_16x16x32_bf16 v[12:15], v[168:171], v[172:175], v[12:15]\n\t" \
    "ds_read_b128 v[116:119], v207 offset:2048\n\t" \
    "v_mfma_f32_16x16x32_bf16 v[16:19], v[156:159], v[176:179], v[16:19]\n\t" \
    "ds_read_b128 v[120:123], v207 offset:4096\n\t" \
    "v_mfma_f32_16x16x32_bf16 v[20:23], v[160:163], v[176:179], v[20:23]\n\t" \
    "ds_read_b128 v[124:127], v207 offset:6144\n\t" \
    "v_mfma_f32_16x16x32_bf16 v[24:27], v[164:167], v[176:179], v[24:27]\n\t" \
    "ds_read_b64_tr_b16 v[132:133], v209 offset:32\n\t" \
    "v_mfma_f32_16x16x32_bf16 v[28:31], v[168:171], v[176:179], v[28:31]\n\t" \
    "ds_read_b64_tr_b16 v[134:135], v209 offset:1824\n\t" \
    "v_mfma_f32_16x16x32_bf16 v[32:35], v[156:159], v[180:183], v[32:35]\n\t" \
    "ds_read_b64_tr_b16 v[136:137], v208 offset:64\n\t" \
    "v_mfma_f32_16x16x32_bf16 v[36:39], v[160:163], v[180:183], v[36:39]\n\t" \
    "ds_read_b64_tr_b16 v[138:139], v208 offset:1856\n\t" \
    "v_mfma_f32_16x16x32_bf16 v[40:43], v[164:167], v[180:183], v[40:43]\n\t" \
    "ds_read_b64_tr_b16 v[140:141], v209 offset:96\n\t" \
    "v_mfma_f32_16x16x32_bf16 v[44:47], v[168:171], v[180:183], v[44:47]\n\t" \
    "ds_read_b64_tr_b16 v[142:143], v209 offset:1888\n\t" \
    "v_mfma_f32_16x16x32_bf16 v[48:51], v[156:159], v[184:187], v[48:51]\n\t" \
    "ds_read_b64_tr_b16 v[144:145], v208 offset:128\n\t" \
    "v_mfma_f32_16x16x32_bf16 v[52:55], v[160:163], v[184:187], v[52:55]\n\t" \
    "ds_read_b64_tr_b16 v[146:147], v208 offset:1920\n\t" \
    "v_mfma_f32_16x16x32_bf16 v[56:59], v[164:167], v[184:187], v[56:59]\n\t" \
    "ds_read_b64_tr_b16 v[148:149], v209 offset:160\n\t" \
    "v_mfma_f32_16x16x32_bf16 v[60:63], v[168:171], v[184:187], v[60:63]\n\t" \
    "ds_read_b64_tr_b16 v[150:151], v209 offset:1952\n\t" \
    "v_mfma_f32_16x16x32_bf16 v[64:67], v[156:159], v[188:191], v[64:67]\n\t" \
    "ds_read_b64_tr_b16 v[152:153], v208 offset:192\n\t" \
    "v_mfma_f32_16x16x32_bf16 v[68:71], v[160:163], v[188:191], v[68:71]\n\t" \
    "ds_read_b64_tr_b16 v[154:155], v208 offset:1984\n\t" \
    "v_mfma_f32_16x16x32_bf16 v[72:75], v[164:167], v[188:191], v[72:75]\n\t" \
    "v_mfma_f32_16x16x32_bf16 v[76:79], v[168:171], v[188:191], v[76:79]\n\t" \
    "s_add_u32 %[bslot], %[bslot], 14336\n\t" \
    "v_mfma_f32_16x16x32_bf16 v[80:83], v[156:159], v[192:195], v[80:83]\n\t" \
    "s_cmp_eq_u32 %[bslot], 86016\n\t" \
    "v_mfma_f32_16x16x32_bf16 v[84:87], v[160:163], v[192:195], v[84:87]\n\t" \
    "s_cselect_b32 %[bslot], 0, %[bslot]\n\t" \
    "v_mfma_f32_16x16x32_bf16 v[88:91], v[164:167], v[192:195], v[88:91]\n\t" \
    "v_add_u32 v204, %[aslot], v201\n\t" \
    "v_mfma_f32_16x16x32_bf16 v[92:95], v[168:171], v[192:195], v[92:95]\n\t" \
    "v_add_u32 v205, %[bslot], v202\n\t" \
    "v_mfma_f32_16x16x32_bf16 v[96:99], v[156:159], v[196:199], v[96:99]\n\t" \
    "v_add_u32 v206, %[bslot], v203\n\t" \
    "v_mfma_f32_16x16x32_bf16 v[100:103], v[160:163], v[196:199], v[100:103]\n\t" \
    "s_sub_u32 %[cnt], %[cnt], 1\n\t" \
    "v_mfma_f32_16x16x32_bf16 v[104:107], v[164:167], v[196:199], v[104:107]\n\t" \
    "s_cmp_lg_u32 %[cnt], 0\n\t" \
    "v_mfma_f32_16x16x32_bf16 v[108:111], v[168:171], v[196:199], v[108:111]\n\t" \
    "s_waitcnt lgkmcnt(0)\n\t" \
    "s_barrier\n\t" \
    "s_cbranch_scc1 1b\n\t" \
    "v_mfma_f32_16x16x32_bf16 v[0:3], v[112:115], v[128:131], v[0:3]\n\t" \
    "ds_read_b128 v[156:159], v204 offset:0\n\t" \
    "v_mfma_f32_16x16x32_bf16 v[4:7], v[116:119], v[128:131], v[4:7]\n\t" \
    "ds_read_b64_tr_b16 v[172:173], v205 offset:0\n\t" \
    "v_mfma_f32_16x16x32_bf16 v[8:11], v[120:123], v[128:131], v[8:11]\n\t" \
    "ds_read_b64_tr_b16 v[174:175], v205 offset:1792\n\t" \
    "v_mfma_f32_16x16x32_bf16 v[12:15], v[124:127], v[128:131], v[12:15]\n\t" \
    "ds_read_b128 v[160:163], v204 offset:2048\n\t" \
    "v_mfma_f32_16x16x32_bf16 v[16:19], v[112:115], v[132:135], v[16:19]\n\t" \
    "ds_read_b128 v[164:167], v204 offset:4096\n\t" \
    "v_mfma_f32_16x16x32_bf16 v[20:23], v[116:119], v[132:135], v[20:23]\n\t" \
    "ds_read_b128 v[168:171], v204 offset:6144\n\t" \
    "v_mfma_f32_16x16x32_bf16 v[24:27], v[120:123], v[132:135], v[24:27]\n\t" \
    "ds_read_b64_tr_b16 v[176:177], v206 offset:32\n\t" \
    "v_mfma_f32_16x16x32_bf16 v[28:31], v[124:127], v[132:135], v[28:31]\n\t" \
    "ds_read_b64_tr_b16 v[178:179], v206 offset:1824\n\t" \
    "v_mfma_f32_16x16x32_bf16 v[32:35], v[112:115], v[136:139], v[32:35]\n\t" \
    "ds_read_b64_tr_b16 v[180:181], v205 offset:64\n\t" \
    "v_mfma_f32_16x16x32_bf16 v[36:39], v[116:119], v[136:139], v[36:39]\n\t" \
    "ds_read_b64_tr_b16 v[182:183], v205 offset:1856\n\t" \
    "v_mfma_f32_16x16x32_bf16 v[40:43], v[120:123], v[136:139], v[40:43]\n\t" \
    "ds_read_b64_tr_b16 v[184:185], v206 offset:96\n\t" \
    "v_mfma_f32_16x16x32_bf16 v[44:47], v[124:127], v[136:139], v[44:47]\n\t" \
    "ds_read_b64_tr_b16 v[186:187], v206 offset:1888\n\t" \
    "v_mfma_f32_16x16x32_bf16 v[48:51], v[112:115], v[140:143], v[48:51]\n\t" \
    "ds_read_b64_tr_b16 v[188:189], v205 offset:128\n\t" \
    "v_mfma_f32_16x16x32_bf16 v[52:55], v[116:119], v[140:143], v[52:55]\n\t" \
    "ds_read_b64_tr_b16 v[190:191], v205 offset:1920\n\t" \
    "v_mfma_f32_16x16x32_bf16 v[56:59], v[120:123], v[140:143], v[56:59]\n\t" \
    "ds_read_b64_tr_b16 v[192:193], v206 offset:160\n\t" \
    "v_mfma_f32_16x16x32_bf16 v[60:63], v[124:127], v[140:143], v[60:63]\n\t" \
    "ds_read_b64_tr_b16 v[194:195], v206 offset:1952\n\t" \
    "v_mfma_f32_16x16x32_bf16 v[64:67], v[112:115], v[144:147], v[64:67]\n\t" \
    "ds_read_b64_tr_b16 v[196:197], v205 offset:192\n\t" \
    "v_mfma_f32_16x16x32_bf16 v[68:71], v[116:119], v[144:147], v[68:71]\n\t" \
    "ds_read_b64_tr_b16 v[198:199], v205 offset:1984\n\t" \
    "v_mfma_f32_16x16x32_bf16 v[72:75], v[120:123], v[144:147], v[72:75]\n\t" \
    "v_mfma_f32_16x16x32_bf16 v[76:79], v[124:127], v[144:147], v[76:79]\n\t" \
    "v_mfma_f32_16x16x32_bf16 v[80:83], v[112:115], v[148:151], v[80:83]\n\t" \
    "v_mfma_f32_16x16x32_bf16 v[84:87], v[116:119], v[148:151], v[84:87]\n\t" \
    "v_mfma_f32_16x16x32_bf16 v[88:91], v[120:123], v[148:151], v[88:91]\n\t" \
    "v_mfma_f32_16x16x32_bf16 v[92:95], v[124:127], v[148:151], v[92:95]\n\t" \
    "v_mfma_f32_16x16x32_bf16 v[96:99], v[112:115], v[152:155], v[96:99]\n\t" \
    "v_mfma_f32_16x16x32_bf16 v[100:103], v[116:119], v[152:155], v[100:103]\n\t" \
    "v_mfma_f32_16x16x32_bf16 v[104:107], v[120:123], v[152:155], v[104:107]\n\t" \
    "v_mfma_f32_16x16x32_bf16 v[108:111], v[124:127], v[152:155], v[108:111]\n\t" \
    "s_waitcnt lgkmcnt(0)\n\t" \
    "s_barrier\n\t" \
    "v_mfma_f32_16x16x32_bf16 v[0:3], v[156:159], v[172:175], v[0:3]\n\t" \
    "v_mfma_f32_16x16x32_bf16 v[4:7], v[160:163], v[172:175], v[4:7]\n\t" \
    "v_mfma_f32_16x16x32_bf16 v[8:11], v[164:167], v[172:175], v[8:11]\n\t" \
    "v_mfma_f32_16x16x32_bf16 v[12:15], v[168:171], v[172:175], v[12:15]\n\t" \
    "v_mfma_f32_16x16x32_bf16 v[16:19], v[156:159], v[176:179], v[16:19]\n\t" \
    "v_mfma_f32_16x16x32_bf16 v[20:23], v[160:163], v[176:179], v[20:23]\n\t" \
    "v_mfma_f32_16x16x32_bf16 v[24:27], v[164:167], v[176:179], v[24:27]\n\t" \
    "v_mfma_f32_16x16x32_bf16 v[28:31], v[168:171], v[176:179], v[28:31]\n\t" \
    "v_mfma_f32_16x16x32_bf16 v[32:35], v[156:159], v[180:183], v[32:35]\n\t" \
    "v_mfma_f32_16x16x32_bf16 v[36:39], v[160:163], v[180:183], v[36:39]\n\t" \
    "v_mfma_f32_16x16x32_bf16 v[40:43], v[164:167], v[180:183], v[40:43]\n\t" \
    "v_mfma_f32_16x16x32_bf16 v[44:47], v[168:171], v[180:183], v[44:47]\n\t" \
    "v_mfma_f32_16x16x32_bf16 v[48:51], v[156:159], v[184:187], v[48:51]\n\t" \
    "v_mfma_f32_16x16x32_bf16 v[52:55], v[160:163], v[184:187], v[52:55]\n\t" \
    "v_mfma_f32_16x16x32_bf16 v[56:59], v[164:167], v[184:187], v[56:59]\n\t" \
    "v_mfma_f32_16x16x32_bf16 v[60:63], v[168:171], v[184:187], v[60:63]\n\t" \
    "v_mfma_f32_16x16x32_bf16 v[64:67], v[156:159], v[188:191], v[64:67]\n\t" \
    "v_mfma_f32_16x16x32_bf16 v[68:71], v[160:163], v[188:191], v[68:71]\n\t" \
    "v_mfma_f32_16x16x32_bf16 v[72:75], v[164:167], v[188:191], v[72:75]\n\t" \
    "v_mfma_f32_16x16x32_bf16 v[76:79], v[168:171], v[188:191], v[76:79]\n\t" \
    "v_mfma_f32_16x16x32_bf16 v[80:83], v[156:159], v[192:195], v[80:83]\n\t" \
    "v_mfma_f32_16x16x32_bf16 v[84:87], v[160:163], v[192:195], v[84:87]\n\t" \
    "v_mfma_f32_16x16x32_bf16 v[88:91], v[164:167], v[192:195], v[88:91]\n\t" \
    "v_mfma_f32_16x16x32_bf16 v[92:95], v[168:171], v[192:195], v[92:95]\n\t" \
    "v_mfma_f32_16x16x32_bf16 v[96:99], v[156:159], v[196:199], v[96:99]\n\t" \
    "v_mfma_f32_16x16x32_bf16 v[100:103], v[160:163], v[196:199], v[100:103]\n\t" \
    "v_mfma_f32_16x16x32_bf16 v[104:107], v[164:167], v[196:199], v[104:107]\n\t" \
    "v_mfma_f32_16x16x32_bf16 v[108:111], v[168:171], v[196:199], v[108:111]\n\t" \
    "s_waitcnt lgkmcnt(0)\n\t" \
    "s_barrier\n\t" \
    "s_nop 7\n\t" \
    "s_nop 7\n\t" \
    ""
#define G224_CLOB "v112","v113","v114","v115","v116","v117","v118","v119","v120","v121","v122","v123","v124","v125","v126","v127","v128","v129","v130","v131","v132","v133","v134","v135","v136","v137","v138","v139","v140","v141","v142","v143","v144","v145","v146","v147","v148","v149","v150","v151","v152","v153","v154","v155","v156","v157","v158","v159","v160","v161","v162","v163","v164","v165","v166","v167","v168","v169","v170","v171","v172","v173","v174","v175","v176","v177","v178","v179","v180","v181","v182","v183","v184","v185","v186","v187","v188","v189","v190","v191","v192","v193","v194","v195","v196","v197","v198","v199","v204","v205","v206","v207","v208","v209", "scc", "memory"
#define LOADER53 \
    "s_add_u32 m0, s47, s53\n\t" \
    "s_nop 0\n\t" \
    "global_load_lds_dwordx4 v5, s[42:43] offset:0\n\t" \
    "global_load_lds_dwordx4 v6, s[42:43] offset:1024\n\t" \
    "global_load_lds_dwordx4 v7, s[42:43] offset:2048\n\t" \
    "s_mov_b32 m0, s46\n\t" \
    "s_add_u32 s46, s46, 14336\n\t" \
    "global_load_lds_dwordx4 v0, s[40:41] offset:-2048\n\t" \
    "global_load_lds_dwordx4 v1, s[40:41] offset:-1024\n\t" \
    "global_load_lds_dwordx4 v2, s[40:41] offset:0\n\t" \
    "global_load_lds_dwordx4 v3, s[40:41] offset:1024\n\t" \
    "global_load_lds_dwordx4 v4, s[40:41] offset:2048\n\t" \
    "s_cmp_eq_u32 s46, s52\n\t" \
    "s_cselect_b32 s46, s51, s46\n\t" \
    "s_add_u32 s40, s40, s49\n\t" \
    "s_addc_u32 s41, s41, 0\n\t" \
    "s_sub_u32 s54, s54, 1\n\t" \
    "s_cmp_eq_u32 s54, 0\n\t" \
    "s_cselect_b32 s50, s56, 0\n\t" \
    "s_sub_u32 s40, s40, s50\n\t" \
    "s_subb_u32 s41, s41, 0\n\t" \
    "s_add_u32 m0, s58, s53\n\t" \
    "s_nop 0\n\t" \
    "global_load_lds_dwordx4 v5, s[44:45] offset:0\n\t" \
    "global_load_lds_dwordx4 v6, s[44:45] offset:1024\n\t" \
    "global_load_lds_dwordx4 v7, s[44:45] offset:2048\n\t" \
    "s_add_u32 s53, s53, 16384\n\t" \
    "s_and_b32 s53, s53, 0xffff\n\t" \
    "s_add_u32 s42, s42, 128\n\t" \
    "s_addc_u32 s43, s43, 0\n\t" \
    "s_add_u32 s44, s44, 128\n\t" \
    "s_addc_u32 s45, s45, 0\n\t" \
    "s_sub_u32 s55, s55, 1\n\t" \
    "s_cmp_eq_u32 s55, 0\n\t" \
    "s_cselect_b32 s50, s57, 0\n\t" \
    "s_sub_u32 s42, s42, s50\n\t" \
    "s_subb_u32 s43, s43, 0\n\t" \
    "s_sub_u32 s44, s44, s50\n\t" \
    "s_subb_u32 s45, s45, 0\n\t" \
    "s_mov_b32 m0, s46\n\t" \
    "s_add_u32 s46, s46, 14336\n\t" \
    "global_load_lds_dwordx4 v0, s[40:41] offset:-2048\n\t" \
    "global_load_lds_dwordx4 v1, s[40:41] offset:-1024\n\t" \
    "global_load_lds_dwordx4 v2, s[40:41] offset:0\n\t" \
    "global_load_lds_dwordx4 v3, s[40:41] offset:1024\n\t" \
    "global_load_lds_dwordx4 v4, s[40:41] offset:2048\n\t" \
    "s_cmp_eq_u32 s46, s52\n\t" \
    "s_cselect_b32 s46, s51, s46\n\t" \
    "s_add_u32 s40, s40, s49\n\t" \
    "s_addc_u32 s41, s41, 0\n\t" \
    "s_sub_u32 s54, s54, 1\n\t" \
    "s_cmp_eq_u32 s54, 0\n\t" \
    "s_cselect_b32 s50, s56, 0\n\t" \
    "s_sub_u32 s40, s40, s50\n\t" \
    "s_subb_u32 s41, s41, 0\n\t" \
    "s_add_u32 m0, s47, s53\n\t" \
    "s_nop 0\n\t" \
    "global_load_lds_dwordx4 v5, s[42:43] offset:0\n\t" \
    "global_load_lds_dwordx4 v6, s[42:43] offset:1024\n\t" \
    "global_load_lds_dwordx4 v7, s[42:43] offset:2048\n\t" \
    "s_mov_b32 m0, s46\n\t" \
    "s_add_u32 s46, s46, 14336\n\t" \
    "global_load_lds_dwordx4 v0, s[40:41] offset:-2048\n\t" \
    "global_load_lds_dwordx4 v1, s[40:41] offset:-1024\n\t" \
    "global_load_lds_dwordx4 v2, s[40:41] offset:0\n\t" \
    "global_load_lds_dwordx4 v3, s[40:41] offset:1024\n\t" \
    "global_load_lds_dwordx4 v4, s[40:41] offset:2048\n\t" \
    "s_cmp_eq_u32 s46, s52\n\t" \
    "s_cselect_b32 s46, s51, s46\n\t" \
    "s_add_u32 s40, s40, s49\n\t" \
    "s_addc_u32 s41, s41, 0\n\t" \
    "s_sub_u32 s54, s54, 1\n\t" \
    "s_cmp_eq_u32 s54, 0\n\t" \
    "s_cselect_b32 s50, s56, 0\n\t" \
    "s_sub_u32 s40, s40, s50\n\t" \
    "s_subb_u32 s41, s41, 0\n\t" \
    "s_add_u32 m0, s58, s53\n\t" \
    "s_nop 0\n\t" \
    "global_load_lds_dwordx4 v5, s[44:45] offset:0\n\t" \
    "global_load_lds_dwordx4 v6, s[44:45] offset:1024\n\t" \
    "global_load_lds_dwordx4 v7, s[44:45] offset:2048\n\t" \
    "s_add_u32 s53, s53, 16384\n\t" \
    "s_and_b32 s53, s53, 0xffff\n\t" \
    "s_add_u32 s42, s42, 128\n\t" \
    "s_addc_u32 s43, s43, 0\n\t" \
    "s_add_u32 s44, s44, 128\n\t" \
    "s_addc_u32 s45, s45, 0\n\t" \
    "s_sub_u32 s55, s55, 1\n\t" \
    "s_cmp_eq_u32 s55, 0\n\t" \
    "s_cselect_b32 s50, s57, 0\n\t" \
    "s_sub_u32 s42, s42, s50\n\t" \
    "s_subb_u32 s43, s43, 0\n\t" \
    "s_sub_u32 s44, s44, s50\n\t" \
    "s_subb_u32 s45, s45, 0\n\t" \
    "s_waitcnt vmcnt(16)\n\t" \
    "s_barrier\n\t" \
    "s_waitcnt vmcnt(11)\n\t" \
    "s_barrier\n\t" \
    "s_mov_b32 m0, s46\n\t" \
    "s_add_u32 s46, s46, 14336\n\t" \
    "global_load_lds_dwordx4 v0, s[40:41] offset:-2048\n\t" \
    "global_load_lds_dwordx4 v1, s[40:41] offset:-1024\n\t" \
    "global_load_lds_dwordx4 v2, s[40:41] offset:0\n\t" \
    "global_load_lds_dwordx4 v3, s[40:41] offset:1024\n\t" \
    "global_load_lds_dwordx4 v4, s[40:41] offset:2048\n\t" \
    "s_cmp_eq_u32 s46, s52\n\t" \
    "s_cselect_b32 s46, s51, s46\n\t" \
    "s_add_u32 s40, s40, s49\n\t" \
    "s_addc_u32 s41, s41, 0\n\t" \
    "s_sub_u32 s54, s54, 1\n\t" \
    "s_cmp_eq_u32 s54, 0\n\t" \
    "s_cselect_b32 s50, s56, 0\n\t" \
    "s_sub_u32 s40, s40, s50\n\t" \
    "s_subb_u32 s41, s41, 0\n\t" \
    "s_add_u32 m0, s47, s53\n\t" \
    "s_nop 0\n\t" \
    "global_load_lds_dwordx4 v5, s[42:43] offset:0\n\t" \
    "global_load_lds_dwordx4 v6, s[42:43] offset:1024\n\t" \
    "global_load_lds_dwordx4 v7, s[42:43] offset:2048\n\t" \
    "s_mov_b32 m0, s46\n\t" \
    "s_add_u32 s46, s46, 14336\n\t" \
    "global_load_lds_dwordx4 v0, s[40:41] offset:-2048\n\t" \
    "global_load_lds_dwordx4 v1, s[40:41] offset:-1024\n\t" \
    "global_load_lds_dwordx4 v2, s[40:41] offset:0\n\t" \
    "global_load_lds_dwordx4 v3, s[40:41] offset:1024\n\t" \
    "global_load_lds_dwordx4 v4, s[40:41] offset:2048\n\t" \
    "s_cmp_eq_u32 s46, s52\n\t" \
    "s_cselect_b32 s46, s51, s46\n\t" \
    "s_add_u32 s40, s40, s49\n\t" \
    "s_addc_u32 s41, s41, 0\n\t" \
    "s_sub_u32 s54, s54, 1\n\t" \
    "s_cmp_eq_u32 s54, 0\n\t" \
    "s_cselect_b32 s50, s56, 0\n\t" \
    "s_sub_u32 s40, s40, s50\n\t" \
    "s_subb_u32 s41, s41, 0\n\t" \
    "s_waitcnt vmcnt(13)\n\t" \
    "s_barrier\n\t" \
    "s_add_u32 m0, s58, s53\n\t" \
    "s_nop 0\n\t" \
    "global_load_lds_dwordx4 v5, s[44:45] offset:0\n\t" \
    "global_load_lds_dwordx4 v6, s[44:45] offset:1024\n\t" \
    "global_load_lds_dwordx4 v7, s[44:45] offset:2048\n\t" \
    "s_add_u32 s53, s53, 16384\n\t" \
    "s_and_b32 s53, s53, 0xffff\n\t" \
    "s_add_u32 s42, s42, 128\n\t" \
    "s_addc_u32 s43, s43, 0\n\t" \
    "s_add_u32 s44, s44, 128\n\t" \
    "s_addc_u32 s45, s45, 0\n\t" \
    "s_sub_u32 s55, s55, 1\n\t" \
    "s_cmp_eq_u32 s55, 0\n\t" \
    "s_cselect_b32 s50, s57, 0\n\t" \
    "s_sub_u32 s42, s42, s50\n\t" \
    "s_subb_u32 s43, s43, 0\n\t" \
    "s_sub_u32 s44, s44, s50\n\t" \
    "s_subb_u32 s45, s45, 0\n\t" \
    "s_mov_b32 m0, s46\n\t" \
    "s_add_u32 s46, s46, 14336\n\t" \
    "global_load_lds_dwordx4 v0, s[40:41] offset:-2048\n\t" \
    "global_load_lds_dwordx4 v1, s[40:41] offset:-1024\n\t" \
    "global_load_lds_dwordx4 v2, s[40:41] offset:0\n\t" \
    "global_load_lds_dwordx4 v3, s[40:41] offset:1024\n\t" \
    "global_load_lds_dwordx4 v4, s[40:41] offset:2048\n\t" \
    "s_cmp_eq_u32 s46, s52\n\t" \
    "s_cselect_b32 s46, s51, s46\n\t" \
    "s_add_u32 s40, s40, s49\n\t" \
    "s_addc_u32 s41, s41, 0\n\t" \
    "s_sub_u32 s54, s54, 1\n\t" \
    "s_cmp_eq_u32 s54, 0\n\t" \
    "s_cselect_b32 s50, s56, 0\n\t" \
    "s_sub_u32 s40, s40, s50\n\t" \
    "s_subb_u32 s41, s41, 0\n\t" \
    "s_add_u32 m0, s47, s53\n\t" \
    "s_nop 0\n\t" \
    "global_load_lds_dwordx4 v5, s[42:43] offset:0\n\t" \
    "global_load_lds_dwordx4 v6, s[42:43] offset:1024\n\t" \
    "global_load_lds_dwordx4 v7, s[42:43] offset:2048\n\t" \
    "s_waitcnt vmcnt(19)\n\t" \
    "s_barrier\n\t" \
    "s_mov_b32 m0, s46\n\t" \
    "s_add_u32 s46, s46, 14336\n\t" \
    "global_load_lds_dwordx4 v0, s[40:41] offset:-2048\n\t" \
    "global_load_lds_dwordx4 v1, s[40:41] offset:-1024\n\t" \
    "global_load_lds_dwordx4 v2, s[40:41] offset:0\n\t" \
    "global_load_lds_dwordx4 v3, s[40:41] offset:1024\n\t" \
    "global_load_lds_dwordx4 v4, s[40:41] offset:2048\n\t" \
    "s_cmp_eq_u32 s46, s52\n\t" \
    "s_cselect_b32 s46, s51, s46\n\t" \
    "s_add_u32 s40, s40, s49\n\t" \
    "s_addc_u32 s41, s41, 0\n\t" \
    "s_sub_u32 s54, s54, 1\n\t" \
    "s_cmp_eq_u32 s54, 0\n\t" \
    "s_cselect_b32 s50, s56, 0\n\t" \
    "s_sub_u32 s40, s40, s50\n\t" \
    "s_subb_u32 s41, s41, 0\n\t" \
    "s_add_u32 m0, s58, s53\n\t" \
    "s_nop 0\n\t" \
    "global_load_lds_dwordx4 v5, s[44:45] offset:0\n\t" \
    "global_load_lds_dwordx4 v6, s[44:45] offset:1024\n\t" \
    "global_load_lds_dwordx4 v7, s[44:45] offset:2048\n\t" \
    "s_add_u32 s53, s53, 16384\n\t" \
    "s_and_b32 s53, s53, 0xffff\n\t" \
    "s_add_u32 s42, s42, 128\n\t" \
    "s_addc_u32 s43, s43, 0\n\t" \
    "s_add_u32 s44, s44, 128\n\t" \
    "s_addc_u32 s45, s45, 0\n\t" \
    "s_sub_u32 s55, s55, 1\n\t" \
    "s_cmp_eq_u32 s55, 0\n\t" \
    "s_cselect_b32 s50, s57, 0\n\t" \
    "s_sub_u32 s42, s42, s50\n\t" \
    "s_subb_u32 s43, s43, 0\n\t" \
    "s_sub_u32 s44, s44, s50\n\t" \
    "s_subb_u32 s45, s45, 0\n\t" \
    "s_mov_b32 m0, s46\n\t" \
    "s_add_u32 s46, s46, 14336\n\t" \
    "global_load_lds_dwordx4 v0, s[40:41] offset:-2048\n\t" \
    "global_load_lds_dwordx4 v1, s[40:41] offset:-1024\n\t" \
    "global_load_lds_dwordx4 v2, s[40:41] offset:0\n\t" \
    "global_load_lds_dwordx4 v3, s[40:41] offset:1024\n\t" \
    "global_load_lds_dwordx4 v4, s[40:41] offset:2048\n\t" \
    "s_cmp_eq_u32 s46, s52\n\t" \
    "s_cselect_b32 s46, s51, s46\n\t" \
    "s_add_u32 s40, s40, s49\n\t" \
    "s_addc_u32 s41, s41, 0\n\t" \
    "s_sub_u32 s54, s54, 1\n\t" \
    "s_cmp_eq_u32 s54, 0\n\t" \
    "s_cselect_b32 s50, s56, 0\n\t" \
    "s_sub_u32 s40, s40, s50\n\t" \
    "s_subb_u32 s41, s41, 0\n\t" \
    "s_waitcnt vmcnt(21)\n\t" \
    "s_barrier\n\t" \
    "s_add_u32 m0, s47, s53\n\t" \
    "s_nop 0\n\t" \
    "global_load_lds_dwordx4 v5, s[42:43] offset:0\n\t" \
    "global_load_lds_dwordx4 v6, s[42:43] offset:1024\n\t" \
    "global_load_lds_dwordx4 v7, s[42:43] offset:2048\n\t" \
    "s_mov_b32 m0, s46\n\t" \
    "s_add_u32 s46, s46, 14336\n\t" \
    "global_load_lds_dwordx4 v0, s[40:41] offset:-2048\n\t" \
    "global_load_lds_dwordx4 v1, s[40:41] offset:-1024\n\t" \
    "global_load_lds_dwordx4 v2, s[40:41] offset:0\n\t" \
    "global_load_lds_dwordx4 v3, s[40:41] offset:1024\n\t" \
    "global_load_lds_dwordx4 v4, s[40:41] offset:2048\n\t" \
    "s_cmp_eq_u32 s46, s52\n\t" \
    "s_cselect_b32 s46, s51, s46\n\t" \
    "s_add_u32 s40, s40, s49\n\t" \
    "s_addc_u32 s41, s41, 0\n\t" \
    "s_sub_u32 s54, s54, 1\n\t" \
    "s_cmp_eq_u32 s54, 0\n\t" \
    "s_cselect_b32 s50, s56, 0\n\t" \
    "s_sub_u32 s40, s40, s50\n\t" \
    "s_subb_u32 s41, s41, 0\n\t" \
    "s_add_u32 m0, s58, s53\n\t" \
    "s_nop 0\n\t" \
    "global_load_lds_dwordx4 v5, s[44:45] offset:0\n\t" \
    "global_load_lds_dwordx4 v6, s[44:45] offset:1024\n\t" \
    "global_load_lds_dwordx4 v7, s[44:45] offset:2048\n\t" \
    "s_add_u32 s53, s53, 16384\n\t" \
    "s_and_b32 s53, s53, 0xffff\n\t" \
    "s_add_u32 s42, s42, 128\n\t" \
    "s_addc_u32 s43, s43, 0\n\t" \
    "s_add_u32 s44, s44, 128\n\t" \
    "s_addc_u32 s45, s45, 0\n\t" \
    "s_sub_u32 s55, s55, 1\n\t" \
    "s_cmp_eq_u32 s55, 0\n\t" \
    "s_cselect_b32 s50, s57, 0\n\t" \
    "s_sub_u32 s42, s42, s50\n\t" \
    "s_subb_u32 s43, s43, 0\n\t" \
    "s_sub_u32 s44, s44, s50\n\t" \
    "s_subb_u32 s45, s45, 0\n\t" \
    "s_waitcnt vmcnt(27)\n\t" \
    "s_barrier\n\t" \
    "s_mov_b32 m0, s46\n\t" \
    "s_add_u32 s46, s46, 14336\n\t" \
    "global_load_lds_dwordx4 v0, s[40:41] offset:-2048\n\t" \
    "global_load_lds_dwordx4 v1, s[40:41] offset:-1024\n\t" \
    "global_load_lds_dwordx4 v2, s[40:41] offset:0\n\t" \
    "global_load_lds_dwordx4 v3, s[40:41] offset:1024\n\t" \
    "global_load_lds_dwordx4 v4, s[40:41] offset:2048\n\t" \
    "s_cmp_eq_u32 s46, s52\n\t" \
    "s_cselect_b32 s46, s51, s46\n\t" \
    "s_add_u32 s40, s40, s49\n\t" \
    "s_addc_u32 s41, s41, 0\n\t" \
    "s_sub_u32 s54, s54, 1\n\t" \
    "s_cmp_eq_u32 s54, 0\n\t" \
    "s_cselect_b32 s50, s56, 0\n\t" \
    "s_sub_u32 s40, s40, s50\n\t" \
    "s_subb_u32 s41, s41, 0\n\t" \
    "s_add_u32 m0, s47, s53\n\t" \
    "s_nop 0\n\t" \
    "global_load_lds_dwordx4 v5, s[42:43] offset:0\n\t" \
    "global_load_lds_dwordx4 v6, s[42:43] offset:1024\n\t" \
    "global_load_lds_dwordx4 v7, s[42:43] offset:2048\n\t" \
    "s_mov_b32 m0, s46\n\t" \
    "s_add_u32 s46, s46, 14336\n\t" \
    "global_load_lds_dwordx4 v0, s[40:41] offset:-2048\n\t" \
    "global_load_lds_dwordx4 v1, s[40:41] offset:-1024\n\t" \
    "global_load_lds_dwordx4 v2, s[40:41] offset:0\n\t" \
    "global_load_lds_dwordx4 v3, s[40:41] offset:1024\n\t" \
    "global_load_lds_dwordx4 v4, s[40:41] offset:2048\n\t" \
    "s_cmp_eq_u32 s46, s52\n\t" \
    "s_cselect_b32 s46, s51, s46\n\t" \
    "s_add_u32 s40, s40, s49\n\t" \
    "s_addc_u32 s41, s41, 0\n\t" \
    "s_sub_u32 s54, s54, 1\n\t" \
    "s_cmp_eq_u32 s54, 0\n\t" \
    "s_cselect_b32 s50, s56, 0\n\t" \
    "s_sub_u32 s40, s40, s50\n\t" \
    "s_subb_u32 s41, s41, 0\n\t" \
    "s_waitcnt vmcnt(29)\n\t" \
    "s_barrier\n\t" \
    "s_add_u32 m0, s58, s53\n\t" \
    "s_nop 0\n\t" \
    "global_load_lds_dwordx4 v5, s[44:45] offset:0\n\t" \
    "global_load_lds_dwordx4 v6, s[44:45] offset:1024\n\t" \
    "global_load_lds_dwordx4 v7, s[44:45] offset:2048\n\t" \
    "s_add_u32 s53, s53, 16384\n\t" \
    "s_and_b32 s53, s53, 0xffff\n\t" \
    "s_add_u32 s42, s42, 128\n\t" \
    "s_addc_u32 s43, s43, 0\n\t" \
    "s_add_u32 s44, s44, 128\n\t" \
    "s_addc_u32 s45, s45, 0\n\t" \
    "s_sub_u32 s55, s55, 1\n\t" \
    "s_cmp_eq_u32 s55, 0\n\t" \
    "s_cselect_b32 s50, s57, 0\n\t" \
    "s_sub_u32 s42, s42, s50\n\t" \
    "s_subb_u32 s43, s43, 0\n\t" \
    "s_sub_u32 s44, s44, s50\n\t" \
    "s_subb_u32 s45, s45, 0\n\t" \
    "s_mov_b32 m0, s46\n\t" \
    "s_add_u32 s46, s46, 14336\n\t" \
    "global_load_lds_dwordx4 v0, s[40:41] offset:-2048\n\t" \
    "global_load_lds_dwordx4 v1, s[40:41] offset:-1024\n\t" \
    "global_load_lds_dwordx4 v2, s[40:41] offset:0\n\t" \
    "global_load_lds_dwordx4 v3, s[40:41] offset:1024\n\t" \
    "global_load_lds_dwordx4 v4, s[40:41] offset:2048\n\t" \
    "s_cmp_eq_u32 s46, s52\n\t" \
    "s_cselect_b32 s46, s51, s46\n\t" \
    "s_add_u32 s40, s40, s49\n\t" \
    "s_addc_u32 s41, s41, 0\n\t" \
    "s_sub_u32 s54, s54, 1\n\t" \
    "s_cmp_eq_u32 s54, 0\n\t" \
    "s_cselect_b32 s50, s56, 0\n\t" \
    "s_sub_u32 s40, s40, s50\n\t" \
    "s_subb_u32 s41, s41, 0\n\t" \
    "s_add_u32 m0, s47, s53\n\t" \
    "s_nop 0\n\t" \
    "global_load_lds_dwordx4 v5, s[42:43] offset:0\n\t" \
    "global_load_lds_dwordx4 v6, s[42:43] offset:1024\n\t" \
    "global_load_lds_dwordx4 v7, s[42:43] offset:2048\n\t" \
    "s_waitcnt vmcnt(32)\n\t" \
    "s_barrier\n\t" \
    "s_mov_b32 m0, s46\n\t" \
    "s_add_u32 s46, s46, 14336\n\t" \
    "global_load_lds_dwordx4 v0, s[40:41] offset:-2048\n\t" \
    "global_load_lds_dwordx4 v1, s[40:41] offset:-1024\n\t" \
    "global_load_lds_dwordx4 v2, s[40:41] offset:0\n\t" \
    "global_load_lds_dwordx4 v3, s[40:41] offset:1024\n\t" \
    "global_load_lds_dwordx4 v4, s[40:41] offset:2048\n\t" \
    "s_cmp_eq_u32 s46, s52\n\t" \
    "s_cselect_b32 s46, s51, s46\n\t" \
    "s_add_u32 s40, s40, s49\n\t" \
    "s_addc_u32 s41, s41, 0\n\t" \
    "s_sub_u32 s54, s54, 1\n\t" \
    "s_cmp_eq_u32 s54, 0\n\t" \
    "s_cselect_b32 s50, s56, 0\n\t" \
    "s_sub_u32 s40, s40, s50\n\t" \
    "s_subb_u32 s41, s41, 0\n\t" \
    "s_add_u32 m0, s58, s53\n\t" \
    "s_nop 0\n\t" \
    "global_load_lds_dwordx4 v5, s[44:45] offset:0\n\t" \
    "global_load_lds_dwordx4 v6, s[44:45] offset:1024\n\t" \
    "global_load_lds_dwordx4 v7, s[44:45] offset:2048\n\t" \
    "s_add_u32 s53, s53, 16384\n\t" \
    "s_and_b32 s53, s53, 0xffff\n\t" \
    "s_add_u32 s42, s42, 128\n\t" \
    "s_addc_u32 s43, s43, 0\n\t" \
    "s_add_u32 s44, s44, 128\n\t" \
    "s_addc_u32 s45, s45, 0\n\t" \
    "s_sub_u32 s55, s55, 1\n\t" \
    "s_cmp_eq_u32 s55, 0\n\t" \
    "s_cselect_b32 s50, s57, 0\n\t" \
    "s_sub_u32 s42, s42, s50\n\t" \
    "s_subb_u32 s43, s43, 0\n\t" \
    "s_sub_u32 s44, s44, s50\n\t" \
    "s_subb_u32 s45, s45, 0\n\t" \
    "s_waitcnt vmcnt(32)\n\t" \
    "s_barrier\n\t" \
    "s_cmp_eq_u32 s48, 0\n\t" \
    "s_cbranch_scc1 2f\n\t" \
    "1:\n\t" \
    "s_mov_b32 m0, s46\n\t" \
    "s_add_u32 s46, s46, 14336\n\t" \
    "global_load_lds_dwordx4 v0, s[40:41] offset:-2048\n\t" \
    "global_load_lds_dwordx4 v1, s[40:41] offset:-1024\n\t" \
    "global_load_lds_dwordx4 v2, s[40:41] offset:0\n\t" \
    "global_load_lds_dwordx4 v3, s[40:41] offset:1024\n\t" \
    "global_load_lds_dwordx4 v4, s[40:41] offset:2048\n\t" \
    "s_cmp_eq_u32 s46, s52\n\t" \
    "s_cselect_b32 s46, s51, s46\n\t" \
    "s_add_u32 s40, s40, s49\n\t" \
    "s_addc_u32 s41, s41, 0\n\t" \
    "s_sub_u32 s54, s54, 1\n\t" \
    "s_cmp_eq_u32 s54, 0\n\t" \
    "s_cselect_b32 s50, s56, 0\n\t" \
    "s_sub_u32 s40, s40, s50\n\t" \
    "s_subb_u32 s41, s41, 0\n\t" \
    "s_add_u32 m0, s47, s53\n\t" \
    "s_nop 0\n\t" \
    "global_load_lds_dwordx4 v5, s[42:43] offset:0\n\t" \
    "global_load_lds_dwordx4 v6, s[42:43] offset:1024\n\t" \
    "global_load_lds_dwordx4 v7, s[42:43] offset:2048\n\t" \
    "s_waitcnt vmcnt(32)\n\t" \
    "s_barrier\n\t" \
    "s_mov_b32 m0, s46\n\t" \
    "s_add_u32 s46, s46, 14336\n\t" \
    "global_load_lds_dwordx4 v0, s[40:41] offset:-2048\n\t" \
    "global_load_lds_dwordx4 v1, s[40:41] offset:-1024\n\t" \
    "global_load_lds_dwordx4 v2, s[40:41] offset:0\n\t" \
    "global_load_lds_dwordx4 v3, s[40:41] offset:1024\n\t" \
    "global_load_lds_dwordx4 v4, s[40:41] offset:2048\n\t" \
    "s_cmp_eq_u32 s46, s52\n\t" \
    "s_cselect_b32 s46, s51, s46\n\t" \
    "s_add_u32 s40, s40, s49\n\t" \
    "s_addc_u32 s41, s41, 0\n\t" \
    "s_sub_u32 s54, s54, 1\n\t" \
    "s_cmp_eq_u32 s54, 0\n\t" \
    "s_cselect_b32 s50, s56, 0\n\t" \
    "s_sub_u32 s40, s40, s50\n\t" \
    "s_subb_u32 s41, s41, 0\n\t" \
    "s_add_u32 m0, s58, s53\n\t" \
    "s_nop 0\n\t" \
    "global_load_lds_dwordx4 v5, s[44:45] offset:0\n\t" \
    "global_load_lds_dwordx4 v6, s[44:45] offset:1024\n\t" \
    "global_load_lds_dwordx4 v7, s[44:45] offset:2048\n\t" \
    "s_add_u32 s53, s53, 16384\n\t" \
    "s_and_b32 s53, s53, 0xffff\n\t" \
    "s_add_u32 s42, s42, 128\n\t" \
    "s_addc_u32 s43, s43, 0\n\t" \
    "s_add_u32 s44, s44, 128\n\t" \
    "s_addc_u32 s45, s45, 0\n\t" \
    "s_sub_u32 s55, s55, 1\n\t" \
    "s_cmp_eq_u32 s55, 0\n\t" \
    "s_cselect_b32 s50, s57, 0\n\t" \
    "s_sub_u32 s42, s42, s50\n\t" \
    "s_subb_u32 s43, s43, 0\n\t" \
    "s_sub_u32 s44, s44, s50\n\t" \
    "s_subb_u32 s45, s45, 0\n\t" \
    "s_sub_u32 s48, s48, 1\n\t" \
    "s_cmp_lg_u32 s48, 0\n\t" \
    "s_waitcnt vmcnt(32)\n\t" \
    "s_barrier\n\t" \
    "s_cbranch_scc1 1b\n\t" \
    "2:\n\t" \
    "s_mov_b32 m0, s46\n\t" \
    "s_add_u32 s46, s46, 14336\n\t" \
    "global_load_lds_dwordx4 v0, s[40:41] offset:-2048\n\t" \
    "global_load_lds_dwordx4 v1, s[40:41] offset:-1024\n\t" \
    "global_load_lds_dwordx4 v2, s[40:41] offset:0\n\t" \
    "global_load_lds_dwordx4 v3, s[40:41] offset:1024\n\t" \
    "global_load_lds_dwordx4 v4, s[40:41] offset:2048\n\t" \
    "s_cmp_eq_u32 s46, s52\n\t" \
    "s_cselect_b32 s46, s51, s46\n\t" \
    "s_add_u32 s40, s40, s49\n\t" \
    "s_addc_u32 s41, s41, 0\n\t" \
    "s_sub_u32 s54, s54, 1\n\t" \
    "s_cmp_eq_u32 s54, 0\n\t" \
    "s_cselect_b32 s50, s56, 0\n\t" \
    "s_sub_u32 s40, s40, s50\n\t" \
    "s_subb_u32 s41, s41, 0\n\t" \
    ""
#define LOADER42 \
    "s_add_u32 m0, s47, s53\n\t" \
    "s_nop 0\n\t" \
    "global_load_lds_dwordx4 v5, s[42:43] offset:0\n\t" \
    "global_load_lds_dwordx4 v6, s[42:43] offset:1024\n\t" \
    "s_mov_b32 m0, s46\n\t" \
    "s_add_u32 s46, s46, 14336\n\t" \
    "global_load_lds_dwordx4 v0, s[40:41] offset:-2048\n\t" \
    "global_load_lds_dwordx4 v1, s[40:41] offset:-1024\n\t" \
    "global_load_lds_dwordx4 v2, s[40:41] offset:0\n\t" \
    "global_load_lds_dwordx4 v3, s[40:41] offset:1024\n\t" \
    "s_cmp_eq_u32 s46, s52\n\t" \
    "s_cselect_b32 s46, s51, s46\n\t" \
    "s_add_u32 s40, s40, s49\n\t" \
    "s_addc_u32 s41, s41, 0\n\t" \
    "s_sub_u32 s54, s54, 1\n\t" \
    "s_cmp_eq_u32 s54, 0\n\t" \
    "s_cselect_b32 s50, s56, 0\n\t" \
    "s_sub_u32 s40, s40, s50\n\t" \
    "s_subb_u32 s41, s41, 0\n\t" \
    "s_add_u32 m0, s58, s53\n\t" \
    "s_nop 0\n\t" \
    "global_load_lds_dwordx4 v5, s[44:45] offset:0\n\t" \
    "global_load_lds_dwordx4 v6, s[44:45] offset:1024\n\t" \
    "s_add_u32 s53, s53, 16384\n\t" \
    "s_and_b32 s53, s53, 0xffff\n\t" \
    "s_add_u32 s42, s42, 128\n\t" \
    "s_addc_u32 s43, s43, 0\n\t" \
    "s_add_u32 s44, s44, 128\n\t" \
    "s_addc_u32 s45, s45, 0\n\t" \
    "s_sub_u32 s55, s55, 1\n\t" \
    "s_cmp_eq_u32 s55, 0\n\t" \
    "s_cselect_b32 s50, s57, 0\n\t" \
    "s_sub_u32 s42, s42, s50\n\t" \
    "s_subb_u32 s43, s43, 0\n\t" \
    "s_sub_u32 s44, s44, s50\n\t" \
    "s_subb_u32 s45, s45, 0\n\t" \
    "s_mov_b32 m0, s46\n\t" \
    "s_add_u32 s46, s46, 14336\n\t" \
    "global_load_lds_dwordx4 v0, s[40:41] offset:-2048\n\t" \
    "global_load_lds_dwordx4 v1, s[40:41] offset:-1024\n\t" \
    "global_load_lds_dwordx4 v2, s[40:41] offset:0\n\t" \
    "global_load_lds_dwordx4 v3, s[40:41] offset:1024\n\t" \
    "s_cmp_eq_u32 s46, s52\n\t" \
    "s_cselect_b32 s46, s51, s46\n\t" \
    "s_add_u32 s40, s40, s49\n\t" \
    "s_addc_u32 s41, s41, 0\n\t" \
    "s_sub_u32 s54, s54, 1\n\t" \
    "s_cmp_eq_u32 s54, 0\n\t" \
    "s_cselect_b32 s50, s56, 0\n\t" \
    "s_sub_u32 s40, s40, s50\n\t" \
    "s_subb_u32 s41, s41, 0\n\t" \
    "s_add_u32 m0, s47, s53\n\t" \
    "s_nop 0\n\t" \
    "global_load_lds_dwordx4 v5, s[42:43] offset:0\n\t" \
    "global_load_lds_dwordx4 v6, s[42:43] offset:1024\n\t" \
    "s_mov_b32 m0, s46\n\t" \
    "s_add_u32 s46, s46, 14336\n\t" \
    "global_load_lds_dwordx4 v0, s[40:41] offset:-2048\n\t" \
    "global_load_lds_dwordx4 v1, s[40:41] offset:-1024\n\t" \
    "global_load_lds_dwordx4 v2, s[40:41] offset:0\n\t" \
    "global_load_lds_dwordx4 v3, s[40:41] offset:1024\n\t" \
    "s_cmp_eq_u32 s46, s52\n\t" \
    "s_cselect_b32 s46, s51, s46\n\t" \
    "s_add_u32 s40, s40, s49\n\t" \
    "s_addc_u32 s41, s41, 0\n\t" \
    "s_sub_u32 s54, s54, 1\n\t" \
    "s_cmp_eq_u32 s54, 0\n\t" \
    "s_cselect_b32 s50, s56, 0\n\t" \
    "s_sub_u32 s40, s40, s50\n\t" \
    "s_subb_u32 s41, s41, 0\n\t" \
    "s_add_u32 m0, s58, s53\n\t" \
    "s_nop 0\n\t" \
    "global_load_lds_dwordx4 v5, s[44:45] offset:0\n\t" \
    "global_load_lds_dwordx4 v6, s[44:45] offset:1024\n\t" \
    "s_add_u32 s53, s53, 16384\n\t" \
    "s_and_b32 s53, s53, 0xffff\n\t" \
    "s_add_u32 s42, s42, 128\n\t" \
    "s_addc_u32 s43, s43, 0\n\t" \
    "s_add_u32 s44, s44, 128\n\t" \
    "s_addc_u32 s45, s45, 0\n\t" \
    "s_sub_u32 s55, s55, 1\n\t" \
    "s_cmp_eq_u32 s55, 0\n\t" \
    "s_cselect_b32 s50, s57, 0\n\t" \
    "s_sub_u32 s42, s42, s50\n\t" \
    "s_subb_u32 s43, s43, 0\n\t" \
    "s_sub_u32 s44, s44, s50\n\t" \
    "s_subb_u32 s45, s45, 0\n\t" \
    "s_waitcnt vmcnt(12)\n\t" \
    "s_barrier\n\t" \
    "s_waitcnt vmcnt(8)\n\t" \
    "s_barrier\n\t" \
    "s_mov_b32 m0, s46\n\t" \
    "s_add_u32 s46, s46, 14336\n\t" \
    "global_load_lds_dwordx4 v0, s[40:41] offset:-2048\n\t" \
    "global_load_lds_dwordx4 v1, s[40:41] offset:-1024\n\t" \
    "global_load_lds_dwordx4 v2, s[40:41] offset:0\n\t" \
    "global_load_lds_dwordx4 v3, s[40:41] offset:1024\n\t" \
    "s_cmp_eq_u32 s46, s52\n\t" \
    "s_cselect_b32 s46, s51, s46\n\t" \
    "s_add_u32 s40, s40, s49\n\t" \
    "s_addc_u32 s41, s41, 0\n\t" \
    "s_sub_u32 s54, s54, 1\n\t" \
    "s_cmp_eq_u32 s54, 0\n\t" \
    "s_cselect_b32 s50, s56, 0\n\t" \
    "s_sub_u32 s40, s40, s50\n\t" \
    "s_subb_u32 s41, s41, 0\n\t" \
    "s_add_u32 m0, s47, s53\n\t" \
    "s_nop 0\n\t" \
    "global_load_lds_dwordx4 v5, s[42:43] offset:0\n\t" \
    "global_load_lds_dwordx4 v6, s[42:43] offset:1024\n\t" \
    "s_mov_b32 m0, s46\n\t" \
    "s_add_u32 s46, s46, 14336\n\t" \
    "global_load_lds_dwordx4 v0, s[40:41] offset:-2048\n\t" \
    "global_load_lds_dwordx4 v1, s[40:41] offset:-1024\n\t" \
    "global_load_lds_dwordx4 v2, s[40:41] offset:0\n\t" \
    "global_load_lds_dwordx4 v3, s[40:41] offset:1024\n\t" \
    "s_cmp_eq_u32 s46, s52\n\t" \
    "s_cselect_b32 s46, s51, s46\n\t" \
    "s_add_u32 s40, s40, s49\n\t" \
    "s_addc_u32 s41, s41, 0\n\t" \
    "s_sub_u32 s54, s54, 1\n\t" \
    "s_cmp_eq_u32 s54, 0\n\t" \
    "s_cselect_b32 s50, s56, 0\n\t" \
    "s_sub_u32 s40, s40, s50\n\t" \
    "s_subb_u32 s41, s41, 0\n\t" \
    "s_waitcnt vmcnt(10)\n\t" \
    "s_barrier\n\t" \
    "s_add_u32 m0, s58, s53\n\t" \
    "s_nop 0\n\t" \
    "global_load_lds_dwordx4 v5, s[44:45] offset:0\n\t" \
    "global_load_lds_dwordx4 v6, s[44:45] offset:1024\n\t" \
    "s_add_u32 s53, s53, 16384\n\t" \
    "s_and_b32 s53, s53, 0xffff\n\t" \
    "s_add_u32 s42, s42, 128\n\t" \
    "s_addc_u32 s43, s43, 0\n\t" \
    "s_add_u32 s44, s44, 128\n\t" \
    "s_addc_u32 s45, s45, 0\n\t" \
    "s_sub_u32 s55, s55, 1\n\t" \
    "s_cmp_eq_u32 s55, 0\n\t" \
    "s_cselect_b32 s50, s57, 0\n\t" \
    "s_sub_u32 s42, s42, s50\n\t" \
    "s_subb_u32 s43, s43, 0\n\t" \
    "s_sub_u32 s44, s44, s50\n\t" \
    "s_subb_u32 s45, s45, 0\n\t" \
    "s_mov_b32 m0, s46\n\t" \
    "s_add_u32 s46, s46, 14336\n\t" \
    "global_load_lds_dwordx4 v0, s[40:41] offset:-2048\n\t" \
    "global_load_lds_dwordx4 v1, s[40:41] offset:-1024\n\t" \
    "global_load_lds_dwordx4 v2, s[40:41] offset:0\n\t" \
    "global_load_lds_dwordx4 v3, s[40:41] offset:1024\n\t" \
    "s_cmp_eq_u32 s46, s52\n\t" \
    "s_cselect_b32 s46, s51, s46\n\t" \
    "s_add_u32 s40, s40, s49\n\t" \
    "s_addc_u32 s41, s41, 0\n\t" \
    "s_sub_u32 s54, s54, 1\n\t" \
    "s_cmp_eq_u32 s54, 0\n\t" \
    "s_cselect_b32 s50, s56, 0\n\t" \
    "s_sub_u32 s40, s40, s50\n\t" \
    "s_subb_u32 s41, s41, 0\n\t" \
    "s_add_u32 m0, s47, s53\n\t" \
    "s_nop 0\n\t" \
    "global_load_lds_dwordx4 v5, s[42:43] offset:0\n\t" \
    "global_load_lds_dwordx4 v6, s[42:43] offset:1024\n\t" \
    "s_waitcnt vmcnt(14)\n\t" \
    "s_barrier\n\t" \
    "s_mov_b32 m0, s46\n\t" \
    "s_add_u32 s46, s46, 14336\n\t" \
    "global_load_lds_dwordx4 v0, s[40:41] offset:-2048\n\t" \
    "global_load_lds_dwordx4 v1, s[40:41] offset:-1024\n\t" \
    "global_load_lds_dwordx4 v2, s[40:41] offset:0\n\t" \
    "global_load_lds_dwordx4 v3, s[40:41] offset:1024\n\t" \
    "s_cmp_eq_u32 s46, s52\n\t" \
    "s_cselect_b32 s46, s51, s46\n\t" \
    "s_add_u32 s40, s40, s49\n\t" \
    "s_addc_u32 s41, s41, 0\n\t" \
    "s_sub_u32 s54, s54, 1\n\t" \
    "s_cmp_eq_u32 s54, 0\n\t" \
    "s_cselect_b32 s50, s56, 0\n\t" \
    "s_sub_u32 s40, s40, s50\n\t" \
    "s_subb_u32 s41, s41, 0\n\t" \
    "s_add_u32 m0, s58, s53\n\t" \
    "s_nop 0\n\t" \
    "global_load_lds_dwordx4 v5, s[44:45] offset:0\n\t" \
    "global_load_lds_dwordx4 v6, s[44:45] offset:1024\n\t" \
    "s_add_u32 s53, s53, 16384\n\t" \
    "s_and_b32 s53, s53, 0xffff\n\t" \
    "s_add_u32 s42, s42, 128\n\t" \
    "s_addc_u32 s43, s43, 0\n\t" \
    "s_add_u32 s44, s44, 128\n\t" \
    "s_addc_u32 s45, s45, 0\n\t" \
    "s_sub_u32 s55, s55, 1\n\t" \
    "s_cmp_eq_u32 s55, 0\n\t" \
    "s_cselect_b32 s50, s57, 0\n\t" \
    "s_sub_u32 s42, s42, s50\n\t" \
    "s_subb_u32 s43, s43, 0\n\t" \
    "s_sub_u32 s44, s44, s50\n\t" \
    "s_subb_u32 s45, s45, 0\n\t" \
    "s_mov_b32 m0, s46\n\t" \
    "s_add_u32 s46, s46, 14336\n\t" \
    "global_load_lds_dwordx4 v0, s[40:41] offset:-2048\n\t" \
    "global_load_lds_dwordx4 v1, s[40:41] offset:-1024\n\t" \
    "global_load_lds_dwordx4 v2, s[40:41] offset:0\n\t" \
    "global_load_lds_dwordx4 v3, s[40:41] offset:1024\n\t" \
    "s_cmp_eq_u32 s46, s52\n\t" \
    "s_cselect_b32 s46, s51, s46\n\t" \
    "s_add_u32 s40, s40, s49\n\t" \
    "s_addc_u32 s41, s41, 0\n\t" \
    "s_sub_u32 s54, s54, 1\n\t" \
    "s_cmp_eq_u32 s54, 0\n\t" \
    "s_cselect_b32 s50, s56, 0\n\t" \
    "s_sub_u32 s40, s40, s50\n\t" \
    "s_subb_u32 s41, s41, 0\n\t" \
    "s_waitcnt vmcnt(16)\n\t" \
    "s_barrier\n\t" \
    "s_add_u32 m0, s47, s53\n\t" \
    "s_nop 0\n\t" \
    "global_load_lds_dwordx4 v5, s[42:43] offset:0\n\t" \
    "global_load_lds_dwordx4 v6, s[42:43] offset:1024\n\t" \
    "s_mov_b32 m0, s46\n\t" \
    "s_add_u32 s46, s46, 14336\n\t" \
    "global_load_lds_dwordx4 v0, s[40:41] offset:-2048\n\t" \
    "global_load_lds_dwordx4 v1, s[40:41] offset:-1024\n\t" \
    "global_load_lds_dwordx4 v2, s[40:41] offset:0\n\t" \
    "global_load_lds_dwordx4 v3, s[40:41] offset:1024\n\t" \
    "s_cmp_eq_u32 s46, s52\n\t" \
    "s_cselect_b32 s46, s51, s46\n\t" \
    "s_add_u32 s40, s40, s49\n\t" \
    "s_addc_u32 s41, s41, 0\n\t" \
    "s_sub_u32 s54, s54, 1\n\t" \
    "s_cmp_eq_u32 s54, 0\n\t" \
    "s_cselect_b32 s50, s56, 0\n\t" \
    "s_sub_u32 s40, s40, s50\n\t" \
    "s_subb_u32 s41, s41, 0\n\t" \
    "s_add_u32 m0, s58, s53\n\t" \
    "s_nop 0\n\t" \
    "global_load_lds_dwordx4 v5, s[44:45] offset:0\n\t" \
    "global_load_lds_dwordx4 v6, s[44:45] offset:1024\n\t" \
    "s_add_u32 s53, s53, 16384\n\t" \
    "s_and_b32 s53, s53, 0xffff\n\t" \
    "s_add_u32 s42, s42, 128\n\t" \
    "s_addc_u32 s43, s43, 0\n\t" \
    "s_add_u32 s44, s44, 128\n\t" \
    "s_addc_u32 s45, s45, 0\n\t" \
    "s_sub_u32 s55, s55, 1\n\t" \
    "s_cmp_eq_u32 s55, 0\n\t" \
    "s_cselect_b32 s50, s57, 0\n\t" \
    "s_sub_u32 s42, s42, s50\n\t" \
    "s_subb_u32 s43, s43, 0\n\t" \
    "s_sub_u32 s44, s44, s50\n\t" \
    "s_subb_u32 s45, s45, 0\n\t" \
    "s_waitcnt vmcnt(20)\n\t" \
    "s_barrier\n\t" \
    "s_mov_b32 m0, s46\n\t" \
    "s_add_u32 s46, s46, 14336\n\t" \
    "global_load_lds_dwordx4 v0, s[40:41] offset:-2048\n\t" \
    "global_load_lds_dwordx4 v1, s[40:41] offset:-1024\n\t" \
    "global_load_lds_dwordx4 v2, s[40:41] offset:0\n\t" \
    "global_load_lds_dwordx4 v3, s[40:41] offset:1024\n\t" \
    "s_cmp_eq_u32 s46, s52\n\t" \
    "s_cselect_b32 s46, s51, s46\n\t" \
    "s_add_u32 s40, s40, s49\n\t" \
    "s_addc_u32 s41, s41, 0\n\t" \
    "s_sub_u32 s54, s54, 1\n\t" \
    "s_cmp_eq_u32 s54, 0\n\t" \
    "s_cselect_b32 s50, s56, 0\n\t" \
    "s_sub_u32 s40, s40, s50\n\t" \
    "s_subb_u32 s41, s41, 0\n\t" \
    "s_add_u32 m0, s47, s53\n\t" \
    "s_nop 0\n\t" \
    "global_load_lds_dwordx4 v5, s[42:43] offset:0\n\t" \
    "global_load_lds_dwordx4 v6, s[42:43] offset:1024\n\t" \
    "s_mov_b32 m0, s46\n\t" \
    "s_add_u32 s46, s46, 14336\n\t" \
    "global_load_lds_dwordx4 v0, s[40:41] offset:-2048\n\t" \
    "global_load_lds_dwordx4 v1, s[40:41] offset:-1024\n\t" \
    "global_load_lds_dwordx4 v2, s[40:41] offset:0\n\t" \
    "global_load_lds_dwordx4 v3, s[40:41] offset:1024\n\t" \
    "s_cmp_eq_u32 s46, s52\n\t" \
    "s_cselect_b32 s46, s51, s46\n\t" \
    "s_add_u32 s40, s40, s49\n\t" \
    "s_addc_u32 s41, s41, 0\n\t" \
    "s_sub_u32 s54, s54, 1\n\t" \
    "s_cmp_eq_u32 s54, 0\n\t" \
    "s_cselect_b32 s50, s56, 0\n\t" \
    "s_sub_u32 s40, s40, s50\n\t" \
    "s_subb_u32 s41, s41, 0\n\t" \
    "s_waitcnt vmcnt(22)\n\t" \
    "s_barrier\n\t" \
    "s_add_u32 m0, s58, s53\n\t" \
    "s_nop 0\n\t" \
    "global_load_lds_dwordx4 v5, s[44:45] offset:0\n\t" \
    "global_load_lds_dwordx4 v6, s[44:45] offset:1024\n\t" \
    "s_add_u32 s53, s53, 16384\n\t" \
    "s_and_b32 s53, s53, 0xffff\n\t" \
    "s_add_u32 s42, s42, 128\n\t" \
    "s_addc_u32 s43, s43, 0\n\t" \
    "s_add_u32 s44, s44, 128\n\t" \
    "s_addc_u32 s45, s45, 0\n\t" \
    "s_sub_u32 s55, s55, 1\n\t" \
    "s_cmp_eq_u32 s55, 0\n\t" \
    "s_cselect_b32 s50, s57, 0\n\t" \
    "s_sub_u32 s42, s42, s50\n\t" \
    "s_subb_u32 s43, s43, 0\n\t" \
    "s_sub_u32 s44, s44, s50\n\t" \
    "s_subb_u32 s45, s45, 0\n\t" \
    "s_mov_b32 m0, s46\n\t" \
    "s_add_u32 s46, s46, 14336\n\t" \
    "global_load_lds_dwordx4 v0, s[40:41] offset:-2048\n\t" \
    "global_load_lds_dwordx4 v1, s[40:41] offset:-1024\n\t" \
    "global_load_lds_dwordx4 v2, s[40:41] offset:0\n\t" \
    "global_load_lds_dwordx4 v3, s[40:41] offset:1024\n\t" \
    "s_cmp_eq_u32 s46, s52\n\t" \
    "s_cselect_b32 s46, s51, s46\n\t" \
    "s_add_u32 s40, s40, s49\n\t" \
    "s_addc_u32 s41, s41, 0\n\t" \
    "s_sub_u32 s54, s54, 1\n\t" \
    "s_cmp_eq_u32 s54, 0\n\t" \
    "s_cselect_b32 s50, s56, 0\n\t" \
    "s_sub_u32 s40, s40, s50\n\t" \
    "s_subb_u32 s41, s41, 0\n\t" \
    "s_add_u32 m0, s47, s53\n\t" \
    "s_nop 0\n\t" \
    "global_load_lds_dwordx4 v5, s[42:43] offset:0\n\t" \
    "global_load_lds_dwordx4 v6, s[42:43] offset:1024\n\t" \
    "s_waitcnt vmcnt(24)\n\t" \
    "s_barrier\n\t" \
    "s_mov_b32 m0, s46\n\t" \
    "s_add_u32 s46, s46, 14336\n\t" \
    "global_load_lds_dwordx4 v0, s[40:41] offset:-2048\n\t" \
    "global_load_lds_dwordx4 v1, s[40:41] offset:-1024\n\t" \
    "global_load_lds_dwordx4 v2, s[40:41] offset:0\n\t" \
    "global_load_lds_dwordx4 v3, s[40:41] offset:1024\n\t" \
    "s_cmp_eq_u32 s46, s52\n\t" \
    "s_cselect_b32 s46, s51, s46\n\t" \
    "s_add_u32 s40, s40, s49\n\t" \
    "s_addc_u32 s41, s41, 0\n\t" \
    "s_sub_u32 s54, s54, 1\n\t" \
    "s_cmp_eq_u32 s54, 0\n\t" \
    "s_cselect_b32 s50, s56, 0\n\t" \
    "s_sub_u32 s40, s40, s50\n\t" \
    "s_subb_u32 s41, s41, 0\n\t" \
    "s_add_u32 m0, s58, s53\n\t" \
    "s_nop 0\n\t" \
    "global_load_lds_dwordx4 v5, s[44:45] offset:0\n\t" \
    "global_load_lds_dwordx4 v6, s[44:45] offset:1024\n\t" \
    "s_add_u32 s53, s53, 16384\n\t" \
    "s_and_b32 s53, s53, 0xffff\n\t" \
    "s_add_u32 s42, s42, 128\n\t" \
    "s_addc_u32 s43, s43, 0\n\t" \
    "s_add_u32 s44, s44, 128\n\t" \
    "s_addc_u32 s45, s45, 0\n\t" \
    "s_sub_u32 s55, s55, 1\n\t" \
    "s_cmp_eq_u32 s55, 0\n\t" \
    "s_cselect_b32 s50, s57, 0\n\t" \
    "s_sub_u32 s42, s42, s50\n\t" \
    "s_subb_u32 s43, s43, 0\n\t" \
    "s_sub_u32 s44, s44, s50\n\t" \
    "s_subb_u32 s45, s45, 0\n\t" \
    "s_waitcnt vmcnt(24)\n\t" \
    "s_barrier\n\t" \
    "s_cmp_eq_u32 s48, 0\n\t" \
    "s_cbranch_scc1 2f\n\t" \
    "1:\n\t" \
    "s_mov_b32 m0, s46\n\t" \
    "s_add_u32 s46, s46, 14336\n\t" \
    "global_load_lds_dwordx4 v0, s[40:41] offset:-2048\n\t" \
    "global_load_lds_dwordx4 v1, s[40:41] offset:-1024\n\t" \
    "global_load_lds_dwordx4 v2, s[40:41] offset:0\n\t" \
    "global_load_lds_dwordx4 v3, s[40:41] offset:1024\n\t" \
    "s_cmp_eq_u32 s46, s52\n\t" \
    "s_cselect_b32 s46, s51, s46\n\t" \
    "s_add_u32 s40, s40, s49\n\t" \
    "s_addc_u32 s41, s41, 0\n\t" \
    "s_sub_u32 s54, s54, 1\n\t" \
    "s_cmp_eq_u32 s54, 0\n\t" \
    "s_cselect_b32 s50, s56, 0\n\t" \
    "s_sub_u32 s40, s40, s50\n\t" \
    "s_subb_u32 s41, s41, 0\n\t" \
    "s_add_u32 m0, s47, s53\n\t" \
    "s_nop 0\n\t" \
    "global_load_lds_dwordx4 v5, s[42:43] offset:0\n\t" \
    "global_load_lds_dwordx4 v6, s[42:43] offset:1024\n\t" \
    "s_waitcnt vmcnt(24)\n\t" \
    "s_barrier\n\t" \
    "s_mov_b32 m0, s46\n\t" \
    "s_add_u32 s46, s46, 14336\n\t" \
    "global_load_lds_dwordx4 v0, s[40:41] offset:-2048\n\t" \
    "global_load_lds_dwordx4 v1, s[40:41] offset:-1024\n\t" \
    "global_load_lds_dwordx4 v2, s[40:41] offset:0\n\t" \
    "global_load_lds_dwordx4 v3, s[40:41] offset:1024\n\t" \
    "s_cmp_eq_u32 s46, s52\n\t" \
    "s_cselect_b32 s46, s51, s46\n\t" \
    "s_add_u32 s40, s40, s49\n\t" \
    "s_addc_u32 s41, s41, 0\n\t" \
    "s_sub_u32 s54, s54, 1\n\t" \
    "s_cmp_eq_u32 s54, 0\n\t" \
    "s_cselect_b32 s50, s56, 0\n\t" \
    "s_sub_u32 s40, s40, s50\n\t" \
    "s_subb_u32 s41, s41, 0\n\t" \
    "s_add_u32 m0, s58, s53\n\t" \
    "s_nop 0\n\t" \
    "global_load_lds_dwordx4 v5, s[44:45] offset:0\n\t" \
    "global_load_lds_dwordx4 v6, s[44:45] offset:1024\n\t" \
    "s_add_u32 s53, s53, 16384\n\t" \
    "s_and_b32 s53, s53, 0xffff\n\t" \
    "s_add_u32 s42, s42, 128\n\t" \
    "s_addc_u32 s43, s43, 0\n\t" \
    "s_add_u32 s44, s44, 128\n\t" \
    "s_addc_u32 s45, s45, 0\n\t" \
    "s_sub_u32 s55, s55, 1\n\t" \
    "s_cmp_eq_u32 s55, 0\n\t" \
    "s_cselect_b32 s50, s57, 0\n\t" \
    "s_sub_u32 s42, s42, s50\n\t" \
    "s_subb_u32 s43, s43, 0\n\t" \
    "s_sub_u32 s44, s44, s50\n\t" \
    "s_subb_u32 s45, s45, 0\n\t" \
    "s_sub_u32 s48, s48, 1\n\t" \
    "s_cmp_lg_u32 s48, 0\n\t" \
    "s_waitcnt vmcnt(24)\n\t" \
    "s_barrier\n\t" \
    "s_cbranch_scc1 1b\n\t" \
    "2:\n\t" \
    "s_mov_b32 m0, s46\n\t" \
    "s_add_u32 s46, s46, 14336\n\t" \
    "global_load_lds_dwordx4 v0, s[40:41] offset:-2048\n\t" \
    "global_load_lds_dwordx4 v1, s[40:41] offset:-1024\n\t" \
    "global_load_lds_dwordx4 v2, s[40:41] offset:0\n\t" \
    "global_load_lds_dwordx4 v3, s[40:41] offset:1024\n\t" \
    "s_cmp_eq_u32 s46, s52\n\t" \
    "s_cselect_b32 s46, s51, s46\n\t" \
    "s_add_u32 s40, s40, s49\n\t" \
    "s_addc_u32 s41, s41, 0\n\t" \
    "s_sub_u32 s54, s54, 1\n\t" \
    "s_cmp_eq_u32 s54, 0\n\t" \
    "s_cselect_b32 s50, s56, 0\n\t" \
    "s_sub_u32 s40, s40, s50\n\t" \
    "s_subb_u32 s41, s41, 0\n\t" \
    ""
#define LOADER_LOOPS(nstep) (((nstep) - 14) / 2)
#define LOADER_CLOB "s50", "scc", "memory"

#define STAMP(id) do { } while (0)
#define STAMPC(id) do { } while (0)
#define STARG
#define STPASS
namespace {
constexpr int A_SLOT = 16384, B_SLOT = 14336, LDS_A = 0, LDS_B = 65536;
constexpr int LDS_BYTES = 163840;
constexpr unsigned WS_EPOCH = 0, WS_ARR = 1024, WS_F1 = 4096, WS_FO = 6144, WS_FP = 8192;
constexpr unsigned WS_Q = 16384, WS_K = WS_Q + 2097152, WS_V = WS_K + 2097152, WS_O = WS_V + 2097152, WS_PQ = WS_O + 2097152;
constexpr unsigned WS_END = WS_PQ + 7340032;
constexpr int AUX_SC1 = 16;
constexpr unsigned SPIN_MAX = 1u << 22;

DI unsigned cvtpk(float lo, float hi) { f32x2 v = {lo, hi}; bf16x2 b = __builtin_convertvector(v, bf16x2); return __builtin_bit_cast(unsigned, b); }
DI float rbf(float x) { return __uint_as_float(cvtpk(0.0f, x)); }
DI void glds16(const void* sbase, unsigned voff, unsigned lds_base) {
    asm volatile("s_mov_b32 m0, %2\n\ts_nop 0\n\tglobal_load_lds_dwordx4 %1, %0" :: "s"(sbase), "v"(voff), "s"(lds_base) : "memory"); }
DI void wait_vm0() { asm volatile("s_waitcnt vmcnt(0)" ::: "memory"); }
DI void bar() { asm volatile("s_waitcnt lgkmcnt(0)\n\ts_barrier" ::: "memory"); }
DI s16x4 trread(LAS const char* p) { return __builtin_bit_cast(s16x4, __builtin_amdgcn_ds_read_tr16_b64_v4i16((LAS s16x4*)p)); }
DI v4u ld_sc1(__amdgpu_buffer_rsrc_t rs, unsigned off) { return __builtin_amdgcn_raw_buffer_load_b128(rs, off, 0, AUX_SC1); }
DI void st_sc1(__amdgpu_buffer_rsrc_t rs, unsigned off, v4u v) { __builtin_amdgcn_raw_buffer_store_b128(v, rs, off, 0, AUX_SC1); }
DI void flag_store(char* ws, unsigned off, u64 tag) { __hip_atomic_store((GAS u64*)(ws + off), tag, __ATOMIC_RELAXED, __HIP_MEMORY_SCOPE_AGENT); }
DI void poll(char* ws, unsigned off, bool active, u64 tag) {
    for (unsigned spins = 0; spins < SPIN_MAX; ++spins) {
        u64 v = tag;
        if (active) v = __hip_atomic_load((GAS u64*)(ws + off), __ATOMIC_RELAXED, __HIP_MEMORY_SCOPE_AGENT);
        if (__all(v == tag)) break;
        __builtin_amdgcn_s_sleep(1);
    }
    asm volatile("" ::: "memory");
}

template <int N> DI void wait_vm() { asm volatile("s_waitcnt vmcnt(%0)" :: "i"(N) : "memory"); }
struct NoTail { static constexpr int count(int, int) { return 0; } template <int W, int K> DI void issue() const {} };
template <class TAIL> constexpr int tail_sum(int w, int k) { int t = 0; for (int i = 0; i <= k; ++i) t += TAIL::count(w, i); return t; }
template <int W, int NB, int NA, class TAIL>
DI void g224_tail(const TAIL& t) {
    constexpr int n = NB + NA;
#define G224_TS(K, RING) t.template issue<W, K>(); if (RING >= 0) wait_vm<(RING >= 0 ? RING + tail_sum<TAIL>(W, K) : 0)>(); asm volatile("s_barrier" ::: "memory");
    G224_TS(0, 3 * n + NB) G224_TS(1, 2 * n + NB) G224_TS(2, n + NB) G224_TS(3, NB) G224_TS(4, 0) G224_TS(5, -1)
    t.template issue<W, 6>(); wait_vm<tail_sum<TAIL>(W, 6) - tail_sum<TAIL>(W, 2)>();
    asm volatile("s_barrier" ::: "memory");
#undef G224_TS
}
template <int NB, int NA>
DI void g224_loader(unsigned lds0, int w, int lane, const char* a0, const char* a1, unsigned lda, const char* bbase, unsigned ldb, int nstep, int rot) {
    const int b0 = 5 * w, i0 = 3 * w;
    unsigned vo[5], va[3];
#pragma unroll
    for (int i = 0; i < 5; ++i) {
        const unsigned o = (b0 + i) * 1024 + lane * 16;
        const unsigned row = o / 448, pc = (o - row * 448) >> 4;
        vo[i] = (i < NB) ? row * ldb + ((pc ^ (((row >> 3) & 1) << 1)) * 16) + 2048 - i * 1024 : 0u;
    }
    const int rl = lane >> 3, ch = lane & 7;
#pragma unroll
    for (int i = 0; i < 3; ++i) {
        const int ii = i0 + i;
        va[i] = (i < NA) ? (ii * 8 + rl) * lda + ((ch ^ ((((ii & 1) * 8 + rl) >> 1) & 7)) * 16) + 2048 - i * 1024 : 0u;
    }
    const unsigned bdst = lds0 + LDS_B + b0 * 1024 + 2048, adst = lds0 + LDS_A + i0 * 1024;
    const int rb = 2 * rot, ra = rot;
    const char* pb = bbase + (size_t)rb * 32 * ldb; const char* pa0 = a0 + ra * 128 - 2048; const char* pa1 = a1 + ra * 128 - 2048;
    unsigned s46 = bdst, s47 = adst, s48 = LOADER_LOOPS(nstep), s49 = 32 * ldb, s51 = bdst, s52 = bdst + 6 * B_SLOT, s53 = 0, s54 = nstep - rb, s55 = nstep / 2 - ra,
             s56 = nstep * 32 * ldb, s57 = (nstep / 2) * 128, s58 = adst + 8192;
    if (NB == 5) asm volatile(LOADER53 : "+{s[40:41]}"(pb), "+{s[42:43]}"(pa0), "+{s[44:45]}"(pa1), "+{s46}"(s46), "+{s48}"(s48), "+{s53}"(s53), "+{s54}"(s54), "+{s55}"(s55)
         : "{v0}"(vo[0]), "{v1}"(vo[1]), "{v2}"(vo[2]), "{v3}"(vo[3]), "{v4}"(vo[4]), "{v5}"(va[0]), "{v6}"(va[1]), "{v7}"(va[2]),
           "{s47}"(s47), "{s49}"(s49), "{s51}"(s51), "{s52}"(s52), "{s56}"(s56), "{s57}"(s57), "{s58}"(s58) : LOADER_CLOB);
    else asm volatile(LOADER42 : "+{s[40:41]}"(pb), "+{s[42:43]}"(pa0), "+{s[44:45]}"(pa1), "+{s46}"(s46), "+{s48}"(s48), "+{s53}"(s53), "+{s54}"(s54), "+{s55}"(s55)
         : "{v0}"(vo[0]), "{v1}"(vo[1]), "{v2}"(vo[2]), "{v3}"(vo[3]), "{v4}"(vo[4]), "{v5}"(va[0]), "{v6}"(va[1]), "{v7}"(va[2]),
           "{s47}"(s47), "{s49}"(s49), "{s51}"(s51), "{s52}"(s52), "{s56}"(s56), "{s57}"(s57), "{s58}"(s58) : LOADER_CLOB);
}
DI void touch(const char* p, unsigned lds_dummy) {
    asm volatile("s_mov_b32 m0, %1\n\ts_nop 0\n\tglobal_load_lds_dword %0, off" :: "v"(p), "s"(lds_dummy) : "memory"); }
constexpr int LDS_DUMMY = LDS_B + 6 * B_SLOT;
constexpr int LDS_CNT = LDS_BYTES - 64;
#define B2_PD 5
#define ATT_PRIO 0
#define ENG_FIRST 2
#define B1_ROT(x, tbl) (2 * (x))
template <class EX, class TAIL>
DI void g224(unsigned lds0, int wid, int lane, const char* a0, const char* a1, unsigned lda, const char* bbase, unsigned ldb, int nstep, int rot, bool swap, f32x16 (&c)[7], EX extra, const TAIL& tail) {
    if (wid == 7) {
        asm volatile("s_barrier\n\ts_barrier" ::: "memory");
        for (int s = 0; s < nstep - 7; ++s) {
            extra(s);
            asm volatile("s_barrier" ::: "memory");
        }
        g224_tail<3, 0, 0>(tail);
    }
    else if (wid == 6) { g224_loader<4, 2>(lds0, 2, lane, a0, a1, lda, bbase, ldb, nstep, rot); g224_tail<2, 4, 2>(tail); }
    else if (wid == 5) { g224_loader<5, 3>(lds0, 1, lane, a0, a1, lda, bbase, ldb, nstep, rot); g224_tail<1, 5, 3>(tail); }
    else if (wid == 4) { g224_loader<5, 3>(lds0, 0, lane, a0, a1, lda, bbase, ldb, nstep, rot); g224_tail<0, 5, 3>(tail); }
    else {
        const int mh = wid & 1, nh = wid >> 1;
        const int r16 = lane & 15, g = lane >> 4, q = (lane & 15) >> 2, p = lane & 3;
        const unsigned aoff0 = lds0 + LDS_A + (mh * 64 + r16) * 128 + ((g ^ ((r16 >> 1) & 7)) * 16);
        const unsigned brow = lds0 + LDS_B + (8 * g + q) * 448 + p * 8 + nh * 224;
        const unsigned gb = (g & 1) * 32;
        const unsigned bE = nh ? brow - gb : brow + gb, bO = nh ? brow + gb : brow - gb;
        unsigned aslot, bslot, cnt = nstep / 2 - 1;
        if (swap) asm volatile(G224_BODY_SWAP
            : "={v[0:15]}"(c[0]), "={v[16:31]}"(c[1]), "={v[32:47]}"(c[2]), "={v[48:63]}"(c[3]), "={v[64:79]}"(c[4]), "={v[80:95]}"(c[5]), "={v[96:111]}"(c[6]),
              [aslot] "=&s"(aslot), [bslot] "=&s"(bslot), [cnt] "+s"(cnt)
            : "{v200}"(aoff0), "{v201}"(aoff0 ^ 64), "{v202}"(bE), "{v203}"(bO) : G224_CLOB);
        else asm volatile(G224_BODY
            : "={v[0:15]}"(c[0]), "={v[16:31]}"(c[1]), "={v[32:47]}"(c[2]), "={v[48:63]}"(c[3]), "={v[64:79]}"(c[4]), "={v[80:95]}"(c[5]), "={v[96:111]}"(c[6]),
              [aslot] "=&s"(aslot), [bslot] "=&s"(bslot), [cnt] "+s"(cnt)
            : "{v200}"(aoff0), "{v201}"(aoff0 ^ 64), "{v202}"(bE), "{v203}"(bO) : G224_CLOB);
    }
}

DI bool last_arriver(LAS char* L, int k, unsigned n, int lane) {
    unsigned old = 0;
    if (lane == 0) old = __hip_atomic_fetch_add((LAS unsigned*)(L + LDS_CNT + k * 4), 1u, __ATOMIC_RELAXED, __HIP_MEMORY_SCOPE_WORKGROUP);
    return __builtin_amdgcn_readfirstlane(old) == n - 1;
}
struct FinalAddr { unsigned so[4]; char* gp[4]; };
DI FinalAddr final_addr(int lane, int m, char* outp  ) {
    FinalAddr f;
#pragma unroll
    for (int k = 0; k < 4; ++k) {
        const int idx = min(k * 64 + lane, 223), row = idx / 14, chn = idx - row * 14;
        f.so[k] = 32768 + m * 4096 + row * 240 + chn * 16;
        f.gp[k] = outp + (size_t)(m * 16 + row) * 1792 + chn * 16;
        asm volatile("" : "+v"(f.so[k]), "+v"(f.gp[k]));
    }
    return f;
}
DI void final_store(LAS char* L, int wid, int lane, f32x4 (&acc)[7], float sign, const FinalAddr& fa) {
    bar();
    const int m = wid & 3;
    if (wid >= 4) {
#pragma unroll
        for (int n = 0; n < 7; ++n) *(LAS f32x4*)(L + (m * 7 + n) * 1024 + lane * 16) = acc[n];
    }
    bar();
    if (wid < 4) {
        LAS char* stg = L + 32768 + m * 4096;
        const int t16 = lane & 15, g = lane >> 4;
        f32x4 o[7];
#pragma unroll
        for (int n = 0; n < 7; ++n) o[n] = *(const LAS f32x4*)(L + (m * 7 + n) * 1024 + lane * 16);
#pragma unroll
        for (int n = 0; n < 7; ++n) {
            v2u w;
            w[0] = cvtpk(acc[n][0] + sign * o[n][0], acc[n][1] + sign * o[n][1]);
            w[1] = cvtpk(acc[n][2] + sign * o[n][2], acc[n][3] + sign * o[n][3]);
            *(LAS v2u*)(stg + t16 * 240 + n * 32 + g * 8) = w;
        }
        asm volatile("s_waitcnt lgkmcnt(0)" ::: "memory");
        v4u v[4];
#pragma unroll
        for (int k = 0; k < 4; ++k) v[k] = *(const LAS v4u*)(L + fa.so[k]);
#pragma unroll
        for (int k = 0; k < 3; ++k) *(GAS v4u*)fa.gp[k] = v[k];
        if (lane < 32) *(GAS v4u*)fa.gp[3] = v[3];
    }
}

DI bf16x8 add_frag(v4u a, v4u b) {
    v4u r;
#pragma unroll
    for (int j = 0; j < 4; ++j) {
        const float lo = __uint_as_float(a[j] << 16) + __uint_as_float(b[j] << 16);
        const float hi = __uint_as_float(a[j] & 0xffff0000u) + __uint_as_float(b[j] & 0xffff0000u);
        r[j] = cvtpk(lo, hi);
    }
    return __builtin_bit_cast(bf16x8, r);
}

DI v4u pack_pair(const f32x16& ca, const f32x16& cb, int m) {
    unsigned a0 = cvtpk(ca[4 * m], ca[4 * m + 1]), a1 = cvtpk(ca[4 * m + 2], ca[4 * m + 3]);
    unsigned b0 = cvtpk(cb[4 * m], cb[4 * m + 1]), b1 = cvtpk(cb[4 * m + 2], cb[4 * m + 3]);
    auto s0 = __builtin_amdgcn_permlane32_swap(a0, b0, false, false);
    auto s1 = __builtin_amdgcn_permlane32_swap(a1, b1, false, false);
    auto t0 = __builtin_amdgcn_permlane16_swap(s0[0], s0[1], false, false);
    auto t1 = __builtin_amdgcn_permlane16_swap(s1[0], s1[1], false, false);
    return (v4u){t0[0], t1[0], t0[1], t1[1]};
}
DI v4u pack_single(const f32x16& ca, int m) {
    unsigned a0 = cvtpk(ca[4 * m], ca[4 * m + 1]), a1 = cvtpk(ca[4 * m + 2], ca[4 * m + 3]);
    auto t0 = __builtin_amdgcn_permlane16_swap(a0, a0, false, false);
    auto t1 = __builtin_amdgcn_permlane16_swap(a1, a1, false, false);
    return (v4u){t0[0], t1[0], t0[1], t1[1]};
}

constexpr unsigned W_POS[22] = {151552, 0, 7168, 94208, 101376, 14336, 21504, 108544, 115712, 122880, 130048, 28672, 35840, 137216, 144384, 65536, 72704,
                                43008, 50176, 57344, 79872, 87040};
constexpr int W_STEP[22] = {0, 0, 0, 1, 1, 2, 2, 2, 2, 3, 3, 4, 4, 4, 4, 5, 5, 6, 6, 6, 6, 6};
struct WTail {
    const char* wre; const char* wim; unsigned wvo, lds0;
    static constexpr int count(int w, int k) { int t = 0; for (int c = 0; c < 22; ++c) if ((c & 3) == w && W_STEP[c] == k) t += 7; return t; }
    DI void chunk(int u, int pq, unsigned pos) const {
        const char* src = (pq ? wim : wre) + u * 64;
#pragma unroll
        for (int k = 0; k < 7; ++k) glds16(src + k * 16 * 896, wvo, __builtin_amdgcn_readfirstlane(lds0 + pos + k * 1024));
    }
    template <int W, int K> DI void issue() const {
#pragma unroll
        for (int c = 0; c < 22; ++c) if ((c & 3) == W && W_STEP[c] == K) chunk(c >> 1, c & 1, W_POS[c]);
    }
};
DI void role_b(char* lds, int x, int i, int wid, int lane, const char* Ax, const char* Wqkv, const char* Wo, const char* Bx, const char* Cre, const char* Cim, const char* Wre, const char* Wim, char* out, char* ws, u64 tag STARG) {
    const unsigned lds0 = (unsigned)(uintptr_t)lds;
    LAS char* L = (LAS char*)lds;
    const int kh = x & 1, ch = i & 1, tb = 8 * (x >> 1) + (i >> 1), cb = 2 * kh + ch;
    const __amdgpu_buffer_rsrc_t rs = __builtin_amdgcn_make_buffer_rsrc(ws, 0, WS_END, 0x00020000);
    f32x16 c[7];
    STAMP(0); STAMPC(0);
    WTail wt;
    wt.wre = Wre + (size_t)cb * 112 * 896; wt.wim = Wim + (size_t)cb * 112 * 896; wt.lds0 = lds0;
    wt.wvo = (lane >> 2) * 896 + (((lane & 3) ^ ((((lane >> 5) ^ (lane >> 4)) & 1) * 2 + (lane >> 5))) * 16);
    g224(lds0, wid, lane, Cre + ((size_t)tb * 64 * 2048 + kh * 1024) * 2, Cim + ((size_t)tb * 64 * 2048 + kh * 1024) * 2, 4096,
         Bx + ((size_t)kh * 1024 * 448 + ch * 224) * 2, 896, 32, B1_ROT(x, i >> 2), false, c,
         [&](int s) { if (s == 0 && lane == 0) flag_store(ws, WS_ARR + blockIdx.x * 8, tag);
         }, wt);
    STAMP(1); STAMPC(1);
    if (wid < 4) {
        const int mh = wid & 1, nh = wid >> 1, g = lane >> 4;
        const unsigned base = WS_PQ + ((kh * 2 + mh) * 128 + tb * 4) * 14336 + (ch * 7) * 1024 + lane * 16;
#pragma unroll
        for (int m = 0; m < 4; ++m) {
            const unsigned bm = base + m * 14336;
            if (nh == 0) {
                st_sc1(rs, bm, pack_pair(c[0], c[1], m)); st_sc1(rs, bm + 1024, pack_pair(c[2], c[3], m)); st_sc1(rs, bm + 2048, pack_pair(c[4], c[5], m));
                const v4u s = pack_single(c[6], m);
                if ((g & 1) == 0) st_sc1(rs, bm + 3072 - lane * 16 + ((g >> 1) * 16 + (lane & 15)) * 16, s);
            } else {
                const v4u s = pack_single(c[0], m);
                if ((g & 1) == 0) st_sc1(rs, bm + 3072 - lane * 16 + ((2 + (g >> 1)) * 16 + (lane & 15)) * 16, s);
                st_sc1(rs, bm + 4096, pack_pair(c[1], c[2], m)); st_sc1(rs, bm + 5120, pack_pair(c[3], c[4], m)); st_sc1(rs, bm + 6144, pack_pair(c[5], c[6], m));
            }
        }
    }
    const int m = wid & 3, pq = wid >> 2;
    const int e16 = lane & 15, g = lane >> 4, rq = e16 >> 2;
    const unsigned wro = e16 * 64 + ((g ^ ((((rq >> 1) ^ rq) & 1) * 2 + (rq >> 1))) * 16);
    auto wpos = [&](int u) { const int uu = u < 11 ? u : u - 11; return pq ? W_POS[2 * uu + 1] : W_POS[2 * uu]; };
    bf16x8 w[3][7];
#pragma unroll
    for (int u = 0; u < 3; ++u)
#pragma unroll
        for (int n = 0; n < 7; ++n) w[u][n] = *(const LAS bf16x8*)(L + wro + wpos(u) + n * 1024);
    STAMP(9);
    bar();
    if (wid >= 4) {
        for (int s = wid - 4; s < 6; s += 4) wt.chunk(11 + (s >> 1), s & 1, s == 0 ? W_POS[0] : s == 1 ? W_POS[1] : s == 2 ? W_POS[2] : s == 3 ? W_POS[3] : s == 4 ? W_POS[4] : W_POS[5]);
        wait_vm0();
    } else {
        wait_vm0();
        STAMP(2);
        if (last_arriver(L, 0, 4, lane)) {
            if (lane == 0) flag_store(ws, WS_FP + (tb * 4 + cb) * 8, tag);
            STAMP(3);
            poll(ws, WS_FP + (tb * 4 + (lane & 3)) * 8, lane < 4, tag);
        }
    }
    STAMP(4);
    bar();
    STAMP(5);
    const unsigned pb = WS_PQ + (pq * 128 + tb * 4 + m) * 14336 + lane * 16;
    constexpr int PD = B2_PD;
    v4u p0[14], p1[14];
#pragma unroll
    for (int u = 0; u < PD; ++u) { p0[u] = ld_sc1(rs, pb + u * 1024); p1[u] = ld_sc1(rs, pb + 2 * 128 * 14336 + u * 1024); }
    asm volatile("" ::: "memory"); __builtin_amdgcn_sched_barrier(0);
    const FinalAddr fa = final_addr(lane, m, out + ((size_t)tb * 64 * 896 + 448 + cb * 112) * 2);
    f32x4 acc[7];
#pragma unroll
    for (int n = 0; n < 7; ++n) acc[n] = (f32x4){0.f, 0.f, 0.f, 0.f};
#pragma unroll
    for (int u = 0; u < 14; ++u) {
        if (u == 1) STAMP(10);
        if (u == 3) STAMP(11);
        if (u == 8) STAMP(12);
        if (u + PD < 14) { p0[u + PD] = ld_sc1(rs, pb + (u + PD) * 1024); p1[u + PD] = ld_sc1(rs, pb + 2 * 128 * 14336 + (u + PD) * 1024); __builtin_amdgcn_sched_barrier(0); }
        const bf16x8 f = add_frag(p0[u], p1[u]);
        __builtin_amdgcn_sched_barrier(0);
#pragma unroll
        for (int n = 0; n < 7; ++n) acc[n] = __builtin_amdgcn_mfma_f32_16x16x32_bf16(w[u % 3][n], f, acc[n], 0, 0, 0);
        if (u + 3 < 14) {
#pragma unroll
            for (int n = 0; n < 7; ++n) w[u % 3][n] = *(const LAS bf16x8*)(L + wro + wpos(u + 3) + n * 1024);
        }
        __builtin_amdgcn_sched_barrier(0);
    }
    STAMP(6);
    final_store(L, wid, lane, acc, -1.0f, fa);
    STAMP(7);
}

#define ASTAMP(id) do { } while (0)
template <class HOOK>
DI void attention(int iq, int head, int lane, __amdgpu_buffer_rsrc_t rs, HOOK after_loads STARG) {
    float SC = 0.18898223650461362f, L2E = 1.4426950408889634f;
    asm volatile("" : "+v"(SC), "+v"(L2E));
    const int a = lane & 31, hi = lane >> 5;
    const unsigned hb = head * 64 * 2048 + lane * 16;
    v4u kf[9][2], vf[9][2];
    const bf16x8 q0 = __builtin_bit_cast(bf16x8, ld_sc1(rs, WS_Q + hb + iq * 2048)), q1 = __builtin_bit_cast(bf16x8, ld_sc1(rs, WS_Q + hb + iq * 2048 + 1024));
#pragma unroll
    for (int jj = 0; jj < 9; ++jj) { const int j = jj == 0 ? 0 : jj == 1 ? 8 : jj - 1; const int kt = max(iq - 8 + j, 0);
        kf[j][0] = ld_sc1(rs, WS_K + hb + kt * 2048); kf[j][1] = ld_sc1(rs, WS_K + hb + kt * 2048 + 1024); }
    asm volatile("" ::: "memory"); __builtin_amdgcn_sched_barrier(0);
    after_loads();
    ASTAMP(9);
    __builtin_amdgcn_sched_barrier(0);
    const f32x16 zero = {};
    f32x16 S[8];
    {
        f32x16 so = __builtin_amdgcn_mfma_f32_32x32x16_bf16(__builtin_bit_cast(bf16x8, kf[0][0]), q0, zero, 0, 0, 0);
        so = __builtin_amdgcn_mfma_f32_32x32x16_bf16(__builtin_bit_cast(bf16x8, kf[0][1]), q1, so, 0, 0, 0);
        f32x16 sd = __builtin_amdgcn_mfma_f32_32x32x16_bf16(__builtin_bit_cast(bf16x8, kf[8][0]), q0, zero, 0, 0, 0);
        sd = __builtin_amdgcn_mfma_f32_32x32x16_bf16(__builtin_bit_cast(bf16x8, kf[8][1]), q1, sd, 0, 0, 0);
        if (iq < 8) {
            asm volatile("s_nop 7\n\ts_nop 7\n\ts_nop 7");
#pragma unroll
            for (int r = 0; r < 16; ++r) { float t; asm volatile("v_mov_b32 %0, 0xff800000" : "=v"(t)); so[r] = t; }
        }
#pragma unroll
        for (int r = 0; r < 16; ++r) { const int b = (r & 3) + 8 * (r >> 2) + 4 * hi; S[0][r] = (b <= a) ? sd[r] : so[r]; }
    }
    asm volatile("" :: "v"(S[0][0])); ASTAMP(10);
#pragma unroll
    for (int j = 1; j < 8; ++j) {
        f32x16 s = __builtin_amdgcn_mfma_f32_32x32x16_bf16(__builtin_bit_cast(bf16x8, kf[j][0]), q0, zero, 0, 0, 0);
        s = __builtin_amdgcn_mfma_f32_32x32x16_bf16(__builtin_bit_cast(bf16x8, kf[j][1]), q1, s, 0, 0, 0);
        if (iq - 8 + j < 0) {
            asm volatile("s_nop 7\n\ts_nop 7\n\ts_nop 7");
#pragma unroll
            for (int r = 0; r < 16; ++r) { float t; asm volatile("v_mov_b32 %0, 0xff800000" : "=v"(t)); s[r] = t; }
        }
        S[j] = s;
    }
    __builtin_amdgcn_sched_barrier(0);
#pragma unroll
    for (int jj = 0; jj < 9; ++jj) { const int j = jj == 0 ? 8 : jj - 1; const int kt = max(iq - 8 + j, 0);
        vf[j][0] = ld_sc1(rs, WS_V + hb + kt * 2048); vf[j][1] = ld_sc1(rs, WS_V + hb + kt * 2048 + 1024); }
    asm volatile("" ::: "memory"); __builtin_amdgcn_sched_barrier(0);
    float mx = S[0][0];
#pragma unroll
    for (int j = 0; j < 8; ++j)
#pragma unroll
        for (int r = 0; r < 16; ++r) mx = __builtin_fmaxf(mx, S[j][r]);
    { auto rr = __builtin_amdgcn_permlane32_swap(__float_as_uint(mx), __float_as_uint(mx), false, false);
      mx = __builtin_fmaxf(__uint_as_float(rr[0]), __uint_as_float(rr[1])); }
    const float nml = -(rbf(rbf(mx) * SC) * L2E);
    asm volatile("" :: "v"(nml)); ASTAMP(11);
    f32x16 O = zero;
#pragma unroll
    for (int j = 0; j < 8; ++j) {
        if (j == 4) { asm volatile("" :: "v"(O[0])); ASTAMP(12); }
        v4u pw[2];
#pragma unroll
        for (int k = 0; k < 8; ++k) {
            const float e0 = __builtin_amdgcn_exp2f(__builtin_fmaf(rbf(rbf(S[j][2 * k]) * SC), L2E, nml));
            const float e1 = __builtin_amdgcn_exp2f(__builtin_fmaf(rbf(rbf(S[j][2 * k + 1]) * SC), L2E, nml));
            pw[k >> 2][k & 3] = cvtpk(e0, e1);
        }
        if (j == 0) {
            v4u pd[2], po[2];
#pragma unroll
            for (int k = 0; k < 8; ++k) {
                const int b = ((2 * k) & 3) + 8 * ((2 * k) >> 2) + 4 * hi;
                const unsigned msk = (b <= a ? 0xffffu : 0u) | (b + 1 <= a ? 0xffff0000u : 0u);
                pd[k >> 2][k & 3] = pw[k >> 2][k & 3] & msk; po[k >> 2][k & 3] = pw[k >> 2][k & 3] & ~msk;
            }
            O = __builtin_amdgcn_mfma_f32_32x32x16_bf16(__builtin_bit_cast(bf16x8, vf[8][0]), __builtin_bit_cast(bf16x8, pd[0]), O, 0, 0, 0);
            O = __builtin_amdgcn_mfma_f32_32x32x16_bf16(__builtin_bit_cast(bf16x8, vf[8][1]), __builtin_bit_cast(bf16x8, pd[1]), O, 0, 0, 0);
            O = __builtin_amdgcn_mfma_f32_32x32x16_bf16(__builtin_bit_cast(bf16x8, vf[0][0]), __builtin_bit_cast(bf16x8, po[0]), O, 0, 0, 0);
            O = __builtin_amdgcn_mfma_f32_32x32x16_bf16(__builtin_bit_cast(bf16x8, vf[0][1]), __builtin_bit_cast(bf16x8, po[1]), O, 0, 0, 0);
        } else {
            O = __builtin_amdgcn_mfma_f32_32x32x16_bf16(__builtin_bit_cast(bf16x8, vf[j][0]), __builtin_bit_cast(bf16x8, pw[0]), O, 0, 0, 0);
            O = __builtin_amdgcn_mfma_f32_32x32x16_bf16(__builtin_bit_cast(bf16x8, vf[j][1]), __builtin_bit_cast(bf16x8, pw[1]), O, 0, 0, 0);
        }
    }
    auto lr = __builtin_amdgcn_permlane32_swap(__float_as_uint(O[12]), __float_as_uint(O[12]), false, false);
    const float rl = 1.0f / __uint_as_float(lr[1]);
    v4u o0, o1;
#pragma unroll
    for (int k = 0; k < 4; ++k) { o0[k] = cvtpk(O[2 * k] * rl, O[2 * k + 1] * rl); o1[k] = cvtpk(O[8 + 2 * k] * rl, O[8 + 2 * k + 1] * rl); }
    if (hi) { o1[2] = 0; o1[3] = 0; }
    const unsigned ob = WS_O + (head * 128 + iq * 2 + (a >> 4)) * 1024 + (hi * 16 + (a & 15)) * 16;
    st_sc1(rs, ob, o0); st_sc1(rs, ob + 512, o1);
}

DI void role_a(char* lds, int x, int ai, int wid, int lane, const char* Ax, const char* Wqkv, const char* Wo, const char* Bx, const char* Cre, const char* Cim, const char* Wre, const char* Wim, char* out, char* ws, u64 tag STARG) {
    const unsigned lds0 = (unsigned)(uintptr_t)lds;
    LAS char* L = (LAS char*)lds;
    const __amdgpu_buffer_rsrc_t rs = __builtin_amdgcn_make_buffer_rsrc(ws, 0, WS_END, 0x00020000);
    const int iq = 8 * x + (ai >> 1), hh = ai & 1, cb = (iq & 1) * 2 + hh, rp = iq >> 1;
    if (ai < 12) {
        const int rb = 4 * (x >> 1) + ai / 3, j = 3 * (x & 1) + ai % 3;
        const bool isv = j >= 4;
        f32x16 c[7];
        STAMP(0); STAMPC(0);
        unsigned ioff[7] = {};
        if (wid < 4) {
            const int mh = wid & 1, nh = (wid >> 1) & 1, l16 = lane & 15, g = lane >> 4;
#pragma unroll
            for (int n = 0; n < 7; ++n) {
                const int u4 = nh * 28 + 4 * n + g, h8 = u4 / 7, du = u4 - 7 * h8;
                const int col = nh * 112 + 16 * n + l16, hv = col / 28, d = col - 28 * hv;
                ioff[n] = lds0 + mh * 4096 + (isv ? hv * 8192 + ((g & 1) * 32 + d) * 16 + (g >> 1) * 8
                                                  : h8 * 8192 + (du >> 2) * 1024 + (((du >> 1) & 1) * 32 + l16) * 16 + (du & 1) * 8);
                asm volatile("" : "+v"(ioff[n]));
            }
        }
        g224(lds0, wid, lane, Ax + (size_t)rb * 128 * 896, Ax + ((size_t)rb * 128 + 64) * 896, 896, Wqkv + j * 448, 2688, 14, 0, isv, c,
             [&](int s) { if (s == 0 && lane == 0) flag_store(ws, WS_ARR + blockIdx.x * 8, tag);
             }, NoTail());
        STAMP(1); STAMPC(1);
        if (wid < 4) {
            if (isv) {
#pragma unroll
                for (int n = 0; n < 7; ++n)
#pragma unroll
                    for (int m = 0; m < 4; ++m) {
                        v2u w; w[0] = cvtpk(c[n][4 * m], c[n][4 * m + 1]); w[1] = cvtpk(c[n][4 * m + 2], c[n][4 * m + 3]);
                        *(LAS v2u*)(uintptr_t)(ioff[n] + (m >> 1) * 2048 + (m & 1) * 1024) = w;
                    }
            } else {
#pragma unroll
                for (int n = 0; n < 7; ++n)
#pragma unroll
                    for (int m = 0; m < 4; ++m) {
                        v2u w; w[0] = cvtpk(c[n][4 * m], c[n][4 * m + 1]); w[1] = cvtpk(c[n][4 * m + 2], c[n][4 * m + 3]);
                        *(LAS v2u*)(uintptr_t)(ioff[n] + (m >> 1) * 2048 + (m & 1) * 256) = w;
                    }
            }
        } else {
            const int t = (wid - 4) * 64 + lane;
            if (!isv) {
#pragma unroll
                for (int k = 0; k < 4; ++k) { const int e = t + 256 * k, h8 = e >> 7, tok = e & 127;
                    *(LAS v2u*)(L + h8 * 8192 + (tok >> 5) * 2048 + 1024 + (32 + (tok & 31)) * 16 + 8) = (v2u){0u, 0u}; }
            } else {
#pragma unroll
                for (int k = 0; k < 2; ++k) { const int e = t + 256 * k, dd = e & 3; const unsigned val = dd == 0 ? 0x3f803f80u : 0u;
                    *(LAS v4u*)(L + (e >> 6) * 8192 + ((e >> 4) & 3) * 2048 + ((e >> 3) & 1) * 1024 + (((e >> 2) & 1) * 32 + 28 + dd) * 16) = (v4u){val, val, val, val}; }
            }
        }
        STAMP(9);
        bar();
        STAMP(10);
        const unsigned gb = (j < 2 ? WS_Q : j < 4 ? WS_K : WS_V) + ((j & 1) * 8 * 64 + rb * 4) * 2048 + lane * 16;
        {
            v4u v[8];
#pragma unroll
            for (int k = 0; k < 8; ++k) v[k] = *(const LAS v4u*)(L + (wid * 8 + k) * 1024 + lane * 16);
#pragma unroll
            for (int k = 0; k < 8; ++k) st_sc1(rs, gb + wid * 64 * 2048 + k * 1024, v[k]);
        }
        STAMP(11);
        wait_vm0();
        STAMP(12);
        asm volatile("s_waitcnt lgkmcnt(0)" ::: "memory");
        if (last_arriver(L, 0, 8, lane) && lane == 0) flag_store(ws, WS_F1 + (rb * 8 + j) * 8, tag);
        bar();
        STAMP(2);
    }
    if (ai >= 12) {
        bar();
        const unsigned dummy = lds0 + LDS_DUMMY;
        const int gw = (ai - 12) * 8 + wid;
        if (gw < 8) {
            u64 cb0, ce0;
            asm volatile("s_getpc_b64 s[60:61]\n1:\n\ts_mov_b64 s[62:63], s[60:61]\n\ts_sub_u32 s60, s60, 1b-fused_code_begin\n\ts_subb_u32 s61, s61, 0\n\t"
                         "s_add_u32 s62, s62, fused_code_end-1b\n\ts_addc_u32 s63, s63, 0" : "={s[60:61]}"(cb0), "={s[62:63]}"(ce0) :: "scc");
            const u64 a = (cb0 & ~127ull) + (u64)(gw * 64 + lane) * 128;
            if (a < ce0) touch((const char*)a, dummy);
        }
        else if (gw < 12) { const int pg = (gw - 8) * 64 + lane; if (pg < 240) touch(ws + (size_t)pg * 65536, dummy); }
        else if (gw == 12) { if (lane < 56) touch(out + (size_t)lane * 65536, dummy); }
        else if (gw == 13) { touch((lane < 7 ? Wre : lane < 14 ? Wim : Wo) + (size_t)(lane < 7 ? lane : lane < 14 ? lane - 7 : min(lane - 14, 6)) * 65536, dummy); }
        const int Lr = (gw & 15) * 64 + lane, tbl = Lr >> 7;
        const char* p = (((Lr >> 6) & 1) ? Cim : Cre) + (size_t)((8 * (x >> 1) + tbl) * 64 + (Lr & 63)) * 4096 + (x & 1) * 2048;
        for (int a = ENG_FIRST + (gw >> 4); a < 16; a += 2) touch(p + ((a + B1_ROT(x, tbl)) & 15) * 128, dummy);
        if (wid == 7 && lane == 0) flag_store(ws, WS_ARR + blockIdx.x * 8, tag);
    }
    const int m = wid & 3, k2 = wid >> 2;
    unsigned ad[2][2][7];
    {
        auto wo_stage = [&]() {
            const unsigned wb = (cb * 224) & ~63u;
            const int rr = lane >> 4;
#pragma unroll
            for (int k = 0; k < 14; ++k) {
                const int r4 = wid * 14 + k;
                const int row = 4 * r4 + rr;
                const unsigned vo = rr * 896 + (((lane & 15) ^ ((row & 7) << 1)) * 16);
                glds16(Wo + (size_t)r4 * 4 * 896 + wb, vo, __builtin_amdgcn_readfirstlane(lds0 + r4 * 1024));
            }
        };
        wo_stage();
        const int rq = iq >> 2;
        const int sel = lane == 0 ? 0 : lane < 4 ? 1 : 2;
        const int rb = sel == 0 ? rq : rq - 2 + ((lane - 1) % 3);
        if (wid == 0) poll(ws, WS_F1 + (max(rb, 0) * 8 + sel * 2 + hh) * 8, lane < 7 && rb >= 0, tag);
        asm volatile("s_barrier" ::: "memory");
        STAMP(3);
        if (wid >= 4) __builtin_amdgcn_s_setprio(ATT_PRIO);
        attention(iq, hh * 8 + wid, lane, rs, [] {} STPASS);
        __builtin_amdgcn_s_setprio(0);
        STAMP(4);
        {
            const int g = lane >> 4, q = (lane & 15) >> 2, p = lane & 3;
            const int s0 = ((cb * 224) & 63) >> 5;
#pragma unroll
            for (int hp = 0; hp < 2; ++hp)
#pragma unroll
                for (int xx = 0; xx < 2; ++xx) {
                    int unit = 4 * (g >> 1) + 2 * xx + (g & 1); if (unit == 7) unit = 6;
                    const int rl = 4 * unit + q, sw = (4 * hp + rl) & 7;
#pragma unroll
                    for (int n = 0; n < 7; ++n) { ad[hp][xx][n] = lds0 + (k2 * 8 * 28 + hp * 28 + rl) * 256 + (((s0 + n) ^ sw) * 32) + p * 8; asm volatile("" : "+v"(ad[hp][xx][n])); }
                }
        }
        wait_vm0();
        if (last_arriver(L, 1, 8, lane)) {
            if (lane == 0) flag_store(ws, WS_FO + (iq * 2 + hh) * 8, tag);
            STAMP(5);
            poll(ws, WS_FO + (rp * 4 + (lane & 3)) * 8, lane < 4, tag);
        }
        bar();
        STAMP(6);
    }
    {
        v4u of[8];
#pragma unroll
        for (int h = 0; h < 8; ++h) of[h] = ld_sc1(rs, WS_O + ((k2 * 8 + h) * 128 + rp * 4 + m) * 1024 + lane * 16);
        asm volatile("" ::: "memory"); __builtin_amdgcn_sched_barrier(0);
        const FinalAddr fa = final_addr(lane, m, out + ((size_t)rp * 64 * 896 + cb * 112) * 2);
        f32x4 acc[7];
#pragma unroll
        for (int n = 0; n < 7; ++n) acc[n] = (f32x4){0.f, 0.f, 0.f, 0.f};
        auto wfrag = [&](int h, int n) {
            const s16x4 lo = trread((LAS const char*)(uintptr_t)(ad[h & 1][0][n] + (h >> 1) * 2 * 7168)), hi2 = trread((LAS const char*)(uintptr_t)(ad[h & 1][1][n] + (h >> 1) * 2 * 7168));
            return (bf16x8){lo[0], lo[1], lo[2], lo[3], hi2[0], hi2[1], hi2[2], hi2[3]};
        };
        bf16x8 w[2][7];
#pragma unroll
        for (int n = 0; n < 7; ++n) w[0][n] = wfrag(0, n);
#pragma unroll
        for (int h = 0; h < 8; ++h) {
            if (h + 1 < 8) {
#pragma unroll
                for (int n = 0; n < 7; ++n) w[(h + 1) & 1][n] = wfrag(h + 1, n);
            }
            __builtin_amdgcn_sched_barrier(0);
#pragma unroll
            for (int n = 0; n < 7; ++n) acc[n] = __builtin_amdgcn_mfma_f32_16x16x32_bf16(w[h & 1][n], __builtin_bit_cast(bf16x8, of[h]), acc[n], 0, 0, 0);
            __builtin_amdgcn_sched_barrier(0);
        }
        final_store(L, wid, lane, acc, 1.0f, fa);
        STAMP(7);
    }
}
}

__global__ void __launch_bounds__(512, 2)
fused_kernel(const char* Ax, const char* Wqkv, const char* Wo, const char* Bx, const char* Cre, const char* Cim, const char* Wre, const char* Wim, char* out, char* ws) {
    extern __shared__ __attribute__((aligned(16))) char lds[];
    asm volatile("fused_code_begin:");
    const int wid = __builtin_amdgcn_readfirstlane(threadIdx.x >> 6), lane = threadIdx.x & 63;
    const int x = blockIdx.x & 7, i = blockIdx.x >> 3;
    STAMP(8);
    const u64 tag = *(const GAS u64*)(ws + WS_EPOCH) + 1;
    if (threadIdx.x < 4) *(LAS unsigned*)((LAS char*)lds + LDS_CNT + threadIdx.x * 4) = 0u;
    if (i < 16) role_b(lds, x, i, wid, lane, Ax, Wqkv, Wo, Bx, Cre, Cim, Wre, Wim, out, ws, tag STPASS);
    else role_a(lds, x, i - 16, wid, lane, Ax, Wqkv, Wo, Bx, Cre, Cim, Wre, Wim, out, ws, tag STPASS);
    asm volatile("fused_code_end:");
    if (blockIdx.x == 0 && wid == 7) {
#pragma unroll
        for (int k = 0; k < 4; ++k) poll(ws, WS_ARR + (k * 64 + lane) * 8, true, tag);
        if (lane == 0) flag_store(ws, WS_EPOCH, tag);
    }
}

extern "C" void kernel_launch(void* const* d_in, const int* in_sizes, int n_in, void* d_out, int out_size, void* d_ws, size_t ws_size, hipStream_t stream) {
    (void)in_sizes; (void)n_in; (void)out_size; (void)ws_size;
    (void)hipFuncSetAttribute((const void*)fused_kernel, hipFuncAttributeMaxDynamicSharedMemorySize, LDS_BYTES);
    fused_kernel<<<256, 512, LDS_BYTES, stream>>>((const char*)d_in[0], (const char*)d_in[1], (const char*)d_in[2], (const char*)d_in[3], (const char*)d_in[4], (const char*)d_in[5],
                                                   (const char*)d_in[6], (const char*)d_in[7], (char*)d_out, (char*)d_ws);
}
```

```cpp
#include <hip/hip_runtime.h>
#include <hip/hip_bf16.h>
#include <stdint.h>

typedef short bf16x8 __attribute__((ext_vector_type(8)));
typedef short s16x4 __attribute__((ext_vector_type(4)));
typedef float f32x2 __attribute__((ext_vector_type(2)));
typedef float f32x4 __attribute__((ext_vector_type(4)));
typedef float f32x16 __attribute__((ext_vector_type(16)));
typedef __bf16 bf16x2 __attribute__((ext_vector_type(2)));
typedef unsigned v4u __attribute__((ext_vector_type(4)));
typedef unsigned v2u __attribute__((ext_vector_type(2)));
typedef unsigned long long u64;
#define LAS __attribute__((address_space(3)))
#define GAS __attribute__((address_space(1)))
#define DI __device__ __forceinline__

#define G224_BODY \
    "v_mov_b32 v0, 0\n\t" \
    "v_mov_b32 v1, 0\n\t" \
    "v_mov_b32 v2, 0\n\t" \
    "v_mov_b32 v3, 0\n\t" \
    "v_mov_b32 v4, 0\n\t" \
    "v_mov_b32 v5, 0\n\t" \
    "v_mov_b32 v6, 0\n\t" \
    "v_mov_b32 v7, 0\n\t" \
    "v_mov_b32 v8, 0\n\t" \
    "v_mov_b32 v9, 0\n\t" \
    "v_mov_b32 v10, 0\n\t" \
    "v_mov_b32 v11, 0\n\t" \
    "v_mov_b32 v12, 0\n\t" \
    "v_mov_b32 v13, 0\n\t" \
    "v_mov_b32 v14, 0\n\t" \
    "v_mov_b32 v15, 0\n\t" \
    "v_mov_b32 v16, 0\n\t" \
    "v_mov_b32 v17, 0\n\t" \
    "v_mov_b32 v18, 0\n\t" \
    "v_mov_b32 v19, 0\n\t" \
    "v_mov_b32 v20, 0\n\t" \
    "v_mov_b32 v21, 0\n\t" \
    "v_mov_b32 v22, 0\n\t" \
    "v_mov_b32 v23, 0\n\t" \
    "v_mov_b32 v24, 0\n\t" \
    "v_mov_b32 v25, 0\n\t" \
    "v_mov_b32 v26, 0\n\t" \
    "v_mov_b32 v27, 0\n\t" \
    "v_mov_b32 v28, 0\n\t" \
    "v_mov_b32 v29, 0\n\t" \
    "v_mov_b32 v30, 0\n\t" \
    "v_mov_b32 v31, 0\n\t" \
    "v_mov_b32 v32, 0\n\t" \
    "v_mov_b32 v33, 0\n\t" \
    "v_mov_b32 v34, 0\n\t" \
    "v_mov_b32 v35, 0\n\t" \
    "v_mov_b32 v36, 0\n\t" \
    "v_mov_b32 v37, 0\n\t" \
    "v_mov_b32 v38, 0\n\t" \
    "v_mov_b32 v39, 0\n\t" \
    "v_mov_b32 v40, 0\n\t" \
    "v_mov_b32 v41, 0\n\t" \
    "v_mov_b32 v42, 0\n\t" \
    "v_mov_b32 v43, 0\n\t" \
    "v_mov_b32 v44, 0\n\t" \
    "v_mov_b32 v45, 0\n\t" \
    "v_mov_b32 v46, 0\n\t" \
    "v_mov_b32 v47, 0\n\t" \
    "v_mov_b32 v48, 0\n\t" \
    "v_mov_b32 v49, 0\n\t" \
    "v_mov_b32 v50, 0\n\t" \
    "v_mov_b32 v51, 0\n\t" \
    "v_mov_b32 v52, 0\n\t" \
    "v_mov_b32 v53, 0\n\t" \
    "v_mov_b32 v54, 0\n\t" \
    "v_mov_b32 v55, 0\n\t" \
    "v_mov_b32 v56, 0\n\t" \
    "v_mov_b32 v57, 0\n\t" \
    "v_mov_b32 v58, 0\n\t" \
    "v_mov_b32 v59, 0\n\t" \
    "v_mov_b32 v60, 0\n\t" \
    "v_mov_b32 v61, 0\n\t" \
    "v_mov_b32 v62, 0\n\t" \
    "v_mov_b32 v63, 0\n\t" \
    "v_mov_b32 v64, 0\n\t" \
    "v_mov_b32 v65, 0\n\t" \
    "v_mov_b32 v66, 0\n\t" \
    "v_mov_b32 v67, 0\n\t" \
    "v_mov_b32 v68, 0\n\t" \
    "v_mov_b32 v69, 0\n\t" \
    "v_mov_b32 v70, 0\n\t" \
    "v_mov_b32 v71, 0\n\t" \
    "v_mov_b32 v72, 0\n\t" \
    "v_mov_b32 v73, 0\n\t" \
    "v_mov_b32 v74, 0\n\t" \
    "v_mov_b32 v75, 0\n\t" \
    "v_mov_b32 v76, 0\n\t" \
    "v_mov_b32 v77, 0\n\t" \
    "v_mov_b32 v78, 0\n\t" \
    "v_mov_b32 v79, 0\n\t" \
    "v_mov_b32 v80, 0\n\t" \
    "v_mov_b32 v81, 0\n\t" \
    "v_mov_b32 v82, 0\n\t" \
    "v_mov_b32 v83, 0\n\t" \
    "v_mov_b32 v84, 0\n\t" \
    "v_mov_b32 v85, 0\n\t" \
    "v_mov_b32 v86, 0\n\t" \
    "v_mov_b32 v87, 0\n\t" \
    "v_mov_b32 v88, 0\n\t" \
    "v_mov_b32 v89, 0\n\t" \
    "v_mov_b32 v90, 0\n\t" \
    "v_mov_b32 v91, 0\n\t" \
    "v_mov_b32 v92, 0\n\t" \
    "v_mov_b32 v93, 0\n\t" \
    "v_mov_b32 v94, 0\n\t" \
    "v_mov_b32 v95, 0\n\t" \
    "v_mov_b32 v96, 0\n\t" \
    "v_mov_b32 v97, 0\n\t" \
    "v_mov_b32 v98, 0\n\t" \
    "v_mov_b32 v99, 0\n\t" \
    "v_mov_b32 v100, 0\n\t" \
    "v_mov_b32 v101, 0\n\t" \
    "v_mov_b32 v102, 0\n\t" \
    "v_mov_b32 v103, 0\n\t" \
    "v_mov_b32 v104, 0\n\t" \
    "v_mov_b32 v105, 0\n\t" \
    "v_mov_b32 v106, 0\n\t" \
    "v_mov_b32 v107, 0\n\t" \
    "v_mov_b32 v108, 0\n\t" \
    "v_mov_b32 v109, 0\n\t" \
    "v_mov_b32 v110, 0\n\t" \
    "v_mov_b32 v111, 0\n\t" \
    "s_waitcnt lgkmcnt(0)\n\t" \
    "s_barrier\n\t" \
    "v_mov_b32 v207, v200\n\t" \
    "v_mov_b32 v208, v202\n\t" \
    "v_mov_b32 v209, v203\n\t" \
    "ds_read_b128 v[112:115], v207 offset:0\n\t" \
    "ds_read_b64_tr_b16 v[128:129], v208 offset:0\n\t" \
    "ds_read_b64_tr_b16 v[130:131], v208 offset:1792\n\t" \
    "ds_read_b128 v[116:119], v207 offset:2048\n\t" \
    "ds_read_b128 v[120:123], v207 offset:4096\n\t" \
    "ds_read_b128 v[124:127], v207 offset:6144\n\t" \
    "ds_read_b64_tr_b16 v[132:133], v209 offset:32\n\t" \
    "ds_read_b64_tr_b16 v[134:135], v209 offset:1824\n\t" \
    "ds_read_b64_tr_b16 v[136:137], v208 offset:64\n\t" \
    "ds_read_b64_tr_b16 v[138:139], v208 offset:1856\n\t" \
    "ds_read_b64_tr_b16 v[140:141], v209 offset:96\n\t" \
    "ds_read_b64_tr_b16 v[142:143], v209 offset:1888\n\t" \
    "ds_read_b64_tr_b16 v[144:145], v208 offset:128\n\t" \
    "ds_read_b64_tr_b16 v[146:147], v208 offset:1920\n\t" \
    "ds_read_b64_tr_b16 v[148:149], v209 offset:160\n\t" \
    "ds_read_b64_tr_b16 v[150:151], v209 offset:1952\n\t" \
    "ds_read_b64_tr_b16 v[152:153], v208 offset:192\n\t" \
    "ds_read_b64_tr_b16 v[154:155], v208 offset:1984\n\t" \
    "s_mov_b32 %[aslot], 0\n\t" \
    "s_mov_b32 %[bslot], 14336\n\t" \
    "v_mov_b32 v204, v201\n\t" \
    "v_add_u32 v205, %[bslot], v202\n\t" \
    "v_add_u32 v206, %[bslot], v203\n\t" \
    "s_waitcnt lgkmcnt(0)\n\t" \
    "s_barrier\n\t" \
    "1:\n\t" \
    "v_mfma_f32_16x16x32_bf16 v[0:3], v[128:131], v[112:115], v[0:3]\n\t" \
    "ds_read_b128 v[156:159], v204 offset:0\n\t" \
    "v_mfma_f32_16x16x32_bf16 v[4:7], v[128:131], v[116:119], v[4:7]\n\t" \
    "ds_read_b64_tr_b16 v[172:173], v205 offset:0\n\t" \
    "v_mfma_f32_16x16x32_bf16 v[8:11], v[128:131], v[120:123], v[8:11]\n\t" \
    "ds_read_b64_tr_b16 v[174:175], v205 offset:1792\n\t" \
    "v_mfma_f32_16x16x32_bf16 v[12:15], v[128:131], v[124:127], v[12:15]\n\t" \
    "ds_read_b128 v[160:163], v204 offset:2048\n\t" \
    "v_mfma_f32_16x16x32_bf16 v[16:19], v[132:135], v[112:115], v[16:19]\n\t" \
    "ds_read_b128 v[164:167], v204 offset:4096\n\t" \
    "v_mfma_f32_16x16x32_bf16 v[20:23], v[132:135], v[116:119], v[20:23]\n\t" \
    "ds_read_b128 v[168:171], v204 offset:6144\n\t" \
    "v_mfma_f32_16x16x32_bf16 v[24:27], v[132:135], v[120:123], v[24:27]\n\t" \
    "ds_read_b64_tr_b16 v[176:177], v206 offset:32\n\t" \
    "v_mfma_f32_16x16x32_bf16 v[28:31], v[132:135], v[124:127], v[28:31]\n\t" \
    "ds_read_b64_tr_b16 v[178:179], v206 offset:1824\n\t" \
    "v_mfma_f32_16x16x32_bf16 v[32:35], v[136:139], v[112:115], v[32:35]\n\t" \
    "ds_read_b64_tr_b16 v[180:181], v205 offset:64\n\t" \
    "v_mfma_f32_16x16x32_bf16 v[36:39], v[136:139], v[116:119], v[36:39]\n\t" \
    "ds_read_b64_tr_b16 v[182:183], v205 offset:1856\n\t" \
    "v_mfma_f32_16x16x32_bf16 v[40:43], v[136:139], v[120:123], v[40:43]\n\t" \
    "ds_read_b64_tr_b16 v[184:185], v206 offset:96\n\t" \
    "v_mfma_f32_16x16x32_bf16 v[44:47], v[136:139], v[124:127], v[44:47]\n\t" \
    "ds_read_b64_tr_b16 v[186:187], v206 offset:1888\n\t" \
    "v_mfma_f32_16x16x32_bf16 v[48:51], v[140:143], v[112:115], v[48:51]\n\t" \
    "ds_read_b64_tr_b16 v[188:189], v205 offset:128\n\t" \
    "v_mfma_f32_16x16x32_bf16 v[52:55], v[140:143], v[116:119], v[52:55]\n\t" \
    "ds_read_b64_tr_b16 v[190:191], v205 offset:1920\n\t" \
    "v_mfma_f32_16x16x32_bf16 v[56:59], v[140:143], v[120:123], v[56:59]\n\t" \
    "ds_read_b64_tr_b16 v[192:193], v206 offset:160\n\t" \
    "v_mfma_f32_16x16x32_bf16 v[60:63], v[140:143], v[124:127], v[60:63]\n\t" \
    "ds_read_b64_tr_b16 v[194:195], v206 offset:1952\n\t" \
    "v_mfma_f32_16x16x32_bf16 v[64:67], v[144:147], v[112:115], v[64:67]\n\t" \
    "ds_read_b64_tr_b16 v[196:197], v205 offset:192\n\t" \
    "v_mfma_f32_16x16x32_bf16 v[68:71], v[144:147], v[116:119], v[68:71]\n\t" \
    "ds_read_b64_tr_b16 v[198:199], v205 offset:1984\n\t" \
    "v_mfma_f32_16x16x32_bf16 v[72:75], v[144:147], v[120:123], v[72:75]\n\t" \
    "v_mfma_f32_16x16x32_bf16 v[76:79], v[144:147], v[124:127], v[76:79]\n\t" \
    "s_add_u32 %[aslot], %[aslot], 16384\n\t" \
    "v_mfma_f32_16x16x32_bf16 v[80:83], v[148:151], v[112:115], v[80:83]\n\t" \
    "s_and_b32 %[aslot], %[aslot], 0xffff\n\t" \
    "v_mfma_f32_16x16x32_bf16 v[84:87], v[148:151], v[116:119], v[84:87]\n\t" \
    "s_add_u32 %[bslot], %[bslot], 14336\n\t" \
    "v_mfma_f32_16x16x32_bf16 v[88:91], v[148:151], v[120:123], v[88:91]\n\t" \
    "s_cmp_eq_u32 %[bslot], 86016\n\t" \
    "v_mfma_f32_16x16x32_bf16 v[92:95], v[148:151], v[124:127], v[92:95]\n\t" \
    "s_cselect_b32 %[bslot], 0, %[bslot]\n\t" \
    "v_mfma_f32_16x16x32_bf16 v[96:99], v[152:155], v[112:115], v[96:99]\n\t" \
    "v_add_u32 v207, %[aslot], v200\n\t" \
    "v_mfma_f32_16x16x32_bf16 v[100:103], v[152:155], v[116:119], v[100:103]\n\t" \
    "v_add_u32 v208, %[bslot], v202\n\t" \
    "v_mfma_f32_16x16x32_bf16 v[104:107], v[152:155], v[120:123], v[104:107]\n\t" \
    "v_add_u32 v209, %[bslot], v203\n\t" \
    "v_mfma_f32_16x16x32_bf16 v[108:111], v[152:155], v[124:127], v[108:111]\n\t" \
    "s_waitcnt lgkmcnt(0)\n\t" \
    "s_barrier\n\t" \
    "v_mfma_f32_16x16x32_bf16 v[0:3], v[172:175], v[156:159], v[0:3]\n\t" \
    "ds_read_b128 v[112:115], v207 offset:0\n\t" \
    "v_mfma_f32_16x16x32_bf16 v[4:7], v[172:175], v[160:163], v[4:7]\n\t" \
    "ds_read_b64_tr_b16 v[128:129], v208 offset:0\n\t" \
    "v_mfma_f32_16x16x32_bf16 v[8:11], v[172:175], v[164:167], v[8:11]\n\t" \
    "ds_read_b64_tr_b16 v[130:131], v208 offset:1792\n\t" \
    "v_mfma_f32_16x16x32_bf16 v[12:15], v[172:175], v[168:171], v[12:15]\n\t" \
    "ds_read_b128 v[116:119], v207 offset:2048\n\t" \
    "v_mfma_f32_16x16x32_bf16 v[16:19], v[176:179], v[156:159], v[16:19]\n\t" \
    "ds_read_b128 v[120:123], v207 offset:4096\n\t" \
    "v_mfma_f32_16x16x32_bf16 v[20:23], v[176:179], v[160:163], v[20:23]\n\t" \
    "ds_read_b128 v[124:127], v207 offset:6144\n\t" \
    "v_mfma_f32_16x16x32_bf16 v[24:27], v[176:179], v[164:167], v[24:27]\n\t" \
    "ds_read_b64_tr_b16 v[132:133], v209 offset:32\n\t" \
    "v_mfma_f32_16x16x32_bf16 v[28:31], v[176:179], v[168:171], v[28:31]\n\t" \
    "ds_read_b64_tr_b16 v[134:135], v209 offset:1824\n\t" \
    "v_mfma_f32_16x16x32_bf16 v[32:35], v[180:183], v[156:159], v[32:35]\n\t" \
    "ds_read_b64_tr_b16 v[136:137], v208 offset:64\n\t" \
    "v_mfma_f32_16x16x32_bf16 v[36:39], v[180:183], v[160:163], v[36:39]\n\t" \
    "ds_read_b64_tr_b16 v[138:139], v208 offset:1856\n\t" \
    "v_mfma_f32_16x16x32_bf16 v[40:43], v[180:183], v[164:167], v[40:43]\n\t" \
    "ds_read_b64_tr_b16 v[140:141], v209 offset:96\n\t" \
    "v_mfma_f32_16x16x32_bf16 v[44:47], v[180:183], v[168:171], v[44:47]\n\t" \
    "ds_read_b64_tr_b16 v[142:143], v209 offset:1888\n\t" \
    "v_mfma_f32_16x16x32_bf16 v[48:51], v[184:187], v[156:159], v[48:51]\n\t" \
    "ds_read_b64_tr_b16 v[144:145], v208 offset:128\n\t" \
    "v_mfma_f32_16x16x32_bf16 v[52:55], v[184:187], v[160:163], v[52:55]\n\t" \
    "ds_read_b64_tr_b16 v[146:147], v208 offset:1920\n\t" \
    "v_mfma_f32_16x16x32_bf16 v[56:59], v[184:187], v[164:167], v[56:59]\n\t" \
    "ds_read_b64_tr_b16 v[148:149], v209 offset:160\n\t" \
    "v_mfma_f32_16x16x32_bf16 v[60:63], v[184:187], v[168:171], v[60:63]\n\t" \
    "ds_read_b64_tr_b16 v[150:151], v209 offset:1952\n\t" \
    "v_mfma_f32_16x16x32_bf16 v[64:67], v[188:191], v[156:159], v[64:67]\n\t" \
    "ds_read_b64_tr_b16 v[152:153], v208 offset:192\n\t" \
    "v_mfma_f32_16x16x32_bf16 v[68:71], v[188:191], v[160:163], v[68:71]\n\t" \
    "ds_read_b64_tr_b16 v[154:155], v208 offset:1984\n\t" \
    "v_mfma_f32_16x16x32_bf16 v[72:75], v[188:191], v[164:167], v[72:75]\n\t" \
    "v_mfma_f32_16x16x32_bf16 v[76:79], v[188:191], v[168:171], v[76:79]\n\t" \
    "s_add_u32 %[bslot], %[bslot], 14336\n\t" \
    "v_mfma_f32_16x16x32_bf16 v[80:83], v[192:195], v[156:159], v[80:83]\n\t" \
    "s_cmp_eq_u32 %[bslot], 86016\n\t" \
    "v_mfma_f32_16x16x32_bf16 v[84:87], v[192:195], v[160:163], v[84:87]\n\t" \
    "s_cselect_b32 %[bslot], 0, %[bslot]\n\t" \
    "v_mfma_f32_16x16x32_bf16 v[88:91], v[192:195], v[164:167], v[88:91]\n\t" \
    "v_add_u32 v204, %[aslot], v201\n\t" \
    "v_mfma_f32_16x16x32_bf16 v[92:95], v[192:195], v[168:171], v[92:95]\n\t" \
    "v_add_u32 v205, %[bslot], v202\n\t" \
    "v_mfma_f32_16x16x32_bf16 v[96:99], v[196:199], v[156:159], v[96:99]\n\t" \
    "v_add_u32 v206, %[bslot], v203\n\t" \
    "v_mfma_f32_16x16x32_bf16 v[100:103], v[196:199], v[160:163], v[100:103]\n\t" \
    "s_sub_u32 %[cnt], %[cnt], 1\n\t" \
    "v_mfma_f32_16x16x32_bf16 v[104:107], v[196:199], v[164:167], v[104:107]\n\t" \
    "s_cmp_lg_u32 %[cnt], 0\n\t" \
    "v_mfma_f32_16x16x32_bf16 v[108:111], v[196:199], v[168:171], v[108:111]\n\t" \
    "s_waitcnt lgkmcnt(0)\n\t" \
    "s_barrier\n\t" \
    "s_cbranch_scc1 1b\n\t" \
    "v_mfma_f32_16x16x32_bf16 v[0:3], v[128:131], v[112:115], v[0:3]\n\t" \
    "ds_read_b128 v[156:159], v204 offset:0\n\t" \
    "v_mfma_f32_16x16x32_bf16 v[4:7], v[128:131], v[116:119], v[4:7]\n\t" \
    "ds_read_b64_tr_b16 v[172:173], v205 offset:0\n\t" \
    "v_mfma_f32_16x16x32_bf16 v[8:11], v[128:131], v[120:123], v[8:11]\n\t" \
    "ds_read_b64_tr_b16 v[174:175], v205 offset:1792\n\t" \
    "v_mfma_f32_16x16x32_bf16 v[12:15], v[128:131], v[124:127], v[12:15]\n\t" \
    "ds_read_b128 v[160:163], v204 offset:2048\n\t" \
    "v_mfma_f32_16x16x32_bf16 v[16:19], v[132:135], v[112:115], v[16:19]\n\t" \
    "ds_read_b128 v[164:167], v204 offset:4096\n\t" \
    "v_mfma_f32_16x16x32_bf16 v[20:23], v[132:135], v[116:119], v[20:23]\n\t" \
    "ds_read_b128 v[168:171], v204 offset:6144\n\t" \
    "v_mfma_f32_16x16x32_bf16 v[24:27], v[132:135], v[120:123], v[24:27]\n\t" \
    "ds_read_b64_tr_b16 v[176:177], v206 offset:32\n\t" \
    "v_mfma_f32_16x16x32_bf16 v[28:31], v[132:135], v[124:127], v[28:31]\n\t" \
    "ds_read_b64_tr_b16 v[178:179], v206 offset:1824\n\t" \
    "v_mfma_f32_16x16x32_bf16 v[32:35], v[136:139], v[112:115], v[32:35]\n\t" \
    "ds_read_b64_tr_b16 v[180:181], v205 offset:64\n\t" \
    "v_mfma_f32_16x16x32_bf16 v[36:39], v[136:139], v[116:119], v[36:39]\n\t" \
    "ds_read_b64_tr_b16 v[182:183], v205 offset:1856\n\t" \
    "v_mfma_f32_16x16x32_bf16 v[40:43], v[136:139], v[120:123], v[40:43]\n\t" \
    "ds_read_b64_tr_b16 v[184:185], v206 offset:96\n\t" \
    "v_mfma_f32_16x16x32_bf16 v[44:47], v[136:139], v[124:127], v[44:47]\n\t" \
    "ds_read_b64_tr_b16 v[186:187], v206 offset:1888\n\t" \
    "v_mfma_f32_16x16x32_bf16 v[48:51], v[140:143], v[112:115], v[48:51]\n\t" \
    "ds_read_b64_tr_b16 v[188:189], v205 offset:128\n\t" \
    "v_mfma_f32_16x16x32_bf16 v[52:55], v[140:143], v[116:119], v[52:55]\n\t" \
    "ds_read_b64_tr_b16 v[190:191], v205 offset:1920\n\t" \
    "v_mfma_f32_16x16x32_bf16 v[56:59], v[140:143], v[120:123], v[56:59]\n\t" \
    "ds_read_b64_tr_b16 v[192:193], v206 offset:160\n\t" \
    "v_mfma_f32_16x16x32_bf16 v[60:63], v[140:143], v[124:127], v[60:63]\n\t" \
    "ds_read_b64_tr_b16 v[194:195], v206 offset:1952\n\t" \
    "v_mfma_f32_16x16x32_bf16 v[64:67], v[144:147], v[112:115], v[64:67]\n\t" \
    "ds_read_b64_tr_b16 v[196:197], v205 offset:192\n\t" \
    "v_mfma_f32_16x16x32_bf16 v[68:71], v[144:147], v[116:119], v[68:71]\n\t" \
    "ds_read_b64_tr_b16 v[198:199], v205 offset:1984\n\t" \
    "v_mfma_f32_16x16x32_bf16 v[72:75], v[144:147], v[120:123], v[72:75]\n\t" \
    "v_mfma_f32_16x16x32_bf16 v[76:79], v[144:147], v[124:127], v[76:79]\n\t" \
    "v_mfma_f32_16x16x32_bf16 v[80:83], v[148:151], v[112:115], v[80:83]\n\t" \
    "v_mfma_f32_16x16x32_bf16 v[84:87], v[148:151], v[116:119], v[84:87]\n\t" \
    "v_mfma_f32_16x16x32_bf16 v[88:91], v[148:151], v[120:123], v[88:91]\n\t" \
    "v_mfma_f32_16x16x32_bf16 v[92:95], v[148:151], v[124:127], v[92:95]\n\t" \
    "v_mfma_f32_16x16x32_bf16 v[96:99], v[152:155], v[112:115], v[96:99]\n\t" \
    "v_mfma_f32_16x16x32_bf16 v[100:103], v[152:155], v[116:119], v[100:103]\n\t" \
    "v_mfma_f32_16x16x32_bf16 v[104:107], v[152:155], v[120:123], v[104:107]\n\t" \
    "v_mfma_f32_16x16x32_bf16 v[108:111], v[152:155], v[124:127], v[108:111]\n\t" \
    "s_waitcnt lgkmcnt(0)\n\t" \
    "s_barrier\n\t" \
    "v_mfma_f32_16x16x32_bf16 v[0:3], v[172:175], v[156:159], v[0:3]\n\t" \
    "v_mfma_f32_16x16x32_bf16 v[4:7], v[172:175], v[160:163], v[4:7]\n\t" \
    "v_mfma_f32_16x16x32_bf16 v[8:11], v[172:175], v[164:167], v[8:11]\n\t" \
    "v_mfma_f32_16x16x32_bf16 v[12:15], v[172:175], v[168:171], v[12:15]\n\t" \
    "v_mfma_f32_16x16x32_bf16 v[16:19], v[176:179], v[156:159], v[16:19]\n\t" \
    "v_mfma_f32_16x16x32_bf16 v[20:23], v[176:179], v[160:163], v[20:23]\n\t" \
    "v_mfma_f32_16x16x32_bf16 v[24:27], v[176:179], v[164:167], v[24:27]\n\t" \
    "v_mfma_f32_16x16x32_bf16 v[28:31], v[176:179], v[168:171], v[28:31]\n\t" \
    "v_mfma_f32_16x16x32_bf16 v[32:35], v[180:183], v[156:159], v[32:35]\n\t" \
    "v_mfma_f32_16x16x32_bf16 v[36:39], v[180:183], v[160:163], v[36:39]\n\t" \
    "v_mfma_f32_16x16x32_bf16 v[40:43], v[180:183], v[164:167], v[40:43]\n\t" \
    "v_mfma_f32_16x16x32_bf16 v[44:47], v[180:183], v[168:171], v[44:47]\n\t" \
    "v_mfma_f32_16x16x32_bf16 v[48:51], v[184:187], v[156:159], v[48:51]\n\t" \
    "v_mfma_f32_16x16x32_bf16 v[52:55], v[184:187], v[160:163], v[52:55]\n\t" \
    "v_mfma_f32_16x16x32_bf16 v[56:59], v[184:187], v[164:167], v[56:59]\n\t" \
    "v_mfma_f32_16x16x32_bf16 v[60:63], v[184:187], v[168:171], v[60:63]\n\t" \
    "v_mfma_f32_16x16x32_bf16 v[64:67], v[188:191], v[156:159], v[64:67]\n\t" \
    "v_mfma_f32_16x16x32_bf16 v[68:71], v[188:191], v[160:163], v[68:71]\n\t" \
    "v_mfma_f32_16x16x32_bf16 v[72:75], v[188:191], v[164:167], v[72:75]\n\t" \
    "v_mfma_f32_16x16x32_bf16 v[76:79], v[188:191], v[168:171], v[76:79]\n\t" \
    "v_mfma_f32_16x16x32_bf16 v[80:83], v[192:195], v[156:159], v[80:83]\n\t" \
    "v_mfma_f32_16x16x32_bf16 v[84:87], v[192:195], v[160:163], v[84:87]\n\t" \
    "v_mfma_f32_16x16x32_bf16 v[88:91], v[192:195], v[164:167], v[88:91]\n\t" \
    "v_mfma_f32_16x16x32_bf16 v[92:95], v[192:195], v[168:171], v[92:95]\n\t" \
    "v_mfma_f32_16x16x32_bf16 v[96:99], v[196:199], v[156:159], v[96:99]\n\t" \
    "v_mfma_f32_16x16x32_bf16 v[100:103], v[196:199], v[160:163], v[100:103]\n\t" \
    "v_mfma_f32_16x16x32_bf16 v[104:107], v[196:199], v[164:167], v[104:107]\n\t" \
    "v_mfma_f32_16x16x32_bf16 v[108:111], v[196:199], v[168:171], v[108:111]\n\t" \
    "s_waitcnt lgkmcnt(0)\n\t" \
    "s_barrier\n\t" \
    "s_nop 7\n\t" \
    "s_nop 7\n\t" \
    ""
#define G224_BODY_SWAP \
    "v_mov_b32 v0, 0\n\t" \
    "v_mov_b32 v1, 0\n\t" \
    "v_mov_b32 v2, 0\n\t" \
    "v_mov_b32 v3, 0\n\t" \
    "v_mov_b32 v4, 0\n\t" \
    "v_mov_b32 v5, 0\n\t" \
    "v_mov_b32 v6, 0\n\t" \
    "v_mov_b32 v7, 0\n\t" \
    "v_mov_b32 v8, 0\n\t" \
    "v_mov_b32 v9, 0\n\t" \
    "v_mov_b32 v10, 0\n\t" \
    "v_mov_b32 v11, 0\n\t" \
    "v_mov_b32 v12, 0\n\t" \
    "v_mov_b32 v13, 0\n\t" \
    "v_mov_b32 v14, 0\n\t" \
    "v_mov_b32 v15, 0\n\t" \
    "v_mov_b32 v16, 0\n\t" \
    "v_mov_b32 v17, 0\n\t" \
    "v_mov_b32 v18, 0\n\t" \
    "v_mov_b32 v19, 0\n\t" \
    "v_mov_b32 v20, 0\n\t" \
    "v_mov_b32 v21, 0\n\t" \
    "v_mov_b32 v22, 0\n\t" \
    "v_mov_b32 v23, 0\n\t" \
    "v_mov_b32 v24, 0\n\t" \
    "v_mov_b32 v25, 0\n\t" \
    "v_mov_b32 v26, 0\n\t" \
    "v_mov_b32 v27, 0\n\t" \
    "v_mov_b32 v28, 0\n\t" \
    "v_mov_b32 v29, 0\n\t" \
    "v_mov_b32 v30, 0\n\t" \
    "v_mov_b32 v31, 0\n\t" \
    "v_mov_b32 v32, 0\n\t" \
    "v_mov_b32 v33, 0\n\t" \
    "v_mov_b32 v34, 0\n\t" \
    "v_mov_b32 v35, 0\n\t" \
    "v_mov_b32 v36, 0\n\t" \
    "v_mov_b32 v37, 0\n\t" \
    "v_mov_b32 v38, 0\n\t" \
    "v_mov_b32 v39, 0\n\t" \
    "v_mov_b32 v40, 0\n\t" \
    "v_mov_b32 v41, 0\n\t" \
    "v_mov_b32 v42, 0\n\t" \
    "v_mov_b32 v43, 0\n\t" \
    "v_mov_b32 v44, 0\n\t" \
    "v_mov_b32 v45, 0\n\t" \
    "v_mov_b32 v46, 0\n\t" \
    "v_mov_b32 v47, 0\n\t" \
    "v_mov_b32 v48, 0\n\t" \
    "v_mov_b32 v49, 0\n\t" \
    "v_mov_b32 v50, 0\n\t" \
    "v_mov_b32 v51, 0\n\t" \
    "v_mov_b32 v52, 0\n\t" \
    "v_mov_b32 v53, 0\n\t" \
    "v_mov_b32 v54, 0\n\t" \
    "v_mov_b32 v55, 0\n\t" \
    "v_mov_b32 v56, 0\n\t" \
    "v_mov_b32 v57, 0\n\t" \
    "v_mov_b32 v58, 0\n\t" \
    "v_mov_b32 v59, 0\n\t" \
    "v_mov_b32 v60, 0\n\t" \
    "v_mov_b32 v61, 0\n\t" \
    "v_mov_b32 v62, 0\n\t" \
    "v_mov_b32 v63, 0\n\t" \
    "v_mov_b32 v64, 0\n\t" \
    "v_mov_b32 v65, 0\n\t" \
    "v_mov_b32 v66, 0\n\t" \
    "v_mov_b32 v67, 0\n\t" \
    "v_mov_b32 v68, 0\n\t" \
    "v_mov_b32 v69, 0\n\t" \
    "v_mov_b32 v70, 0\n\t" \
    "v_mov_b32 v71, 0\n\t" \
    "v_mov_b32 v72, 0\n\t" \
    "v_mov_b32 v73, 0\n\t" \
    "v_mov_b32 v74, 0\n\t" \
    "v_mov_b32 v75, 0\n\t" \
    "v_mov_b32 v76, 0\n\t" \
    "v_mov_b32 v77, 0\n\t" \
    "v_mov_b32 v78, 0\n\t" \
    "v_mov_b32 v79, 0\n\t" \
    "v_mov_b32 v80, 0\n\t" \
    "v_mov_b32 v81, 0\n\t" \
    "v_mov_b32 v82, 0\n\t" \
    "v_mov_b32 v83, 0\n\t" \
    "v_mov_b32 v84, 0\n\t" \
    "v_mov_b32 v85, 0\n\t" \
    "v_mov_b32 v86, 0\n\t" \
    "v_mov_b32 v87, 0\n\t" \
    "v_mov_b32 v88, 0\n\t" \
    "v_mov_b32 v89, 0\n\t" \
    "v_mov_b32 v90, 0\n\t" \
    "v_mov_b32 v91, 0\n\t" \
    "v_mov_b32 v92, 0\n\t" \
    "v_mov_b32 v93, 0\n\t" \
    "v_mov_b32 v94, 0\n\t" \
    "v_mov_b32 v95, 0\n\t" \
    "v_mov_b32 v96, 0\n\t" \
    "v_mov_b32 v97, 0\n\t" \
    "v_mov_b32 v98, 0\n\t" \
    "v_mov_b32 v99, 0\n\t" \
    "v_mov_b32 v100, 0\n\t" \
    "v_mov_b32 v101, 0\n\t" \
    "v_mov_b32 v102, 0\n\t" \
    "v_mov_b32 v103, 0\n\t" \
    "v_mov_b32 v104, 0\n\t" \
    "v_mov_b32 v105, 0\n\t" \
    "v_mov_b32 v106, 0\n\t" \
    "v_mov_b32 v107, 0\n\t" \
    "v_mov_b32 v108, 0\n\t" \
    "v_mov_b32 v109, 0\n\t" \
    "v_mov_b32 v110, 0\n\t" \
    "v_mov_b32 v111, 0\n\t" \
    "s_waitcnt lgkmcnt(0)\n\t" \
    "s_barrier\n\t" \
    "v_mov_b32 v207, v200\n\t" \
    "v_mov_b32 v208, v202\n\t" \
    "v_mov_b32 v209, v203\n\t" \
    "ds_read_b128 v[112:115], v207 offset:0\n\t" \
    "ds_read_b64_tr_b16 v[128:129], v208 offset:0\n\t" \
    "ds_read_b64_tr_b16 v[130:131], v208 offset:1792\n\t" \
    "ds_read_b128 v[116:119], v207 offset:2048\n\t" \
    "ds_read_b128 v[120:123], v207 offset:4096\n\t" \
    "ds_read_b128 v[124:127], v207 offset:6144\n\t" \
    "ds_read_b64_tr_b16 v[132:133], v209 offset:32\n\t" \
    "ds_read_b64_tr_b16 v[134:135], v209 offset:1824\n\t" \
    "ds_read_b64_tr_b16 v[136:137], v208 offset:64\n\t" \
    "ds_read_b64_tr_b16 v[138:139], v208 offset:1856\n\t" \
    "ds_read_b64_tr_b16 v[140:141], v209 offset:96\n\t" \
    "ds_read_b64_tr_b16 v[142:143], v209 offset:1888\n\t" \
    "ds_read_b64_tr_b16 v[144:145], v208 offset:128\n\t" \
    "ds_read_b64_tr_b16 v[146:147], v208 offset:1920\n\t" \
    "ds_read_b64_tr_b16 v[148:149], v209 offset:160\n\t" \
    "ds_read_b64_tr_b16 v[150:151], v209 offset:1952\n\t" \
    "ds_read_b64_tr_b16 v[152:153], v208 offset:192\n\t" \
    "ds_read_b64_tr_b16 v[154:155], v208 offset:1984\n\t" \
    "s_mov_b32 %[aslot], 0\n\t" \
    "s_mov_b32 %[bslot], 14336\n\t" \
    "v_mov_b32 v204, v201\n\t" \
    "v_add_u32 v205, %[bslot], v202\n\t" \
    "v_add_u32 v206, %[bslot], v203\n\t" \
    "s_waitcnt lgkmcnt(0)\n\t" \
    "s_barrier\n\t" \
    "1:\n\t" \
    "v_mfma_f32_16x16x32_bf16 v[0:3], v[112:115], v[128:131], v[0:3]\n\t" \
    "ds_read_b128 v[156:159], v204 offset:0\n\t" \
    "v_mfma_f32_16x16x32_bf16 v[4:7], v[116:119], v[128:131], v[4:7]\n\t" \
    "ds_read_b64_tr_b16 v[172:173], v205 offset:0\n\t" \
    "v_mfma_f32_16x16x32_bf16 v[8:11], v[120:123], v[128:131], v[8:11]\n\t" \
    "ds_read_b64_tr_b16 v[174:175], v205 offset:1792\n\t" \
    "v_mfma_f32_16x16x32_bf16 v[12:15], v[124:127], v[128:131], v[12:15]\n\t" \
    "ds_read_b128 v[160:163], v204 offset:2048\n\t" \
    "v_mfma_f32_16x16x32_bf16 v[16:19], v[112:115], v[132:135], v[16:19]\n\t" \
    "ds_read_b128 v[164:167], v204 offset:4096\n\t" \
    "v_mfma_f32_16x16x32_bf16 v[20:23], v[116:119], v[132:135], v[20:23]\n\t" \
    "ds_read_b128 v[168:171], v204 offset:6144\n\t" \
    "v_mfma_f32_16x16x32_bf16 v[24:27], v[120:123], v[132:135], v[24:27]\n\t" \
    "ds_read_b64_tr_b16 v[176:177], v206 offset:32\n\t" \
    "v_mfma_f32_16x16x32_bf16 v[28:31], v[124:127], v[132:135], v[28:31]\n\t" \
    "ds_read_b64_tr_b16 v[178:179], v206 offset:1824\n\t" \
    "v_mfma_f32_16x16x32_bf16 v[32:35], v[112:115], v[136:139], v[32:35]\n\t" \
    "ds_read_b64_tr_b16 v[180:181], v205 offset:64\n\t" \
    "v_mfma_f32_16x16x32_bf16 v[36:39], v[116:119], v[136:139], v[36:39]\n\t" \
    "ds_read_b64_tr_b16 v[182:183], v205 offset:1856\n\t" \
    "v_mfma_f32_16x16x32_bf16 v[40:43], v[120:123], v[136:139], v[40:43]\n\t" \
    "ds_read_b64_tr_b16 v[184:185], v206 offset:96\n\t" \
    "v_mfma_f32_16x16x32_bf16 v[44:47], v[124:127], v[136:139], v[44:47]\n\t" \
    "ds_read_b64_tr_b16 v[186:187], v206 offset:1888\n\t" \
    "v_mfma_f32_16x16x32_bf16 v[48:51], v[112:115], v[140:143], v[48:51]\n\t" \
    "ds_read_b64_tr_b16 v[188:189], v205 offset:128\n\t" \
    "v_mfma_f32_16x16x32_bf16 v[52:55], v[116:119], v[140:143], v[52:55]\n\t" \
    "ds_read_b64_tr_b16 v[190:191], v205 offset:1920\n\t" \
    "v_mfma_f32_16x16x32_bf16 v[56:59], v[120:123], v[140:143], v[56:59]\n\t" \
    "ds_read_b64_tr_b16 v[192:193], v206 offset:160\n\t" \
    "v_mfma_f32_16x16x32_bf16 v[60:63], v[124:127], v[140:143], v[60:63]\n\t" \
    "ds_read_b64_tr_b16 v[194:195], v206 offset:1952\n\t" \
    "v_mfma_f32_16x16x32_bf16 v[64:67], v[112:115], v[144:147], v[64:67]\n\t" \
    "ds_read_b64_tr_b16 v[196:197], v205 offset:192\n\t" \
    "v_mfma_f32_16x16x32_bf16 v[68:71], v[116:119], v[144:147], v[68:71]\n\t" \
    "ds_read_b64_tr_b16 v[198:199], v205 offset:1984\n\t" \
    "v_mfma_f32_16x16x32_bf16 v[72:75], v[120:123], v[144:147], v[72:75]\n\t" \
    "v_mfma_f32_16x16x32_bf16 v[76:79], v[124:127], v[144:147], v[76:79]\n\t" \
    "s_add_u32 %[aslot], %[aslot], 16384\n\t" \
    "v_mfma_f32_16x16x32_bf16 v[80:83], v[112:115], v[148:151], v[80:83]\n\t" \
    "s_and_b32 %[aslot], %[aslot], 0xffff\n\t" \
    "v_mfma_f32_16x16x32_bf16 v[84:87], v[116:119], v[148:151], v[84:87]\n\t" \
    "s_add_u32 %[bslot], %[bslot], 14336\n\t" \
    "v_mfma_f32_16x16x32_bf16 v[88:91], v[120:123], v[148:151], v[88:91]\n\t" \
    "s_cmp_eq_u32 %[bslot], 86016\n\t" \
    "v_mfma_f32_16x16x32_bf16 v[92:95], v[124:127], v[148:151], v[92:95]\n\t" \
    "s_cselect_b32 %[bslot], 0, %[bslot]\n\t" \
    "v_mfma_f32_16x16x32_bf16 v[96:99], v[112:115], v[152:155], v[96:99]\n\t" \
    "v_add_u32 v207, %[aslot], v200\n\t" \
    "v_mfma_f32_16x16x32_bf16 v[100:103], v[116:119], v[152:155], v[100:103]\n\t" \
    "v_add_u32 v208, %[bslot], v202\n\t" \
    "v_mfma_f32_16x16x32_bf16 v[104:107], v[120:123], v[152:155], v[104:107]\n\t" \
    "v_add_u32 v209, %[bslot], v203\n\t" \
    "v_mfma_f32_16x16x32_bf16 v[108:111], v[124:127], v[152:155], v[108:111]\n\t" \
    "s_waitcnt lgkmcnt(0)\n\t" \
    "s_barrier\n\t" \
    "v_mfma_f32_16x16x32_bf16 v[0:3], v[156:159], v[172:175], v[0:3]\n\t" \
    "ds_read_b128 v[112:115], v207 offset:0\n\t" \
    "v_mfma_f32_16x16x32_bf16 v[4:7], v[160:163], v[172:175], v[4:7]\n\t" \
    "ds_read_b64_tr_b16 v[128:129], v208 offset:0\n\t" \
    "v_mfma_f32_16x16x32_bf16 v[8:11], v[164:167], v[172:175], v[8:11]\n\t" \
    "ds_read_b64_tr_b16 v[130:131], v208 offset:1792\n\t" \
    "v_mfma_f32_16x16x32_bf16 v[12:15], v[168:171], v[172:175], v[12:15]\n\t" \
    "ds_read_b128 v[116:119], v207 offset:2048\n\t" \
    "v_mfma_f32_16x16x32_bf16 v[16:19], v[156:159], v[176:179], v[16:19]\n\t" \
    "ds_read_b128 v[120:123], v207 offset:4096\n\t" \
    "v_mfma_f32_16x16x32_bf16 v[20:23], v[160:163], v[176:179], v[20:23]\n\t" \
    "ds_read_b128 v[124:127], v207 offset:6144\n\t" \
    "v_mfma_f32_16x16x32_bf16 v[24:27], v[164:167], v[176:179], v[24:27]\n\t" \
    "ds_read_b64_tr_b16 v[132:133], v209 offset:32\n\t" \
    "v_mfma_f32_16x16x32_bf16 v[28:31], v[168:171], v[176:179], v[28:31]\n\t" \
    "ds_read_b64_tr_b16 v[134:135], v209 offset:1824\n\t" \
    "v_mfma_f32_16x16x32_bf16 v[32:35], v[156:159], v[180:183], v[32:35]\n\t" \
    "ds_read_b64_tr_b16 v[136:137], v208 offset:64\n\t" \
    "v_mfma_f32_16x16x32_bf16 v[36:39], v[160:163], v[180:183], v[36:39]\n\t" \
    "ds_read_b64_tr_b16 v[138:139], v208 offset:1856\n\t" \
    "v_mfma_f32_16x16x32_bf16 v[40:43], v[164:167], v[180:183], v[40:43]\n\t" \
    "ds_read_b64_tr_b16 v[140:141], v209 offset:96\n\t" \
    "v_mfma_f32_16x16x32_bf16 v[44:47], v[168:171], v[180:183], v[44:47]\n\t" \
    "ds_read_b64_tr_b16 v[142:143], v209 offset:1888\n\t" \
    "v_mfma_f32_16x16x32_bf16 v[48:51], v[156:159], v[184:187], v[48:51]\n\t" \
    "ds_read_b64_tr_b16 v[144:145], v208 offset:128\n\t" \
    "v_mfma_f32_16x16x32_bf16 v[52:55], v[160:163], v[184:187], v[52:55]\n\t" \
    "ds_read_b64_tr_b16 v[146:147], v208 offset:1920\n\t" \
    "v_mfma_f32_16x16x32_bf16 v[56:59], v[164:167], v[184:187], v[56:59]\n\t" \
    "ds_read_b64_tr_b16 v[148:149], v209 offset:160\n\t" \
    "v_mfma_f32_16x16x32_bf16 v[60:63], v[168:171], v[184:187], v[60:63]\n\t" \
    "ds_read_b64_tr_b16 v[150:151], v209 offset:1952\n\t" \
    "v_mfma_f32_16x16x32_bf16 v[64:67], v[156:159], v[188:191], v[64:67]\n\t" \
    "ds_read_b64_tr_b16 v[152:153], v208 offset:192\n\t" \
    "v_mfma_f32_16x16x32_bf16 v[68:71], v[160:163], v[188:191], v[68:71]\n\t" \
    "ds_read_b64_tr_b16 v[154:155], v208 offset:1984\n\t" \
    "v_mfma_f32_16x16x32_bf16 v[72:75], v[164:167], v[188:191], v[72:75]\n\t" \
    "v_mfma_f32_16x16x32_bf16 v[76:79], v[168:171], v[188:191], v[76:79]\n\t" \
    "s_add_u32 %[bslot], %[bslot], 14336\n\t" \
    "v_mfma_f32_16x16x32_bf16 v[80:83], v[156:159], v[192:195], v[80:83]\n\t" \
    "s_cmp_eq_u32 %[bslot], 86016\n\t" \
    "v_mfma_f32_16x16x32_bf16 v[84:87], v[160:163], v[192:195], v[84:87]\n\t" \
    "s_cselect_b32 %[bslot], 0, %[bslot]\n\t" \
    "v_mfma_f32_16x16x32_bf16 v[88:91], v[164:167], v[192:195], v[88:91]\n\t" \
    "v_add_u32 v204, %[aslot], v201\n\t" \
    "v_mfma_f32_16x16x32_bf16 v[92:95], v[168:171], v[192:195], v[92:95]\n\t" \
    "v_add_u32 v205, %[bslot], v202\n\t" \
    "v_mfma_f32_16x16x32_bf16 v[96:99], v[156:159], v[196:199], v[96:99]\n\t" \
    "v_add_u32 v206, %[bslot], v203\n\t" \
    "v_mfma_f32_16x16x32_bf16 v[100:103], v[160:163], v[196:199], v[100:103]\n\t" \
    "s_sub_u32 %[cnt], %[cnt], 1\n\t" \
    "v_mfma_f32_16x16x32_bf16 v[104:107], v[164:167], v[196:199], v[104:107]\n\t" \
    "s_cmp_lg_u32 %[cnt], 0\n\t" \
    "v_mfma_f32_16x16x32_bf16 v[108:111], v[168:171], v[196:199], v[108:111]\n\t" \
    "s_waitcnt lgkmcnt(0)\n\t" \
    "s_barrier\n\t" \
    "s_cbranch_scc1 1b\n\t" \
    "v_mfma_f32_16x16x32_bf16 v[0:3], v[112:115], v[128:131], v[0:3]\n\t" \
    "ds_read_b128 v[156:159], v204 offset:0\n\t" \
    "v_mfma_f32_16x16x32_bf16 v[4:7], v[116:119], v[128:131], v[4:7]\n\t" \
    "ds_read_b64_tr_b16 v[172:173], v205 offset:0\n\t" \
    "v_mfma_f32_16x16x32_bf16 v[8:11], v[120:123], v[128:131], v[8:11]\n\t" \
    "ds_read_b64_tr_b16 v[174:175], v205 offset:1792\n\t" \
    "v_mfma_f32_16x16x32_bf16 v[12:15], v[124:127], v[128:131], v[12:15]\n\t" \
    "ds_read_b128 v[160:163], v204 offset:2048\n\t" \
    "v_mfma_f32_16x16x32_bf16 v[16:19], v[112:115], v[132:135], v[16:19]\n\t" \
    "ds_read_b128 v[164:167], v204 offset:4096\n\t" \
    "v_mfma_f32_16x16x32_bf16 v[20:23], v[116:119], v[132:135], v[20:23]\n\t" \
    "ds_read_b128 v[168:171], v204 offset:6144\n\t" \
    "v_mfma_f32_16x16x32_bf16 v[24:27], v[120:123], v[132:135], v[24:27]\n\t" \
    "ds_read_b64_tr_b16 v[176:177], v206 offset:32\n\t" \
    "v_mfma_f32_16x16x32_bf16 v[28:31], v[124:127], v[132:135], v[28:31]\n\t" \
    "ds_read_b64_tr_b16 v[178:179], v206 offset:1824\n\t" \
    "v_mfma_f32_16x16x32_bf16 v[32:35], v[112:115], v[136:139], v[32:35]\n\t" \
    "ds_read_b64_tr_b16 v[180:181], v205 offset:64\n\t" \
    "v_mfma_f32_16x16x32_bf16 v[36:39], v[116:119], v[136:139], v[36:39]\n\t" \
    "ds_read_b64_tr_b16 v[182:183], v205 offset:1856\n\t" \
    "v_mfma_f32_16x16x32_bf16 v[40:43], v[120:123], v[136:139], v[40:43]\n\t" \
    "ds_read_b64_tr_b16 v[184:185], v206 offset:96\n\t" \
    "v_mfma_f32_16x16x32_bf16 v[44:47], v[124:127], v[136:139], v[44:47]\n\t" \
    "ds_read_b64_tr_b16 v[186:187], v206 offset:1888\n\t" \
    "v_mfma_f32_16x16x32_bf16 v[48:51], v[112:115], v[140:143], v[48:51]\n\t" \
    "ds_read_b64_tr_b16 v[188:189], v205 offset:128\n\t" \
    "v_mfma_f32_16x16x32_bf16 v[52:55], v[116:119], v[140:143], v[52:55]\n\t" \
    "ds_read_b64_tr_b16 v[190:191], v205 offset:1920\n\t" \
    "v_mfma_f32_16x16x32_bf16 v[56:59], v[120:123], v[140:143], v[56:59]\n\t" \
    "ds_read_b64_tr_b16 v[192:193], v206 offset:160\n\t" \
    "v_mfma_f32_16x16x32_bf16 v[60:63], v[124:127], v[140:143], v[60:63]\n\t" \
    "ds_read_b64_tr_b16 v[194:195], v206 offset:1952\n\t" \
    "v_mfma_f32_16x16x32_bf16 v[64:67], v[112:115], v[144:147], v[64:67]\n\t" \
    "ds_read_b64_tr_b16 v[196:197], v205 offset:192\n\t" \
    "v_mfma_f32_16x16x32_bf16 v[68:71], v[116:119], v[144:147], v[68:71]\n\t" \
    "ds_read_b64_tr_b16 v[198:199], v205 offset:1984\n\t" \
    "v_mfma_f32_16x16x32_bf16 v[72:75], v[120:123], v[144:147], v[72:75]\n\t" \
    "v_mfma_f32_16x16x32_bf16 v[76:79], v[124:127], v[144:147], v[76:79]\n\t" \
    "v_mfma_f32_16x16x32_bf16 v[80:83], v[112:115], v[148:151], v[80:83]\n\t" \
    "v_mfma_f32_16x16x32_bf16 v[84:87], v[116:119], v[148:151], v[84:87]\n\t" \
    "v_mfma_f32_16x16x32_bf16 v[88:91], v[120:123], v[148:151], v[88:91]\n\t" \
    "v_mfma_f32_16x16x32_bf16 v[92:95], v[124:127], v[148:151], v[92:95]\n\t" \
    "v_mfma_f32_16x16x32_bf16 v[96:99], v[112:115], v[152:155], v[96:99]\n\t" \
    "v_mfma_f32_16x16x32_bf16 v[100:103], v[116:119], v[152:155], v[100:103]\n\t" \
    "v_mfma_f32_16x16x32_bf16 v[104:107], v[120:123], v[152:155], v[104:107]\n\t" \
    "v_mfma_f32_16x16x32_bf16 v[108:111], v[124:127], v[152:155], v[108:111]\n\t" \
    "s_waitcnt lgkmcnt(0)\n\t" \
    "s_barrier\n\t" \
    "v_mfma_f32_16x16x32_bf16 v[0:3], v[156:159], v[172:175], v[0:3]\n\t" \
    "v_mfma_f32_16x16x32_bf16 v[4:7], v[160:163], v[172:175], v[4:7]\n\t" \
    "v_mfma_f32_16x16x32_bf16 v[8:11], v[164:167], v[172:175], v[8:11]\n\t" \
    "v_mfma_f32_16x16x32_bf16 v[12:15], v[168:171], v[172:175], v[12:15]\n\t" \
    "v_mfma_f32_16x16x32_bf16 v[16:19], v[156:159], v[176:179], v[16:19]\n\t" \
    "v_mfma_f32_16x16x32_bf16 v[20:23], v[160:163], v[176:179], v[20:23]\n\t" \
    "v_mfma_f32_16x16x32_bf16 v[24:27], v[164:167], v[176:179], v[24:27]\n\t" \
    "v_mfma_f32_16x16x32_bf16 v[28:31], v[168:171], v[176:179], v[28:31]\n\t" \
    "v_mfma_f32_16x16x32_bf16 v[32:35], v[156:159], v[180:183], v[32:35]\n\t" \
    "v_mfma_f32_16x16x32_bf16 v[36:39], v[160:163], v[180:183], v[36:39]\n\t" \
    "v_mfma_f32_16x16x32_bf16 v[40:43], v[164:167], v[180:183], v[40:43]\n\t" \
    "v_mfma_f32_16x16x32_bf16 v[44:47], v[168:171], v[180:183], v[44:47]\n\t" \
    "v_mfma_f32_16x16x32_bf16 v[48:51], v[156:159], v[184:187], v[48:51]\n\t" \
    "v_mfma_f32_16x16x32_bf16 v[52:55], v[160:163], v[184:187], v[52:55]\n\t" \
    "v_mfma_f32_16x16x32_bf16 v[56:59], v[164:167], v[184:187], v[56:59]\n\t" \
    "v_mfma_f32_16x16x32_bf16 v[60:63], v[168:171], v[184:187], v[60:63]\n\t" \
    "v_mfma_f32_16x16x32_bf16 v[64:67], v[156:159], v[188:191], v[64:67]\n\t" \
    "v_mfma_f32_16x16x32_bf16 v[68:71], v[160:163], v[188:191], v[68:71]\n\t" \
    "v_mfma_f32_16x16x32_bf16 v[72:75], v[164:167], v[188:191], v[72:75]\n\t" \
    "v_mfma_f32_16x16x32_bf16 v[76:79], v[168:171], v[188:191], v[76:79]\n\t" \
    "v_mfma_f32_16x16x32_bf16 v[80:83], v[156:159], v[192:195], v[80:83]\n\t" \
    "v_mfma_f32_16x16x32_bf16 v[84:87], v[160:163], v[192:195], v[84:87]\n\t" \
    "v_mfma_f32_16x16x32_bf16 v[88:91], v[164:167], v[192:195], v[88:91]\n\t" \
    "v_mfma_f32_16x16x32_bf16 v[92:95], v[168:171], v[192:195], v[92:95]\n\t" \
    "v_mfma_f32_16x16x32_bf16 v[96:99], v[156:159], v[196:199], v[96:99]\n\t" \
    "v_mfma_f32_16x16x32_bf16 v[100:103], v[160:163], v[196:199], v[100:103]\n\t" \
    "v_mfma_f32_16x16x32_bf16 v[104:107], v[164:167], v[196:199], v[104:107]\n\t" \
    "v_mfma_f32_16x16x32_bf16 v[108:111], v[168:171], v[196:199], v[108:111]\n\t" \
    "s_waitcnt lgkmcnt(0)\n\t" \
    "s_barrier\n\t" \
    "s_nop 7\n\t" \
    "s_nop 7\n\t" \
    ""
#define G224_CLOB "v112","v113","v114","v115","v116","v117","v118","v119","v120","v121","v122","v123","v124","v125","v126","v127","v128","v129","v130","v131","v132","v133","v134","v135","v136","v137","v138","v139","v140","v141","v142","v143","v144","v145","v146","v147","v148","v149","v150","v151","v152","v153","v154","v155","v156","v157","v158","v159","v160","v161","v162","v163","v164","v165","v166","v167","v168","v169","v170","v171","v172","v173","v174","v175","v176","v177","v178","v179","v180","v181","v182","v183","v184","v185","v186","v187","v188","v189","v190","v191","v192","v193","v194","v195","v196","v197","v198","v199","v204","v205","v206","v207","v208","v209", "scc", "memory"
#define LOADER53 \
    "s_add_u32 m0, s47, s53\n\t" \
    "s_nop 0\n\t" \
    "global_load_lds_dwordx4 v5, s[42:43] offset:0\n\t" \
    "global_load_lds_dwordx4 v6, s[42:43] offset:1024\n\t" \
    "global_load_lds_dwordx4 v7, s[42:43] offset:2048\n\t" \
    "s_mov_b32 m0, s46\n\t" \
    "s_add_u32 s46, s46, 14336\n\t" \
    "global_load_lds_dwordx4 v0, s[40:41] offset:-2048\n\t" \
    "global_load_lds_dwordx4 v1, s[40:41] offset:-1024\n\t" \
    "global_load_lds_dwordx4 v2, s[40:41] offset:0\n\t" \
    "global_load_lds_dwordx4 v3, s[40:41] offset:1024\n\t" \
    "global_load_lds_dwordx4 v4, s[40:41] offset:2048\n\t" \
    "s_cmp_eq_u32 s46, s52\n\t" \
    "s_cselect_b32 s46, s51, s46\n\t" \
    "s_add_u32 s40, s40, s49\n\t" \
    "s_addc_u32 s41, s41, 0\n\t" \
    "s_sub_u32 s54, s54, 1\n\t" \
    "s_cmp_eq_u32 s54, 0\n\t" \
    "s_cselect_b32 s50, s56, 0\n\t" \
    "s_sub_u32 s40, s40, s50\n\t" \
    "s_subb_u32 s41, s41, 0\n\t" \
    "s_add_u32 m0, s58, s53\n\t" \
    "s_nop 0\n\t" \
    "global_load_lds_dwordx4 v5, s[44:45] offset:0\n\t" \
    "global_load_lds_dwordx4 v6, s[44:45] offset:1024\n\t" \
    "global_load_lds_dwordx4 v7, s[44:45] offset:2048\n\t" \
    "s_add_u32 s53, s53, 16384\n\t" \
    "s_and_b32 s53, s53, 0xffff\n\t" \
    "s_add_u32 s42, s42, 128\n\t" \
    "s_addc_u32 s43, s43, 0\n\t" \
    "s_add_u32 s44, s44, 128\n\t" \
    "s_addc_u32 s45, s45, 0\n\t" \
    "s_sub_u32 s55, s55, 1\n\t" \
    "s_cmp_eq_u32 s55, 0\n\t" \
    "s_cselect_b32 s50, s57, 0\n\t" \
    "s_sub_u32 s42, s42, s50\n\t" \
    "s_subb_u32 s43, s43, 0\n\t" \
    "s_sub_u32 s44, s44, s50\n\t" \
    "s_subb_u32 s45, s45, 0\n\t" \
    "s_mov_b32 m0, s46\n\t" \
    "s_add_u32 s46, s46, 14336\n\t" \
    "global_load_lds_dwordx4 v0, s[40:41] offset:-2048\n\t" \
    "global_load_lds_dwordx4 v1, s[40:41] offset:-1024\n\t" \
    "global_load_lds_dwordx4 v2, s[40:41] offset:0\n\t" \
    "global_load_lds_dwordx4 v3, s[40:41] offset:1024\n\t" \
    "global_load_lds_dwordx4 v4, s[40:41] offset:2048\n\t" \
    "s_cmp_eq_u32 s46, s52\n\t" \
    "s_cselect_b32 s46, s51, s46\n\t" \
    "s_add_u32 s40, s40, s49\n\t" \
    "s_addc_u32 s41, s41, 0\n\t" \
    "s_sub_u32 s54, s54, 1\n\t" \
    "s_cmp_eq_u32 s54, 0\n\t" \
    "s_cselect_b32 s50, s56, 0\n\t" \
    "s_sub_u32 s40, s40, s50\n\t" \
    "s_subb_u32 s41, s41, 0\n\t" \
    "s_add_u32 m0, s47, s53\n\t" \
    "s_nop 0\n\t" \
    "global_load_lds_dwordx4 v5, s[42:43] offset:0\n\t" \
    "global_load_lds_dwordx4 v6, s[42:43] offset:1024\n\t" \
    "global_load_lds_dwordx4 v7, s[42:43] offset:2048\n\t" \
    "s_mov_b32 m0, s46\n\t" \
    "s_add_u32 s46, s46, 14336\n\t" \
    "global_load_lds_dwordx4 v0, s[40:41] offset:-2048\n\t" \
    "global_load_lds_dwordx4 v1, s[40:41] offset:-1024\n\t" \
    "global_load_lds_dwordx4 v2, s[40:41] offset:0\n\t" \
    "global_load_lds_dwordx4 v3, s[40:41] offset:1024\n\t" \
    "global_load_lds_dwordx4 v4, s[40:41] offset:2048\n\t" \
    "s_cmp_eq_u32 s46, s52\n\t" \
    "s_cselect_b32 s46, s51, s46\n\t" \
    "s_add_u32 s40, s40, s49\n\t" \
    "s_addc_u32 s41, s41, 0\n\t" \
    "s_sub_u32 s54, s54, 1\n\t" \
    "s_cmp_eq_u32 s54, 0\n\t" \
    "s_cselect_b32 s50, s56, 0\n\t" \
    "s_sub_u32 s40, s40, s50\n\t" \
    "s_subb_u32 s41, s41, 0\n\t" \
    "s_add_u32 m0, s58, s53\n\t" \
    "s_nop 0\n\t" \
    "global_load_lds_dwordx4 v5, s[44:45] offset:0\n\t" \
    "global_load_lds_dwordx4 v6, s[44:45] offset:1024\n\t" \
    "global_load_lds_dwordx4 v7, s[44:45] offset:2048\n\t" \
    "s_add_u32 s53, s53, 16384\n\t" \
    "s_and_b32 s53, s53, 0xffff\n\t" \
    "s_add_u32 s42, s42, 128\n\t" \
    "s_addc_u32 s43, s43, 0\n\t" \
    "s_add_u32 s44, s44, 128\n\t" \
    "s_addc_u32 s45, s45, 0\n\t" \
    "s_sub_u32 s55, s55, 1\n\t" \
    "s_cmp_eq_u32 s55, 0\n\t" \
    "s_cselect_b32 s50, s57, 0\n\t" \
    "s_sub_u32 s42, s42, s50\n\t" \
    "s_subb_u32 s43, s43, 0\n\t" \
    "s_sub_u32 s44, s44, s50\n\t" \
    "s_subb_u32 s45, s45, 0\n\t" \
    "s_waitcnt vmcnt(16)\n\t" \
    "s_barrier\n\t" \
    "s_waitcnt vmcnt(11)\n\t" \
    "s_barrier\n\t" \
    "s_mov_b32 m0, s46\n\t" \
    "s_add_u32 s46, s46, 14336\n\t" \
    "global_load_lds_dwordx4 v0, s[40:41] offset:-2048\n\t" \
    "global_load_lds_dwordx4 v1, s[40:41] offset:-1024\n\t" \
    "global_load_lds_dwordx4 v2, s[40:41] offset:0\n\t" \
    "global_load_lds_dwordx4 v3, s[40:41] offset:1024\n\t" \
    "global_load_lds_dwordx4 v4, s[40:41] offset:2048\n\t" \
    "s_cmp_eq_u32 s46, s52\n\t" \
    "s_cselect_b32 s46, s51, s46\n\t" \
    "s_add_u32 s40, s40, s49\n\t" \
    "s_addc_u32 s41, s41, 0\n\t" \
    "s_sub_u32 s54, s54, 1\n\t" \
    "s_cmp_eq_u32 s54, 0\n\t" \
    "s_cselect_b32 s50, s56, 0\n\t" \
    "s_sub_u32 s40, s40, s50\n\t" \
    "s_subb_u32 s41, s41, 0\n\t" \
    "s_add_u32 m0, s47, s53\n\t" \
    "s_nop 0\n\t" \
    "global_load_lds_dwordx4 v5, s[42:43] offset:0\n\t" \
    "global_load_lds_dwordx4 v6, s[42:43] offset:1024\n\t" \
    "global_load_lds_dwordx4 v7, s[42:43] offset:2048\n\t" \
    "s_mov_b32 m0, s46\n\t" \
    "s_add_u32 s46, s46, 14336\n\t" \
    "global_load_lds_dwordx4 v0, s[40:41] offset:-2048\n\t" \
    "global_load_lds_dwordx4 v1, s[40:41] offset:-1024\n\t" \
    "global_load_lds_dwordx4 v2, s[40:41] offset:0\n\t" \
    "global_load_lds_dwordx4 v3, s[40:41] offset:1024\n\t" \
    "global_load_lds_dwordx4 v4, s[40:41] offset:2048\n\t" \
    "s_cmp_eq_u32 s46, s52\n\t" \
    "s_cselect_b32 s46, s51, s46\n\t" \
    "s_add_u32 s40, s40, s49\n\t" \
    "s_addc_u32 s41, s41, 0\n\t" \
    "s_sub_u32 s54, s54, 1\n\t" \
    "s_cmp_eq_u32 s54, 0\n\t" \
    "s_cselect_b32 s50, s56, 0\n\t" \
    "s_sub_u32 s40, s40, s50\n\t" \
    "s_subb_u32 s41, s41, 0\n\t" \
    "s_waitcnt vmcnt(13)\n\t" \
    "s_barrier\n\t" \
    "s_add_u32 m0, s58, s53\n\t" \
    "s_nop 0\n\t" \
    "global_load_lds_dwordx4 v5, s[44:45] offset:0\n\t" \
    "global_load_lds_dwordx4 v6, s[44:45] offset:1024\n\t" \
    "global_load_lds_dwordx4 v7, s[44:45] offset:2048\n\t" \
    "s_add_u32 s53, s53, 16384\n\t" \
    "s_and_b32 s53, s53, 0xffff\n\t" \
    "s_add_u32 s42, s42, 128\n\t" \
    "s_addc_u32 s43, s43, 0\n\t" \
    "s_add_u32 s44, s44, 128\n\t" \
    "s_addc_u32 s45, s45, 0\n\t" \
    "s_sub_u32 s55, s55, 1\n\t" \
    "s_cmp_eq_u32 s55, 0\n\t" \
    "s_cselect_b32 s50, s57, 0\n\t" \
    "s_sub_u32 s42, s42, s50\n\t" \
    "s_subb_u32 s43, s43, 0\n\t" \
    "s_sub_u32 s44, s44, s50\n\t" \
    "s_subb_u32 s45, s45, 0\n\t" \
    "s_mov_b32 m0, s46\n\t" \
    "s_add_u32 s46, s46, 14336\n\t" \
    "global_load_lds_dwordx4 v0, s[40:41] offset:-2048\n\t" \
    "global_load_lds_dwordx4 v1, s[40:41] offset:-1024\n\t" \
    "global_load_lds_dwordx4 v2, s[40:41] offset:0\n\t" \
    "global_load_lds_dwordx4 v3, s[40:41] offset:1024\n\t" \
    "global_load_lds_dwordx4 v4, s[40:41] offset:2048\n\t" \
    "s_cmp_eq_u32 s46, s52\n\t" \
    "s_cselect_b32 s46, s51, s46\n\t" \
    "s_add_u32 s40, s40, s49\n\t" \
    "s_addc_u32 s41, s41, 0\n\t" \
    "s_sub_u32 s54, s54, 1\n\t" \
    "s_cmp_eq_u32 s54, 0\n\t" \
    "s_cselect_b32 s50, s56, 0\n\t" \
    "s_sub_u32 s40, s40, s50\n\t" \
    "s_subb_u32 s41, s41, 0\n\t" \
    "s_add_u32 m0, s47, s53\n\t" \
    "s_nop 0\n\t" \
    "global_load_lds_dwordx4 v5, s[42:43] offset:0\n\t" \
    "global_load_lds_dwordx4 v6, s[42:43] offset:1024\n\t" \
    "global_load_lds_dwordx4 v7, s[42:43] offset:2048\n\t" \
    "s_waitcnt vmcnt(19)\n\t" \
    "s_barrier\n\t" \
    "s_mov_b32 m0, s46\n\t" \
    "s_add_u32 s46, s46, 14336\n\t" \
    "global_load_lds_dwordx4 v0, s[40:41] offset:-2048\n\t" \
    "global_load_lds_dwordx4 v1, s[40:41] offset:-1024\n\t" \
    "global_load_lds_dwordx4 v2, s[40:41] offset:0\n\t" \
    "global_load_lds_dwordx4 v3, s[40:41] offset:1024\n\t" \
    "global_load_lds_dwordx4 v4, s[40:41] offset:2048\n\t" \
    "s_cmp_eq_u32 s46, s52\n\t" \
    "s_cselect_b32 s46, s51, s46\n\t" \
    "s_add_u32 s40, s40, s49\n\t" \
    "s_addc_u32 s41, s41, 0\n\t" \
    "s_sub_u32 s54, s54, 1\n\t" \
    "s_cmp_eq_u32 s54, 0\n\t" \
    "s_cselect_b32 s50, s56, 0\n\t" \
    "s_sub_u32 s40, s40, s50\n\t" \
    "s_subb_u32 s41, s41, 0\n\t" \
    "s_add_u32 m0, s58, s53\n\t" \
    "s_nop 0\n\t" \
    "global_load_lds_dwordx4 v5, s[44:45] offset:0\n\t" \
    "global_load_lds_dwordx4 v6, s[44:45] offset:1024\n\t" \
    "global_load_lds_dwordx4 v7, s[44:45] offset:2048\n\t" \
    "s_add_u32 s53, s53, 16384\n\t" \
    "s_and_b32 s53, s53, 0xffff\n\t" \
    "s_add_u32 s42, s42, 128\n\t" \
    "s_addc_u32 s43, s43, 0\n\t" \
    "s_add_u32 s44, s44, 128\n\t" \
    "s_addc_u32 s45, s45, 0\n\t" \
    "s_sub_u32 s55, s55, 1\n\t" \
    "s_cmp_eq_u32 s55, 0\n\t" \
    "s_cselect_b32 s50, s57, 0\n\t" \
    "s_sub_u32 s42, s42, s50\n\t" \
    "s_subb_u32 s43, s43, 0\n\t" \
    "s_sub_u32 s44, s44, s50\n\t" \
    "s_subb_u32 s45, s45, 0\n\t" \
    "s_mov_b32 m0, s46\n\t" \
    "s_add_u32 s46, s46, 14336\n\t" \
    "global_load_lds_dwordx4 v0, s[40:41] offset:-2048\n\t" \
    "global_load_lds_dwordx4 v1, s[40:41] offset:-1024\n\t" \
    "global_load_lds_dwordx4 v2, s[40:41] offset:0\n\t" \
    "global_load_lds_dwordx4 v3, s[40:41] offset:1024\n\t" \
    "global_load_lds_dwordx4 v4, s[40:41] offset:2048\n\t" \
    "s_cmp_eq_u32 s46, s52\n\t" \
    "s_cselect_b32 s46, s51, s46\n\t" \
    "s_add_u32 s40, s40, s49\n\t" \
    "s_addc_u32 s41, s41, 0\n\t" \
    "s_sub_u32 s54, s54, 1\n\t" \
    "s_cmp_eq_u32 s54, 0\n\t" \
    "s_cselect_b32 s50, s56, 0\n\t" \
    "s_sub_u32 s40, s40, s50\n\t" \
    "s_subb_u32 s41, s41, 0\n\t" \
    "s_waitcnt vmcnt(21)\n\t" \
    "s_barrier\n\t" \
    "s_add_u32 m0, s47, s53\n\t" \
    "s_nop 0\n\t" \
    "global_load_lds_dwordx4 v5, s[42:43] offset:0\n\t" \
    "global_load_lds_dwordx4 v6, s[42:43] offset:1024\n\t" \
    "global_load_lds_dwordx4 v7, s[42:43] offset:2048\n\t" \
    "s_mov_b32 m0, s46\n\t" \
    "s_add_u32 s46, s46, 14336\n\t" \
    "global_load_lds_dwordx4 v0, s[40:41] offset:-2048\n\t" \
    "global_load_lds_dwordx4 v1, s[40:41] offset:-1024\n\t" \
    "global_load_lds_dwordx4 v2, s[40:41] offset:0\n\t" \
    "global_load_lds_dwordx4 v3, s[40:41] offset:1024\n\t" \
    "global_load_lds_dwordx4 v4, s[40:41] offset:2048\n\t" \
    "s_cmp_eq_u32 s46, s52\n\t" \
    "s_cselect_b32 s46, s51, s46\n\t" \
    "s_add_u32 s40, s40, s49\n\t" \
    "s_addc_u32 s41, s41, 0\n\t" \
    "s_sub_u32 s54, s54, 1\n\t" \
    "s_cmp_eq_u32 s54, 0\n\t" \
    "s_cselect_b32 s50, s56, 0\n\t" \
    "s_sub_u32 s40, s40, s50\n\t" \
    "s_subb_u32 s41, s41, 0\n\t" \
    "s_add_u32 m0, s58, s53\n\t" \
    "s_nop 0\n\t" \
    "global_load_lds_dwordx4 v5, s[44:45] offset:0\n\t" \
    "global_load_lds_dwordx4 v6, s[44:45] offset:1024\n\t" \
    "global_load_lds_dwordx4 v7, s[44:45] offset:2048\n\t" \
    "s_add_u32 s53, s53, 16384\n\t" \
    "s_and_b32 s53, s53, 0xffff\n\t" \
    "s_add_u32 s42, s42, 128\n\t" \
    "s_addc_u32 s43, s43, 0\n\t" \
    "s_add_u32 s44, s44, 128\n\t" \
    "s_addc_u32 s45, s45, 0\n\t" \
    "s_sub_u32 s55, s55, 1\n\t" \
    "s_cmp_eq_u32 s55, 0\n\t" \
    "s_cselect_b32 s50, s57, 0\n\t" \
    "s_sub_u32 s42, s42, s50\n\t" \
    "s_subb_u32 s43, s43, 0\n\t" \
    "s_sub_u32 s44, s44, s50\n\t" \
    "s_subb_u32 s45, s45, 0\n\t" \
    "s_waitcnt vmcnt(27)\n\t" \
    "s_barrier\n\t" \
    "s_mov_b32 m0, s46\n\t" \
    "s_add_u32 s46, s46, 14336\n\t" \
    "global_load_lds_dwordx4 v0, s[40:41] offset:-2048\n\t" \
    "global_load_lds_dwordx4 v1, s[40:41] offset:-1024\n\t" \
    "global_load_lds_dwordx4 v2, s[40:41] offset:0\n\t" \
    "global_load_lds_dwordx4 v3, s[40:41] offset:1024\n\t" \
    "global_load_lds_dwordx4 v4, s[40:41] offset:2048\n\t" \
    "s_cmp_eq_u32 s46, s52\n\t" \
    "s_cselect_b32 s46, s51, s46\n\t" \
    "s_add_u32 s40, s40, s49\n\t" \
    "s_addc_u32 s41, s41, 0\n\t" \
    "s_sub_u32 s54, s54, 1\n\t" \
    "s_cmp_eq_u32 s54, 0\n\t" \
    "s_cselect_b32 s50, s56, 0\n\t" \
    "s_sub_u32 s40, s40, s50\n\t" \
    "s_subb_u32 s41, s41, 0\n\t" \
    "s_add_u32 m0, s47, s53\n\t" \
    "s_nop 0\n\t" \
    "global_load_lds_dwordx4 v5, s[42:43] offset:0\n\t" \
    "global_load_lds_dwordx4 v6, s[42:43] offset:1024\n\t" \
    "global_load_lds_dwordx4 v7, s[42:43] offset:2048\n\t" \
    "s_mov_b32 m0, s46\n\t" \
    "s_add_u32 s46, s46, 14336\n\t" \
    "global_load_lds_dwordx4 v0, s[40:41] offset:-2048\n\t" \
    "global_load_lds_dwordx4 v1, s[40:41] offset:-1024\n\t" \
    "global_load_lds_dwordx4 v2, s[40:41] offset:0\n\t" \
    "global_load_lds_dwordx4 v3, s[40:41] offset:1024\n\t" \
    "global_load_lds_dwordx4 v4, s[40:41] offset:2048\n\t" \
    "s_cmp_eq_u32 s46, s52\n\t" \
    "s_cselect_b32 s46, s51, s46\n\t" \
    "s_add_u32 s40, s40, s49\n\t" \
    "s_addc_u32 s41, s41, 0\n\t" \
    "s_sub_u32 s54, s54, 1\n\t" \
    "s_cmp_eq_u32 s54, 0\n\t" \
    "s_cselect_b32 s50, s56, 0\n\t" \
    "s_sub_u32 s40, s40, s50\n\t" \
    "s_subb_u32 s41, s41, 0\n\t" \
    "s_waitcnt vmcnt(29)\n\t" \
    "s_barrier\n\t" \
    "s_add_u32 m0, s58, s53\n\t" \
    "s_nop 0\n\t" \
    "global_load_lds_dwordx4 v5, s[44:45] offset:0\n\t" \
    "global_load_lds_dwordx4 v6, s[44:45] offset:1024\n\t" \
    "global_load_lds_dwordx4 v7, s[44:45] offset:2048\n\t" \
    "s_add_u32 s53, s53, 16384\n\t" \
    "s_and_b32 s53, s53, 0xffff\n\t" \
    "s_add_u32 s42, s42, 128\n\t" \
    "s_addc_u32 s43, s43, 0\n\t" \
    "s_add_u32 s44, s44, 128\n\t" \
    "s_addc_u32 s45, s45, 0\n\t" \
    "s_sub_u32 s55, s55, 1\n\t" \
    "s_cmp_eq_u32 s55, 0\n\t" \
    "s_cselect_b32 s50, s57, 0\n\t" \
    "s_sub_u32 s42, s42, s50\n\t" \
    "s_subb_u32 s43, s43, 0\n\t" \
    "s_sub_u32 s44, s44, s50\n\t" \
    "s_subb_u32 s45, s45, 0\n\t" \
    "s_mov_b32 m0, s46\n\t" \
    "s_add_u32 s46, s46, 14336\n\t" \
    "global_load_lds_dwordx4 v0, s[40:41] offset:-2048\n\t" \
    "global_load_lds_dwordx4 v1, s[40:41] offset:-1024\n\t" \
    "global_load_lds_dwordx4 v2, s[40:41] offset:0\n\t" \
    "global_load_lds_dwordx4 v3, s[40:41] offset:1024\n\t" \
    "global_load_lds_dwordx4 v4, s[40:41] offset:2048\n\t" \
    "s_cmp_eq_u32 s46, s52\n\t" \
    "s_cselect_b32 s46, s51, s46\n\t" \
    "s_add_u32 s40, s40, s49\n\t" \
    "s_addc_u32 s41, s41, 0\n\t" \
    "s_sub_u32 s54, s54, 1\n\t" \
    "s_cmp_eq_u32 s54, 0\n\t" \
    "s_cselect_b32 s50, s56, 0\n\t" \
    "s_sub_u32 s40, s40, s50\n\t" \
    "s_subb_u32 s41, s41, 0\n\t" \
    "s_add_u32 m0, s47, s53\n\t" \
    "s_nop 0\n\t" \
    "global_load_lds_dwordx4 v5, s[42:43] offset:0\n\t" \
    "global_load_lds_dwordx4 v6, s[42:43] offset:1024\n\t" \
    "global_load_lds_dwordx4 v7, s[42:43] offset:2048\n\t" \
    "s_waitcnt vmcnt(32)\n\t" \
    "s_barrier\n\t" \
    "s_mov_b32 m0, s46\n\t" \
    "s_add_u32 s46, s46, 14336\n\t" \
    "global_load_lds_dwordx4 v0, s[40:41] offset:-2048\n\t" \
    "global_load_lds_dwordx4 v1, s[40:41] offset:-1024\n\t" \
    "global_load_lds_dwordx4 v2, s[40:41] offset:0\n\t" \
    "global_load_lds_dwordx4 v3, s[40:41] offset:1024\n\t" \
    "global_load_lds_dwordx4 v4, s[40:41] offset:2048\n\t" \
    "s_cmp_eq_u32 s46, s52\n\t" \
    "s_cselect_b32 s46, s51, s46\n\t" \
    "s_add_u32 s40, s40, s49\n\t" \
    "s_addc_u32 s41, s41, 0\n\t" \
    "s_sub_u32 s54, s54, 1\n\t" \
    "s_cmp_eq_u32 s54, 0\n\t" \
    "s_cselect_b32 s50, s56, 0\n\t" \
    "s_sub_u32 s40, s40, s50\n\t" \
    "s_subb_u32 s41, s41, 0\n\t" \
    "s_add_u32 m0, s58, s53\n\t" \
    "s_nop 0\n\t" \
    "global_load_lds_dwordx4 v5, s[44:45] offset:0\n\t" \
    "global_load_lds_dwordx4 v6, s[44:45] offset:1024\n\t" \
    "global_load_lds_dwordx4 v7, s[44:45] offset:2048\n\t" \
    "s_add_u32 s53, s53, 16384\n\t" \
    "s_and_b32 s53, s53, 0xffff\n\t" \
    "s_add_u32 s42, s42, 128\n\t" \
    "s_addc_u32 s43, s43, 0\n\t" \
    "s_add_u32 s44, s44, 128\n\t" \
    "s_addc_u32 s45, s45, 0\n\t" \
    "s_sub_u32 s55, s55, 1\n\t" \
    "s_cmp_eq_u32 s55, 0\n\t" \
    "s_cselect_b32 s50, s57, 0\n\t" \
    "s_sub_u32 s42, s42, s50\n\t" \
    "s_subb_u32 s43, s43, 0\n\t" \
    "s_sub_u32 s44, s44, s50\n\t" \
    "s_subb_u32 s45, s45, 0\n\t" \
    "s_waitcnt vmcnt(32)\n\t" \
    "s_barrier\n\t" \
    "s_cmp_eq_u32 s48, 0\n\t" \
    "s_cbranch_scc1 2f\n\t" \
    "1:\n\t" \
    "s_mov_b32 m0, s46\n\t" \
    "s_add_u32 s46, s46, 14336\n\t" \
    "global_load_lds_dwordx4 v0, s[40:41] offset:-2048\n\t" \
    "global_load_lds_dwordx4 v1, s[40:41] offset:-1024\n\t" \
    "global_load_lds_dwordx4 v2, s[40:41] offset:0\n\t" \
    "global_load_lds_dwordx4 v3, s[40:41] offset:1024\n\t" \
    "global_load_lds_dwordx4 v4, s[40:41] offset:2048\n\t" \
    "s_cmp_eq_u32 s46, s52\n\t" \
    "s_cselect_b32 s46, s51, s46\n\t" \
    "s_add_u32 s40, s40, s49\n\t" \
    "s_addc_u32 s41, s41, 0\n\t" \
    "s_sub_u32 s54, s54, 1\n\t" \
    "s_cmp_eq_u32 s54, 0\n\t" \
    "s_cselect_b32 s50, s56, 0\n\t" \
    "s_sub_u32 s40, s40, s50\n\t" \
    "s_subb_u32 s41, s41, 0\n\t" \
    "s_add_u32 m0, s47, s53\n\t" \
    "s_nop 0\n\t" \
    "global_load_lds_dwordx4 v5, s[42:43] offset:0\n\t" \
    "global_load_lds_dwordx4 v6, s[42:43] offset:1024\n\t" \
    "global_load_lds_dwordx4 v7, s[42:43] offset:2048\n\t" \
    "s_waitcnt vmcnt(32)\n\t" \
    "s_barrier\n\t" \
    "s_mov_b32 m0, s46\n\t" \
    "s_add_u32 s46, s46, 14336\n\t" \
    "global_load_lds_dwordx4 v0, s[40:41] offset:-2048\n\t" \
    "global_load_lds_dwordx4 v1, s[40:41] offset:-1024\n\t" \
    "global_load_lds_dwordx4 v2, s[40:41] offset:0\n\t" \
    "global_load_lds_dwordx4 v3, s[40:41] offset:1024\n\t" \
    "global_load_lds_dwordx4 v4, s[40:41] offset:2048\n\t" \
    "s_cmp_eq_u32 s46, s52\n\t" \
    "s_cselect_b32 s46, s51, s46\n\t" \
    "s_add_u32 s40, s40, s49\n\t" \
    "s_addc_u32 s41, s41, 0\n\t" \
    "s_sub_u32 s54, s54, 1\n\t" \
    "s_cmp_eq_u32 s54, 0\n\t" \
    "s_cselect_b32 s50, s56, 0\n\t" \
    "s_sub_u32 s40, s40, s50\n\t" \
    "s_subb_u32 s41, s41, 0\n\t" \
    "s_add_u32 m0, s58, s53\n\t" \
    "s_nop 0\n\t" \
    "global_load_lds_dwordx4 v5, s[44:45] offset:0\n\t" \
    "global_load_lds_dwordx4 v6, s[44:45] offset:1024\n\t" \
    "global_load_lds_dwordx4 v7, s[44:45] offset:2048\n\t" \
    "s_add_u32 s53, s53, 16384\n\t" \
    "s_and_b32 s53, s53, 0xffff\n\t" \
    "s_add_u32 s42, s42, 128\n\t" \
    "s_addc_u32 s43, s43, 0\n\t" \
    "s_add_u32 s44, s44, 128\n\t" \
    "s_addc_u32 s45, s45, 0\n\t" \
    "s_sub_u32 s55, s55, 1\n\t" \
    "s_cmp_eq_u32 s55, 0\n\t" \
    "s_cselect_b32 s50, s57, 0\n\t" \
    "s_sub_u32 s42, s42, s50\n\t" \
    "s_subb_u32 s43, s43, 0\n\t" \
    "s_sub_u32 s44, s44, s50\n\t" \
    "s_subb_u32 s45, s45, 0\n\t" \
    "s_sub_u32 s48, s48, 1\n\t" \
    "s_cmp_lg_u32 s48, 0\n\t" \
    "s_waitcnt vmcnt(32)\n\t" \
    "s_barrier\n\t" \
    "s_cbranch_scc1 1b\n\t" \
    "2:\n\t" \
    "s_mov_b32 m0, s46\n\t" \
    "s_add_u32 s46, s46, 14336\n\t" \
    "global_load_lds_dwordx4 v0, s[40:41] offset:-2048\n\t" \
    "global_load_lds_dwordx4 v1, s[40:41] offset:-1024\n\t" \
    "global_load_lds_dwordx4 v2, s[40:41] offset:0\n\t" \
    "global_load_lds_dwordx4 v3, s[40:41] offset:1024\n\t" \
    "global_load_lds_dwordx4 v4, s[40:41] offset:2048\n\t" \
    "s_cmp_eq_u32 s46, s52\n\t" \
    "s_cselect_b32 s46, s51, s46\n\t" \
    "s_add_u32 s40, s40, s49\n\t" \
    "s_addc_u32 s41, s41, 0\n\t" \
    "s_sub_u32 s54, s54, 1\n\t" \
    "s_cmp_eq_u32 s54, 0\n\t" \
    "s_cselect_b32 s50, s56, 0\n\t" \
    "s_sub_u32 s40, s40, s50\n\t" \
    "s_subb_u32 s41, s41, 0\n\t" \
    ""
#define LOADER42 \
    "s_add_u32 m0, s47, s53\n\t" \
    "s_nop 0\n\t" \
    "global_load_lds_dwordx4 v5, s[42:43] offset:0\n\t" \
    "global_load_lds_dwordx4 v6, s[42:43] offset:1024\n\t" \
    "s_mov_b32 m0, s46\n\t" \
    "s_add_u32 s46, s46, 14336\n\t" \
    "global_load_lds_dwordx4 v0, s[40:41] offset:-2048\n\t" \
    "global_load_lds_dwordx4 v1, s[40:41] offset:-1024\n\t" \
    "global_load_lds_dwordx4 v2, s[40:41] offset:0\n\t" \
    "global_load_lds_dwordx4 v3, s[40:41] offset:1024\n\t" \
    "s_cmp_eq_u32 s46, s52\n\t" \
    "s_cselect_b32 s46, s51, s46\n\t" \
    "s_add_u32 s40, s40, s49\n\t" \
    "s_addc_u32 s41, s41, 0\n\t" \
    "s_sub_u32 s54, s54, 1\n\t" \
    "s_cmp_eq_u32 s54, 0\n\t" \
    "s_cselect_b32 s50, s56, 0\n\t" \
    "s_sub_u32 s40, s40, s50\n\t" \
    "s_subb_u32 s41, s41, 0\n\t" \
    "s_add_u32 m0, s58, s53\n\t" \
    "s_nop 0\n\t" \
    "global_load_lds_dwordx4 v5, s[44:45] offset:0\n\t" \
    "global_load_lds_dwordx4 v6, s[44:45] offset:1024\n\t" \
    "s_add_u32 s53, s53, 16384\n\t" \
    "s_and_b32 s53, s53, 0xffff\n\t" \
    "s_add_u32 s42, s42, 128\n\t" \
    "s_addc_u32 s43, s43, 0\n\t" \
    "s_add_u32 s44, s44, 128\n\t" \
    "s_addc_u32 s45, s45, 0\n\t" \
    "s_sub_u32 s55, s55, 1\n\t" \
    "s_cmp_eq_u32 s55, 0\n\t" \
    "s_cselect_b32 s50, s57, 0\n\t" \
    "s_sub_u32 s42, s42, s50\n\t" \
    "s_subb_u32 s43, s43, 0\n\t" \
    "s_sub_u32 s44, s44, s50\n\t" \
    "s_subb_u32 s45, s45, 0\n\t" \
    "s_mov_b32 m0, s46\n\t" \
    "s_add_u32 s46, s46, 14336\n\t" \
    "global_load_lds_dwordx4 v0, s[40:41] offset:-2048\n\t" \
    "global_load_lds_dwordx4 v1, s[40:41] offset:-1024\n\t" \
    "global_load_lds_dwordx4 v2, s[40:41] offset:0\n\t" \
    "global_load_lds_dwordx4 v3, s[40:41] offset:1024\n\t" \
    "s_cmp_eq_u32 s46, s52\n\t" \
    "s_cselect_b32 s46, s51, s46\n\t" \
    "s_add_u32 s40, s40, s49\n\t" \
    "s_addc_u32 s41, s41, 0\n\t" \
    "s_sub_u32 s54, s54, 1\n\t" \
    "s_cmp_eq_u32 s54, 0\n\t" \
    "s_cselect_b32 s50, s56, 0\n\t" \
    "s_sub_u32 s40, s40, s50\n\t" \
    "s_subb_u32 s41, s41, 0\n\t" \
    "s_add_u32 m0, s47, s53\n\t" \
    "s_nop 0\n\t" \
    "global_load_lds_dwordx4 v5, s[42:43] offset:0\n\t" \
    "global_load_lds_dwordx4 v6, s[42:43] offset:1024\n\t" \
    "s_mov_b32 m0, s46\n\t" \
    "s_add_u32 s46, s46, 14336\n\t" \
    "global_load_lds_dwordx4 v0, s[40:41] offset:-2048\n\t" \
    "global_load_lds_dwordx4 v1, s[40:41] offset:-1024\n\t" \
    "global_load_lds_dwordx4 v2, s[40:41] offset:0\n\t" \
    "global_load_lds_dwordx4 v3, s[40:41] offset:1024\n\t" \
    "s_cmp_eq_u32 s46, s52\n\t" \
    "s_cselect_b32 s46, s51, s46\n\t" \
    "s_add_u32 s40, s40, s49\n\t" \
    "s_addc_u32 s41, s41, 0\n\t" \
    "s_sub_u32 s54, s54, 1\n\t" \
    "s_cmp_eq_u32 s54, 0\n\t" \
    "s_cselect_b32 s50, s56, 0\n\t" \
    "s_sub_u32 s40, s40, s50\n\t" \
    "s_subb_u32 s41, s41, 0\n\t" \
    "s_add_u32 m0, s58, s53\n\t" \
    "s_nop 0\n\t" \
    "global_load_lds_dwordx4 v5, s[44:45] offset:0\n\t" \
    "global_load_lds_dwordx4 v6, s[44:45] offset:1024\n\t" \
    "s_add_u32 s53, s53, 16384\n\t" \
    "s_and_b32 s53, s53, 0xffff\n\t" \
    "s_add_u32 s42, s42, 128\n\t" \
    "s_addc_u32 s43, s43, 0\n\t" \
    "s_add_u32 s44, s44, 128\n\t" \
    "s_addc_u32 s45, s45, 0\n\t" \
    "s_sub_u32 s55, s55, 1\n\t" \
    "s_cmp_eq_u32 s55, 0\n\t" \
    "s_cselect_b32 s50, s57, 0\n\t" \
    "s_sub_u32 s42, s42, s50\n\t" \
    "s_subb_u32 s43, s43, 0\n\t" \
    "s_sub_u32 s44, s44, s50\n\t" \
    "s_subb_u32 s45, s45, 0\n\t" \
    "s_waitcnt vmcnt(12)\n\t" \
    "s_barrier\n\t" \
    "s_waitcnt vmcnt(8)\n\t" \
    "s_barrier\n\t" \
    "s_mov_b32 m0, s46\n\t" \
    "s_add_u32 s46, s46, 14336\n\t" \
    "global_load_lds_dwordx4 v0, s[40:41] offset:-2048\n\t" \
    "global_load_lds_dwordx4 v1, s[40:41] offset:-1024\n\t" \
    "global_load_lds_dwordx4 v2, s[40:41] offset:0\n\t" \
    "global_load_lds_dwordx4 v3, s[40:41] offset:1024\n\t" \
    "s_cmp_eq_u32 s46, s52\n\t" \
    "s_cselect_b32 s46, s51, s46\n\t" \
    "s_add_u32 s40, s40, s49\n\t" \
    "s_addc_u32 s41, s41, 0\n\t" \
    "s_sub_u32 s54, s54, 1\n\t" \
    "s_cmp_eq_u32 s54, 0\n\t" \
    "s_cselect_b32 s50, s56, 0\n\t" \
    "s_sub_u32 s40, s40, s50\n\t" \
    "s_subb_u32 s41, s41, 0\n\t" \
    "s_add_u32 m0, s47, s53\n\t" \
    "s_nop 0\n\t" \
    "global_load_lds_dwordx4 v5, s[42:43] offset:0\n\t" \
    "global_load_lds_dwordx4 v6, s[42:43] offset:1024\n\t" \
    "s_mov_b32 m0, s46\n\t" \
    "s_add_u32 s46, s46, 14336\n\t" \
    "global_load_lds_dwordx4 v0, s[40:41] offset:-2048\n\t" \
    "global_load_lds_dwordx4 v1, s[40:41] offset:-1024\n\t" \
    "global_load_lds_dwordx4 v2, s[40:41] offset:0\n\t" \
    "global_load_lds_dwordx4 v3, s[40:41] offset:1024\n\t" \
    "s_cmp_eq_u32 s46, s52\n\t" \
    "s_cselect_b32 s46, s51, s46\n\t" \
    "s_add_u32 s40, s40, s49\n\t" \
    "s_addc_u32 s41, s41, 0\n\t" \
    "s_sub_u32 s54, s54, 1\n\t" \
    "s_cmp_eq_u32 s54, 0\n\t" \
    "s_cselect_b32 s50, s56, 0\n\t" \
    "s_sub_u32 s40, s40, s50\n\t" \
    "s_subb_u32 s41, s41, 0\n\t" \
    "s_waitcnt vmcnt(10)\n\t" \
    "s_barrier\n\t" \
    "s_add_u32 m0, s58, s53\n\t" \
    "s_nop 0\n\t" \
    "global_load_lds_dwordx4 v5, s[44:45] offset:0\n\t" \
    "global_load_lds_dwordx4 v6, s[44:45] offset:1024\n\t" \
    "s_add_u32 s53, s53, 16384\n\t" \
    "s_and_b32 s53, s53, 0xffff\n\t" \
    "s_add_u32 s42, s42, 128\n\t" \
    "s_addc_u32 s43, s43, 0\n\t" \
    "s_add_u32 s44, s44, 128\n\t" \
    "s_addc_u32 s45, s45, 0\n\t" \
    "s_sub_u32 s55, s55, 1\n\t" \
    "s_cmp_eq_u32 s55, 0\n\t" \
    "s_cselect_b32 s50, s57, 0\n\t" \
    "s_sub_u32 s42, s42, s50\n\t" \
    "s_subb_u32 s43, s43, 0\n\t" \
    "s_sub_u32 s44, s44, s50\n\t" \
    "s_subb_u32 s45, s45, 0\n\t" \
    "s_mov_b32 m0, s46\n\t" \
    "s_add_u32 s46, s46, 14336\n\t" \
    "global_load_lds_dwordx4 v0, s[40:41] offset:-2048\n\t" \
    "global_load_lds_dwordx4 v1, s[40:41] offset:-1024\n\t" \
    "global_load_lds_dwordx4 v2, s[40:41] offset:0\n\t" \
    "global_load_lds_dwordx4 v3, s[40:41] offset:1024\n\t" \
    "s_cmp_eq_u32 s46, s52\n\t" \
    "s_cselect_b32 s46, s51, s46\n\t" \
    "s_add_u32 s40, s40, s49\n\t" \
    "s_addc_u32 s41, s41, 0\n\t" \
    "s_sub_u32 s54, s54, 1\n\t" \
    "s_cmp_eq_u32 s54, 0\n\t" \
    "s_cselect_b32 s50, s56, 0\n\t" \
    "s_sub_u32 s40, s40, s50\n\t" \
    "s_subb_u32 s41, s41, 0\n\t" \
    "s_add_u32 m0, s47, s53\n\t" \
    "s_nop 0\n\t" \
    "global_load_lds_dwordx4 v5, s[42:43] offset:0\n\t" \
    "global_load_lds_dwordx4 v6, s[42:43] offset:1024\n\t" \
    "s_waitcnt vmcnt(14)\n\t" \
    "s_barrier\n\t" \
    "s_mov_b32 m0, s46\n\t" \
    "s_add_u32 s46, s46, 14336\n\t" \
    "global_load_lds_dwordx4 v0, s[40:41] offset:-2048\n\t" \
    "global_load_lds_dwordx4 v1, s[40:41] offset:-1024\n\t" \
    "global_load_lds_dwordx4 v2, s[40:41] offset:0\n\t" \
    "global_load_lds_dwordx4 v3, s[40:41] offset:1024\n\t" \
    "s_cmp_eq_u32 s46, s52\n\t" \
    "s_cselect_b32 s46, s51, s46\n\t" \
    "s_add_u32 s40, s40, s49\n\t" \
    "s_addc_u32 s41, s41, 0\n\t" \
    "s_sub_u32 s54, s54, 1\n\t" \
    "s_cmp_eq_u32 s54, 0\n\t" \
    "s_cselect_b32 s50, s56, 0\n\t" \
    "s_sub_u32 s40, s40, s50\n\t" \
    "s_subb_u32 s41, s41, 0\n\t" \
    "s_add_u32 m0, s58, s53\n\t" \
    "s_nop 0\n\t" \
    "global_load_lds_dwordx4 v5, s[44:45] offset:0\n\t" \
    "global_load_lds_dwordx4 v6, s[44:45] offset:1024\n\t" \
    "s_add_u32 s53, s53, 16384\n\t" \
    "s_and_b32 s53, s53, 0xffff\n\t" \
    "s_add_u32 s42, s42, 128\n\t" \
    "s_addc_u32 s43, s43, 0\n\t" \
    "s_add_u32 s44, s44, 128\n\t" \
    "s_addc_u32 s45, s45, 0\n\t" \
    "s_sub_u32 s55, s55, 1\n\t" \
    "s_cmp_eq_u32 s55, 0\n\t" \
    "s_cselect_b32 s50, s57, 0\n\t" \
    "s_sub_u32 s42, s42, s50\n\t" \
    "s_subb_u32 s43, s43, 0\n\t" \
    "s_sub_u32 s44, s44, s50\n\t" \
    "s_subb_u32 s45, s45, 0\n\t" \
    "s_mov_b32 m0, s46\n\t" \
    "s_add_u32 s46, s46, 14336\n\t" \
    "global_load_lds_dwordx4 v0, s[40:41] offset:-2048\n\t" \
    "global_load_lds_dwordx4 v1, s[40:41] offset:-1024\n\t" \
    "global_load_lds_dwordx4 v2, s[40:41] offset:0\n\t" \
    "global_load_lds_dwordx4 v3, s[40:41] offset:1024\n\t" \
    "s_cmp_eq_u32 s46, s52\n\t" \
    "s_cselect_b32 s46, s51, s46\n\t" \
    "s_add_u32 s40, s40, s49\n\t" \
    "s_addc_u32 s41, s41, 0\n\t" \
    "s_sub_u32 s54, s54, 1\n\t" \
    "s_cmp_eq_u32 s54, 0\n\t" \
    "s_cselect_b32 s50, s56, 0\n\t" \
    "s_sub_u32 s40, s40, s50\n\t" \
    "s_subb_u32 s41, s41, 0\n\t" \
    "s_waitcnt vmcnt(16)\n\t" \
    "s_barrier\n\t" \
    "s_add_u32 m0, s47, s53\n\t" \
    "s_nop 0\n\t" \
    "global_load_lds_dwordx4 v5, s[42:43] offset:0\n\t" \
    "global_load_lds_dwordx4 v6, s[42:43] offset:1024\n\t" \
    "s_mov_b32 m0, s46\n\t" \
    "s_add_u32 s46, s46, 14336\n\t" \
    "global_load_lds_dwordx4 v0, s[40:41] offset:-2048\n\t" \
    "global_load_lds_dwordx4 v1, s[40:41] offset:-1024\n\t" \
    "global_load_lds_dwordx4 v2, s[40:41] offset:0\n\t" \
    "global_load_lds_dwordx4 v3, s[40:41] offset:1024\n\t" \
    "s_cmp_eq_u32 s46, s52\n\t" \
    "s_cselect_b32 s46, s51, s46\n\t" \
    "s_add_u32 s40, s40, s49\n\t" \
    "s_addc_u32 s41, s41, 0\n\t" \
    "s_sub_u32 s54, s54, 1\n\t" \
    "s_cmp_eq_u32 s54, 0\n\t" \
    "s_cselect_b32 s50, s56, 0\n\t" \
    "s_sub_u32 s40, s40, s50\n\t" \
    "s_subb_u32 s41, s41, 0\n\t" \
    "s_add_u32 m0, s58, s53\n\t" \
    "s_nop 0\n\t" \
    "global_load_lds_dwordx4 v5, s[44:45] offset:0\n\t" \
    "global_load_lds_dwordx4 v6, s[44:45] offset:1024\n\t" \
    "s_add_u32 s53, s53, 16384\n\t" \
    "s_and_b32 s53, s53, 0xffff\n\t" \
    "s_add_u32 s42, s42, 128\n\t" \
    "s_addc_u32 s43, s43, 0\n\t" \
    "s_add_u32 s44, s44, 128\n\t" \
    "s_addc_u32 s45, s45, 0\n\t" \
    "s_sub_u32 s55, s55, 1\n\t" \
    "s_cmp_eq_u32 s55, 0\n\t" \
    "s_cselect_b32 s50, s57, 0\n\t" \
    "s_sub_u32 s42, s42, s50\n\t" \
    "s_subb_u32 s43, s43, 0\n\t" \
    "s_sub_u32 s44, s44, s50\n\t" \
    "s_subb_u32 s45, s45, 0\n\t" \
    "s_waitcnt vmcnt(20)\n\t" \
    "s_barrier\n\t" \
    "s_mov_b32 m0, s46\n\t" \
    "s_add_u32 s46, s46, 14336\n\t" \
    "global_load_lds_dwordx4 v0, s[40:41] offset:-2048\n\t" \
    "global_load_lds_dwordx4 v1, s[40:41] offset:-1024\n\t" \
    "global_load_lds_dwordx4 v2, s[40:41] offset:0\n\t" \
    "global_load_lds_dwordx4 v3, s[40:41] offset:1024\n\t" \
    "s_cmp_eq_u32 s46, s52\n\t" \
    "s_cselect_b32 s46, s51, s46\n\t" \
    "s_add_u32 s40, s40, s49\n\t" \
    "s_addc_u32 s41, s41, 0\n\t" \
    "s_sub_u32 s54, s54, 1\n\t" \
    "s_cmp_eq_u32 s54, 0\n\t" \
    "s_cselect_b32 s50, s56, 0\n\t" \
    "s_sub_u32 s40, s40, s50\n\t" \
    "s_subb_u32 s41, s41, 0\n\t" \
    "s_add_u32 m0, s47, s53\n\t" \
    "s_nop 0\n\t" \
    "global_load_lds_dwordx4 v5, s[42:43] offset:0\n\t" \
    "global_load_lds_dwordx4 v6, s[42:43] offset:1024\n\t" \
    "s_mov_b32 m0, s46\n\t" \
    "s_add_u32 s46, s46, 14336\n\t" \
    "global_load_lds_dwordx4 v0, s[40:41] offset:-2048\n\t" \
    "global_load_lds_dwordx4 v1, s[40:41] offset:-1024\n\t" \
    "global_load_lds_dwordx4 v2, s[40:41] offset:0\n\t" \
    "global_load_lds_dwordx4 v3, s[40:41] offset:1024\n\t" \
    "s_cmp_eq_u32 s46, s52\n\t" \
    "s_cselect_b32 s46, s51, s46\n\t" \
    "s_add_u32 s40, s40, s49\n\t" \
    "s_addc_u32 s41, s41, 0\n\t" \
    "s_sub_u32 s54, s54, 1\n\t" \
    "s_cmp_eq_u32 s54, 0\n\t" \
    "s_cselect_b32 s50, s56, 0\n\t" \
    "s_sub_u32 s40, s40, s50\n\t" \
    "s_subb_u32 s41, s41, 0\n\t" \
    "s_waitcnt vmcnt(22)\n\t" \
    "s_barrier\n\t" \
    "s_add_u32 m0, s58, s53\n\t" \
    "s_nop 0\n\t" \
    "global_load_lds_dwordx4 v5, s[44:45] offset:0\n\t" \
    "global_load_lds_dwordx4 v6, s[44:45] offset:1024\n\t" \
    "s_add_u32 s53, s53, 16384\n\t" \
    "s_and_b32 s53, s53, 0xffff\n\t" \
    "s_add_u32 s42, s42, 128\n\t" \
    "s_addc_u32 s43, s43, 0\n\t" \
    "s_add_u32 s44, s44, 128\n\t" \
    "s_addc_u32 s45, s45, 0\n\t" \
    "s_sub_u32 s55, s55, 1\n\t" \
    "s_cmp_eq_u32 s55, 0\n\t" \
    "s_cselect_b32 s50, s57, 0\n\t" \
    "s_sub_u32 s42, s42, s50\n\t" \
    "s_subb_u32 s43, s43, 0\n\t" \
    "s_sub_u32 s44, s44, s50\n\t" \
    "s_subb_u32 s45, s45, 0\n\t" \
    "s_mov_b32 m0, s46\n\t" \
    "s_add_u32 s46, s46, 14336\n\t" \
    "global_load_lds_dwordx4 v0, s[40:41] offset:-2048\n\t" \
    "global_load_lds_dwordx4 v1, s[40:41] offset:-1024\n\t" \
    "global_load_lds_dwordx4 v2, s[40:41] offset:0\n\t" \
    "global_load_lds_dwordx4 v3, s[40:41] offset:1024\n\t" \
    "s_cmp_eq_u32 s46, s52\n\t" \
    "s_cselect_b32 s46, s51, s46\n\t" \
    "s_add_u32 s40, s40, s49\n\t" \
    "s_addc_u32 s41, s41, 0\n\t" \
    "s_sub_u32 s54, s54, 1\n\t" \
    "s_cmp_eq_u32 s54, 0\n\t" \
    "s_cselect_b32 s50, s56, 0\n\t" \
    "s_sub_u32 s40, s40, s50\n\t" \
    "s_subb_u32 s41, s41, 0\n\t" \
    "s_add_u32 m0, s47, s53\n\t" \
    "s_nop 0\n\t" \
    "global_load_lds_dwordx4 v5, s[42:43] offset:0\n\t" \
    "global_load_lds_dwordx4 v6, s[42:43] offset:1024\n\t" \
    "s_waitcnt vmcnt(24)\n\t" \
    "s_barrier\n\t" \
    "s_mov_b32 m0, s46\n\t" \
    "s_add_u32 s46, s46, 14336\n\t" \
    "global_load_lds_dwordx4 v0, s[40:41] offset:-2048\n\t" \
    "global_load_lds_dwordx4 v1, s[40:41] offset:-1024\n\t" \
    "global_load_lds_dwordx4 v2, s[40:41] offset:0\n\t" \
    "global_load_lds_dwordx4 v3, s[40:41] offset:1024\n\t" \
    "s_cmp_eq_u32 s46, s52\n\t" \
    "s_cselect_b32 s46, s51, s46\n\t" \
    "s_add_u32 s40, s40, s49\n\t" \
    "s_addc_u32 s41, s41, 0\n\t" \
    "s_sub_u32 s54, s54, 1\n\t" \
    "s_cmp_eq_u32 s54, 0\n\t" \
    "s_cselect_b32 s50, s56, 0\n\t" \
    "s_sub_u32 s40, s40, s50\n\t" \
    "s_subb_u32 s41, s41, 0\n\t" \
    "s_add_u32 m0, s58, s53\n\t" \
    "s_nop 0\n\t" \
    "global_load_lds_dwordx4 v5, s[44:45] offset:0\n\t" \
    "global_load_lds_dwordx4 v6, s[44:45] offset:1024\n\t" \
    "s_add_u32 s53, s53, 16384\n\t" \
    "s_and_b32 s53, s53, 0xffff\n\t" \
    "s_add_u32 s42, s42, 128\n\t" \
    "s_addc_u32 s43, s43, 0\n\t" \
    "s_add_u32 s44, s44, 128\n\t" \
    "s_addc_u32 s45, s45, 0\n\t" \
    "s_sub_u32 s55, s55, 1\n\t" \
    "s_cmp_eq_u32 s55, 0\n\t" \
    "s_cselect_b32 s50, s57, 0\n\t" \
    "s_sub_u32 s42, s42, s50\n\t" \
    "s_subb_u32 s43, s43, 0\n\t" \
    "s_sub_u32 s44, s44, s50\n\t" \
    "s_subb_u32 s45, s45, 0\n\t" \
    "s_waitcnt vmcnt(24)\n\t" \
    "s_barrier\n\t" \
    "s_cmp_eq_u32 s48, 0\n\t" \
    "s_cbranch_scc1 2f\n\t" \
    "1:\n\t" \
    "s_mov_b32 m0, s46\n\t" \
    "s_add_u32 s46, s46, 14336\n\t" \
    "global_load_lds_dwordx4 v0, s[40:41] offset:-2048\n\t" \
    "global_load_lds_dwordx4 v1, s[40:41] offset:-1024\n\t" \
    "global_load_lds_dwordx4 v2, s[40:41] offset:0\n\t" \
    "global_load_lds_dwordx4 v3, s[40:41] offset:1024\n\t" \
    "s_cmp_eq_u32 s46, s52\n\t" \
    "s_cselect_b32 s46, s51, s46\n\t" \
    "s_add_u32 s40, s40, s49\n\t" \
    "s_addc_u32 s41, s41, 0\n\t" \
    "s_sub_u32 s54, s54, 1\n\t" \
    "s_cmp_eq_u32 s54, 0\n\t" \
    "s_cselect_b32 s50, s56, 0\n\t" \
    "s_sub_u32 s40, s40, s50\n\t" \
    "s_subb_u32 s41, s41, 0\n\t" \
    "s_add_u32 m0, s47, s53\n\t" \
    "s_nop 0\n\t" \
    "global_load_lds_dwordx4 v5, s[42:43] offset:0\n\t" \
    "global_load_lds_dwordx4 v6, s[42:43] offset:1024\n\t" \
    "s_waitcnt vmcnt(24)\n\t" \
    "s_barrier\n\t" \
    "s_mov_b32 m0, s46\n\t" \
    "s_add_u32 s46, s46, 14336\n\t" \
    "global_load_lds_dwordx4 v0, s[40:41] offset:-2048\n\t" \
    "global_load_lds_dwordx4 v1, s[40:41] offset:-1024\n\t" \
    "global_load_lds_dwordx4 v2, s[40:41] offset:0\n\t" \
    "global_load_lds_dwordx4 v3, s[40:41] offset:1024\n\t" \
    "s_cmp_eq_u32 s46, s52\n\t" \
    "s_cselect_b32 s46, s51, s46\n\t" \
    "s_add_u32 s40, s40, s49\n\t" \
    "s_addc_u32 s41, s41, 0\n\t" \
    "s_sub_u32 s54, s54, 1\n\t" \
    "s_cmp_eq_u32 s54, 0\n\t" \
    "s_cselect_b32 s50, s56, 0\n\t" \
    "s_sub_u32 s40, s40, s50\n\t" \
    "s_subb_u32 s41, s41, 0\n\t" \
    "s_add_u32 m0, s58, s53\n\t" \
    "s_nop 0\n\t" \
    "global_load_lds_dwordx4 v5, s[44:45] offset:0\n\t" \
    "global_load_lds_dwordx4 v6, s[44:45] offset:1024\n\t" \
    "s_add_u32 s53, s53, 16384\n\t" \
    "s_and_b32 s53, s53, 0xffff\n\t" \
    "s_add_u32 s42, s42, 128\n\t" \
    "s_addc_u32 s43, s43, 0\n\t" \
    "s_add_u32 s44, s44, 128\n\t" \
    "s_addc_u32 s45, s45, 0\n\t" \
    "s_sub_u32 s55, s55, 1\n\t" \
    "s_cmp_eq_u32 s55, 0\n\t" \
    "s_cselect_b32 s50, s57, 0\n\t" \
    "s_sub_u32 s42, s42, s50\n\t" \
    "s_subb_u32 s43, s43, 0\n\t" \
    "s_sub_u32 s44, s44, s50\n\t" \
    "s_subb_u32 s45, s45, 0\n\t" \
    "s_sub_u32 s48, s48, 1\n\t" \
    "s_cmp_lg_u32 s48, 0\n\t" \
    "s_waitcnt vmcnt(24)\n\t" \
    "s_barrier\n\t" \
    "s_cbranch_scc1 1b\n\t" \
    "2:\n\t" \
    "s_mov_b32 m0, s46\n\t" \
    "s_add_u32 s46, s46, 14336\n\t" \
    "global_load_lds_dwordx4 v0, s[40:41] offset:-2048\n\t" \
    "global_load_lds_dwordx4 v1, s[40:41] offset:-1024\n\t" \
    "global_load_lds_dwordx4 v2, s[40:41] offset:0\n\t" \
    "global_load_lds_dwordx4 v3, s[40:41] offset:1024\n\t" \
    "s_cmp_eq_u32 s46, s52\n\t" \
    "s_cselect_b32 s46, s51, s46\n\t" \
    "s_add_u32 s40, s40, s49\n\t" \
    "s_addc_u32 s41, s41, 0\n\t" \
    "s_sub_u32 s54, s54, 1\n\t" \
    "s_cmp_eq_u32 s54, 0\n\t" \
    "s_cselect_b32 s50, s56, 0\n\t" \
    "s_sub_u32 s40, s40, s50\n\t" \
    "s_subb_u32 s41, s41, 0\n\t" \
    ""
#define LOADER_LOOPS(nstep) (((nstep) - 14) / 2)
#define LOADER_CLOB "s50", "scc", "memory"

namespace {
constexpr int A_SLOT = 16384, B_SLOT = 14336, LDS_A = 0, LDS_B = 65536;
constexpr int LDS_BYTES = 163840;
constexpr unsigned WS_EPOCH = 0, WS_ARR = 1024, WS_F1 = 4096, WS_FO = 6144, WS_FP = 8192;
constexpr unsigned WS_Q = 16384, WS_K = WS_Q + 2097152, WS_V = WS_K + 2097152, WS_O = WS_V + 2097152, WS_PQ = WS_O + 2097152;
constexpr unsigned WS_END = WS_PQ + 7340032;
constexpr int AUX_SC1 = 16;
constexpr unsigned SPIN_MAX = 1u << 20;

DI unsigned cvtpk(float lo, float hi) { f32x2 v = {lo, hi}; bf16x2 b = __builtin_convertvector(v, bf16x2); return __builtin_bit_cast(unsigned, b); }
DI float rbf(float x) { return __uint_as_float(cvtpk(0.0f, x)); }
DI void glds16(const void* sbase, unsigned voff, unsigned lds_base) {
    asm volatile("s_mov_b32 m0, %2\n\ts_nop 0\n\tglobal_load_lds_dwordx4 %1, %0" :: "s"(sbase), "v"(voff), "s"(lds_base) : "memory"); }
DI void wait_vm0() { asm volatile("s_waitcnt vmcnt(0)" ::: "memory"); }
DI void bar() { asm volatile("s_waitcnt lgkmcnt(0)\n\ts_barrier" ::: "memory"); }
DI s16x4 trread(LAS const char* p) { return __builtin_bit_cast(s16x4, __builtin_amdgcn_ds_read_tr16_b64_v4i16((LAS s16x4*)p)); }
DI v4u ld_sc1(__amdgpu_buffer_rsrc_t rs, unsigned off) { return __builtin_amdgcn_raw_buffer_load_b128(rs, off, 0, AUX_SC1); }
DI void st_sc1(__amdgpu_buffer_rsrc_t rs, unsigned off, v4u v) { __builtin_amdgcn_raw_buffer_store_b128(v, rs, off, 0, AUX_SC1); }
DI void flag_store(char* ws, unsigned off, u64 tag) { __hip_atomic_store((GAS u64*)(ws + off), tag, __ATOMIC_RELAXED, __HIP_MEMORY_SCOPE_AGENT); }
DI void poll(char* ws, unsigned off, bool active, u64 tag) {
    GAS u64* p = (GAS u64*)(ws + (active ? off : 0u));
    for (unsigned spins = 0; spins < SPIN_MAX; ++spins) {
        const u64 v = __hip_atomic_load(p, __ATOMIC_RELAXED, __HIP_MEMORY_SCOPE_AGENT);
        if (__all(!active || v == tag)) break;
        __builtin_amdgcn_s_sleep(1);
    }
    asm volatile("" ::: "memory");
}

template <int N> DI void wait_vm() { asm volatile("s_waitcnt vmcnt(%0)" :: "i"(N) : "memory"); }
struct NoTail { static constexpr int count(int, int) { return 0; } template <int W, int K> DI void issue() const {} };
template <class TAIL> constexpr int tail_sum(int w, int k) { int t = 0; for (int i = 0; i <= k; ++i) t += TAIL::count(w, i); return t; }
template <int W, int NB, int NA, class TAIL>
DI void g224_tail(const TAIL& t) {
    constexpr int n = NB + NA;
#define G224_TS(K, RING) t.template issue<W, K>(); if (RING >= 0) wait_vm<(RING >= 0 ? RING + tail_sum<TAIL>(W, K) : 0)>(); asm volatile("s_barrier" ::: "memory");
    G224_TS(0, 3 * n + NB) G224_TS(1, 2 * n + NB) G224_TS(2, n + NB) G224_TS(3, NB) G224_TS(4, 0) G224_TS(5, -1)
    t.template issue<W, 6>(); wait_vm<tail_sum<TAIL>(W, 6) - tail_sum<TAIL>(W, 2)>();
    asm volatile("s_barrier" ::: "memory");
#undef G224_TS
}
template <int NB, int NA>
DI void g224_loader(unsigned lds0, int w, int lane, const char* a0, const char* a1, unsigned lda, const char* bbase, unsigned ldb, int nstep, int rot) {
    const int b0 = 5 * w, i0 = 3 * w;
    unsigned vo[5], va[3];
#pragma unroll
    for (int i = 0; i < 5; ++i) {
        const unsigned o = (b0 + i) * 1024 + lane * 16;
        const unsigned row = o / 448, pc = (o - row * 448) >> 4;
        vo[i] = (i < NB) ? row * ldb + ((pc ^ (((row >> 3) & 1) << 1)) * 16) + 2048 - i * 1024 : 0u;
    }
    const int rl = lane >> 3, ch = lane & 7;
#pragma unroll
    for (int i = 0; i < 3; ++i) {
        const int ii = i0 + i;
        va[i] = (i < NA) ? (ii * 8 + rl) * lda + ((ch ^ ((((ii & 1) * 8 + rl) >> 1) & 7)) * 16) + 2048 - i * 1024 : 0u;
    }
    const unsigned bdst = lds0 + LDS_B + b0 * 1024 + 2048, adst = lds0 + LDS_A + i0 * 1024;
    const int rb = 2 * rot, ra = rot;
    const char* pb = bbase + (size_t)rb * 32 * ldb; const char* pa0 = a0 + ra * 128 - 2048; const char* pa1 = a1 + ra * 128 - 2048;
    unsigned s46 = bdst, s47 = adst, s48 = LOADER_LOOPS(nstep), s49 = 32 * ldb, s51 = bdst, s52 = bdst + 6 * B_SLOT, s53 = 0, s54 = nstep - rb, s55 = nstep / 2 - ra,
             s56 = nstep * 32 * ldb, s57 = (nstep / 2) * 128, s58 = adst + 8192;
    if constexpr (NB == 5) asm volatile(LOADER53 : "+{s[40:41]}"(pb), "+{s[42:43]}"(pa0), "+{s[44:45]}"(pa1), "+{s46}"(s46), "+{s48}"(s48), "+{s53}"(s53), "+{s54}"(s54), "+{s55}"(s55)
         : "{v0}"(vo[0]), "{v1}"(vo[1]), "{v2}"(vo[2]), "{v3}"(vo[3]), "{v4}"(vo[4]), "{v5}"(va[0]), "{v6}"(va[1]), "{v7}"(va[2]),
           "{s47}"(s47), "{s49}"(s49), "{s51}"(s51), "{s52}"(s52), "{s56}"(s56), "{s57}"(s57), "{s58}"(s58) : LOADER_CLOB);
    else asm volatile(LOADER42 : "+{s[40:41]}"(pb), "+{s[42:43]}"(pa0), "+{s[44:45]}"(pa1), "+{s46}"(s46), "+{s48}"(s48), "+{s53}"(s53), "+{s54}"(s54), "+{s55}"(s55)
         : "{v0}"(vo[0]), "{v1}"(vo[1]), "{v2}"(vo[2]), "{v3}"(vo[3]), "{v4}"(vo[4]), "{v5}"(va[0]), "{v6}"(va[1]), "{v7}"(va[2]),
           "{s47}"(s47), "{s49}"(s49), "{s51}"(s51), "{s52}"(s52), "{s56}"(s56), "{s57}"(s57), "{s58}"(s58) : LOADER_CLOB);
}
DI void touch(const char* p, unsigned lds_dummy) {
    asm volatile("s_mov_b32 m0, %1\n\ts_nop 0\n\tglobal_load_lds_dword %0, off" :: "v"(p), "s"(lds_dummy) : "memory");
}
constexpr int LDS_DUMMY = LDS_B + 6 * B_SLOT;
constexpr int LDS_CNT = LDS_BYTES - 64;
constexpr int LDS_TAG = LDS_BYTES - 32;
constexpr int B2_PD = 5;
constexpr int ENG_FIRST = 2;
DI int a1_rot(int x) { return 2 * (x >> 1); }
DI int b1_rot(int x) { return 2 * x; }
template <class FI, class EX, class TAIL>
DI void g224(unsigned lds0, int wid, int lane, const char* a0, const char* a1, unsigned lda, const char* bbase, unsigned ldb, int nstep, int rot, bool swap, f32x16 (&c)[7], FI first, EX extra, const TAIL& tail) {
    if (wid == 7) {
        first();
        asm volatile("s_barrier\n\ts_barrier" ::: "memory");
        for (int s = 0; s < nstep - 7; ++s) {
            extra(s);
            asm volatile("s_barrier" ::: "memory");
        }
        g224_tail<3, 0, 0>(tail);
    }
    else if (wid == 6) { g224_loader<4, 2>(lds0, 2, lane, a0, a1, lda, bbase, ldb, nstep, rot); g224_tail<2, 4, 2>(tail); }
    else if (wid == 5) { g224_loader<5, 3>(lds0, 1, lane, a0, a1, lda, bbase, ldb, nstep, rot); g224_tail<1, 5, 3>(tail); }
    else if (wid == 4) { g224_loader<5, 3>(lds0, 0, lane, a0, a1, lda, bbase, ldb, nstep, rot); g224_tail<0, 5, 3>(tail); }
    else {
        const int mh = wid & 1, nh = wid >> 1;
        const int r16 = lane & 15, g = lane >> 4, q = (lane & 15) >> 2, p = lane & 3;
        const unsigned aoff0 = lds0 + LDS_A + (mh * 64 + r16) * 128 + ((g ^ ((r16 >> 1) & 7)) * 16);
        const unsigned brow = lds0 + LDS_B + (8 * g + q) * 448 + p * 8 + nh * 224;
        const unsigned gb = (g & 1) * 32;
        const unsigned bE = nh ? brow - gb : brow + gb, bO = nh ? brow + gb : brow - gb;
        unsigned aslot, bslot, cnt = nstep / 2 - 1;
        if (swap) asm volatile(G224_BODY_SWAP
            : "={v[0:15]}"(c[0]), "={v[16:31]}"(c[1]), "={v[32:47]}"(c[2]), "={v[48:63]}"(c[3]), "={v[64:79]}"(c[4]), "={v[80:95]}"(c[5]), "={v[96:111]}"(c[6]),
              [aslot] "=&s"(aslot), [bslot] "=&s"(bslot), [cnt] "+s"(cnt)
            : "{v200}"(aoff0), "{v201}"(aoff0 ^ 64), "{v202}"(bE), "{v203}"(bO) : G224_CLOB);
        else asm volatile(G224_BODY
            : "={v[0:15]}"(c[0]), "={v[16:31]}"(c[1]), "={v[32:47]}"(c[2]), "={v[48:63]}"(c[3]), "={v[64:79]}"(c[4]), "={v[80:95]}"(c[5]), "={v[96:111]}"(c[6]),
              [aslot] "=&s"(aslot), [bslot] "=&s"(bslot), [cnt] "+s"(cnt)
            : "{v200}"(aoff0), "{v201}"(aoff0 ^ 64), "{v202}"(bE), "{v203}"(bO) : G224_CLOB);
    }
}

DI void open_launch(LAS char* L, char* ws, int lane) {
    if (lane == 0) {
        const u64 t = __hip_atomic_load((GAS u64*)(ws + WS_EPOCH), __ATOMIC_RELAXED, __HIP_MEMORY_SCOPE_AGENT) + 1;
        *(LAS u64*)(L + LDS_TAG) = t;
        flag_store(ws, WS_ARR + blockIdx.x * 8, t);
    }
    asm volatile("s_waitcnt lgkmcnt(0)" ::: "memory");
}
DI u64 get_tag(LAS char* L) { return *(volatile LAS u64*)(L + LDS_TAG); }
DI bool last_arriver(LAS char* L, int k, unsigned n, int lane) {
    unsigned old = 0;
    if (lane == 0) old = __hip_atomic_fetch_add((LAS unsigned*)(L + LDS_CNT + k * 4), 1u, __ATOMIC_RELAXED, __HIP_MEMORY_SCOPE_WORKGROUP);
    return __builtin_amdgcn_readfirstlane(old) == n - 1;
}
struct FinalAddr { unsigned so[2]; char* gp[2]; };
DI FinalAddr final_addr(int lane, int wid, unsigned stg, char* outp  ) {
    FinalAddr f;
    const int m = wid & 3, hi = wid >> 2;
#pragma unroll
    for (int k = 0; k < 2; ++k) {
        const int idx = k * 64 + lane, row = hi ? min(idx, 95) / 6 : idx >> 3, chn = hi ? min(idx, 95) - 6 * row : idx & 7;
        f.so[k] = stg + wid * 2304 + row * 144 + chn * 16;
        f.gp[k] = outp + (size_t)(m * 16 + row) * 1792 + hi * 128 + chn * 16;
        asm volatile("" : "+v"(f.so[k]), "+v"(f.gp[k]));
    }
    return f;
}
template <bool NEED_BAR>
DI void final_store(LAS char* L, int wid, int lane, f32x4 (&acc)[7], float sign, unsigned red, unsigned stg, const FinalAddr& fa) {
    if (NEED_BAR) bar();
    const int m = wid & 3, t16 = lane & 15, g = lane >> 4;
    LAS char* rx = L + red + m * 7168 + lane * 16;
    LAS char* st = L + stg + wid * 2304 + t16 * 144 + g * 8;
    if (wid < 4) {
#pragma unroll
        for (int n = 4; n < 7; ++n) *(LAS f32x4*)(rx + n * 1024) = acc[n];
        bar();
        f32x4 o[4];
#pragma unroll
        for (int n = 0; n < 4; ++n) o[n] = *(const LAS f32x4*)(rx + n * 1024);
#pragma unroll
        for (int n = 0; n < 4; ++n) {
            v2u w; w[0] = cvtpk(acc[n][0] + sign * o[n][0], acc[n][1] + sign * o[n][1]); w[1] = cvtpk(acc[n][2] + sign * o[n][2], acc[n][3] + sign * o[n][3]);
            *(LAS v2u*)(st + n * 32) = w;
        }
    } else {
#pragma unroll
        for (int n = 0; n < 4; ++n) *(LAS f32x4*)(rx + n * 1024) = acc[n];
        bar();
        f32x4 o[3];
#pragma unroll
        for (int n = 0; n < 3; ++n) o[n] = *(const LAS f32x4*)(rx + (4 + n) * 1024);
#pragma unroll
        for (int n = 0; n < 3; ++n) {
            v2u w; w[0] = cvtpk(o[n][0] + sign * acc[4 + n][0], o[n][1] + sign * acc[4 + n][1]); w[1] = cvtpk(o[n][2] + sign * acc[4 + n][2], o[n][3] + sign * acc[4 + n][3]);
            *(LAS v2u*)(st + n * 32) = w;
        }
    }
    asm volatile("s_waitcnt lgkmcnt(0)" ::: "memory");
    const v4u v0 = *(const LAS v4u*)(L + fa.so[0]), v1 = *(const LAS v4u*)(L + fa.so[1]);
    *(GAS v4u*)fa.gp[0] = v0;
    if (wid < 4 || lane < 32) *(GAS v4u*)fa.gp[1] = v1;
}

DI v4u pack_pair(const f32x16& ca, const f32x16& cb, int m) {
    unsigned a0 = cvtpk(ca[4 * m], ca[4 * m + 1]), a1 = cvtpk(ca[4 * m + 2], ca[4 * m + 3]);
    unsigned b0 = cvtpk(cb[4 * m], cb[4 * m + 1]), b1 = cvtpk(cb[4 * m + 2], cb[4 * m + 3]);
    auto s0 = __builtin_amdgcn_permlane32_swap(a0, b0, false, false);
    auto s1 = __builtin_amdgcn_permlane32_swap(a1, b1, false, false);
    auto t0 = __builtin_amdgcn_permlane16_swap(s0[0], s0[1], false, false);
    auto t1 = __builtin_amdgcn_permlane16_swap(s1[0], s1[1], false, false);
    return (v4u){t0[0], t1[0], t0[1], t1[1]};
}
DI v4u pack_single(const f32x16& ca, int m) {
    unsigned a0 = cvtpk(ca[4 * m], ca[4 * m + 1]), a1 = cvtpk(ca[4 * m + 2], ca[4 * m + 3]);
    auto t0 = __builtin_amdgcn_permlane16_swap(a0, a0, false, false);
    auto t1 = __builtin_amdgcn_permlane16_swap(a1, a1, false, false);
    return (v4u){t0[0], t1[0], t0[1], t1[1]};
}

constexpr unsigned W_POS[22] = {151552, 0, 7168, 94208, 101376, 14336, 21504, 108544, 115712, 122880, 130048, 28672, 35840, 137216, 144384, 65536, 72704,
                                43008, 50176, 57344, 79872, 87040};
constexpr int W_STEP[22] = {0, 0, 0, 1, 1, 2, 2, 2, 2, 3, 3, 4, 4, 4, 4, 5, 5, 6, 6, 6, 6, 6};
struct WTail {
    const char* wre; const char* wim; unsigned wvo, lds0;
    static constexpr int count(int w, int k) { int t = 0; for (int c = 0; c < 22; ++c) if ((c & 3) == w && W_STEP[c] == k) t += 7; return t; }
    DI void chunk(int u, int pq, unsigned pos) const {
        const char* src = (pq ? wim : wre) + u * 64;
#pragma unroll
        for (int k = 0; k < 7; ++k) glds16(src + k * 16 * 896, wvo, __builtin_amdgcn_readfirstlane(lds0 + pos + k * 1024));
    }
    template <int W, int K> DI void issue() const {
#pragma unroll
        for (int c = 0; c < 22; ++c) if ((c & 3) == W && W_STEP[c] == K) chunk(c >> 1, c & 1, W_POS[c]);
    }
};
DI void role_b(char* lds, int x, int i, int wid, int lane, const char* Ax, const char* Wqkv, const char* Wo, const char* Bx, const char* Cre, const char* Cim, const char* Wre, const char* Wim, char* out, char* ws) {
    const unsigned lds0 = (unsigned)(uintptr_t)lds;
    LAS char* L = (LAS char*)lds;
    const int kh = x & 1, ch = i & 1, tb = 8 * (x >> 1) + (i >> 1), cb = 2 * kh + ch;
    const __amdgpu_buffer_rsrc_t rs = __builtin_amdgcn_make_buffer_rsrc(ws, 0, WS_END, 0x00020000);
    f32x16 c[7];

    WTail wt;
    wt.wre = Wre + (size_t)cb * 112 * 896; wt.wim = Wim + (size_t)cb * 112 * 896; wt.lds0 = lds0;
    wt.wvo = (lane >> 2) * 896 + (((lane & 3) ^ ((((lane >> 5) ^ (lane >> 4)) & 1) * 2 + (lane >> 5))) * 16);
    g224(lds0, wid, lane, Cre + ((size_t)tb * 64 * 2048 + kh * 1024) * 2, Cim + ((size_t)tb * 64 * 2048 + kh * 1024) * 2, 4096,
         Bx + ((size_t)kh * 1024 * 448 + ch * 224) * 2, 896, 32, b1_rot(x), false, c,
         [&] { open_launch(L, ws, lane); },
         [&](int s) {
            if (s == 20 && blockIdx.x == 0) {
                const u64 tg = get_tag(L);
                for (unsigned spins = 0; spins < SPIN_MAX; ++spins) {
                    bool ok = true;
#pragma unroll
                    for (int k = 0; k < 4; ++k) ok &= __hip_atomic_load((GAS u64*)(ws + WS_ARR + (k * 64 + lane) * 8), __ATOMIC_RELAXED, __HIP_MEMORY_SCOPE_AGENT) == tg;
                    if (__all(ok)) break;
                    __builtin_amdgcn_s_sleep(4);
                }
                if (lane == 0) flag_store(ws, WS_EPOCH, tg);
            }
         }, wt);

    const u64 tag = get_tag(L);
    if (wid < 4) {
        const int mh = wid & 1, nh = wid >> 1, g = lane >> 4;
        const unsigned base = WS_PQ + ((kh * 2 + mh) * 128 + tb * 4) * 14336 + (ch * 7) * 1024 + lane * 16;
#pragma unroll
        for (int m = 0; m < 4; ++m) {
            const unsigned bm = base + m * 14336;
            if (nh == 0) {
                st_sc1(rs, bm, pack_pair(c[0], c[1], m)); st_sc1(rs, bm + 1024, pack_pair(c[2], c[3], m)); st_sc1(rs, bm + 2048, pack_pair(c[4], c[5], m));
                const v4u s = pack_single(c[6], m);
                if ((g & 1) == 0) st_sc1(rs, bm + 3072 - lane * 16 + ((g >> 1) * 16 + (lane & 15)) * 16, s);
            } else {
                const v4u s = pack_single(c[0], m);
                if ((g & 1) == 0) st_sc1(rs, bm + 3072 - lane * 16 + ((2 + (g >> 1)) * 16 + (lane & 15)) * 16, s);
                st_sc1(rs, bm + 4096, pack_pair(c[1], c[2], m)); st_sc1(rs, bm + 5120, pack_pair(c[3], c[4], m)); st_sc1(rs, bm + 6144, pack_pair(c[5], c[6], m));
            }
        }
    }
    const int m = wid & 3, pq = wid >> 2;
    const int e16 = lane & 15, g = lane >> 4, rq = e16 >> 2;
    const unsigned wro = e16 * 64 + ((g ^ ((((rq >> 1) ^ rq) & 1) * 2 + (rq >> 1))) * 16);
    auto wpos = [&](int u) { const int uu = u < 11 ? u : u - 11; return pq ? W_POS[2 * uu + 1] : W_POS[2 * uu]; };
    bf16x8 w[3][7];
#pragma unroll
    for (int u = 0; u < 3; ++u)
#pragma unroll
        for (int n = 0; n < 7; ++n) w[u][n] = *(const LAS bf16x8*)(L + wro + wpos(u) + n * 1024);

    bar();
    if (wid >= 4) {
        for (int s = wid - 4; s < 6; s += 4) wt.chunk(11 + (s >> 1), s & 1, s == 0 ? W_POS[0] : s == 1 ? W_POS[1] : s == 2 ? W_POS[2] : s == 3 ? W_POS[3] : s == 4 ? W_POS[4] : W_POS[5]);
        wait_vm0();
    } else {
        wait_vm0();

        if (last_arriver(L, 0, 4, lane)) {
            if (lane == 0) flag_store(ws, WS_FP + (tb * 4 + cb) * 8, tag);

            poll(ws, WS_FP + (tb * 4 + (lane & 3)) * 8, lane < 4, tag);
        }
    }

    bar();

    const unsigned pb = WS_PQ + (pq * 128 + tb * 4 + m) * 14336 + lane * 16;
    constexpr int PD = B2_PD;
    v4u p0[14], p1[14];
    auto pay = [&](int u) { p0[u] = ld_sc1(rs, pb + u * 1024); p1[u] = ld_sc1(rs, pb + 2 * 128 * 14336 + u * 1024); };
#pragma unroll
    for (int u = 0; u < PD; ++u) pay(u);
    asm volatile("" ::: "memory"); __builtin_amdgcn_sched_barrier(0);
    const FinalAddr fa = final_addr(lane, wid, 32768, out + ((size_t)tb * 64 * 896 + 448 + cb * 112) * 2);
    f32x4 acc[7];
#pragma unroll
    for (int n = 0; n < 7; ++n) acc[n] = (f32x4){0.f, 0.f, 0.f, 0.f};
#pragma unroll
    for (int u = 0; u < 14; ++u) {
        if (u + PD < 14) { pay(u + PD); __builtin_amdgcn_sched_barrier(0); }
#pragma unroll
        for (int n = 0; n < 7; ++n) acc[n] = __builtin_amdgcn_mfma_f32_16x16x32_bf16(w[u % 3][n], __builtin_bit_cast(bf16x8, p0[u]), acc[n], 0, 0, 0);
#pragma unroll
        for (int n = 0; n < 7; ++n) acc[n] = __builtin_amdgcn_mfma_f32_16x16x32_bf16(w[u % 3][n], __builtin_bit_cast(bf16x8, p1[u]), acc[n], 0, 0, 0);
        if (u + 3 < 14) {
#pragma unroll
            for (int n = 0; n < 7; ++n) w[u % 3][n] = *(const LAS bf16x8*)(L + wro + wpos(u + 3) + n * 1024);
        }
        __builtin_amdgcn_sched_barrier(0);
    }

    final_store<true>(L, wid, lane, acc, -1.0f, 0, 32768, fa);

}

DI void attention(int iq, int head, int lane, __amdgpu_buffer_rsrc_t rs) {
    float SC = 0.18898223650461362f, L2E = 1.4426950408889634f;
    asm volatile("" : "+v"(SC), "+v"(L2E));
    const int a = lane & 31, hi = lane >> 5;
    const unsigned hb = head * 64 * 2048 + lane * 16;
    v4u kf[9][2], vf[9][2];
    const bf16x8 q0 = __builtin_bit_cast(bf16x8, ld_sc1(rs, WS_Q + hb + iq * 2048)), q1 = __builtin_bit_cast(bf16x8, ld_sc1(rs, WS_Q + hb + iq * 2048 + 1024));
#pragma unroll
    for (int jj = 0; jj < 9; ++jj) { const int j = jj == 0 ? 0 : jj == 1 ? 8 : jj - 1; const int kt = max(iq - 8 + j, 0);
        kf[j][0] = ld_sc1(rs, WS_K + hb + kt * 2048); kf[j][1] = ld_sc1(rs, WS_K + hb + kt * 2048 + 1024); }
    asm volatile("" ::: "memory"); __builtin_amdgcn_sched_barrier(0);
    const f32x16 zero = {};
    f32x16 S[8], so, sd;
    so = __builtin_amdgcn_mfma_f32_32x32x16_bf16(__builtin_bit_cast(bf16x8, kf[0][0]), q0, zero, 0, 0, 0);
    so = __builtin_amdgcn_mfma_f32_32x32x16_bf16(__builtin_bit_cast(bf16x8, kf[0][1]), q1, so, 0, 0, 0);
    sd = __builtin_amdgcn_mfma_f32_32x32x16_bf16(__builtin_bit_cast(bf16x8, kf[8][0]), q0, zero, 0, 0, 0);
    sd = __builtin_amdgcn_mfma_f32_32x32x16_bf16(__builtin_bit_cast(bf16x8, kf[8][1]), q1, sd, 0, 0, 0);
#pragma unroll
    for (int j = 1; j < 8; ++j) {
        S[j] = __builtin_amdgcn_mfma_f32_32x32x16_bf16(__builtin_bit_cast(bf16x8, kf[j][0]), q0, zero, 0, 0, 0);
        S[j] = __builtin_amdgcn_mfma_f32_32x32x16_bf16(__builtin_bit_cast(bf16x8, kf[j][1]), q1, S[j], 0, 0, 0);
    }
    __builtin_amdgcn_sched_barrier(0);
#pragma unroll
    for (int jj = 0; jj < 9; ++jj) { const int j = jj == 0 ? 8 : jj - 1; const int kt = max(iq - 8 + j, 0);
        vf[j][0] = ld_sc1(rs, WS_V + hb + kt * 2048); vf[j][1] = ld_sc1(rs, WS_V + hb + kt * 2048 + 1024); }
    asm volatile("" ::: "memory"); __builtin_amdgcn_sched_barrier(0);
    if (iq < 8) {
        asm volatile("s_nop 7\n\ts_nop 7\n\ts_nop 7");
#pragma unroll
        for (int r = 0; r < 16; ++r) { float t; asm volatile("v_mov_b32 %0, 0xff800000" : "=v"(t)); so[r] = t; }
#pragma unroll
        for (int j = 1; j < 8; ++j) if (iq - 8 + j < 0) {
#pragma unroll
            for (int r = 0; r < 16; ++r) { float t; asm volatile("v_mov_b32 %0, 0xff800000" : "=v"(t)); S[j][r] = t; }
        }
    }
#pragma unroll
    for (int r = 0; r < 16; ++r) { const int b = (r & 3) + 8 * (r >> 2) + 4 * hi; S[0][r] = (b <= a) ? sd[r] : so[r]; }
    float mx = S[0][0];
#pragma unroll
    for (int j = 0; j < 8; ++j)
#pragma unroll
        for (int r = 0; r < 16; ++r) mx = __builtin_fmaxf(mx, S[j][r]);
    { auto rr = __builtin_amdgcn_permlane32_swap(__float_as_uint(mx), __float_as_uint(mx), false, false);
      mx = __builtin_fmaxf(__uint_as_float(rr[0]), __uint_as_float(rr[1])); }
    const float nml = -(rbf(rbf(mx) * SC) * L2E);
    f32x16 O = zero;
#pragma unroll
    for (int j = 0; j < 8; ++j) {
        v4u pw[2];
#pragma unroll
        for (int k = 0; k < 8; ++k) {
            const f32x2 y = (f32x2){rbf(S[j][2 * k]), rbf(S[j][2 * k + 1])} * (f32x2){SC, SC};
            const f32x2 z = __builtin_elementwise_fma((f32x2){rbf(y[0]), rbf(y[1])}, (f32x2){L2E, L2E}, (f32x2){nml, nml});
            const float e0 = __builtin_amdgcn_exp2f(z[0]), e1 = __builtin_amdgcn_exp2f(z[1]);
            pw[k >> 2][k & 3] = cvtpk(e0, e1);
        }
        if (j == 0) {
            v4u pd[2], po[2];
#pragma unroll
            for (int k = 0; k < 8; ++k) {
                const int b = ((2 * k) & 3) + 8 * ((2 * k) >> 2) + 4 * hi;
                const unsigned msk = (b <= a ? 0xffffu : 0u) | (b + 1 <= a ? 0xffff0000u : 0u);
                pd[k >> 2][k & 3] = pw[k >> 2][k & 3] & msk; po[k >> 2][k & 3] = pw[k >> 2][k & 3] & ~msk;
            }
            O = __builtin_amdgcn_mfma_f32_32x32x16_bf16(__builtin_bit_cast(bf16x8, vf[8][0]), __builtin_bit_cast(bf16x8, pd[0]), O, 0, 0, 0);
            O = __builtin_amdgcn_mfma_f32_32x32x16_bf16(__builtin_bit_cast(bf16x8, vf[8][1]), __builtin_bit_cast(bf16x8, pd[1]), O, 0, 0, 0);
            O = __builtin_amdgcn_mfma_f32_32x32x16_bf16(__builtin_bit_cast(bf16x8, vf[0][0]), __builtin_bit_cast(bf16x8, po[0]), O, 0, 0, 0);
            O = __builtin_amdgcn_mfma_f32_32x32x16_bf16(__builtin_bit_cast(bf16x8, vf[0][1]), __builtin_bit_cast(bf16x8, po[1]), O, 0, 0, 0);
        } else {
            O = __builtin_amdgcn_mfma_f32_32x32x16_bf16(__builtin_bit_cast(bf16x8, vf[j][0]), __builtin_bit_cast(bf16x8, pw[0]), O, 0, 0, 0);
            O = __builtin_amdgcn_mfma_f32_32x32x16_bf16(__builtin_bit_cast(bf16x8, vf[j][1]), __builtin_bit_cast(bf16x8, pw[1]), O, 0, 0, 0);
        }
    }
    auto lr = __builtin_amdgcn_permlane32_swap(__float_as_uint(O[12]), __float_as_uint(O[12]), false, false);
    const float rl = 1.0f / __uint_as_float(lr[1]);
    v4u o0, o1;
#pragma unroll
    for (int k = 0; k < 4; ++k) { o0[k] = cvtpk(O[2 * k] * rl, O[2 * k + 1] * rl); o1[k] = cvtpk(O[8 + 2 * k] * rl, O[8 + 2 * k + 1] * rl); }
    if (hi) { o1[2] = 0; o1[3] = 0; }
    const unsigned ob = WS_O + (head * 128 + iq * 2 + (a >> 4)) * 1024 + (hi * 16 + (a & 15)) * 16;
    st_sc1(rs, ob, o0); st_sc1(rs, ob + 512, o1);
}

DI void role_a(char* lds, int x, int ai, int wid, int lane, const char* Ax, const char* Wqkv, const char* Wo, const char* Bx, const char* Cre, const char* Cim, const char* Wre, const char* Wim, char* out, char* ws) {
    const unsigned lds0 = (unsigned)(uintptr_t)lds;
    LAS char* L = (LAS char*)lds;
    const __amdgpu_buffer_rsrc_t rs = __builtin_amdgcn_make_buffer_rsrc(ws, 0, WS_END, 0x00020000);
    const int iq = 8 * x + (ai >> 1), hh = ai & 1, cb = (iq & 1) * 2 + hh, rp = iq >> 1;
    if (ai < 12) {
        const int rb = 4 * (x >> 1) + ai / 3, j = 3 * (x & 1) + ai % 3;
        const bool isv = j >= 4;
        f32x16 c[7];

        unsigned ioff[7] = {};
        if (wid < 4) {
            const int mh = wid & 1, nh = (wid >> 1) & 1, l16 = lane & 15, g = lane >> 4;
#pragma unroll
            for (int n = 0; n < 7; ++n) {
                const int u4 = nh * 28 + 4 * n + g, h8 = u4 / 7, du = u4 - 7 * h8;
                const int col = nh * 112 + 16 * n + l16, hv = col / 28, d = col - 28 * hv;
                ioff[n] = lds0 + mh * 4096 + (isv ? hv * 8192 + ((g & 1) * 32 + d) * 16 + (g >> 1) * 8
                                                  : h8 * 8192 + (du >> 2) * 1024 + (((du >> 1) & 1) * 32 + l16) * 16 + (du & 1) * 8);
                asm volatile("" : "+v"(ioff[n]));
            }
        }
        g224(lds0, wid, lane, Ax + (size_t)rb * 128 * 896, Ax + ((size_t)rb * 128 + 64) * 896, 896, Wqkv + j * 448, 2688, 14, a1_rot(x), isv, c,
             [&] { open_launch(L, ws, lane); },
             [](int) {}, NoTail());

        if (wid < 4) {
            if (isv) {
#pragma unroll
                for (int n = 0; n < 7; ++n)
#pragma unroll
                    for (int m = 0; m < 4; ++m) {
                        v2u w; w[0] = cvtpk(c[n][4 * m], c[n][4 * m + 1]); w[1] = cvtpk(c[n][4 * m + 2], c[n][4 * m + 3]);
                        *(LAS v2u*)(uintptr_t)(ioff[n] + (m >> 1) * 2048 + (m & 1) * 1024) = w;
                    }
            } else {
#pragma unroll
                for (int n = 0; n < 7; ++n)
#pragma unroll
                    for (int m = 0; m < 4; ++m) {
                        v2u w; w[0] = cvtpk(c[n][4 * m], c[n][4 * m + 1]); w[1] = cvtpk(c[n][4 * m + 2], c[n][4 * m + 3]);
                        *(LAS v2u*)(uintptr_t)(ioff[n] + (m >> 1) * 2048 + (m & 1) * 256) = w;
                    }
            }
        } else {
            const int t = (wid - 4) * 64 + lane;
            if (!isv) {
#pragma unroll
                for (int k = 0; k < 4; ++k) { const int e = t + 256 * k, h8 = e >> 7, tok = e & 127;
                    *(LAS v2u*)(L + h8 * 8192 + (tok >> 5) * 2048 + 1024 + (32 + (tok & 31)) * 16 + 8) = (v2u){0u, 0u}; }
            } else {
#pragma unroll
                for (int k = 0; k < 2; ++k) { const int e = t + 256 * k, dd = e & 3; const unsigned val = dd == 0 ? 0x3f803f80u : 0u;
                    *(LAS v4u*)(L + (e >> 6) * 8192 + ((e >> 4) & 3) * 2048 + ((e >> 3) & 1) * 1024 + (((e >> 2) & 1) * 32 + 28 + dd) * 16) = (v4u){val, val, val, val}; }
            }
        }

        bar();

        const unsigned gb = (j < 2 ? WS_Q : j < 4 ? WS_K : WS_V) + ((j & 1) * 8 * 64 + rb * 4) * 2048 + lane * 16;
        {
            v4u v[8];
#pragma unroll
            for (int k = 0; k < 8; ++k) v[k] = *(const LAS v4u*)(L + (wid * 8 + k) * 1024 + lane * 16);
#pragma unroll
            for (int k = 0; k < 8; ++k) st_sc1(rs, gb + wid * 64 * 2048 + k * 1024, v[k]);
        }

        wait_vm0();

        asm volatile("s_waitcnt lgkmcnt(0)" ::: "memory");
        if (last_arriver(L, 0, 8, lane) && lane == 0) flag_store(ws, WS_F1 + (rb * 8 + j) * 8, get_tag(L));
        bar();

    }
    if (ai >= 12) {
        if (wid == 7) open_launch(L, ws, lane);
        const unsigned dummy = lds0 + LDS_DUMMY;
        const int gw = (ai - 12) * 8 + wid;
        if (gw < 8) {
            u64 cb0, ce0;
            asm volatile("s_getpc_b64 s[60:61]\n1:\n\ts_mov_b64 s[62:63], s[60:61]\n\ts_sub_u32 s60, s60, 1b-fused_code_begin\n\ts_subb_u32 s61, s61, 0\n\t"
                         "s_add_u32 s62, s62, fused_code_end-1b\n\ts_addc_u32 s63, s63, 0" : "={s[60:61]}"(cb0), "={s[62:63]}"(ce0) :: "scc");
            const u64 a = (cb0 & ~127ull) + (u64)(gw * 64 + lane) * 128;
            if (a < ce0) touch((const char*)a, dummy);
        }
        else if (gw < 12) { const int pg = (gw - 8) * 64 + lane; if (pg < 240) touch(ws + (size_t)pg * 65536, dummy); }
        else if (gw == 12) { if (lane < 56) touch(out + (size_t)lane * 65536, dummy); }
        else if (gw == 13) { touch((lane < 7 ? Wre : lane < 14 ? Wim : Wo) + (size_t)(lane < 7 ? lane : lane < 14 ? lane - 7 : min(lane - 14, 6)) * 65536, dummy); }
        const int Lr = (gw & 15) * 64 + lane, tbl = Lr >> 7;
        const char* p = (((Lr >> 6) & 1) ? Cim : Cre) + (size_t)((8 * (x >> 1) + tbl) * 64 + (Lr & 63)) * 4096 + (x & 1) * 2048;
        auto eng_w = [&] { for (int k = gw * 8 + x; k < 147; k += 256) { const int Ln = k * 64 + lane;
            touch((Ln < 3136 ? Wre + (size_t)Ln * 128 : Ln < 6272 ? Wim + (size_t)(Ln - 3136) * 128 : Wo + (size_t)(Ln - 6272) * 128), dummy); } };
        for (int a = ENG_FIRST + (gw >> 4); a < 16; a += 2) touch(p + ((a + b1_rot(x)) & 15) * 128, dummy);
        eng_w();
        bar();
    }
    const u64 tag = get_tag(L);
    const int m = wid & 3, k2 = wid >> 2;
    unsigned ad[2][2][7];
    {
        auto wo_stage = [&]() {
            const unsigned wb = (cb * 224) & ~63u;
            const int rr = lane >> 4;
#pragma unroll
            for (int k = 0; k < 14; ++k) {
                const int r4 = wid * 14 + k;
                const int row = 4 * r4 + rr;
                const unsigned vo = rr * 896 + (((lane & 15) ^ ((row & 7) << 1)) * 16);
                glds16(Wo + (size_t)r4 * 4 * 896 + wb, vo, __builtin_amdgcn_readfirstlane(lds0 + r4 * 1024));
            }
        };
        wo_stage();
        const int rq = iq >> 2;
        const int sel = lane == 0 ? 0 : lane < 4 ? 1 : 2;
        const int rb = sel == 0 ? rq : rq - 2 + ((lane - 1) % 3);
        if (wid == 0) poll(ws, WS_F1 + (max(rb, 0) * 8 + sel * 2 + hh) * 8, lane < 7 && rb >= 0, tag);
        asm volatile("s_barrier" ::: "memory");

        attention(iq, hh * 8 + wid, lane, rs);

        {
            const int g = lane >> 4, q = (lane & 15) >> 2, p = lane & 3;
            const int s0 = ((cb * 224) & 63) >> 5;
#pragma unroll
            for (int hp = 0; hp < 2; ++hp)
#pragma unroll
                for (int xx = 0; xx < 2; ++xx) {
                    int unit = 4 * (g >> 1) + 2 * xx + (g & 1); if (unit == 7) unit = 6;
                    const int rl = 4 * unit + q, sw = (4 * hp + rl) & 7;
#pragma unroll
                    for (int n = 0; n < 7; ++n) { ad[hp][xx][n] = lds0 + (k2 * 8 * 28 + hp * 28 + rl) * 256 + (((s0 + n) ^ sw) * 32) + p * 8; asm volatile("" : "+v"(ad[hp][xx][n])); }
                }
        }
        wait_vm0();
        if (last_arriver(L, 1, 8, lane)) {
            if (lane == 0) flag_store(ws, WS_FO + (iq * 2 + hh) * 8, tag);

            poll(ws, WS_FO + (rp * 4 + (lane & 3)) * 8, lane < 4, tag);
        }
        bar();

    }
    {
        v4u of[8];
#pragma unroll
        for (int h = 0; h < 8; ++h) of[h] = ld_sc1(rs, WS_O + ((k2 * 8 + h) * 128 + rp * 4 + m) * 1024 + lane * 16);
        asm volatile("" ::: "memory"); __builtin_amdgcn_sched_barrier(0);
        const FinalAddr fa = final_addr(lane, wid, 114688 + 28672, out + ((size_t)rp * 64 * 896 + cb * 112) * 2);
        f32x4 acc[7];
#pragma unroll
        for (int n = 0; n < 7; ++n) acc[n] = (f32x4){0.f, 0.f, 0.f, 0.f};
        auto wfrag = [&](int h, int n) {
            const s16x4 lo = trread((LAS const char*)(uintptr_t)(ad[h & 1][0][n] + (h >> 1) * 2 * 7168)), hi2 = trread((LAS const char*)(uintptr_t)(ad[h & 1][1][n] + (h >> 1) * 2 * 7168));
            return (bf16x8){lo[0], lo[1], lo[2], lo[3], hi2[0], hi2[1], hi2[2], hi2[3]};
        };
        bf16x8 w[2][7];
#pragma unroll
        for (int n = 0; n < 7; ++n) w[0][n] = wfrag(0, n);
#pragma unroll
        for (int h = 0; h < 8; ++h) {
            if (h + 1 < 8) {
#pragma unroll
                for (int n = 0; n < 7; ++n) w[(h + 1) & 1][n] = wfrag(h + 1, n);
            }
            __builtin_amdgcn_sched_barrier(0);
#pragma unroll
            for (int n = 0; n < 7; ++n) acc[n] = __builtin_amdgcn_mfma_f32_16x16x32_bf16(w[h & 1][n], __builtin_bit_cast(bf16x8, of[h]), acc[n], 0, 0, 0);
            __builtin_amdgcn_sched_barrier(0);
        }
        final_store<false>(L, wid, lane, acc, 1.0f, 114688, 114688 + 28672, fa);

    }
}
}

__global__ void __launch_bounds__(512, 2)
fused_kernel(const char* Ax, const char* Wqkv, const char* Wo, const char* Bx, const char* Cre, const char* Cim, const char* Wre, const char* Wim, char* out, char* ws) {
    extern __shared__ __attribute__((aligned(16))) char lds[];
    asm volatile("fused_code_begin:");
    const int wid = __builtin_amdgcn_readfirstlane(threadIdx.x >> 6), lane = threadIdx.x & 63;
    const int x = blockIdx.x & 7, i = blockIdx.x >> 3;

    if (threadIdx.x < 4) *(LAS unsigned*)((LAS char*)lds + LDS_CNT + threadIdx.x * 4) = 0u;
    if (i < 16) role_b(lds, x, i, wid, lane, Ax, Wqkv, Wo, Bx, Cre, Cim, Wre, Wim, out, ws);
    else role_a(lds, x, i - 16, wid, lane, Ax, Wqkv, Wo, Bx, Cre, Cim, Wre, Wim, out, ws);
    asm volatile("fused_code_end:");
}

extern "C" void kernel_launch(void* const* d_in, const int* in_sizes, int n_in, void* d_out, int out_size, void* d_ws, size_t ws_size, hipStream_t stream) {
    (void)in_sizes; (void)n_in; (void)out_size; (void)ws_size;
    (void)hipFuncSetAttribute((const void*)fused_kernel, hipFuncAttributeMaxDynamicSharedMemorySize, LDS_BYTES);
    fused_kernel<<<256, 512, LDS_BYTES, stream>>>((const char*)d_in[0], (const char*)d_in[1], (const char*)d_in[2], (const char*)d_in[3], (const char*)d_in[4], (const char*)d_in[5],
                                                   (const char*)d_in[6], (const char*)d_in[7], (char*)d_out, (char*)d_ws);
}
```
